```python
import torch

def _softmax(x, axis=-1):
    return torch.softmax(x, dim=axis)

def _gelu(x):
    return torch.nn.functional.gelu(x, approximate='tanh')

def _silu(x):
    return torch.nn.functional.silu(x)

def _sigmoid(x):
    return torch.sigmoid(x)

def _layernorm(x, axis=-1, eps=1e-05):
    m = x.mean(dim=axis, keepdim=True)
    v = x.var(dim=axis, keepdim=True, unbiased=False)
    return (x - m) / torch.sqrt(v + eps)

def _scatter_add(tgt, idx, src, axis=0):
    return tgt.clone().index_add_(axis, idx, src)

def _segment_sum(src, seg, n):
    out = torch.zeros((n,) + src.shape[1:], dtype=src.dtype)
    return out.index_add_(0, seg, src)

def _pad(x, pw):
    flat = tuple((v for pair in reversed(pw) for v in pair))
    return torch.nn.functional.pad(x, flat)
kernel_name = 'interleave_layers__memory_aug_recurrent_mem_tr__linear_kernel_hedgehog__prompt'

def setup_inputs():

    _w = lambda a: (lambda t: t.to(torch.bfloat16) if t.is_floating_point() else t)(torch.as_tensor(a))

    def _A_setup():
        B, S, Dm = (1, 2048, 640)
        H, Dh = (32, 192)
        M, seg = (8, 32)
        seg = min(seg, S)
        S = S // seg * seg
        return {'x': _w((lambda *s: torch.randn(*s, dtype=torch.bfloat16) * 0.05)(B, S, Dm)), 'mem0': _w((lambda *s: torch.randn(*s, dtype=torch.bfloat16) * 0.8)(B, M, Dm)), 'Wq': _w((lambda *s: torch.randn(*s, dtype=torch.bfloat16) * 1.6)(Dm, H, Dh) * Dm ** (-0.5)), 'Wk': _w((lambda *s: torch.randn(*s, dtype=torch.bfloat16) * 1.6)(Dm, H, Dh) * Dm ** (-0.5)), 'Wv': _w((lambda *s: torch.randn(*s, dtype=torch.bfloat16) * 0.8)(Dm, H, Dh) * Dm ** (-0.5)), 'Wo': _w((lambda *s: torch.randn(*s, dtype=torch.bfloat16) * 0.8)(H, Dh, Dm) * (H * Dh) ** (-0.5)), 'seg_len': seg}
    _A_in = _A_setup()

    def _B_setup():
        B, S, D, H = (1, 2048, 640, 32)
        Dh = D // H
        return {'x': _w((lambda *s: torch.randn(*s, dtype=torch.bfloat16) * 0.05)(B, S, D)), 'wqkv': _w((lambda *s: torch.randn(*s, dtype=torch.bfloat16) * 0.4)(D, 3 * D)), 'w_phi': _w((lambda *s: torch.randn(*s, dtype=torch.bfloat16) * 0.4)(H, Dh, Dh) * Dh ** (-0.5)), 'wo': _w((lambda *s: torch.randn(*s, dtype=torch.bfloat16) * 0.05)(D, D))}
    _B_in = _B_setup()
    return {'A_x': _A_in['x'], 'A_mem0': _A_in['mem0'], 'A_Wq': _A_in['Wq'], 'A_Wk': _A_in['Wk'], 'A_Wv': _A_in['Wv'], 'A_Wo': _A_in['Wo'], 'A_seg_len': _A_in['seg_len'], 'B_x': _B_in['x'], 'B_wqkv': _B_in['wqkv'], 'B_w_phi': _B_in['w_phi'], 'B_wo': _B_in['wo']}

def reference(A_x, A_mem0, A_Wq, A_Wk, A_Wv, A_Wo, A_seg_len, B_x, B_wqkv, B_w_phi, B_wo):

    def _A_ref(x, mem0, Wq, Wk, Wv, Wo, seg_len):
        B, S, Dm = x.shape
        H, Dh = (Wq.shape[1], Wq.shape[2])
        M = mem0.shape[1]
        scale = Dh ** (-0.5)
        n_seg = S // seg_len
        L = M + seg_len
        idx = torch.arange(L)
        mask = (idx[:, None] < M) | (idx[:, None] >= idx[None, :])
        mem = mem0
        outs = []
        for s in range(n_seg):
            xs = x[:, s * seg_len:(s + 1) * seg_len, :].clone()
            h = torch.concatenate([mem, xs], axis=1)
            q = torch.einsum('btd,dhe->bhte', h, Wq)
            k = torch.einsum('btd,dhe->bhte', h, Wk)
            v = torch.einsum('btd,dhe->bhte', h, Wv)
            scores = torch.einsum('bhqe,bhke->bhqk', q, k) * scale
            scores = torch.where(mask[None, None], scores, -1000000000.0)
            p = _softmax(scores, axis=-1)
            o = torch.einsum('bhqk,bhke->bhqe', p, v)
            o = torch.einsum('bhte,hed->btd', o, Wo)
            mem = o[:, :M, :].clone()
            outs.append(o[:, M:, :])
        return torch.concatenate(outs, axis=1)

    def _B_ref(x, wqkv, w_phi, wo):
        B, S, D = x.shape
        H = 32
        Dh = D // H
        qkv = (lambda x, *d: x.permute(*d))((x @ wqkv).reshape(B, S, 3, H, Dh), 2, 0, 3, 1, 4)
        q, k, v = (qkv[0], qkv[1], qkv[2])
        pq = torch.einsum('bhsd,hdf->bhsf', q, w_phi)
        pk = torch.einsum('bhsd,hdf->bhsf', k, w_phi)
        phi_q = torch.concatenate([_softmax(pq, axis=-1), _softmax(-pq, axis=-1)], axis=-1)
        phi_k = torch.concatenate([_softmax(pk, axis=-1), _softmax(-pk, axis=-1)], axis=-1)
        kv = torch.einsum('bhsf,bhsd->bhfd', phi_k, v)
        z = torch.einsum('bhsf->bhf', phi_k)
        num = torch.einsum('bhqf,bhfd->bhqd', phi_q, kv)
        den = torch.einsum('bhqf,bhf->bhq', phi_q, z)[..., None] + 1e-06
        o = num / den
        return (lambda x, *d: x.permute(*d))(o, 0, 2, 1, 3).reshape(B, S, D) @ wo
    x = A_x
    x = x + _A_ref(_layernorm(x, axis=-1), A_mem0, A_Wq, A_Wk, A_Wv, A_Wo, A_seg_len)
    x = x + _B_ref(_layernorm(x, axis=-1), B_wqkv, B_w_phi, B_wo)
    x = x + _A_ref(_layernorm(x, axis=-1), A_mem0, A_Wq, A_Wk, A_Wv, A_Wo, A_seg_len)
    x = x + _B_ref(_layernorm(x, axis=-1), B_wqkv, B_w_phi, B_wo)
    return x
```

```cpp
#include <hip/hip_runtime.h>
#include <hip/hip_bf16.h>
#include <stdint.h>
#include <type_traits>

typedef unsigned short u16;
typedef unsigned int u32;
typedef unsigned long long u64;
using bf16x8 = __attribute__((ext_vector_type(8))) short;
using f32x4 = __attribute__((ext_vector_type(4))) float;

constexpr int S_TOK = 2048, DM = 640, NH = 32, DH = 192;
constexpr int MEM = 8, SEG = 32, NSEG = S_TOK / SEG;
constexpr int HD = NH * DH;

__device__ __forceinline__ float bf2f(u16 v) { return __uint_as_float(((u32)v) << 16); }
__device__ __forceinline__ u32 pk2bf(float lo, float hi) {
    typedef __attribute__((ext_vector_type(2))) float f32x2_t;
    typedef __attribute__((ext_vector_type(2))) __bf16 bf16x2_t;
    bf16x2_t r = __builtin_convertvector((f32x2_t){lo, hi}, bf16x2_t);
    return __builtin_bit_cast(u32, r);
}
__device__ __forceinline__ float rbf(float f) { return __uint_as_float(pk2bf(f, f) << 16); }
typedef __attribute__((ext_vector_type(8))) __bf16 bf8;
typedef __attribute__((ext_vector_type(16))) __bf16 bf16v;
typedef __attribute__((ext_vector_type(4))) __bf16 bf4;
typedef __attribute__((ext_vector_type(4))) short s16x4;
using u32x4 = __attribute__((ext_vector_type(4))) u32;
using u32x2 = __attribute__((ext_vector_type(2))) u32;
typedef __attribute__((address_space(1))) u32 gu32;
#define LDS3 __attribute__((address_space(3)))

constexpr int SLOT = 16384;
constexpr int P_SLOT0 = 0, M_SLOT0 = 16, S_SLOT = 48, T_SLOT = 52, N_SLOT = 54;
constexpr size_t HOP_BYTES = (size_t)N_SLOT * SLOT;
constexpr size_t PX_BUF = 4 * 32 * 640, PX_BYTES = 8 * PX_BUF;
constexpr int LA_LDS = 153088 + 512 + 256;
constexpr int SPIN_CAP = 1 << 16;
#define D1I 135
#define D2I 68
#define MG1 10
#define MG2 12
#define ROTC 0
#define DUP 4
#define PACE 6
#define CLAG 50
#define ELATE 30

__device__ __forceinline__ void st16(void* p, u32x4 v) {
    asm volatile("global_store_dwordx4 %0, %1, off sc1\n\ts_nop 2" :: "v"(p), "v"(v) : "memory");
}
typedef __attribute__((address_space(1))) u32x4 gu32x4;
__device__ __forceinline__ u32x4 ld16(const char* base, u32 off, int imm) { return __builtin_amdgcn_global_load_b128((gu32x4*)(base + off + imm), "agent"); }
__device__ __forceinline__ u32 nosent(u32 v) { return min(v, 0xFFFFFFFEu); }
__device__ __forceinline__ u32 rt32() { return (u32)__builtin_amdgcn_s_memrealtime(); }
__device__ __forceinline__ void wait_rt(u32 t) {
    int d = (int)(t - rt32());
    if (d > 4096) return;
    while (d > 10) { if (d > 20) __builtin_amdgcn_s_sleep(2); d = (int)(t - rt32()); }
    asm volatile("s_cmp_lt_i32 %0, 1\n\ts_cbranch_scc1 2f\n1:\n\ts_sub_i32 %0, %0, 1\n\ts_cmp_gt_i32 %0, 0\n\ts_cbranch_scc1 1b\n2:" : "+s"(d) :: "scc");
}
template <int N> __device__ __forceinline__ void dma_kb(const char* src, u32 lds_dst, u32 lane16) {
#pragma unroll
    for (int i = 0; i < N; i += 4) {
        u32 keep;
        if (N - i >= 4)
            asm volatile("s_mov_b32 %0, m0\n\ts_mov_b32 m0, %3\n\ts_nop 0\n\tglobal_load_lds_dwordx4 %1, %2\n\tglobal_load_lds_dwordx4 %1, %2 offset:1024\n\t"
                         "global_load_lds_dwordx4 %1, %2 offset:2048\n\tglobal_load_lds_dwordx4 %1, %2 offset:3072\n\ts_mov_b32 m0, %0"
                         : "=&s"(keep) : "v"(lane16), "s"(src + i * 1024), "s"(lds_dst + i * 1024) : "memory");
        else if (N - i == 2)
            asm volatile("s_mov_b32 %0, m0\n\ts_mov_b32 m0, %3\n\ts_nop 0\n\tglobal_load_lds_dwordx4 %1, %2\n\tglobal_load_lds_dwordx4 %1, %2 offset:1024\n\ts_mov_b32 m0, %0"
                         : "=&s"(keep) : "v"(lane16), "s"(src + i * 1024), "s"(lds_dst + i * 1024) : "memory");
        else
            asm volatile("s_mov_b32 %0, m0\n\ts_mov_b32 m0, %3\n\ts_nop 0\n\tglobal_load_lds_dwordx4 %1, %2\n\ts_mov_b32 m0, %0"
                         : "=&s"(keep) : "v"(lane16), "s"(src + i * 1024), "s"(lds_dst + i * 1024) : "memory");
    }
}
__device__ __forceinline__ void st16s(char* base, u32 off, u32x4 v) {
    asm volatile("global_store_dwordx4 %0, %1, %2 sc1\n\ts_nop 2" :: "v"(off), "v"(v), "s"(base) : "memory");
}
__device__ __forceinline__ void arm16(char* base, u32 off) {
    u32x4 v;
    asm volatile("v_mov_b64 %0, -1\n\tv_mov_b64 %1, -1" : "=v"(((u64*)&v)[0]), "=v"(((u64*)&v)[1]));
    st16s(base, off, v);
}
template <int B, int E, typename F> __device__ __forceinline__ void static_for(F&& f) {
    if constexpr (B < E) { f(std::integral_constant<int, B>()); static_for<B + 1, E>(f); }
}
#define CI(x) decltype(x)::value
#define RAW_BARRIER() do { asm volatile("s_waitcnt lgkmcnt(0)" ::: "memory"); __builtin_amdgcn_s_barrier(); asm volatile("" ::: "memory"); } while (0)
#define VM0() asm volatile("s_waitcnt vmcnt(0)" ::: "memory")
#define SCHEDB() __builtin_amdgcn_sched_barrier(0)
__device__ __forceinline__ bf8 lo8(const bf16v& v) { return __builtin_shufflevector(v, v, 0, 1, 2, 3, 4, 5, 6, 7); }
__device__ __forceinline__ bf8 hi8(const bf16v& v) { return __builtin_shufflevector(v, v, 8, 9, 10, 11, 12, 13, 14, 15); }
__device__ __forceinline__ bf16v cat8(bf8 a, bf8 b) { return __builtin_shufflevector(a, b, 0, 1, 2, 3, 4, 5, 6, 7, 8, 9, 10, 11, 12, 13, 14, 15); }
#define MMA_VV(c, a, b) asm volatile("v_mfma_f32_16x16x32_bf16 %0, %1, %2, %0" : "+v"(c) : "v"(a), "v"(b))
#define MMA_AV(c, a, b) asm volatile("v_mfma_f32_16x16x32_bf16 %0, %1, %2, %0" : "+v"(c) : "a"(a), "v"(b))
#define MMA_VA(c, a, b) asm volatile("v_mfma_f32_16x16x32_bf16 %0, %1, %2, %0" : "+v"(c) : "v"(a), "a"(b))
#define MMA0_VV(c, a, b) asm volatile("v_mfma_f32_16x16x32_bf16 %0, %1, %2, 0" : "=&v"(c) : "v"(a), "v"(b))
#define MMA0_AV(c, a, b) asm volatile("v_mfma_f32_16x16x32_bf16 %0, %1, %2, 0" : "=&v"(c) : "a"(a), "v"(b))
#define MMA0_VA(c, a, b) asm volatile("v_mfma_f32_16x16x32_bf16 %0, %1, %2, 0" : "=&v"(c) : "v"(a), "a"(b))
#define MMA16(c, a, b) asm volatile("v_mfma_f32_16x16x16_bf16 %0, %1, %2, %0" : "+v"(c) : "v"(a), "v"(b))
#define MMA16_0(c, a, b) asm volatile("v_mfma_f32_16x16x16_bf16 %0, %1, %2, 0" : "=&v"(c) : "v"(a), "v"(b))
#define SMM_V(c, a, b, i) asm volatile("v_smfmac_f32_16x16x64_bf16 %0, %1, %2, %3" : "+v"(c) : "v"(a), "v"(b), "v"(i))
#define SMM_A(c, a, b, i) asm volatile("v_smfmac_f32_16x16x64_bf16 %0, %1, %2, %3" : "+v"(c) : "v"(a), "a"(b), "v"(i))
#define NOPV() asm volatile("s_nop 1")
#define PIN(x) asm volatile("" : "+v"(x))
#define SETTLE() asm volatile("s_nop 7\n\ts_nop 3")
__device__ __forceinline__ bf8 pack8(f32x4 a, f32x4 b) {
    u32x4 r = {pk2bf(a[0], a[1]), pk2bf(a[2], a[3]), pk2bf(b[0], b[1]), pk2bf(b[2], b[3])};
    return __builtin_bit_cast(bf8, r);
}
__device__ __forceinline__ u32 max4(u32 m, u32x4 g) { m = max(max(m, g[0]), g[1]); return max(max(m, g[2]), g[3]); }
#define ZERO4(c) asm volatile("v_mov_b64 %0, 0\n\tv_mov_b64 %1, 0" : "=v"(((u64*)&(c))[0]), "=v"(((u64*)&(c))[1]))
__device__ __forceinline__ bf8 lds8(const char* shm, int off) { return *(const bf8*)(shm + off); }
__device__ __forceinline__ bf16v ldfrag(const char* p) { return cat8(*(const bf8*)p, *(const bf8*)(p + 1024)); }
template <int CTRL> __device__ __forceinline__ float fdpp(float x) {
    return __builtin_bit_cast(float, __builtin_amdgcn_mov_dpp(__builtin_bit_cast(int, x), CTRL, 0xf, 0xf, true));
}
__device__ __forceinline__ float halfsum(float a, float b) {
    auto r = __builtin_amdgcn_permlane32_swap(__float_as_uint(a), __float_as_uint(b), false, false);
    return __uint_as_float(r[0]) + __uint_as_float(r[1]);
}
__device__ __forceinline__ void st4_lane0(char* base, u32 v) {
    u32 z, d;
    asm volatile("s_mov_b64 exec, 1\n\tv_mov_b32 %0, 0\n\tv_mov_b32 %1, %3\n\tglobal_store_dword %0, %1, %2 sc1\n\ts_mov_b64 exec, -1" : "=&v"(z), "=&v"(d) : "s"(base), "s"(v) : "memory");
}
__device__ __forceinline__ int track(int d, int sample) {
    if (sample <= 0) return d;
    return sample >= d ? min(sample, d + DUP) : (d - sample > 2 ? d - ((d - sample) >> 1) : d);
}
__device__ __forceinline__ u32 sched_peek(const char* p) { return __hip_atomic_load((const gu32*)p, __ATOMIC_RELAXED, __HIP_MEMORY_SCOPE_AGENT); }
__device__ __forceinline__ u32 sched_get(u32 v, const char* p, int& cap) {
    for (int spin = 0; v == ~0u; ++spin) {
        if (spin >= cap) { cap = 1; break; }
        __builtin_amdgcn_s_sleep(3);
        v = sched_peek(p);
    }
    return __builtin_amdgcn_readfirstlane(v);
}

#define TRDECL
#define TR(i)
#define TRV(i, v)
#define TRFLUSH(role)
#define TRC(i)
struct LaParams {
    const char* wqk; const char* wvo; const char* xn; const u16* xres; u16* x1;
    char* hop; char* px; u32* tick; const u32* flip; u32* dbg;
    int layer, pad;
};
__device__ __forceinline__ char* align2m(char* p) { return p + ((size_t)(-(intptr_t)p) & ((2u << 20) - 1)); }

constexpr int Q_XA = 0, Q_PART = 20480, Q_TR = 48640, Q_WL = 54784, Q_ST = 153088, Q_PS = 44;

#define DPP1(op, ror) op "_dpp %0, %0, %0 row_ror:" ror " row_mask:0xf bank_mask:0xf\n\t"
#define DPP2(op, ror) DPP1(op, ror) op "_dpp %1, %1, %1 row_ror:" ror " row_mask:0xf bank_mask:0xf\n\ts_nop 0\n\t"
__device__ __forceinline__ void st16m(char* base, u32 off, u32x4 v, u64 mask) {
    asm volatile("s_mov_b64 exec, %3\n\tglobal_store_dwordx4 %0, %1, %2 sc1\n\ts_mov_b64 exec, -1\n\ts_nop 1" :: "v"(off), "v"(v), "s"(base), "s"(mask) : "memory");
}
__device__ __forceinline__ u32 pk2bf_abs(float lo, float hi) {
    u32 r;
    asm("v_cvt_pk_bf16_f32 %0, |%1|, |%2|" : "=v"(r) : "v"(lo), "v"(hi));
    return r;
}
constexpr float SM_C = 0.07216878364870322f * 1.4426950408889634f, NEG_BIG = -0x1p100f;
constexpr u64 SM_MASK = 0x0155015501550155ull;
__device__ __forceinline__ void softmax_mem(const float* p, char* dst, const u32 off) {
    f32x4 a[4];
#pragma unroll
    for (int i = 0; i < 4; ++i) a[i] = *(const f32x4*)(p + i * 40 * Q_PS);
    f32x4 x = (a[0] + a[1]) + (a[2] + a[3]);
    const float ml = fmaxf(fmaxf(x[0], x[1]), fmaxf(x[2], x[3])) * SM_C;
    float mx = ml, e0, e1, e2, e3;
    asm("v_fma_f32 %1, %5, %9, -%0\n\tv_fma_f32 %2, %6, %9, -%0\n\tv_fma_f32 %3, %7, %9, -%0\n\tv_fma_f32 %4, %8, %9, -%0\n\t"
        DPP1("v_max_f32", "8") "v_exp_f32 %1, %1\n\ts_nop 0\n\t" DPP1("v_max_f32", "4") "v_exp_f32 %2, %2\n\ts_nop 0\n\t"
        DPP1("v_max_f32", "2") "v_exp_f32 %3, %3\n\ts_nop 0\n\t" DPP1("v_max_f32", "1") "v_exp_f32 %4, %4\n\ts_nop 0"
        : "+v"(mx), "=&v"(e0), "=&v"(e1), "=&v"(e2), "=&v"(e3) : "v"(x[0]), "v"(x[1]), "v"(x[2]), "v"(x[3]), "s"(SM_C));
    const float f = __builtin_amdgcn_exp2f(ml - mx);
    float sm = ((e0 + e1) + (e2 + e3)) * f;
    asm("s_nop 1\n\t" DPP1("v_add_f32", "8") "v_mul_f32 %1, %1, %5\n\ts_nop 0\n\t" DPP1("v_add_f32", "4") "v_mul_f32 %2, %2, %5\n\ts_nop 0\n\t"
        DPP1("v_add_f32", "2") "v_mul_f32 %3, %3, %5\n\ts_nop 0\n\t" DPP1("v_add_f32", "1") "v_mul_f32 %4, %4, %5"
        : "+v"(sm), "+v"(e0), "+v"(e1), "+v"(e2), "+v"(e3) : "v"(f));
    const float inv = __builtin_amdgcn_rcpf(sm);
    const u32 d0 = pk2bf_abs(e0 * inv, e1 * inv), d1 = pk2bf_abs(e2 * inv, e3 * inv);
    u32 n0, n1;
    asm("s_nop 0\n\tv_mov_b32_dpp %0, %2 quad_perm:[1,0,3,2] row_mask:0xf bank_mask:0xf\n\tv_mov_b32_dpp %1, %3 quad_perm:[1,0,3,2] row_mask:0xf bank_mask:0xf"
        : "=&v"(n0), "=&v"(n1) : "v"(d0), "v"(d1));
    st16m(dst, off, (u32x4){d0, d1, n0, n1}, SM_MASK);
}
__device__ __forceinline__ void softmax_tok(const char* shm, const int q0, int lane, char* dst) {
    asm volatile("" : "+v"(lane));
    const int r = lane >> 4, j = lane & 15;
    const float* p = (const float*)(shm + Q_PART) + (q0 + r) * Q_PS + min(j, 10) * 4;
    f32x4 x[2];
    float mx[2], sm[2];
#pragma unroll
    for (int n = 0; n < 2; ++n) {
        f32x4 a[4];
#pragma unroll
        for (int i = 0; i < 4; ++i) a[i] = *(const f32x4*)(p + (i * 40 + n * 4) * Q_PS);
        x[n] = (a[0] + a[1]) + (a[2] + a[3]);
        mx[n] = fmaxf(fmaxf(x[n][0], x[n][1]), fmaxf(x[n][2], x[n][3])) * SM_C;
    }
    asm("s_nop 1\n\t" DPP2("v_max_f32", "8") DPP2("v_max_f32", "4") DPP2("v_max_f32", "2") DPP2("v_max_f32", "1") : "+v"(mx[0]), "+v"(mx[1]));
#pragma unroll
    for (int n = 0; n < 2; ++n) {
#pragma unroll
        for (int i = 0; i < 4; ++i) x[n][i] = __builtin_amdgcn_exp2f(fmaf(x[n][i], SM_C, -mx[n]));
        sm[n] = (x[n][0] + x[n][1]) + (x[n][2] + x[n][3]);
    }
    asm("s_nop 1\n\t" DPP2("v_add_f32", "8") DPP2("v_add_f32", "4") DPP2("v_add_f32", "2") DPP2("v_add_f32", "1") : "+v"(sm[0]), "+v"(sm[1]));
#pragma unroll
    for (int n = 0; n < 2; ++n) {
        const float inv = __builtin_amdgcn_rcpf(sm[n]);
        const u32 d0 = pk2bf_abs(x[n][0] * inv, x[n][1] * inv), d1 = pk2bf_abs(x[n][2] * inv, x[n][3] * inv);
        u32 n0, n1;
        asm("s_nop 1\n\tv_mov_b32_dpp %0, %2 quad_perm:[1,0,3,2] row_mask:0xf bank_mask:0xf\n\tv_mov_b32_dpp %1, %3 quad_perm:[1,0,3,2] row_mask:0xf bank_mask:0xf"
            : "=&v"(n0), "=&v"(n1) : "v"(d0), "v"(d1));
        st16m(dst, (j < 8 ? n * 256 + j * 32 : 512 + n * 64) + r * 16, (u32x4){d0, d1, n0, n1}, SM_MASK);
    }
}

__host__ __device__ constexpr int qk_slot(int T, int ks) { return ks < 5 ? ks * 6 + T : 30 + (T >> 1) * 10 + (ks - 5) * 2 + (T & 1); }
__host__ __device__ constexpr int lds_slot(int n) { return n < 7 ? 5 * n + 2 : (n == 7 ? 38 : (n == 8 ? 41 : (n == 9 ? 46 : (n == 10 ? 51 : 55)))); }
__host__ __device__ constexpr int slot_lds(int sl) { for (int n = 0; n < 12; ++n) if (lds_slot(n) == sl) return n; return -1; }
__host__ __device__ constexpr int slot_frag(int sl) { if (slot_lds(sl) >= 0) return 48 + slot_lds(sl); int n = 0; for (int i = 0; i < 12; ++i) n += lds_slot(i) < sl; return sl - n; }
__host__ __device__ constexpr int qk_frag(int T, int ks) { return slot_frag(qk_slot(T, ks)); }
__host__ __device__ constexpr int slot_T(int sl) { return sl < 30 ? sl % 6 : ((sl - 30) / 10) * 2 + (sl & 1); }
__host__ __device__ constexpr int slot_ks(int sl) { return sl < 30 ? sl / 6 : 5 + ((sl - 30) % 10) / 2; }
__host__ __device__ constexpr int lds_rank(int T, int ks) { int n = 0; for (int t = 0; t < T; ++t) n += qk_frag(t, ks) >= 48; return n; }
__host__ __device__ constexpr int lds_nth(int ks, int n) { for (int t = 0; t < 6; ++t) if (qk_frag(t, ks) >= 48 && lds_rank(t, ks) == n) return qk_frag(t, ks) - 48; return -1; }

__device__ __forceinline__ void qk_role(const LaParams& p, const int set, const int h, const int phys, char* shm) {
    const int lane = threadIdx.x & 63, w = __builtin_amdgcn_readfirstlane(threadIdx.x >> 6);
    const int fr = lane & 15, g = lane >> 4;
    const u32 lds0 = (u32)(uintptr_t)(LDS3 char*)shm;
    const int sidx = (lane & 8) ? 0xEEEE : 0x4444;
    int cap = SPIN_CAP;
    char* mine = p.hop + phys * 8192;
    const char* wl = shm + Q_WL + w * 24576 + lane * 16;
    const char* xa = shm + Q_XA + lane * 16;

    bf16v WA[32], WV[16];
    {
        const char* wb = p.wqk + ((size_t)(h * 4 + w) * 60) * 2048 + lane * 16;
        dma_kb<24>(p.wqk + ((size_t)(h * 4 + w) * 60 + 48) * 2048, lds0 + Q_WL + w * 24576, lane * 16);
        dma_kb<5>(p.xn + (size_t)set * 40960 + w * 5120, lds0 + Q_XA + w * 5120, lane * 16);
#pragma unroll
        for (int i = 0; i < 32; ++i) WA[i] = ldfrag(wb + i * 2048);
#pragma unroll
        for (int i = 0; i < 16; ++i) WV[i] = ldfrag(wb + (32 + i) * 2048);
    }
    TRDECL;
    bf8 QX[2], KX[2];
    u32x2 QY[2], KY[2];
    u32 peek = ~0u;
    int d1 = D1I, d2 = D2I;
    if (w == 3) *(u64*)(shm + Q_ST + lane * 8) = 0;
    auto xproj = [&](const int snext, const int sprev) {
        f32x4 acc[6][2];
        TR(8);
        const char* xs1 = p.xn + (size_t)(snext - 2) * 40960 + 20480 + w * 5120;
        const char* xs0 = p.xn + (size_t)min(snext, NSEG - 1) * 40960 + w * 5120;
        u32 xl = lds0 + Q_XA + w * 5120;
        asm volatile("" : "+s"(xs0), "+s"(xs1), "+s"(xl));
        bf8 x0 = *(const bf8*)(xa), x1 = *(const bf8*)(xa + 1024), lw[2];
        lw[0] = *(const bf8*)(wl);
        static_for<0, 20>([&](auto K) {
            constexpr int k = CI(K), l0 = lds_nth(((k + 1) >> 1) % 10, 0), l1 = lds_nth(((k + 1) >> 1) % 10, 1);
            bf8 n0, n1, nw[2];
            if constexpr (k == 9) {
                TR(9);
                VM0(); RAW_BARRIER();
                TR(10);
            }
            static_for<0, 6>([&](auto TT) {
                constexpr int T = CI(TT), id = qk_frag(T, k >> 1), rk = lds_rank(T, k >> 1);
                if constexpr (k < 19) {
                    if constexpr (T == 0) n0 = *(const bf8*)(xa + (k + 1) * 2048);
                    if constexpr (T == 1) n1 = *(const bf8*)(xa + (k + 1) * 2048 + 1024);
                    if constexpr (T == 2 && l0 >= 0) nw[0] = *(const bf8*)(wl + l0 * 2048 + ((k + 1) & 1) * 1024);
                    if constexpr (T == 3 && l1 >= 0) nw[1] = *(const bf8*)(wl + l1 * 2048 + ((k + 1) & 1) * 1024);
                }
                if constexpr (k < 3 && (T == 3 || T == 5) && 2 * k + (T == 5) < 5)
                    dma_kb<1>(xs1 + (2 * k + (T == 5)) * 1024, xl + 20480 + (2 * k + (T == 5)) * 1024, lane * 16);
                if constexpr (k >= 9 && k < 14 && T == 4)
                    dma_kb<1>(xs0 + (k - 9) * 1024, xl + (k - 9) * 1024, lane * 16);
                if constexpr (k == 18 && T == 4) peek = sched_peek(mine + (S_SLOT + w) * SLOT + max(sprev, 0) * 128 + 4);
                SCHEDB();
                if constexpr (id < 32) {
                    const bf8 wf = (k & 1) ? hi8(WA[id]) : lo8(WA[id]);
                    if constexpr (k == 0) { MMA0_AV(acc[T][0], wf, x0); MMA0_AV(acc[T][1], wf, x1); } else { MMA_AV(acc[T][0], wf, x0); MMA_AV(acc[T][1], wf, x1); }
                } else {
                    bf8 wf;
                    if constexpr (id < 48) wf = (k & 1) ? hi8(WV[id - 32]) : lo8(WV[id - 32]); else wf = lw[rk];
                    if constexpr (k == 0) { MMA0_VV(acc[T][0], wf, x0); MMA0_VV(acc[T][1], wf, x1); } else { MMA_VV(acc[T][0], wf, x0); MMA_VV(acc[T][1], wf, x1); }
                }
                SCHEDB();
            });
            if constexpr (k < 19) { x0 = n0; x1 = n1; if constexpr (l0 >= 0) lw[0] = nw[0]; if constexpr (l1 >= 0) lw[1] = nw[1]; }
        });
        TR(11);
        int ln = lane;
        asm volatile("" : "+v"(ln));
        f32x4 s[3];
#pragma unroll
        for (int i = 0; i < 4; ++i) s[0][i] = (ln >> 4) * 4 + i > (ln & 15) ? NEG_BIG : 0.f;
        s[2] = s[0];
        float* pp = (float*)(shm + Q_PART) + (w * 40 + 8 + (ln & 15)) * Q_PS + (ln >> 4) * 8;
        PIN(s[0]); PIN(s[2]);
        RAW_BARRIER();
#pragma unroll
        for (int T = 0; T < 6; ++T) { PIN(acc[T][0]); PIN(acc[T][1]); }
#pragma unroll
        for (int tt = 0; tt < 2; ++tt) { QX[tt] = pack8(acc[0][tt], acc[2][tt]); KX[tt] = pack8(acc[1][tt], acc[3][tt]); PIN(QX[tt]); PIN(KX[tt]); }
        NOPV();
        MMA_VV(s[0], KX[0], QX[0]); MMA0_VV(s[1], KX[0], QX[1]); MMA_VV(s[2], KX[1], QX[1]);
#pragma unroll
        for (int tt = 0; tt < 2; ++tt) {
            PIN(acc[4][tt]); PIN(acc[5][tt]);
            QY[tt] = (u32x2){pk2bf(acc[4][tt][0], acc[4][tt][1]), pk2bf(acc[4][tt][2], acc[4][tt][3])};
            KY[tt] = (u32x2){pk2bf(acc[5][tt][0], acc[5][tt][1]), pk2bf(acc[5][tt][2], acc[5][tt][3])};
            PIN(QY[tt]); PIN(KY[tt]);
        }
        NOPV();
        MMA16(s[0], KY[0], QY[0]); MMA16(s[1], KY[0], QY[1]); MMA16(s[2], KY[1], QY[1]);
        asm volatile("s_nop 7");
        PIN(s[0]); PIN(s[1]); PIN(s[2]);
        *(f32x4*)(pp) = s[0];
        *(f32x4*)(pp + 16 * Q_PS) = s[1];
        *(f32x4*)(pp + 16 * Q_PS + 4) = s[2];
    };
    VM0(); RAW_BARRIER();
    xproj(set + 2, set - 1);
    {
        const f32x4 neg = {NEG_BIG, NEG_BIG, NEG_BIG, NEG_BIG};
        *(f32x4*)((float*)(shm + Q_PART) + (w * 40 + 8 + fr) * Q_PS + g * 8 + 4) = neg;
        if (lane < 40) *(f32x4*)((float*)(shm + Q_PART) + (w * 40 + lane) * Q_PS + 40) = neg;
    }
    float* pw0 = (float*)(shm + Q_PART) + (w * 40 + (fr & 7)) * Q_PS + g * 8 + (fr >> 3) * 4;
    float* pw2 = fr < 8 && g >= 2 ? (float*)(shm + Q_PART) + (w * 40 + fr) * Q_PS + 24 + g * 4 : (float*)(shm + Q_TR + w * 1536) + lane * 4;
    const float* prd = (const float*)(shm + Q_PART) + ((w & 1) * 4 + g) * Q_PS + min(fr, 10) * 4;
    u32 trw = lds0 + Q_TR + w * 1536 + lane * 8, trr = lds0 + Q_TR + w * 1536 + (fr & 3) * 128 + (4 * g + (fr >> 2)) * 8;
    asm volatile("" : "+v"(trw), "+v"(trr));
    const u32 poff = (fr < 8 ? (w & 1) * 256 + fr * 32 : 512 + (w & 1) * 64) + g * 16;

    for (int s = set; s < NSEG; s += 2) {
        const int rot = (s >> 1) & 3;
        TR(0);
        u32 ytime = 0;
        const char* mb0 = mine + (M_SLOT0 + rot * 8 + 2 * w) * SLOT + 2048;
        const char* mb1 = mb0 + SLOT;
        char* pdst = mine + (P_SLOT0 + rot * 4 + (h >> 3)) * SLOT + (h & 7) * 640;
        asm volatile("" : "+s"(mb0), "+s"(mb1), "+s"(pdst));
        if (s > 0) { ytime = sched_get(peek, mine + (S_SLOT + w) * SLOT + (s - 1) * 128 + 4, cap); wait_rt(ytime); }
        TR(1); TRV(7, ytime);
        f32x4 acc[6];
        u32x4 A[10];
        bf8 Ll[2], Lh[2];
        s16x4 F[3];
        u32 r0[4], r1[4], d[2];
        float v[4];
        int tries = 0;
        auto fin = [&](const int pr, const int i) {
            if (i == 0) { PIN(acc[2 * pr]); PIN(acc[2 * pr + 1]); }
            if (i < 4) { auto r = __builtin_amdgcn_permlane32_swap(__float_as_uint(acc[2 * pr][i]), __float_as_uint(acc[2 * pr + 1][i]), false, false); r0[i] = r[0]; r1[i] = r[1]; }
            if (i >= 1 && i < 5) { asm("v_add_f32 %0, %1, %2" : "=v"(v[i - 1]) : "v"(r0[i - 1]), "v"(r1[i - 1])); }
            if (i == 4) d[0] = pk2bf(v[0], v[1]);
            if (i == 5) {
                d[1] = pk2bf(v[2], v[3]);
                *(LDS3 u32x2*)(uintptr_t)(trw + pr * 512) = (u32x2){d[0], d[1]};
                F[pr] = __builtin_amdgcn_ds_read_tr16_b64_v4i16((LDS3 s16x4*)(uintptr_t)(trr + pr * 512));
            }
        };
        auto slot = [&](auto SL) {
            constexpr int sl = CI(SL), T = slot_T(sl), ks = slot_ks(sl), id = slot_frag(sl);
            if constexpr (id < 32) SMM_A(acc[T], A[ks], WA[id], sidx);
            else if constexpr (id < 48) SMM_V(acc[T], A[ks], WV[id - 32], sidx);
            else { const bf16v L = cat8(Ll[id & 1], Lh[id & 1]); SMM_V(acc[T], A[ks], L, sidx); }
            SCHEDB();
            if constexpr (sl == 0) TRC(6);
            constexpr int n0 = slot_lds(sl), n1 = slot_lds(sl - 1);
            if constexpr (n0 >= 0 && n0 < 10) Ll[n0 & 1] = *(const bf8*)(wl + (n0 + 2) * 2048);
            if constexpr (n1 >= 0 && n1 < 10) Lh[n1 & 1] = *(const bf8*)(wl + (n1 + 2) * 2048 + 1024);
            if constexpr (sl >= 43 && sl < 49) fin(0, sl - 43);
            if constexpr (sl >= 53 && sl < 59) fin(1, sl - 53);
            SCHEDB();
        };
        auto gather = [&]() {
            u32 vo = lane * 16;
            asm volatile("" : "+v"(vo) :: "memory");
#pragma unroll
            for (int ks = 0; ks < 10; ++ks) {
                A[ks] = ld16(ks < 5 ? mb0 : mb1, vo, (ks % 5 - 2) * 1024);
                SCHEDB();
                if (ks < 6) ZERO4(acc[ks]);
                if (ks == 6 || ks == 7) { Ll[ks & 1] = *(const bf8*)(wl + (ks & 1) * 2048); Lh[ks & 1] = *(const bf8*)(wl + (ks & 1) * 2048 + 1024); }
                if (ks < 9) asm volatile("s_nop %0" :: "n"(PACE));
                SCHEDB();
            }
        };
        gather();
        static_for<0, 49>(slot);
        if (__builtin_expect(__any(__builtin_isunordered(v[0], v[1]) || __builtin_isunordered(v[2], v[3])), 0)) {
            SETTLE();
            while (true) {
                gather();
                u32 m = 0;
#pragma unroll
                for (int ks = 0; ks < 10; ++ks) m = max4(m, A[ks]);
                ++tries;
                if (!__any(m == ~0u)) break;
                if (tries >= cap) { cap = 1; break; }
                __builtin_amdgcn_s_sleep(4);
            }
            static_for<0, 49>(slot);
        }
        TR(2); TRV(6, tries + 1);
        static_for<49, 51>(slot);
        if (__builtin_expect(h == 0 && w == 3, 0)) {
            u32 base = (tries == 0 && s > 0) ? ytime : rt32() + ELATE;
            u32 ln8 = lane;
            asm volatile("" : "+v"(ln8));
            ln8 *= 8192;
            const u32x2 rec = {nosent(base + d1 + MG1), nosent(base + d1 + MG1 + d2 + MG2)};
            asm volatile("s_mov_b64 exec, 0xff\n\tglobal_store_dwordx2 %0, %1, %2 sc1\n\ts_mov_b64 exec, -1" :: "v"(ln8), "v"(rec), "s"(p.hop + S_SLOT * SLOT + s * 128) : "memory");
        }
        static_for<51, 60>(slot);
        TRC(7);
        bf8 F01 = __builtin_bit_cast(bf8, __builtin_shufflevector(F[0], F[1], 0, 1, 2, 3, 4, 5, 6, 7));
        f32x4 s0, s1, s2, u0, u1;
        asm volatile("s_nop 6");
#pragma unroll
        for (int i = 0; i < 6; ++i) fin(2, i);
        PIN(F01); NOPV();
        MMA0_VV(s1, KX[1], F01); MMA0_VV(s0, KX[0], F01); MMA0_VV(s2, F01, F01); MMA0_VV(u0, F01, QX[0]); MMA0_VV(u1, F01, QX[1]);
        asm volatile("s_nop 3");
        MMA16(s1, KY[1], F[2]); MMA16(s0, KY[0], F[2]); MMA16(s2, F[2], F[2]); MMA16(u0, F[2], QY[0]); MMA16(u1, F[2], QY[1]);
        asm volatile("s_nop 4\n\tv_mov_b32_dpp %0, %4 row_ror:8 row_mask:0xf bank_mask:0xc\n\tv_mov_b32_dpp %1, %5 row_ror:8 row_mask:0xf bank_mask:0xc\n\t"
                     "v_mov_b32_dpp %2, %6 row_ror:8 row_mask:0xf bank_mask:0xc\n\tv_mov_b32_dpp %3, %7 row_ror:8 row_mask:0xf bank_mask:0xc"
                     : "+v"(s0[0]), "+v"(s0[1]), "+v"(s0[2]), "+v"(s0[3]) : "v"(s1[0]), "v"(s1[1]), "v"(s1[2]), "v"(s1[3]));
        PIN(s2); *(f32x4*)pw2 = s2; SCHEDB();
        *(f32x4*)pw0 = s0; SCHEDB();
        if (__builtin_expect(w < 2, 1)) {
            RAW_BARRIER();
            TR(3);
            softmax_mem(prd, pdst, poff);
            TR(4);
            if (w == 1) st4_lane0(mine + (T_SLOT + 1) * SLOT + (16 + h) * 128, tries == 0 && s > 0 ? rt32() - ytime : 0);
        } else {
            RAW_BARRIER();
            TR(3);
            if (h == 0 && w == 3) {
                u32 lo = lane;
                asm volatile("" : "+v"(lo));
                int sta = *(const int*)(shm + Q_ST + lo * 4), stb = *(const int*)(shm + Q_ST + 256 + lo * 4);
                asm("s_nop 1\n\t" DPP2("v_max_i32", "8") DPP2("v_max_i32", "4") DPP2("v_max_i32", "2") DPP2("v_max_i32", "1") : "+v"(sta), "+v"(stb));
                const int m1 = max(__builtin_amdgcn_readlane(stb, 16), __builtin_amdgcn_readlane(stb, 32));
                const int m2 = max(max(max(__builtin_amdgcn_readlane(sta, 0), __builtin_amdgcn_readlane(sta, 16)), max(__builtin_amdgcn_readlane(sta, 32), __builtin_amdgcn_readlane(sta, 48))),
                                   __builtin_amdgcn_readlane(stb, 0));
                d1 = track(d1, m1); d2 = track(d2, m2);
                u32 keep;
                asm volatile("s_mov_b32 %0, m0\n\ts_mov_b32 m0, %4\n\ts_nop 0\n\tglobal_load_lds_dword %1, %2 sc1\n\ts_add_u32 m0, m0, 256\n\ts_nop 0\n\tglobal_load_lds_dword %1, %3 sc1\n\ts_mov_b32 m0, %0"
                             : "=&s"(keep) : "v"(lo * 128), "s"(mine + T_SLOT * SLOT), "s"(mine + (T_SLOT + 1) * SLOT), "s"(lds0 + Q_ST), "s"(d1), "s"(d2) : "memory", "scc");
            }
            if (w == 2 && s >= 2 && lane < 40) arm16(mine + (P_SLOT0 + ((rot + 3) & 3) * 4 + (h >> 3)) * SLOT + (h & 7) * 640, lane * 16);
            if (s >= 4 && lane < 40) {
                char* o = p.px + (size_t)(set * 4 + ((rot + 2) & 3)) * PX_BUF + ((w - 2) * 64 + h) * 640;
                arm16(o, lane * 16); arm16(o + 32 * 640, lane * 16);
            }
        }
        {
            int ln = lane;
            asm volatile("" : "+v"(ln));
            PIN(u0); PIN(u1);
            f32x4 u;
#pragma unroll
            for (int i = 0; i < 4; ++i) u[i] = __uint_as_float(__builtin_amdgcn_permlane32_swap(__float_as_uint(u0[i]), __float_as_uint(u1[i]), false, false)[1]);
            *(f32x4*)((float*)(shm + Q_PART) + (w * 40 + 8 + (ln >> 5) * 16 + (ln & 15)) * Q_PS + 32 + ((ln >> 4) & 1) * 4) = u;
        }
        RAW_BARRIER();
        softmax_tok(shm, 8 + 8 * w, lane, p.px + (size_t)(set * 4 + rot) * PX_BUF + (w * 32 + h) * 640);
        TR(5); TRFLUSH(set * 128 + h);
        if (s + 2 >= NSEG) break;
        RAW_BARRIER();
        xproj(s + 4, s + 1);
    }
}

constexpr int C_YL = 0, C_RED = 20480, C_EX = 28672, C_XN = 32768;
__device__ __forceinline__ void comb_role(const LaParams& p, const int set, const int c, const int phys, char* shm) {
    const int lane = threadIdx.x & 63, w = __builtin_amdgcn_readfirstlane(threadIdx.x >> 6);
    const int fr = lane & 15, g = lane >> 4;
    const u32 lds0 = (u32)(uintptr_t)(LDS3 char*)shm;
    const int sidx = (lane & 8) ? 0xEEEE : 0x4444;
    int cap = SPIN_CAP;
    char* mine = p.hop + phys * 8192;
    char* other = p.hop + (phys ^ 1) * 8192;
    char* yl = shm + C_YL + lane * 16;

    bf16v WA[32], WV[8];
    {
        const char* wb = p.wvo + ((size_t)(c * 4 + w) * 40) * 2048 + lane * 16;
        dma_kb<10>(p.xn + (size_t)set * 40960 + w * 10240, lds0 + C_XN + w * 10240, lane * 16);
#pragma unroll
        for (int i = 0; i < 32; ++i) WA[i] = ldfrag(wb + i * 2048);
#pragma unroll
        for (int i = 0; i < 8; ++i) WV[i] = ldfrag(wb + (32 + i) * 2048);
    }
    auto yx = [&](bf8 (&yn)[4], const int sy, auto&& extra) {
        f32x4 acc[4][2];
        const char* xa = shm + C_XN + ((sy >> 1) & 1) * 40960 + lane * 16;
        const char* xs = p.xn + (size_t)min(sy + 2, NSEG - 1) * 40960 + w * 10240;
        u32 xl = lds0 + C_XN + (~(sy >> 1) & 1) * 40960 + w * 10240;
        asm volatile("" : "+s"(xs), "+s"(xl));
        bf8 x0 = *(const bf8*)(xa), x1 = *(const bf8*)(xa + 1024);
#pragma unroll
        for (int k = 0; k < 20; ++k) {
            bf8 n0, n1;
#pragma unroll
            for (int t = 0; t < 4; ++t) {
                if (k < 19 && t == 0) n0 = *(const bf8*)(xa + (k + 1) * 2048);
                if (k < 19 && t == 1) n1 = *(const bf8*)(xa + (k + 1) * 2048 + 1024);
                if (t >= 2 && k < 5) dma_kb<1>(xs + (2 * k + t - 2) * 1024, xl + (2 * k + t - 2) * 1024, lane * 16);
                if (t >= 2 && k >= 5 && k < 15) extra(2 * k + t - 12);
                SCHEDB();
                const int id = (k >> 1) * 4 + t;
                if (id < 32) {
                    const bf8 wf = (k & 1) ? hi8(WA[id & 31]) : lo8(WA[id & 31]);
                    if (k == 0) { MMA0_VA(acc[t][0], x0, wf); MMA0_VA(acc[t][1], x1, wf); } else { MMA_VA(acc[t][0], x0, wf); MMA_VA(acc[t][1], x1, wf); }
                } else {
                    const bf8 wf = (k & 1) ? hi8(WV[id & 7]) : lo8(WV[id & 7]);
                    MMA_VV(acc[t][0], x0, wf); MMA_VV(acc[t][1], x1, wf);
                }
                SCHEDB();
            }
            if (k < 19) { x0 = n0; x1 = n1; }
        }
        SETTLE();
#pragma unroll
        for (int t = 0; t < 4; ++t) { PIN(acc[t][0]); PIN(acc[t][1]); yn[t] = pack8(acc[t][0], acc[t][1]); }
    };
    VM0(); RAW_BARRIER();
    {
        bf8 yn[4];
        yx(yn, set, [](int) {});
#pragma unroll
        for (int t = 0; t < 4; ++t) *(bf8*)(yl + (w * 5 + t) * 1024) = yn[t];
    }
    const u32 pofs = (fr >> 3) * 640 + ((fr >> 2) & 1) * 256 + g * 64 + (fr & 3) * 16;
    const u32 mofs = (2 * g + (fr >> 3)) * 640 + 512 + (fr & 7) * 16;
    u32 peek = ~0u;

    for (int s = set; s < NSEG; s += 2) {
        const int rot = (s >> 1) & 3;
        TRDECL; TR(0);
        const char* mb0 = mine + (M_SLOT0 + rot * 8 + 2 * w) * SLOT + 2048;
        const char* mb1 = mb0 + SLOT;
        asm volatile("" : "+s"(mb0), "+s"(mb1));
        if (s > 0) wait_rt(sched_get(peek, mine + (S_SLOT + w) * SLOT + (s - 1) * 128 + 4, cap) + CLAG);
        TR(1);
        {
            f32x4 acc[4];
            u32x4 A[10];
            auto gather = [&]() {
                u32 vo = lane * 16;
                asm volatile("" : "+v"(vo) :: "memory");
#pragma unroll
                for (int ks = 0; ks < 10; ++ks) {
                    A[ks] = ld16(ks < 5 ? mb0 : mb1, vo, (ks % 5 - 2) * 1024);
                    SCHEDB();
                    if (ks < 4) ZERO4(acc[ks]);
                    if (ks < 9) asm volatile("s_nop %0" :: "n"(PACE));
                    SCHEDB();
                }
            };
            auto mproj = [&]() {
#pragma unroll
                for (int ks = 0; ks < 10; ++ks)
#pragma unroll
                    for (int t = 0; t < 4; ++t) { if (ks < 8) SMM_A(acc[t], A[ks], WA[(ks * 4 + t) & 31], sidx); else SMM_V(acc[t], A[ks], WV[(ks * 4 + t) & 7], sidx); }
                asm volatile("s_nop 7");
#pragma unroll
                for (int t = 0; t < 4; ++t) PIN(acc[t]);
            };
            gather();
            mproj();
            if (__builtin_expect(__any(__builtin_isunordered(acc[0][0], acc[0][1]) || __builtin_isunordered(acc[0][2], acc[0][3])), 0)) {
                for (int tries = 1;; ++tries) {
                    gather();
                    u32 m = 0;
#pragma unroll
                    for (int ks = 0; ks < 10; ++ks) m = max4(m, A[ks]);
                    if (!__any(m == ~0u)) break;
                    if (tries >= cap) { cap = 1; break; }
                    __builtin_amdgcn_s_sleep(8);
                }
                mproj();
            }
            peek = sched_peek(mine + (S_SLOT + w) * SLOT + s * 128);
#pragma unroll
            for (int pr = 0; pr < 2; ++pr) {
                float v[4];
#pragma unroll
                for (int i = 0; i < 4; ++i) v[i] = halfsum(acc[2 * pr][i], acc[2 * pr + 1][i]);
                *(u32x2*)(shm + C_YL + (w * 5 + 4) * 1024 + ((2 * pr + (g >> 1)) * 16 + fr) * 16 + (g & 1) * 8) = (u32x2){pk2bf(v[0], v[1]), pk2bf(v[2], v[3])};
            }
        }
        RAW_BARRIER();
        TR(2);
        if (s < NSEG - 1) {
            const char* pb[4];
#pragma unroll
            for (int i = 0; i < 4; ++i) { pb[i] = mine + (P_SLOT0 + rot * 4 + ((i + c * ROTC) & 3)) * SLOT; asm volatile("" : "+s"(pb[i])); }
            auto ldy = [&](const int f) { return *(const bf8*)(yl + (((f / 5 + c * ROTC) & 3) * 5 + f % 5) * 1024); };
            auto ldp = [&](const int f, const u32 vo, const u32 vm) { return f % 5 == 4 ? ld16(pb[f / 5], vm, 0) : ld16(pb[f / 5], vo, (f % 5) * 1280); };
            u32 xt;
            auto partial = [&](auto NF, auto F0, f32x4& acc, auto&& late) {
                constexpr int nf = CI(NF), f0 = CI(F0);
                bf8 Y[nf];
#pragma unroll
                for (int i = 0; i < nf; ++i) Y[i] = ldy(f0 + i);
#pragma unroll
                for (int i = 0; i < nf; ++i) PIN(Y[i]);
                u32 vo = pofs, vm = mofs;
                asm volatile("" : "+v"(vo), "+v"(vm));
                xt = sched_get(peek, mine + (S_SLOT + w) * SLOT + s * 128, cap);
                TR(3);
                wait_rt(xt);
                TRV(7, xt); TRC(6);
                asm volatile("" ::: "memory");
                u32x4 P[nf];
#pragma unroll
                for (int i = 0; i < nf; ++i) { P[i] = ldp(f0 + i, vo, vm); SCHEDB(); }
#pragma unroll
                for (int i = 0; i < nf; ++i) {
                    if (i == nf - 2) late();
                    if (i == 0) MMA0_VV(acc, Y[i], P[i]); else MMA_VV(acc, Y[i], P[i]);
                    if (i == 0) TRC(7);
                    SCHEDB();
                }
                TRC(8);
            };
            f32x4 acc;
            if (w > 0) {
                if (w == 1) partial(std::integral_constant<int, 3>(), std::integral_constant<int, 11>(), acc, [] {});
                else if (w == 2) partial(std::integral_constant<int, 3>(), std::integral_constant<int, 14>(), acc, [] {});
                else partial(std::integral_constant<int, 3>(), std::integral_constant<int, 17>(), acc, [] {});
                asm volatile("s_nop 7" : "+v"(acc));
                *(f32x4*)(shm + C_EX + (w - 1) * 1024 + lane * 16) = acc;
                RAW_BARRIER();
            } else {
                const int rn = ((s + 1) >> 1) & 3;
                u32 dst = g * 2 * SLOT + (lane & 7) * 16;
                char* dstb = other + (M_SLOT0 + rn * 8 + c / 40) * SLOT + (c % 40) * 128;
                u32 exa = lds0 + C_EX + lane * 16;
                asm volatile("" : "+s"(dstb), "+v"(dst), "+v"(exa));
                f32x4 h1, h2, h3;
                partial(std::integral_constant<int, 11>(), std::integral_constant<int, 0>(), acc, [&] {
                    __builtin_amdgcn_s_barrier();
                    asm volatile("" ::: "memory");
                    h1 = *(LDS3 const f32x4*)(uintptr_t)exa; h2 = *(LDS3 const f32x4*)(uintptr_t)(exa + 1024); h3 = *(LDS3 const f32x4*)(uintptr_t)(exa + 2048);
                    SCHEDB();
                });
                h1 = (h1 + h2) + h3;
                asm volatile("s_nop 2" : "+v"(acc), "+v"(h1));
                acc += h1;
                float v0, v1;
                auto merge = [&]() {
                    auto a = __builtin_amdgcn_permlane32_swap(__float_as_uint(acc[0]), __float_as_uint(acc[2]), false, false);
                    auto b = __builtin_amdgcn_permlane32_swap(__float_as_uint(acc[1]), __float_as_uint(acc[3]), false, false);
                    v0 = __uint_as_float(a[0]) + fdpp<0x128>(__uint_as_float(a[1]));
                    v1 = __uint_as_float(b[0]) + fdpp<0x128>(__uint_as_float(b[1]));
                };
                merge();
                int tries = 0;
                if (__builtin_expect(__any(__builtin_isunordered(v0, v1)), 0)) {
                    while (true) {
                        u32 vo = pofs, vm = mofs, m = 0;
                        asm volatile("" : "+v"(vo), "+v"(vm) :: "memory");
                        u32x4 P[20];
#pragma unroll
                        for (int i = 0; i < 20; ++i) P[i] = ldp(i, vo, vm);
#pragma unroll
                        for (int i = 0; i < 20; ++i) {
                            const bf8 y = ldy(i);
                            if (i == 0) MMA0_VV(acc, y, P[i]); else MMA_VV(acc, y, P[i]);
                            m = max4(m, P[i]);
                        }
                        asm volatile("s_nop 7" : "+v"(acc));
                        ++tries;
                        if (!__any(m == ~0u)) break;
                        if (tries >= cap) { cap = 1; break; }
                        __builtin_amdgcn_s_sleep(4);
                    }
                    merge();
                }
                TRV(6, tries + 1);
                const u32 dw = nosent(pk2bf(v0, v1));
                auto e = __builtin_amdgcn_permlane32_swap(dw, dw, false, false);
                auto e0 = __builtin_amdgcn_permlane16_swap(e[0], e[0], false, false);
                auto e1 = __builtin_amdgcn_permlane16_swap(e[1], e[1], false, false);
                st16m(dstb, dst, (u32x4){e0[0], e1[0], e0[1], e1[1]}, 0x00ff00ff00ff00ffull);
                TR(4);
                st4_lane0(mine + (T_SLOT + (c >> 6)) * SLOT + (c & 63) * 128, tries == 0 && s > 0 ? rt32() - xt : 0);
                if (s >= 1 && fr < 8) arm16(dstb + (((rn + 3) & 3) - rn) * 8 * SLOT, dst);
            }
        } else asm volatile("" :: "v"(peek));
        RAW_BARRIER();
        auto tokens = [&](auto MORE) {
            f32x4 oa[4];
            bf8 ya[4];
            u32x4 P[20];
            u32 vo = pofs, vm = mofs;
            asm volatile("" : "+v"(vo), "+v"(vm));
            const char* pb[4];
#pragma unroll
            for (int i = 0; i < 4; ++i) { pb[i] = p.px + (size_t)(set * 4 + rot) * PX_BUF + (i * 32 + w * 8) * 640; asm volatile("" : "+s"(pb[i])); }
            auto ldp = [&](const int i) { P[i] = i % 5 == 4 ? ld16(pb[i / 5], vm, 0) : ld16(pb[i / 5], vo, (i % 5) * 1280); };
            int ln = lane;
            asm volatile("" : "+v"(ln));
            const size_t ro = (size_t)(s * SEG + w * 8 + ((ln >> 1) & 7)) * DM + c * 8 + (ln & 1) * 4;
            const u64 res = *(const u64*)(p.xres + ro);
            TRC(9);
            if constexpr (CI(MORE)) yx(ya, s + 2, ldp);
            else {
#pragma unroll
                for (int i = 0; i < 20; ++i) ldp(i);
            }
            TRC(10);
            if constexpr (CI(MORE)) peek = sched_peek(mine + (S_SLOT + w) * SLOT + (s + 1) * 128 + 4);
            bf8 y[5];
#pragma unroll
            for (int j = 0; j < 5; ++j) y[j] = *(const bf8*)(yl + (w * 5 + j) * 1024);
            auto outx = [&]() {
#pragma unroll
                for (int i = 0; i < 20; ++i) PIN(P[i]);
                NOPV();
#pragma unroll
                for (int j = 0; j < 5; ++j)
#pragma unroll
                    for (int q = 0; q < 4; ++q) { if (j == 0) MMA0_VV(oa[q], y[j], P[q * 5 + j]); else MMA_VV(oa[q], y[j], P[q * 5 + j]); }
                asm volatile("s_nop 7");
#pragma unroll
                for (int q = 0; q < 4; ++q) PIN(oa[q]);
            };
            outx();
            if (__builtin_expect(__any(__builtin_isunordered(oa[0][0], oa[1][0]) || __builtin_isunordered(oa[2][0], oa[3][0])), 0)) {
                for (int tries = 1;; ++tries) {
                    asm volatile("" : "+v"(vo), "+v"(vm) :: "memory");
                    u32 m = 0;
#pragma unroll
                    for (int i = 0; i < 20; ++i) ldp(i);
#pragma unroll
                    for (int i = 0; i < 20; ++i) m = max4(m, P[i]);
                    if (!__any(m == ~0u)) break;
                    if (tries >= cap) { cap = 1; break; }
                    __builtin_amdgcn_s_sleep(8);
                }
                outx();
            }
            const bool own = (fr < 8) == (g < 2);
#pragma unroll
            for (int q = 0; q < 4; q += 2) {
                f32x4 o;
#pragma unroll
                for (int i = 0; i < 4; ++i) { const float t = fdpp<0x128>(oa[q + 1][i]); o[i] = own ? oa[q][i] : t; }
                *(f32x4*)(shm + C_RED + ((w * 2 + (g >> 1)) * 32 + (q + !own) * 8 + (fr & 7)) * 32 + (g & 1) * 16) = o;
            }
            if constexpr (CI(MORE)) {
#pragma unroll
                for (int t = 0; t < 4; ++t) *(bf8*)(yl + (w * 5 + t) * 1024) = ya[t];
            }
            RAW_BARRIER();
            TR(5); TRFLUSH(set * 128 + 32 + c);
            f32x4 a = *(const f32x4*)(shm + C_RED + (ln >> 4) * 2048 + w * 256 + (ln & 15) * 16) + *(const f32x4*)(shm + C_RED + (ln >> 4) * 2048 + 1024 + w * 256 + (ln & 15) * 16);
#pragma unroll
            for (int i = 0; i < 4; ++i) {
                auto t = __builtin_amdgcn_permlane16_swap(__float_as_uint(a[i]), __float_as_uint(a[i]), false, false);
                const float z = __uint_as_float(t[0]) + __uint_as_float(t[1]);
                a[i] = halfsum(z, z);
            }
            const float v0 = bf2f((u16)res) + rbf(a[0]), v1 = bf2f((u16)(res >> 16)) + rbf(a[1]);
            const float v2 = bf2f((u16)(res >> 32)) + rbf(a[2]), v3 = bf2f((u16)(res >> 48)) + rbf(a[3]);
            if (ln < 16) *(u64*)(p.x1 + ro) = (u64)pk2bf(v0, v1) | ((u64)pk2bf(v2, v3) << 32);
        };
        if (s + 2 >= NSEG) { tokens(std::integral_constant<int, 0>()); break; }
        tokens(std::integral_constant<int, 1>());
    }
}

__global__ void __launch_bounds__(256, 1) la_kernel(LaParams p) {
    extern __shared__ __attribute__((aligned(16))) char shm[];
    p.hop = align2m(p.hop) + (size_t)p.layer * HOP_BYTES;
    if (threadIdx.x == 0) {
        u32 xcc; asm volatile("s_getreg_b32 %0, hwreg(HW_REG_XCC_ID)" : "=s"(xcc));
        int role = 255;
        for (int a = 0; a < 8; ++a) {
            const u32 v = (xcc + a) & 7;
            const u32 k = __hip_atomic_fetch_add((gu32*)(p.tick + v * 32), 1u, __ATOMIC_RELAXED, __HIP_MEMORY_SCOPE_AGENT);
            if (k < 32) { role = v * 32 + k; break; }
        }
        *(int*)shm = role;
    }
    __syncthreads();
    const int role = __builtin_amdgcn_readfirstlane(*(int*)shm);
    __syncthreads();
    const int set = role >> 7, id = (role >> 5) & 3, k = role & 31;
    const int phys = set ^ (int)(__builtin_amdgcn_readfirstlane(*p.flip) & 1);
    if (k < 8) qk_role(p, set, id * 8 + k, phys, shm);
    else if (k < 28) comb_role(p, set, id * 20 + k - 8, phys, shm);
}
template <bool FRAG>
__device__ __forceinline__ void ln_rows(const u16* __restrict__ x, u16* __restrict__ y, int blk) {
    int row = blk * 4 + (threadIdx.x >> 6), lane = threadIdx.x & 63;
    const u16* xr = x + (size_t)row * DM;
    float v[10];
    float s = 0.f;
#pragma unroll
    for (int i = 0; i < 5; ++i) {
        u32 w = *(const u32*)(xr + (i * 64 + lane) * 2);
        v[2 * i] = bf2f(w & 0xffff); v[2 * i + 1] = bf2f(w >> 16);
        s += v[2 * i] + v[2 * i + 1];
    }
#pragma unroll
    for (int o = 32; o > 0; o >>= 1) s += __shfl_xor(s, o);
    float mean_f = s * (1.0f / DM);
    float q = 0.f;
#pragma unroll
    for (int i = 0; i < 10; ++i) { float d = v[i] - mean_f; q += d * d; }
#pragma unroll
    for (int o = 32; o > 0; o >>= 1) q += __shfl_xor(q, o);
    float m = rbf(mean_f);
    float var = rbf(q * (1.0f / DM));
    float den = rbf(sqrtf(rbf(var + 1e-5f)));
#pragma unroll
    for (int i = 0; i < 5; ++i) {
        const u32 o = pk2bf(rbf(v[2 * i] - m) / den, rbf(v[2 * i + 1] - m) / den);
        const int dw = i * 64 + lane;
        if (!FRAG) { *(u32*)(y + (size_t)row * DM + dw * 2) = o; continue; }
        const int k = dw >> 4, D = dw & 7, g = ((dw >> 3) & 1) * 2 + ((D >> 1) & 1), pos = (D & 1) * 2 + (D >> 2);
        const int tok = row & 31;
        ((u32*)y)[(size_t)(row >> 5) * 10240 + (k * 2 + (tok >> 4)) * 256 + (g * 16 + (tok & 15)) * 4 + pos] = o;
    }
}
template <bool FRAG>
__global__ void __launch_bounds__(256) layernorm_kernel(const u16* __restrict__ x, u16* __restrict__ y) { ln_rows<FRAG>(x, y, blockIdx.x); }

__device__ __forceinline__ int kperm(int g, int e) { return 16 * (g >> 1) + 8 * ((e & 3) >> 1) + 4 * (g & 1) + 2 * (e >> 2) + (e & 1); }

struct TrJob { const u16* in; u16* out; int C; int tiles_c; int tile0; int R; };
struct PrepParams {
    TrJob j[2];
    const u16* Wq; const u16* Wk; const u16* Wv; const u16* Wo; const u16* mem0; const u16* x;
    char* wqk; char* wvo; u16* xn; char* hop; char* px; u32* ctl;
    int n_vo, n_qk, n_tr, n_ln, n_fill, pad;
};
constexpr int CTL_WORDS = 2 * 256 + 32;
__global__ void __launch_bounds__(256) prep_kernel(PrepParams q) {
    __shared__ __attribute__((aligned(16))) u16 sm[192 * 72];
    int b = blockIdx.x;
    const int t = threadIdx.x, lane = t & 63, w = t >> 6, fr = lane & 15, g = lane >> 4;
    if (b < q.n_vo) {
        const int h = b / 10, cb = b % 10;
#pragma unroll
        for (int i = 0; i < 6; ++i) {
            const int id = t + i * 256, e = id >> 3, ch = id & 7;
            *(u32x4*)(sm + e * 72 + ch * 8) = *(const u32x4*)(q.Wo + ((size_t)h * DH + e) * DM + cb * 64 + ch * 8);
        }
        __syncthreads();
        bf8 B[4][6];
#pragma unroll
        for (int n = 0; n < 4; ++n)
#pragma unroll
            for (int k = 0; k < 6; ++k) {
                const u16* a = sm + (k * 32 + g * 8 + (fr >> 2)) * 72 + n * 16 + (fr & 3) * 4;
                s16x4 lo = __builtin_amdgcn_ds_read_tr16_b64_v4i16((LDS3 s16x4*)a);
                s16x4 hi = __builtin_amdgcn_ds_read_tr16_b64_v4i16((LDS3 s16x4*)(a + 4 * 72));
                B[n][k] = __builtin_bit_cast(bf8, __builtin_shufflevector(lo, hi, 0, 1, 2, 3, 4, 5, 6, 7));
            }
        for (int k32 = w * 5; k32 < w * 5 + 5; ++k32) {
            bf8 A[2][6];
#pragma unroll
            for (int u = 0; u < 2; ++u) {
                const u16* row = q.Wv + (size_t)(k32 * 32 + kperm(fr >> 2, u * 4 + (fr & 3))) * HD + h * DH + g * 8;
#pragma unroll
                for (int k = 0; k < 6; ++k) A[u][k] = *(const bf8*)(row + k * 32);
            }
#pragma unroll
            for (int n = 0; n < 4; ++n) {
                f32x4 c0 = {}, c1 = {};
#pragma unroll
                for (int k = 0; k < 6; ++k) {
                    c0 = __builtin_amdgcn_mfma_f32_16x16x32_bf16(A[0][k], B[n][k], c0, 0, 0, 0);
                    c1 = __builtin_amdgcn_mfma_f32_16x16x32_bf16(A[1][k], B[n][k], c1, 0, 0, 0);
                }
                const int c = cb * 8 + n * 2 + (fr >> 3);
                const size_t frag = (size_t)(c * 4 + (h >> 3)) * 40 + (k32 >> 1) * 4 + ((h & 7) >> 1);
                *(bf8*)(q.wvo + frag * 2048 + (k32 & 1) * 1024 + (g * 16 + (h & 1) * 8 + (fr & 7)) * 16) = pack8(c0, c1);
            }
        }
        return;
    }
    b -= q.n_vo;
    if (b < q.n_qk) {
        const int mt = b / 320, h = (b / 10) & 31, ks = b % 10;
        const u16* W = mt ? q.Wk : q.Wq;
#pragma unroll
        for (int i = 0; i < 6; ++i) {
            const int id = t + i * 256, d = id / 24, ch = id % 24;
            *(u32x4*)(sm + d * 200 + ch * 8) = *(const u32x4*)(W + (size_t)(ks * 64 + d) * HD + h * DH + ch * 8);
        }
        __syncthreads();
#pragma unroll
        for (int pr = 0; pr < 3; ++pr)
#pragma unroll
            for (int hf = 0; hf < 2; ++hf) {
                const u16* a = sm + (hf * 32) * 200 + (w * 3 + pr) * 16 + (fr & 3) * 4;
                s16x4 lo = __builtin_amdgcn_ds_read_tr16_b64_v4i16((LDS3 s16x4*)(a + kperm(g, fr >> 2) * 200));
                s16x4 hi = __builtin_amdgcn_ds_read_tr16_b64_v4i16((LDS3 s16x4*)(a + kperm(g, 4 + (fr >> 2)) * 200));
                const size_t frag = (size_t)(h * 4 + w) * 60 + qk_frag(2 * pr + mt, ks);
                *(bf8*)(q.wqk + frag * 2048 + hf * 1024 + lane * 16) = __builtin_bit_cast(bf8, __builtin_shufflevector(lo, hi, 0, 1, 2, 3, 4, 5, 6, 7));
            }
        return;
    }
    b -= q.n_qk;
    if (b < q.n_tr) {
        u16 (*tile)[66] = (u16 (*)[66])sm;
        const TrJob jb = b >= q.j[1].tile0 ? q.j[1] : q.j[0];
        const int tb = b - jb.tile0, bc = (tb % jb.tiles_c) * 64, br = (tb / jb.tiles_c) * 64;
#pragma unroll
        for (int i = 0; i < 8; ++i) {
            int id = t + i * 256, r = id >> 5, c = (id & 31) * 2;
            u32 v = *(const u32*)(jb.in + (size_t)(br + r) * jb.C + bc + c);
            tile[r][c] = (u16)v; tile[r][c + 1] = (u16)(v >> 16);
        }
        __syncthreads();
#pragma unroll
        for (int i = 0; i < 8; ++i) {
            int id = t + i * 256, c = id >> 5, r = (id & 31) * 2;
            *(u32*)(jb.out + (size_t)(bc + c) * jb.R + br + r) = (u32)tile[r][c] | ((u32)tile[r + 1][c] << 16);
        }
        return;
    }
    b -= q.n_tr;
    if (b < q.n_ln) { ln_rows<true>(q.x, q.xn, b); return; }
    b -= q.n_ln;
    const u32x4 SENT = {~0u, ~0u, ~0u, ~0u};
    q.hop = align2m(q.hop);
    if (b < q.n_fill) {
        for (u32 i = b * 256 + t; i < 2 * HOP_BYTES / 16; i += q.n_fill * 256) {
            const u32 sl = (i / (SLOT / 16)) % N_SLOT;
            if (sl < M_SLOT0 || sl >= M_SLOT0 + 8) st16(q.hop + (size_t)i * 16, SENT);
        }
        for (u32 i = b * 256 + t; i < 2 * PX_BYTES / 16; i += q.n_fill * 256) st16(q.px + (size_t)i * 16, SENT);
        if (b == 0) { q.ctl[t] = 0; q.ctl[256 + t] = 0; }
        return;
    }
    if (w == 0) {
        u32 lat[2] = {0, 0};
        for (int i = 0; i < 9; ++i)
#pragma unroll
            for (int sd = 0; sd < 2; ++sd) {
                char* a = q.hop + M_SLOT0 * SLOT + sd * 8192 + 6144 + lane * 16;
                st16(a, SENT);
                VM0();
                const u32 t0 = (u32)__builtin_readcyclecounter();
                u32x4 v;
                asm volatile("global_load_dwordx4 %0, %1, off sc1\n\ts_waitcnt vmcnt(0)" : "=&v"(v) : "v"(a) : "memory");
                const u32 t1 = (u32)__builtin_readcyclecounter();
                if (i > 0) lat[sd] += min(t1 - t0, 2000u);
            }
        u32 xcc; asm volatile("s_getreg_b32 %0, hwreg(HW_REG_XCC_ID)" : "=s"(xcc));
        if (lane == 0) { const u32 f = ((xcc >> 2) & 1) ^ (lat[1] < lat[0] ? 1u : 0u); q.ctl[512] = f; *(u32*)sm = f; q.ctl[513] = lat[0]; q.ctl[514] = lat[1]; }
    }
    __syncthreads();
    const u32 flip = *(u32*)sm;
    for (int i = t; i < 2 * 8 * 2 * 320; i += 256) {
        const int ly = i / 5120, sl = (i / 640) & 7, sd = (i / 320) & 1, e = i % 320;
        u32x4 v = SENT;
        if (sd == (int)flip) {
            v = *(const u32x4*)(q.mem0 + (e & 7) * DM + ((sl & 1) * 40 + (e >> 3)) * 8);
            v = (u32x4){nosent(v[0]), nosent(v[1]), nosent(v[2]), nosent(v[3])};
        }
        st16(q.hop + (size_t)ly * HOP_BYTES + (M_SLOT0 + sl) * SLOT + sd * 8192 + e * 16, v);
    }
}
namespace g256 {
constexpr int BM = 256, BK = 64, HALF = 128, NXCD = 8, WGM = 8, HT = HALF * BK, SHM_B = 8 * HT * 2;
__device__ __forceinline__ int lds_byte(int r, int c) {
    int st = (r >> 4) * 2 + (c >> 5), rr = r & 15, cc = c & 31, ob = rr * 64 + cc * 2;
    return st * 1024 + (ob ^ (((ob >> 9) & 1) << 5));
}
__device__ __forceinline__ void stage_rc(int b, int& R, int& C) {
    int st = b / 1024, sb = b % 1024, swz = sb ^ (((sb >> 9) & 1) << 5);
    R = (st >> 1) * 16 + swz / 64; C = (st & 1) * 32 + (swz % 64) / 2;
}
}
template <int EPI, int BM, int BN, int NS>
__global__ void __launch_bounds__(256) gemmd_kernel(const u16* __restrict__ A, const u16* __restrict__ Bt,
                                                    u16* __restrict__ C, const u16* __restrict__ res,
                                                    int M, int N, int K) {
    constexpr int MT = BM / 32, NT = BN / 32, LA = BM / 32, LB = BN / 32, SA_B = BM * 128, SB_B = BN * 128, ST_B = SA_B + SB_B;
    __shared__ __attribute__((aligned(16))) char sm[NS * ST_B];
    const int t = threadIdx.x, lane = t & 63, w = t >> 6, wr = w >> 1, wc = w & 1;
    const int nx = N / BN, ny = M / BM, xc = blockIdx.x & 7, kx = blockIdx.x >> 3;
    int bxi, byi, bzi = 0;
    if (EPI == 2) { const int per = nx * ny; bzi = xc + 8 * (kx / per); bxi = (kx % per) % nx; byi = (kx % per) / nx; }
    else { bxi = kx % nx; byi = xc * (ny / 8) + kx / nx; }
    const int bm = byi * BM, bn = bxi * BN;
    const int fr = lane & 15, fq = lane >> 4;
    f32x4 acc[MT][NT] = {};
    const int lda = K;
    if (EPI == 2) { K /= 8; A += (size_t)bzi * K; Bt += (size_t)bzi * K; }
    const int nt = K / 64;
    const u16* srcA[LA]; const u16* srcB[LB];
#pragma unroll
    for (int i = 0; i < LA; ++i) { int r, c; g256::stage_rc(t * 16 + i * 4096, r, c); srcA[i] = A + (size_t)(bm + r) * lda + c; }
#pragma unroll
    for (int i = 0; i < LB; ++i) { int r, c; g256::stage_rc(t * 16 + i * 4096, r, c); srcB[i] = Bt + (size_t)(bn + r) * lda + c; }
    auto issue = [&](int kt) {
        char* dst = sm + (kt % NS) * ST_B + w * 1024;
#pragma unroll
        for (int i = 0; i < LA; ++i) __builtin_amdgcn_global_load_lds((const u32*)(srcA[i] + kt * 64), (u32*)(dst + i * 4096), 16, 0, 0);
#pragma unroll
        for (int i = 0; i < LB; ++i) __builtin_amdgcn_global_load_lds((const u32*)(srcB[i] + kt * 64), (u32*)(dst + SA_B + i * 4096), 16, 0, 0);
    };
#pragma unroll
    for (int p = 0; p < NS - 1; ++p) issue(p);
    for (int kt = 0; kt < nt; ++kt) {
        const int rem = nt - 1 - kt;
        if (rem >= NS - 2) asm volatile("s_waitcnt vmcnt(%0)" :: "n"((NS - 2) * (LA + LB)) : "memory");
        else if (NS > 3 && rem == 2) asm volatile("s_waitcnt vmcnt(%0)" :: "n"(2 * (LA + LB)) : "memory");
        else if (rem == 1) asm volatile("s_waitcnt vmcnt(%0)" :: "n"(LA + LB) : "memory");
        else asm volatile("s_waitcnt vmcnt(0)" ::: "memory");
        __builtin_amdgcn_s_barrier();
        asm volatile("" ::: "memory");
        if (kt + NS - 1 < nt) issue(kt + NS - 1);
        const char* sa = sm + (kt % NS) * ST_B; const char* sb = sa + SA_B;
#pragma unroll
        for (int kk = 0; kk < 2; ++kk) {
            bf16x8 af[MT], bfr[NT];
#pragma unroll
            for (int m = 0; m < MT; ++m) af[m] = *(const bf16x8*)(sa + g256::lds_byte(wr * (BM / 2) + m * 16 + fr, kk * 32 + fq * 8));
#pragma unroll
            for (int n = 0; n < NT; ++n) bfr[n] = *(const bf16x8*)(sb + g256::lds_byte(wc * (BN / 2) + n * 16 + fr, kk * 32 + fq * 8));
#pragma unroll
            for (int m = 0; m < MT; ++m)
#pragma unroll
                for (int n = 0; n < NT; ++n)
                    acc[m][n] = __builtin_amdgcn_mfma_f32_16x16x32_bf16(bfr[n], af[m], acc[m][n], 0, 0, 0);
        }
        asm volatile("s_waitcnt lgkmcnt(0)" ::: "memory");
    }
#pragma unroll
    for (int m = 0; m < MT; ++m)
#pragma unroll
        for (int n = 0; n < NT; ++n) {
            const int row = bm + wr * (BM / 2) + m * 16 + fr, col = bn + wc * (BN / 2) + n * 16 + fq * 4;
            f32x4 v = acc[m][n];
            if (EPI == 2) { *(f32x4*)((float*)C + ((size_t)bzi * M + row) * N + col) = v; continue; }
            if (EPI == 1) {
                u64 r = *(const u64*)(res + (size_t)row * N + col);
                v[0] = bf2f((u16)r) + rbf(v[0]); v[1] = bf2f((u16)(r >> 16)) + rbf(v[1]);
                v[2] = bf2f((u16)(r >> 32)) + rbf(v[2]); v[3] = bf2f((u16)(r >> 48)) + rbf(v[3]);
            }
            *(u64*)(C + (size_t)row * N + col) = (u64)pk2bf(v[0], v[1]) | ((u64)pk2bf(v[2], v[3]) << 32);
        }
}

constexpr int BCH = 128;
constexpr int NCHUNK = S_TOK / BCH;

__device__ __forceinline__ void load20(const u16* p, float* o) {
#pragma unroll
    for (int i = 0; i < 5; ++i) {
        u64 w = *(const u64*)(p + 4 * i);
        o[4 * i] = bf2f((u16)w); o[4 * i + 1] = bf2f((u16)(w >> 16)); o[4 * i + 2] = bf2f((u16)(w >> 32)); o[4 * i + 3] = bf2f((u16)(w >> 48));
    }
}
template <int CTRL> __device__ __forceinline__ float qdpp(float x) {
    return __builtin_bit_cast(float, __builtin_amdgcn_mov_dpp(__builtin_bit_cast(int, x), CTRL, 0xf, 0xf, true));
}
__device__ __forceinline__ float quad_max(float x) { x = fmaxf(x, qdpp<0xB1>(x)); return fmaxf(x, qdpp<0x4E>(x)); }
__device__ __forceinline__ float quad_min(float x) { x = fminf(x, qdpp<0xB1>(x)); return fminf(x, qdpp<0x4E>(x)); }
__device__ __forceinline__ float quad_sum(float x) { x += qdpp<0xB1>(x); return x + qdpp<0x4E>(x); }
__device__ __forceinline__ void hedgehog_phi_q(const float* in20, const float (&wr)[20][5], float* pos5, float* neg5) {
    float p[5] = {};
#pragma unroll
    for (int d = 0; d < 20; ++d)
#pragma unroll
        for (int f = 0; f < 5; ++f) p[f] += in20[d] * wr[d][f];
#pragma unroll
    for (int f = 0; f < 5; ++f) p[f] = rbf(p[f]);
    float mx = fmaxf(fmaxf(fmaxf(p[0], p[1]), fmaxf(p[2], p[3])), p[4]);
    float mn = fminf(fminf(fminf(p[0], p[1]), fminf(p[2], p[3])), p[4]);
    mx = quad_max(mx); mn = quad_min(mn);
    float s1 = 0.f, s2 = 0.f;
#pragma unroll
    for (int f = 0; f < 5; ++f) { pos5[f] = __expf(p[f] - mx); s1 += pos5[f]; neg5[f] = __expf(mn - p[f]); s2 += neg5[f]; }
    s1 = quad_sum(s1); s2 = quad_sum(s2);
    float i1 = 1.0f / s1, i2 = 1.0f / s2;
#pragma unroll
    for (int f = 0; f < 5; ++f) { pos5[f] = rbf(pos5[f] * i1); neg5[f] = rbf(neg5[f] * i2); }
}

__global__ void __launch_bounds__(256) bkv_kernel(const u16* __restrict__ qkvb, const u16* __restrict__ wphi,
                                                  float* __restrict__ part  ) {
    constexpr int TS = BCH + 8;
    __shared__ __attribute__((aligned(16))) u16 phT[48][TS];
    __shared__ __attribute__((aligned(16))) u16 vT[32][TS];
    const int c = blockIdx.x, h = blockIdx.y, t = threadIdx.x, qd = t & 3, tl = t >> 2;
    __shared__ float wp[400];
    for (int i = t; i < 400; i += 256) wp[i] = bf2f(wphi[h * 400 + i]);
    __syncthreads();
    float wr[20][5];
#pragma unroll
    for (int d = 0; d < 20; ++d)
#pragma unroll
        for (int f = 0; f < 5; ++f) wr[d][f] = wp[d * 20 + qd * 5 + f];
    for (int i = t; i < 8 * TS; i += 256) (&phT[40][0])[i] = 0;
    for (int i = t; i < 11 * TS; i += 256) (&vT[21][0])[i] = 0;
#pragma unroll
    for (int ps = 0; ps < BCH / 64; ++ps) {
        const int tk = ps * 64 + tl, tok = c * BCH + tk;
        float kin[20], pos[5], neg[5];
        load20(qkvb + (size_t)tok * 1920 + 640 + h * 20, kin);
        const u16* vp = qkvb + (size_t)tok * 1920 + 1280 + h * 20 + qd * 5;
        u16 vv[5];
#pragma unroll
        for (int i = 0; i < 5; ++i) vv[i] = vp[i];
        hedgehog_phi_q(kin, wr, pos, neg);
#pragma unroll
        for (int f = 0; f < 5; ++f) {
            phT[qd * 5 + f][tk] = (u16)(__float_as_uint(pos[f]) >> 16);
            phT[20 + qd * 5 + f][tk] = (u16)(__float_as_uint(neg[f]) >> 16);
            vT[qd * 5 + f][tk] = vv[f];
        }
        if (qd == 0) vT[20][tk] = 0x3f80;
    }
    __syncthreads();
    const int lane = t & 63, w = t >> 6, fr = lane & 15, fq = lane >> 4;
    for (int tile = w; tile < 6; tile += 4) {
        const int dt = tile / 3, ft = tile % 3;
        f32x4 acc = {};
#pragma unroll
        for (int ks = 0; ks < BCH / 32; ++ks) {
            bf16x8 a = *(const bf16x8*)&vT[dt * 16 + fr][ks * 32 + fq * 8];
            bf16x8 b = *(const bf16x8*)&phT[ft * 16 + fr][ks * 32 + fq * 8];
            acc = __builtin_amdgcn_mfma_f32_16x16x32_bf16(a, b, acc, 0, 0, 0);
        }
        const int f = ft * 16 + fr, d0 = dt * 16 + fq * 4;
        if (f < 40 && d0 < 24) *(f32x4*)(part + ((size_t)c * 32 + h) * 960 + f * 24 + d0) = acc;
    }
}

__global__ void __launch_bounds__(256) bout_kernel(const u16* __restrict__ qkvb, const u16* __restrict__ wphi,
                                                   const float* __restrict__ part, u16* __restrict__ ob) {
    __shared__ __attribute__((aligned(16))) u16 kzT[32][72];
    __shared__ __attribute__((aligned(16))) u16 ph[BCH][72];
    __shared__ float wp[400];
    const int h = blockIdx.y, t = threadIdx.x, qd = t & 3, tl = t >> 2;
    for (int i = t; i < 32 * 72; i += 256) (&kzT[0][0])[i] = 0;
    for (int i = t; i < 400; i += 256) wp[i] = bf2f(wphi[h * 400 + i]);
    f32x4 a = {};
    if (t < 240) {
#pragma unroll
        for (int c = 0; c < NCHUNK; ++c) a += *(const f32x4*)(part + ((size_t)c * 32 + h) * 960 + t * 4);
    }
    float qin[BCH / 64][20];
#pragma unroll
    for (int ps = 0; ps < BCH / 64; ++ps) load20(qkvb + (size_t)(blockIdx.x * BCH + ps * 64 + tl) * 1920 + h * 20, qin[ps]);
    __syncthreads();
    if (t < 240) {
        const int f = t / 6, d0 = (t % 6) * 4;
#pragma unroll
        for (int e = 0; e < 4; ++e) kzT[d0 + e][f] = (u16)(pk2bf(a[e], 0.f) & 0xffff);
    }
    float wr[20][5];
#pragma unroll
    for (int d = 0; d < 20; ++d)
#pragma unroll
        for (int f = 0; f < 5; ++f) wr[d][f] = wp[d * 20 + qd * 5 + f];
#pragma unroll
    for (int ps = 0; ps < BCH / 64; ++ps) {
        float pos[5], neg[5];
        hedgehog_phi_q(qin[ps], wr, pos, neg);
        u16* row = ph[ps * 64 + tl];
#pragma unroll
        for (int f = 0; f < 5; ++f) {
            row[qd * 5 + f] = (u16)(__float_as_uint(pos[f]) >> 16);
            row[20 + qd * 5 + f] = (u16)(__float_as_uint(neg[f]) >> 16);
        }
        *(u64*)&row[40 + qd * 4] = 0; *(u32*)&row[56 + qd * 2] = 0;
    }
    __syncthreads();
    const int lane = t & 63, w = t >> 6, fr = lane & 15, fq = lane >> 4;
    bf16x8 ka0[2], ka1[2];
#pragma unroll
    for (int dt = 0; dt < 2; ++dt) { ka0[dt] = *(const bf16x8*)&kzT[dt * 16 + fr][fq * 8]; ka1[dt] = *(const bf16x8*)&kzT[dt * 16 + fr][32 + fq * 8]; }
#pragma unroll
    for (int tt = w; tt < BCH / 16; tt += 4) {
        bf16x8 b0 = *(const bf16x8*)&ph[tt * 16 + fr][fq * 8];
        bf16x8 b1 = *(const bf16x8*)&ph[tt * 16 + fr][32 + fq * 8];
        f32x4 acc[2];
#pragma unroll
        for (int dt = 0; dt < 2; ++dt) {
            acc[dt] = __builtin_amdgcn_mfma_f32_16x16x32_bf16(ka0[dt], b0, (f32x4){}, 0, 0, 0);
            acc[dt] = __builtin_amdgcn_mfma_f32_16x16x32_bf16(ka1[dt], b1, acc[dt], 0, 0, 0);
        }
        const float den = rbf(rbf(__shfl(acc[1][0], 16 + fr)) + 1e-6f);
        u16* dst = ob + (size_t)(blockIdx.x * BCH + tt * 16 + fr) * 640 + h * 20;
        *(u64*)(dst + fq * 4) = (u64)pk2bf(rbf(acc[0][0]) / den, rbf(acc[0][1]) / den) | ((u64)pk2bf(rbf(acc[0][2]) / den, rbf(acc[0][3]) / den) << 32);
        if (fq == 0) *(u64*)(dst + 16) = (u64)pk2bf(rbf(acc[1][0]) / den, rbf(acc[1][1]) / den) | ((u64)pk2bf(rbf(acc[1][2]) / den, rbf(acc[1][3]) / den) << 32);
    }
}

extern "C" void kernel_launch(void* const* d_in, const int* in_sizes, int n_in,
                              void* d_out, int out_size, void* d_ws, size_t ws_size,
                              hipStream_t stream) {
    const u16* A_x = (const u16*)d_in[0];
    const u16* B_wphi = (const u16*)d_in[9];
    u16* out = (u16*)d_out;

    char* ws = (char*)d_ws;
    size_t off = 0;
    auto alloc = [&](size_t bytes) { char* p = ws + off; off += (bytes + 255) & ~(size_t)255; return p; };
    u32* dbg = (u32*)alloc(2 << 20);
    u32* ctl = (u32*)alloc(CTL_WORDS * 4);
    char* hop = alloc(4u << 20);
    char* px = alloc(2 * PX_BYTES);
    char* wqk = alloc((size_t)32 * 4 * 60 * 2048);
    char* wvo = alloc((size_t)80 * 4 * 40 * 2048);
    u16* BwqkvT = (u16*)alloc((size_t)1920 * DM * 2);
    u16* BwoT = (u16*)alloc((size_t)DM * DM * 2);
    u16* xnf = (u16*)alloc((size_t)S_TOK * DM * 2);
    u16* xn = (u16*)alloc((size_t)S_TOK * DM * 2);
    u16* xa = (u16*)alloc((size_t)S_TOK * DM * 2);
    u16* xb = (u16*)alloc((size_t)S_TOK * DM * 2);
    u16* qkvb = (u16*)alloc((size_t)S_TOK * 1920 * 2);
    float* bpart = (float*)alloc((size_t)NCHUNK * 32 * 960 * 4);
    u16* ob = (u16*)alloc((size_t)S_TOK * DM * 2);

    hipFuncSetAttribute((const void*)la_kernel, hipFuncAttributeMaxDynamicSharedMemorySize, LA_LDS);
    {
        PrepParams q;
        q.j[0].in = (const u16*)d_in[8]; q.j[0].out = BwqkvT; q.j[0].C = 1920; q.j[0].tiles_c = 30; q.j[0].tile0 = 0; q.j[0].R = DM;
        q.j[1].in = (const u16*)d_in[10]; q.j[1].out = BwoT; q.j[1].C = DM; q.j[1].tiles_c = 10; q.j[1].tile0 = 300; q.j[1].R = DM;
        q.Wq = (const u16*)d_in[2]; q.Wk = (const u16*)d_in[3]; q.Wv = (const u16*)d_in[4]; q.Wo = (const u16*)d_in[5];
        q.mem0 = (const u16*)d_in[1]; q.x = A_x;
        q.wqk = wqk; q.wvo = wvo; q.xn = xnf; q.hop = hop; q.px = px; q.ctl = ctl;
        q.n_vo = 320; q.n_qk = 640; q.n_tr = 400; q.n_ln = S_TOK / 4; q.n_fill = 128; q.pad = 0;
        prep_kernel<<<q.n_vo + q.n_qk + q.n_tr + q.n_ln + q.n_fill + 1, 256, 0, stream>>>(q);
    }
    const u16* xcur = A_x;
    for (int layer = 0; layer < 2; ++layer) {
        if (layer > 0) layernorm_kernel<true><<<S_TOK / 4, 256, 0, stream>>>(xcur, xnf);
        LaParams p;
        p.wqk = wqk; p.wvo = wvo; p.xn = (const char*)xnf; p.xres = xcur; p.x1 = xa;
        p.hop = hop; p.layer = layer; p.pad = 0; p.px = px + (size_t)layer * PX_BYTES; p.tick = ctl + layer * 256; p.flip = ctl + 512; p.dbg = dbg;
        la_kernel<<<256, 256, LA_LDS, stream>>>(p);
        layernorm_kernel<false><<<S_TOK / 4, 256, 0, stream>>>(xa, xn);
        gemmd_kernel<0, 128, 128, 3><<<(1920 / 128) * (S_TOK / 128), 256, 0, stream>>>(xn, BwqkvT, qkvb, nullptr, S_TOK, 1920, DM);
        bkv_kernel<<<dim3(NCHUNK, 32), 256, 0, stream>>>(qkvb, B_wphi, bpart);
        bout_kernel<<<dim3(S_TOK / BCH, 32), 256, 0, stream>>>(qkvb, B_wphi, bpart, ob);
        u16* dst = layer == 0 ? xb : out;
        gemmd_kernel<1, 64, 64, 3><<<(DM / 64) * (S_TOK / 64), 256, 0, stream>>>(ob, BwoT, dst, xa, S_TOK, DM, DM);
        xcur = xb;
    }
}
```

```cpp
#include <hip/hip_runtime.h>
#include <hip/hip_bf16.h>
#include <stdint.h>
#include <type_traits>

typedef unsigned short u16;
typedef unsigned int u32;
typedef unsigned long long u64;
using bf16x8 = __attribute__((ext_vector_type(8))) short;
using f32x4 = __attribute__((ext_vector_type(4))) float;

constexpr int S_TOK = 2048, DM = 640, NH = 32, DH = 192;
constexpr int MEM = 8, SEG = 32, NSEG = S_TOK / SEG;
constexpr int HD = NH * DH;

__device__ __forceinline__ float bf2f(u16 v) { return __uint_as_float(((u32)v) << 16); }
__device__ __forceinline__ u32 pk2bf(float lo, float hi) {
    typedef __attribute__((ext_vector_type(2))) float f32x2_t;
    typedef __attribute__((ext_vector_type(2))) __bf16 bf16x2_t;
    bf16x2_t r = __builtin_convertvector((f32x2_t){lo, hi}, bf16x2_t);
    return __builtin_bit_cast(u32, r);
}
__device__ __forceinline__ float rbf(float f) { return __uint_as_float(pk2bf(f, f) << 16); }
typedef __attribute__((ext_vector_type(8))) __bf16 bf8;
typedef __attribute__((ext_vector_type(16))) __bf16 bf16v;
typedef __attribute__((ext_vector_type(4))) __bf16 bf4;
typedef __attribute__((ext_vector_type(4))) short s16x4;
using u32x4 = __attribute__((ext_vector_type(4))) u32;
using u32x2 = __attribute__((ext_vector_type(2))) u32;
typedef __attribute__((address_space(1))) u32 gu32;
typedef __attribute__((address_space(1))) u64 gu64;
#define LDS3 __attribute__((address_space(3)))

constexpr int SLOT = 16384;
constexpr int P_SLOT0 = 0, M_SLOT0 = 16, S_SLOT = 48, T_SLOT = 53, N_SLOT = 55;
constexpr size_t HOP_BYTES = (size_t)N_SLOT * SLOT, M0_OFF = 2 * HOP_BYTES;
constexpr size_t PX_BUF = 4 * 32 * 640, PX_BYTES = 8 * PX_BUF;
constexpr int LA_LDS = 155648 + 4096 + 256;
constexpr int SPIN_CAP = 1 << 16;
#define D1I 135
#define D2I 68
#define MG1 10
#define MG2 12
#define ROTC 0
#define DUP 4
#define PACE 6
#define CLAG 50
#define T_PX 45
#define T_X1 215
#define T_OB 480
#define T_TOK 900
#define B_LAG 30
#define ELATE 30

__device__ __forceinline__ void st16(void* p, u32x4 v) {
    asm volatile("global_store_dwordx4 %0, %1, off sc1\n\ts_nop 2" :: "v"(p), "v"(v) : "memory");
}
typedef __attribute__((address_space(1))) u32x4 gu32x4;
__device__ __forceinline__ u32x4 ld16(const char* base, u32 off, int imm) { return __builtin_amdgcn_global_load_b128((gu32x4*)(base + off + imm), "agent"); }
__device__ __forceinline__ u32 nosent(u32 v) { return min(v, 0xFFFFFFFEu); }
__device__ __forceinline__ u32 rt32() { return (u32)__builtin_amdgcn_s_memrealtime(); }
__device__ __forceinline__ void wait_rt(u32 t) {
    int d = (int)(t - rt32());
    if (d > 4096) return;
    while (d > 10) { if (d > 20) __builtin_amdgcn_s_sleep(2); d = (int)(t - rt32()); }
    asm volatile("s_cmp_lt_i32 %0, 1\n\ts_cbranch_scc1 2f\n1:\n\ts_sub_i32 %0, %0, 1\n\ts_cmp_gt_i32 %0, 0\n\ts_cbranch_scc1 1b\n2:" : "+s"(d) :: "scc");
}
template <int N> __device__ __forceinline__ void dma_kb(const char* src, u32 lds_dst, u32 lane16) {
#pragma unroll
    for (int i = 0; i < N; i += 4) {
        u32 keep;
        if (N - i >= 4)
            asm volatile("s_mov_b32 %0, m0\n\ts_mov_b32 m0, %3\n\ts_nop 0\n\tglobal_load_lds_dwordx4 %1, %2 sc1\n\tglobal_load_lds_dwordx4 %1, %2 offset:1024 sc1\n\t"
                         "global_load_lds_dwordx4 %1, %2 offset:2048 sc1\n\tglobal_load_lds_dwordx4 %1, %2 offset:3072 sc1\n\ts_mov_b32 m0, %0"
                         : "=&s"(keep) : "v"(lane16), "s"(src + i * 1024), "s"(lds_dst + i * 1024) : "memory");
        else if (N - i == 2)
            asm volatile("s_mov_b32 %0, m0\n\ts_mov_b32 m0, %3\n\ts_nop 0\n\tglobal_load_lds_dwordx4 %1, %2 sc1\n\tglobal_load_lds_dwordx4 %1, %2 offset:1024 sc1\n\ts_mov_b32 m0, %0"
                         : "=&s"(keep) : "v"(lane16), "s"(src + i * 1024), "s"(lds_dst + i * 1024) : "memory");
        else
            asm volatile("s_mov_b32 %0, m0\n\ts_mov_b32 m0, %3\n\ts_nop 0\n\tglobal_load_lds_dwordx4 %1, %2 sc1\n\ts_mov_b32 m0, %0"
                         : "=&s"(keep) : "v"(lane16), "s"(src + i * 1024), "s"(lds_dst + i * 1024) : "memory");
    }
}
__device__ __forceinline__ void st16s(char* base, u32 off, u32x4 v) {
    asm volatile("global_store_dwordx4 %0, %1, %2 sc1\n\ts_nop 2" :: "v"(off), "v"(v), "s"(base) : "memory");
}
__device__ __forceinline__ void arm16(char* base, u32 off) {
    u32x4 v;
    asm volatile("v_mov_b64 %0, -1\n\tv_mov_b64 %1, -1" : "=v"(((u64*)&v)[0]), "=v"(((u64*)&v)[1]));
    st16s(base, off, v);
}
template <int B, int E, typename F> __device__ __forceinline__ void static_for(F&& f) {
    if constexpr (B < E) { f(std::integral_constant<int, B>()); static_for<B + 1, E>(f); }
}
#define CI(x) decltype(x)::value
#define RAW_BARRIER() do { asm volatile("s_waitcnt lgkmcnt(0)" ::: "memory"); __builtin_amdgcn_s_barrier(); asm volatile("" ::: "memory"); } while (0)
#define VM0() asm volatile("s_waitcnt vmcnt(0)" ::: "memory")
#define SCHEDB() __builtin_amdgcn_sched_barrier(0)
__device__ __forceinline__ bf8 lo8(const bf16v& v) { return __builtin_shufflevector(v, v, 0, 1, 2, 3, 4, 5, 6, 7); }
__device__ __forceinline__ bf8 hi8(const bf16v& v) { return __builtin_shufflevector(v, v, 8, 9, 10, 11, 12, 13, 14, 15); }
__device__ __forceinline__ bf16v cat8(bf8 a, bf8 b) { return __builtin_shufflevector(a, b, 0, 1, 2, 3, 4, 5, 6, 7, 8, 9, 10, 11, 12, 13, 14, 15); }
#define MMA_VV(c, a, b) asm volatile("v_mfma_f32_16x16x32_bf16 %0, %1, %2, %0" : "+v"(c) : "v"(a), "v"(b))
#define MMA_AV(c, a, b) asm volatile("v_mfma_f32_16x16x32_bf16 %0, %1, %2, %0" : "+v"(c) : "a"(a), "v"(b))
#define MMA_VA(c, a, b) asm volatile("v_mfma_f32_16x16x32_bf16 %0, %1, %2, %0" : "+v"(c) : "v"(a), "a"(b))
#define MMA0_VV(c, a, b) asm volatile("v_mfma_f32_16x16x32_bf16 %0, %1, %2, 0" : "=&v"(c) : "v"(a), "v"(b))
#define MMA0_AV(c, a, b) asm volatile("v_mfma_f32_16x16x32_bf16 %0, %1, %2, 0" : "=&v"(c) : "a"(a), "v"(b))
#define MMA0_VA(c, a, b) asm volatile("v_mfma_f32_16x16x32_bf16 %0, %1, %2, 0" : "=&v"(c) : "v"(a), "a"(b))
#define MMA16(c, a, b) asm volatile("v_mfma_f32_16x16x16_bf16 %0, %1, %2, %0" : "+v"(c) : "v"(a), "v"(b))
#define MMA16_0(c, a, b) asm volatile("v_mfma_f32_16x16x16_bf16 %0, %1, %2, 0" : "=&v"(c) : "v"(a), "v"(b))
#define SMM_V(c, a, b, i) asm volatile("v_smfmac_f32_16x16x64_bf16 %0, %1, %2, %3" : "+v"(c) : "v"(a), "v"(b), "v"(i))
#define SMM_A(c, a, b, i) asm volatile("v_smfmac_f32_16x16x64_bf16 %0, %1, %2, %3" : "+v"(c) : "v"(a), "a"(b), "v"(i))
#define NOPV() asm volatile("s_nop 1")
#define PIN(x) asm volatile("" : "+v"(x))
#define SETTLE() asm volatile("s_nop 7\n\ts_nop 3")
__device__ __forceinline__ bf8 pack8(f32x4 a, f32x4 b) {
    u32x4 r = {pk2bf(a[0], a[1]), pk2bf(a[2], a[3]), pk2bf(b[0], b[1]), pk2bf(b[2], b[3])};
    return __builtin_bit_cast(bf8, r);
}
__device__ __forceinline__ u32 max4(u32 m, u32x4 g) { m = max(max(m, g[0]), g[1]); return max(max(m, g[2]), g[3]); }
#define ZERO4(c) asm volatile("v_mov_b64 %0, 0\n\tv_mov_b64 %1, 0" : "=v"(((u64*)&(c))[0]), "=v"(((u64*)&(c))[1]))
__device__ __forceinline__ bf8 lds8(const char* shm, int off) { return *(const bf8*)(shm + off); }
__device__ __forceinline__ bf16v ldfrag(const char* p) { return cat8(*(const bf8*)p, *(const bf8*)(p + 1024)); }
template <int CTRL> __device__ __forceinline__ float fdpp(float x) {
    return __builtin_bit_cast(float, __builtin_amdgcn_mov_dpp(__builtin_bit_cast(int, x), CTRL, 0xf, 0xf, true));
}
__device__ __forceinline__ float halfsum(float a, float b) {
    auto r = __builtin_amdgcn_permlane32_swap(__float_as_uint(a), __float_as_uint(b), false, false);
    return __uint_as_float(r[0]) + __uint_as_float(r[1]);
}
__device__ __forceinline__ float grp_sum(float x) {
    auto t = __builtin_amdgcn_permlane16_swap(__float_as_uint(x), __float_as_uint(x), false, false);
    x = __uint_as_float(t[0]) + __uint_as_float(t[1]);
    return halfsum(x, x);
}
__device__ __forceinline__ float grp_max(float x) {
    auto t = __builtin_amdgcn_permlane16_swap(__float_as_uint(x), __float_as_uint(x), false, false);
    x = fmaxf(__uint_as_float(t[0]), __uint_as_float(t[1]));
    auto u = __builtin_amdgcn_permlane32_swap(__float_as_uint(x), __float_as_uint(x), false, false);
    return fmaxf(__uint_as_float(u[0]), __uint_as_float(u[1]));
}
__device__ __forceinline__ float bflo(u32 v) { return __uint_as_float(v << 16); }
__device__ __forceinline__ float bfhi(u32 v) { return __uint_as_float(v & 0xffff0000u); }
__device__ __forceinline__ void st4_lane0(char* base, u32 v) {
    u32 z, d;
    asm volatile("s_mov_b64 exec, 1\n\tv_mov_b32 %0, 0\n\tv_mov_b32 %1, %3\n\tglobal_store_dword %0, %1, %2 sc1\n\ts_mov_b64 exec, -1" : "=&v"(z), "=&v"(d) : "s"(base), "s"(v) : "memory");
}
__device__ __forceinline__ int track(int d, int sample) {
    if (sample <= 0) return d;
    return sample >= d ? min(sample, d + DUP) : (d - sample > 2 ? d - ((d - sample) >> 1) : d);
}
__device__ __forceinline__ u32 sched_peek(const char* p) { return __hip_atomic_load((const gu32*)p, __ATOMIC_RELAXED, __HIP_MEMORY_SCOPE_AGENT); }
__device__ __forceinline__ u32 sched_get(u32 v, const char* p, int& cap) {
    for (int spin = 0; v == ~0u; ++spin) {
        if (spin >= cap) { cap = 1; break; }
        __builtin_amdgcn_s_sleep(3);
        v = sched_peek(p);
    }
    return __builtin_amdgcn_readfirstlane(v);
}

#define TRDECL
#define TR(i)
#define TRV(i, v)
#define TRFLUSH(role)
#define TRC(i)
constexpr size_t ACT = (size_t)S_TOK * DM * 2;
constexpr size_t O_DBG = 0, O_CTL = 2u << 20, O_ST = O_CTL + 4096, O_FL = O_ST + 128 * 1280, O_PX = O_FL + 3 * 192 * 128, O_X1 = O_PX + 2 * PX_BYTES,
                 O_OB = O_X1 + 2 * ACT, O_XN = O_OB + 2 * ACT, O_XR = O_XN + 2 * ACT, O_WB = O_XR + 2 * ACT, O_WO = O_WB + 32 * 81920,
                 O_WQK = O_WO + 10 * 81920, O_WVO = O_WQK + (size_t)32 * 4 * 60 * 2048, O_HOP = O_WVO + (size_t)80 * 4 * 40 * 2048, WS_BYTES = O_HOP + (4u << 20);
#define TT(role, i)
struct LaParams { char* ws; u16* out; char* hop; };
__device__ __forceinline__ char* wsp(const LaParams& p) { char* b = p.ws; asm volatile("" : "+s"(b)); return b; }
template <int N> __device__ __forceinline__ void wait_flags(const char* f, const u32 (&off)[N], int& cap) {
    for (int spin = 0;; ++spin) {
        u32 m = 0;
#pragma unroll
        for (int i = 0; i < N; ++i) m = max(m, sched_peek(f + off[i]));
        if (!__any(m == ~0u)) break;
        if (spin >= cap) { cap = 1; break; }
        __builtin_amdgcn_s_sleep(4);
    }
    asm volatile("" ::: "memory");
}
__device__ __forceinline__ void wait_all_tokens(const LaParams& p, u32 lane, int& cap) {
    asm volatile("" : "+v"(lane));
    const u32 off[3] = {lane * 128, lane * 128 + 8192, min(lane + 128, 159u) * 128};
    wait_flags<3>(wsp(p) + O_FL + 192 * 128, off, cap);
}
__device__ __forceinline__ int get_side(const LaParams& p, int& cap) { return sched_get(sched_peek(wsp(p) + O_FL + 384 * 128), wsp(p) + O_FL + 384 * 128, cap) & 1; }
__device__ __forceinline__ char* align2m(char* p) { return p + ((size_t)(-(intptr_t)p) & ((2u << 20) - 1)); }

constexpr int Q_XA = 0, Q_PART = 20480, Q_TR = 48640, Q_WL = 54784, Q_ST = 153088, Q_PS = 44;

#define DPP1(op, ror) op "_dpp %0, %0, %0 row_ror:" ror " row_mask:0xf bank_mask:0xf\n\t"
#define DPP2(op, ror) DPP1(op, ror) op "_dpp %1, %1, %1 row_ror:" ror " row_mask:0xf bank_mask:0xf\n\ts_nop 0\n\t"
__device__ __forceinline__ void st16m(char* base, u32 off, u32x4 v, u64 mask) {
    asm volatile("s_mov_b64 exec, %3\n\tglobal_store_dwordx4 %0, %1, %2 sc1\n\ts_mov_b64 exec, -1\n\ts_nop 1" :: "v"(off), "v"(v), "s"(base), "s"(mask) : "memory");
}
__device__ __forceinline__ void st8m(char* base, u32 off, u32x2 v, u64 mask) {
    asm volatile("s_mov_b64 exec, %3\n\tglobal_store_dwordx2 %0, %1, %2 sc1\n\ts_mov_b64 exec, -1" :: "v"(off), "v"(v), "s"(base), "s"(mask) : "memory");
}
__device__ __forceinline__ u32 pk2bf_abs(float lo, float hi) {
    u32 r;
    asm("v_cvt_pk_bf16_f32 %0, |%1|, |%2|" : "=v"(r) : "v"(lo), "v"(hi));
    return r;
}
constexpr float SM_C = 0.07216878364870322f * 1.4426950408889634f, NEG_BIG = -0x1p100f;
constexpr u64 SM_MASK = 0x0155015501550155ull;
__device__ __forceinline__ void softmax_mem(const float* p, char* dst, const u32 off) {
    f32x4 a[4];
#pragma unroll
    for (int i = 0; i < 4; ++i) a[i] = *(const f32x4*)(p + i * 40 * Q_PS);
    f32x4 x = (a[0] + a[1]) + (a[2] + a[3]);
    const float ml = fmaxf(fmaxf(x[0], x[1]), fmaxf(x[2], x[3])) * SM_C;
    float mx = ml, e0, e1, e2, e3;
    asm("v_fma_f32 %1, %5, %9, -%0\n\tv_fma_f32 %2, %6, %9, -%0\n\tv_fma_f32 %3, %7, %9, -%0\n\tv_fma_f32 %4, %8, %9, -%0\n\t"
        DPP1("v_max_f32", "8") "v_exp_f32 %1, %1\n\ts_nop 0\n\t" DPP1("v_max_f32", "4") "v_exp_f32 %2, %2\n\ts_nop 0\n\t"
        DPP1("v_max_f32", "2") "v_exp_f32 %3, %3\n\ts_nop 0\n\t" DPP1("v_max_f32", "1") "v_exp_f32 %4, %4\n\ts_nop 0"
        : "+v"(mx), "=&v"(e0), "=&v"(e1), "=&v"(e2), "=&v"(e3) : "v"(x[0]), "v"(x[1]), "v"(x[2]), "v"(x[3]), "s"(SM_C));
    const float f = __builtin_amdgcn_exp2f(ml - mx);
    float sm = ((e0 + e1) + (e2 + e3)) * f;
    asm("s_nop 1\n\t" DPP1("v_add_f32", "8") "v_mul_f32 %1, %1, %5\n\ts_nop 0\n\t" DPP1("v_add_f32", "4") "v_mul_f32 %2, %2, %5\n\ts_nop 0\n\t"
        DPP1("v_add_f32", "2") "v_mul_f32 %3, %3, %5\n\ts_nop 0\n\t" DPP1("v_add_f32", "1") "v_mul_f32 %4, %4, %5"
        : "+v"(sm), "+v"(e0), "+v"(e1), "+v"(e2), "+v"(e3) : "v"(f));
    const float inv = __builtin_amdgcn_rcpf(sm);
    const u32 d0 = pk2bf_abs(e0 * inv, e1 * inv), d1 = pk2bf_abs(e2 * inv, e3 * inv);
    u32 n0, n1;
    asm("s_nop 0\n\tv_mov_b32_dpp %0, %2 quad_perm:[1,0,3,2] row_mask:0xf bank_mask:0xf\n\tv_mov_b32_dpp %1, %3 quad_perm:[1,0,3,2] row_mask:0xf bank_mask:0xf"
        : "=&v"(n0), "=&v"(n1) : "v"(d0), "v"(d1));
    st16m(dst, off, (u32x4){d0, d1, n0, n1}, SM_MASK);
}
__device__ __forceinline__ void softmax_tok(const char* shm, const int q0, int lane, char* dst) {
    asm volatile("" : "+v"(lane));
    const int r = lane >> 4, j = lane & 15;
    const float* p = (const float*)(shm + Q_PART) + (q0 + r) * Q_PS + min(j, 10) * 4;
    f32x4 x[2];
    float mx[2], sm[2];
#pragma unroll
    for (int n = 0; n < 2; ++n) {
        f32x4 a[4];
#pragma unroll
        for (int i = 0; i < 4; ++i) a[i] = *(const f32x4*)(p + (i * 40 + n * 4) * Q_PS);
        x[n] = (a[0] + a[1]) + (a[2] + a[3]);
        mx[n] = fmaxf(fmaxf(x[n][0], x[n][1]), fmaxf(x[n][2], x[n][3])) * SM_C;
    }
    asm("s_nop 1\n\t" DPP2("v_max_f32", "8") DPP2("v_max_f32", "4") DPP2("v_max_f32", "2") DPP2("v_max_f32", "1") : "+v"(mx[0]), "+v"(mx[1]));
#pragma unroll
    for (int n = 0; n < 2; ++n) {
#pragma unroll
        for (int i = 0; i < 4; ++i) x[n][i] = __builtin_amdgcn_exp2f(fmaf(x[n][i], SM_C, -mx[n]));
        sm[n] = (x[n][0] + x[n][1]) + (x[n][2] + x[n][3]);
    }
    asm("s_nop 1\n\t" DPP2("v_add_f32", "8") DPP2("v_add_f32", "4") DPP2("v_add_f32", "2") DPP2("v_add_f32", "1") : "+v"(sm[0]), "+v"(sm[1]));
#pragma unroll
    for (int n = 0; n < 2; ++n) {
        const float inv = __builtin_amdgcn_rcpf(sm[n]);
        const u32 d0 = pk2bf_abs(x[n][0] * inv, x[n][1] * inv), d1 = pk2bf_abs(x[n][2] * inv, x[n][3] * inv);
        u32 n0, n1;
        asm("s_nop 1\n\tv_mov_b32_dpp %0, %2 quad_perm:[1,0,3,2] row_mask:0xf bank_mask:0xf\n\tv_mov_b32_dpp %1, %3 quad_perm:[1,0,3,2] row_mask:0xf bank_mask:0xf"
            : "=&v"(n0), "=&v"(n1) : "v"(d0), "v"(d1));
        st16m(dst, (j < 8 ? n * 256 + j * 32 : 512 + n * 64) + r * 16, (u32x4){d0, d1, n0, n1}, SM_MASK);
    }
}

__host__ __device__ constexpr int qk_slot(int T, int ks) { return ks < 5 ? ks * 6 + T : 30 + (T >> 1) * 10 + (ks - 5) * 2 + (T & 1); }
__host__ __device__ constexpr int lds_slot(int n) { return n < 7 ? 5 * n + 2 : (n == 7 ? 38 : (n == 8 ? 41 : (n == 9 ? 46 : (n == 10 ? 51 : 55)))); }
__host__ __device__ constexpr int slot_lds(int sl) { for (int n = 0; n < 12; ++n) if (lds_slot(n) == sl) return n; return -1; }
__host__ __device__ constexpr int slot_frag(int sl) { if (slot_lds(sl) >= 0) return 48 + slot_lds(sl); int n = 0; for (int i = 0; i < 12; ++i) n += lds_slot(i) < sl; return sl - n; }
__host__ __device__ constexpr int qk_frag(int T, int ks) { return slot_frag(qk_slot(T, ks)); }
__host__ __device__ constexpr int slot_T(int sl) { return sl < 30 ? sl % 6 : ((sl - 30) / 10) * 2 + (sl & 1); }
__host__ __device__ constexpr int slot_ks(int sl) { return sl < 30 ? sl / 6 : 5 + ((sl - 30) % 10) / 2; }
__host__ __device__ constexpr int lds_rank(int T, int ks) { int n = 0; for (int t = 0; t < T; ++t) n += qk_frag(t, ks) >= 48; return n; }
__host__ __device__ constexpr int lds_nth(int ks, int n) { for (int t = 0; t < 6; ++t) if (qk_frag(t, ks) >= 48 && lds_rank(t, ks) == n) return qk_frag(t, ks) - 48; return -1; }

__device__ __forceinline__ void qk_role(const LaParams& p, const int set, const int h, char* shm) {
    const int lane = threadIdx.x & 63, w = __builtin_amdgcn_readfirstlane(threadIdx.x >> 6);
    const int fr = lane & 15, g = lane >> 4;
    const u32 lds0 = (u32)(uintptr_t)(LDS3 char*)shm;
    const int sidx = (lane & 8) ? 0xEEEE : 0x4444;
    int cap = SPIN_CAP;
    char* hop = p.hop;
    char* mine = hop;
    int phys = 0;
    char* px = (wsp(p) + O_PX);
    const char* xn = (wsp(p) + O_XN);
    const char* wl = shm + Q_WL + w * 24576 + lane * 16;
    const char* xa = shm + Q_XA + lane * 16;

    bf16v WA[32], WV[16];
    {
        const char* wb = (p.ws + O_WQK) + ((size_t)(h * 4 + w) * 60) * 2048 + lane * 16;
        dma_kb<24>((wsp(p) + O_WQK) + ((size_t)(h * 4 + w) * 60 + 48) * 2048, lds0 + Q_WL + w * 24576, lane * 16);
        dma_kb<5>(xn + (size_t)set * 40960 + w * 5120, lds0 + Q_XA + w * 5120, lane * 16);
#pragma unroll
        for (int i = 0; i < 32; ++i) WA[i] = ldfrag(wb + i * 2048);
#pragma unroll
        for (int i = 0; i < 16; ++i) WV[i] = ldfrag(wb + (32 + i) * 2048);
    }
    TRDECL;
    bf8 QX[2], KX[2];
    u32x2 QY[2], KY[2];
    u32 peek = ~0u;
    int d1 = D1I, d2 = D2I;
    if (w == 3) *(u64*)(shm + Q_ST + lane * 8) = 0;
    auto xproj = [&](const int snext, const int sprev) {
        f32x4 acc[6][2];
        TR(8);
        const char* xs1 = xn + (size_t)(snext - 2) * 40960 + 20480 + w * 5120;
        const char* xs0 = xn + (size_t)min(snext, NSEG - 1) * 40960 + w * 5120;
        u32 xl = lds0 + Q_XA + w * 5120;
        asm volatile("" : "+s"(xs0), "+s"(xs1), "+s"(xl));
        bf8 x0 = *(const bf8*)(xa), x1 = *(const bf8*)(xa + 1024), lw[2];
        lw[0] = *(const bf8*)(wl);
        static_for<0, 20>([&](auto K) {
            constexpr int k = CI(K), l0 = lds_nth(((k + 1) >> 1) % 10, 0), l1 = lds_nth(((k + 1) >> 1) % 10, 1);
            bf8 n0, n1, nw[2];
            if constexpr (k == 9) {
                TR(9);
                VM0(); RAW_BARRIER();
                TR(10);
            }
            static_for<0, 6>([&](auto TT) {
                constexpr int T = CI(TT), id = qk_frag(T, k >> 1), rk = lds_rank(T, k >> 1);
                if constexpr (k < 19) {
                    if constexpr (T == 0) n0 = *(const bf8*)(xa + (k + 1) * 2048);
                    if constexpr (T == 1) n1 = *(const bf8*)(xa + (k + 1) * 2048 + 1024);
                    if constexpr (T == 2 && l0 >= 0) nw[0] = *(const bf8*)(wl + l0 * 2048 + ((k + 1) & 1) * 1024);
                    if constexpr (T == 3 && l1 >= 0) nw[1] = *(const bf8*)(wl + l1 * 2048 + ((k + 1) & 1) * 1024);
                }
                if constexpr (k < 3 && (T == 3 || T == 5) && 2 * k + (T == 5) < 5)
                    dma_kb<1>(xs1 + (2 * k + (T == 5)) * 1024, xl + 20480 + (2 * k + (T == 5)) * 1024, lane * 16);
                if constexpr (k >= 9 && k < 14 && T == 4)
                    dma_kb<1>(xs0 + (k - 9) * 1024, xl + (k - 9) * 1024, lane * 16);
                if constexpr (k == 18 && T == 4) peek = sched_peek(mine + (S_SLOT + w) * SLOT + max(sprev, 0) * 128 + 4);
                SCHEDB();
                if constexpr (id < 32) {
                    const bf8 wf = (k & 1) ? hi8(WA[id]) : lo8(WA[id]);
                    if constexpr (k == 0) { MMA0_AV(acc[T][0], wf, x0); MMA0_AV(acc[T][1], wf, x1); } else { MMA_AV(acc[T][0], wf, x0); MMA_AV(acc[T][1], wf, x1); }
                } else {
                    bf8 wf;
                    if constexpr (id < 48) wf = (k & 1) ? hi8(WV[id - 32]) : lo8(WV[id - 32]); else wf = lw[rk];
                    if constexpr (k == 0) { MMA0_VV(acc[T][0], wf, x0); MMA0_VV(acc[T][1], wf, x1); } else { MMA_VV(acc[T][0], wf, x0); MMA_VV(acc[T][1], wf, x1); }
                }
                SCHEDB();
            });
            if constexpr (k < 19) { x0 = n0; x1 = n1; if constexpr (l0 >= 0) lw[0] = nw[0]; if constexpr (l1 >= 0) lw[1] = nw[1]; }
        });
        TR(11);
        int ln = lane;
        asm volatile("" : "+v"(ln));
        f32x4 s[3];
#pragma unroll
        for (int i = 0; i < 4; ++i) s[0][i] = (ln >> 4) * 4 + i > (ln & 15) ? NEG_BIG : 0.f;
        s[2] = s[0];
        float* pp = (float*)(shm + Q_PART) + (w * 40 + 8 + (ln & 15)) * Q_PS + (ln >> 4) * 8;
        PIN(s[0]); PIN(s[2]);
        RAW_BARRIER();
#pragma unroll
        for (int T = 0; T < 6; ++T) { PIN(acc[T][0]); PIN(acc[T][1]); }
#pragma unroll
        for (int tt = 0; tt < 2; ++tt) { QX[tt] = pack8(acc[0][tt], acc[2][tt]); KX[tt] = pack8(acc[1][tt], acc[3][tt]); PIN(QX[tt]); PIN(KX[tt]); }
        NOPV();
        MMA_VV(s[0], KX[0], QX[0]); MMA0_VV(s[1], KX[0], QX[1]); MMA_VV(s[2], KX[1], QX[1]);
#pragma unroll
        for (int tt = 0; tt < 2; ++tt) {
            PIN(acc[4][tt]); PIN(acc[5][tt]);
            QY[tt] = (u32x2){pk2bf(acc[4][tt][0], acc[4][tt][1]), pk2bf(acc[4][tt][2], acc[4][tt][3])};
            KY[tt] = (u32x2){pk2bf(acc[5][tt][0], acc[5][tt][1]), pk2bf(acc[5][tt][2], acc[5][tt][3])};
            PIN(QY[tt]); PIN(KY[tt]);
        }
        NOPV();
        MMA16(s[0], KY[0], QY[0]); MMA16(s[1], KY[0], QY[1]); MMA16(s[2], KY[1], QY[1]);
        asm volatile("s_nop 7");
        PIN(s[0]); PIN(s[1]); PIN(s[2]);
        *(f32x4*)(pp) = s[0];
        *(f32x4*)(pp + 16 * Q_PS) = s[1];
        *(f32x4*)(pp + 16 * Q_PS + 4) = s[2];
    };
    {
        const f32x4 neg = {NEG_BIG, NEG_BIG, NEG_BIG, NEG_BIG};
        *(f32x4*)((float*)(shm + Q_PART) + (w * 40 + 8 + fr) * Q_PS + g * 8 + 4) = neg;
        if (lane < 40) *(f32x4*)((float*)(shm + Q_PART) + (w * 40 + lane) * Q_PS + 40) = neg;
    }
    float* pw0 = (float*)(shm + Q_PART) + (w * 40 + (fr & 7)) * Q_PS + g * 8 + (fr >> 3) * 4;
    float* pw2 = fr < 8 && g >= 2 ? (float*)(shm + Q_PART) + (w * 40 + fr) * Q_PS + 24 + g * 4 : (float*)(shm + Q_TR + w * 1536) + lane * 4;
    const float* prd = (const float*)(shm + Q_PART) + ((w & 1) * 4 + g) * Q_PS + min(fr, 10) * 4;
    u32 trw = lds0 + Q_TR + w * 1536 + lane * 8, trr = lds0 + Q_TR + w * 1536 + (fr & 3) * 128 + (4 * g + (fr >> 2)) * 8;
    asm volatile("" : "+v"(trw), "+v"(trr));
    const u32 poff = (fr < 8 ? (w & 1) * 256 + fr * 32 : 512 + (w & 1) * 64) + g * 16;

    for (int layer = 0; layer < 2; ++layer) {
    if (layer > 0) {
        if (w == 0) {
            __builtin_amdgcn_s_sleep(127);
            wait_rt(sched_get(~0u, mine + S_SLOT * SLOT + 63 * 128, cap) + T_TOK);
            u32 ln = min(lane, 39);
            asm volatile("" : "+v"(ln));
            const u32 off[1] = {(set * 20 + ln + (ln >= 20 ? 20 : 0)) * 128};
            wait_flags<1>(wsp(p) + O_FL, off, cap);
        }
        RAW_BARRIER();
        hop += HOP_BYTES; mine += HOP_BYTES; px += PX_BYTES; xn += ACT;
        TT(set * 128 + h, 1);
        dma_kb<5>(xn + (size_t)set * 40960 + w * 5120, lds0 + Q_XA + w * 5120, lane * 16);
    }
    VM0(); RAW_BARRIER();
    TT(set * 128 + h, 2);
    xproj(set + 2, set - 1);
    if (layer == 0) { phys = set ^ get_side(p, cap); mine += phys * 8192; }
    TT(set * 128 + h, 3);
    for (int s = set; s < NSEG; s += 2) {
        const int rot = (s >> 1) & 3;
        TR(0);
        u32 ytime = 0;
        const char* mb0 = (s > 0 ? mine + (M_SLOT0 + rot * 8) * SLOT : p.hop + M0_OFF + phys * 8192) + 2 * w * SLOT + 2048;
        const char* mb1 = mb0 + SLOT;
        char* pdst = mine + (P_SLOT0 + rot * 4 + (h >> 3)) * SLOT + (h & 7) * 640;
        asm volatile("" : "+s"(mb0), "+s"(mb1), "+s"(pdst));
        if (s > 0) { ytime = sched_get(peek, mine + (S_SLOT + w) * SLOT + (s - 1) * 128 + 4, cap); wait_rt(ytime); }
        TR(1); TRV(7, ytime);
        f32x4 acc[6];
        u32x4 A[10];
        bf8 Ll[2], Lh[2];
        s16x4 F[3];
        u32 r0[4], r1[4], d[2];
        float v[4];
        int tries = 0;
        auto fin = [&](const int pr, const int i) {
            if (i == 0) { PIN(acc[2 * pr]); PIN(acc[2 * pr + 1]); }
            if (i < 4) { auto r = __builtin_amdgcn_permlane32_swap(__float_as_uint(acc[2 * pr][i]), __float_as_uint(acc[2 * pr + 1][i]), false, false); r0[i] = r[0]; r1[i] = r[1]; }
            if (i >= 1 && i < 5) { asm("v_add_f32 %0, %1, %2" : "=v"(v[i - 1]) : "v"(r0[i - 1]), "v"(r1[i - 1])); }
            if (i == 4) d[0] = pk2bf(v[0], v[1]);
            if (i == 5) {
                d[1] = pk2bf(v[2], v[3]);
                *(LDS3 u32x2*)(uintptr_t)(trw + pr * 512) = (u32x2){d[0], d[1]};
                F[pr] = __builtin_amdgcn_ds_read_tr16_b64_v4i16((LDS3 s16x4*)(uintptr_t)(trr + pr * 512));
            }
        };
        auto slot = [&](auto SL) {
            constexpr int sl = CI(SL), T = slot_T(sl), ks = slot_ks(sl), id = slot_frag(sl);
            if constexpr (id < 32) SMM_A(acc[T], A[ks], WA[id], sidx);
            else if constexpr (id < 48) SMM_V(acc[T], A[ks], WV[id - 32], sidx);
            else { const bf16v L = cat8(Ll[id & 1], Lh[id & 1]); SMM_V(acc[T], A[ks], L, sidx); }
            SCHEDB();
            if constexpr (sl == 0) TRC(6);
            constexpr int n0 = slot_lds(sl), n1 = slot_lds(sl - 1);
            if constexpr (n0 >= 0 && n0 < 10) Ll[n0 & 1] = *(const bf8*)(wl + (n0 + 2) * 2048);
            if constexpr (n1 >= 0 && n1 < 10) Lh[n1 & 1] = *(const bf8*)(wl + (n1 + 2) * 2048 + 1024);
            if constexpr (sl >= 43 && sl < 49) fin(0, sl - 43);
            if constexpr (sl >= 53 && sl < 59) fin(1, sl - 53);
            SCHEDB();
        };
        auto gather = [&]() {
            u32 vo = lane * 16;
            asm volatile("" : "+v"(vo) :: "memory");
#pragma unroll
            for (int ks = 0; ks < 10; ++ks) {
                A[ks] = ld16(ks < 5 ? mb0 : mb1, vo, (ks % 5 - 2) * 1024);
                SCHEDB();
                if (ks < 6) ZERO4(acc[ks]);
                if (ks == 6 || ks == 7) { Ll[ks & 1] = *(const bf8*)(wl + (ks & 1) * 2048); Lh[ks & 1] = *(const bf8*)(wl + (ks & 1) * 2048 + 1024); }
                if (ks < 9) asm volatile("s_nop %0" :: "n"(PACE));
                SCHEDB();
            }
        };
        gather();
        static_for<0, 49>(slot);
        if (__builtin_expect(__any(__builtin_isunordered(v[0], v[1]) || __builtin_isunordered(v[2], v[3])), 0)) {
            SETTLE();
            while (true) {
                gather();
                u32 m = 0;
#pragma unroll
                for (int ks = 0; ks < 10; ++ks) m = max4(m, A[ks]);
                ++tries;
                if (!__any(m == ~0u)) break;
                if (tries >= cap) { cap = 1; break; }
                __builtin_amdgcn_s_sleep(4);
            }
            static_for<0, 49>(slot);
        }
        TR(2); TRV(6, tries + 1);
        static_for<49, 51>(slot);
        if (__builtin_expect(h == 0 && w == 3, 0)) {
            u32 base = (tries == 0 && s > 0) ? ytime : rt32() + ELATE;
            u32 ln8 = lane;
            asm volatile("" : "+v"(ln8));
            ln8 *= 8192;
            const u32x2 rec = {nosent(base + d1 + MG1), nosent(base + d1 + MG1 + d2 + MG2)};
            asm volatile("s_mov_b64 exec, 0x3ff\n\tglobal_store_dwordx2 %0, %1, %2 sc1\n\ts_mov_b64 exec, -1" :: "v"(ln8), "v"(rec), "s"(hop + S_SLOT * SLOT + s * 128) : "memory");
        }
        static_for<51, 60>(slot);
        TRC(7);
        bf8 F01 = __builtin_bit_cast(bf8, __builtin_shufflevector(F[0], F[1], 0, 1, 2, 3, 4, 5, 6, 7));
        f32x4 s0, s1, s2, u0, u1;
        asm volatile("s_nop 6");
#pragma unroll
        for (int i = 0; i < 6; ++i) fin(2, i);
        PIN(F01); NOPV();
        MMA0_VV(s1, KX[1], F01); MMA0_VV(s0, KX[0], F01); MMA0_VV(s2, F01, F01); MMA0_VV(u0, F01, QX[0]); MMA0_VV(u1, F01, QX[1]);
        asm volatile("s_nop 3");
        MMA16(s1, KY[1], F[2]); MMA16(s0, KY[0], F[2]); MMA16(s2, F[2], F[2]); MMA16(u0, F[2], QY[0]); MMA16(u1, F[2], QY[1]);
        asm volatile("s_nop 4\n\tv_mov_b32_dpp %0, %4 row_ror:8 row_mask:0xf bank_mask:0xc\n\tv_mov_b32_dpp %1, %5 row_ror:8 row_mask:0xf bank_mask:0xc\n\t"
                     "v_mov_b32_dpp %2, %6 row_ror:8 row_mask:0xf bank_mask:0xc\n\tv_mov_b32_dpp %3, %7 row_ror:8 row_mask:0xf bank_mask:0xc"
                     : "+v"(s0[0]), "+v"(s0[1]), "+v"(s0[2]), "+v"(s0[3]) : "v"(s1[0]), "v"(s1[1]), "v"(s1[2]), "v"(s1[3]));
        PIN(s2); *(f32x4*)pw2 = s2; SCHEDB();
        *(f32x4*)pw0 = s0; SCHEDB();
        if (__builtin_expect(w < 2, 1)) {
            RAW_BARRIER();
            TR(3);
            softmax_mem(prd, pdst, poff);
            TR(4);
            if (w == 1) st4_lane0(mine + (T_SLOT + 1) * SLOT + (16 + h) * 128, tries == 0 && s > 0 ? rt32() - ytime : 0);
        } else {
            RAW_BARRIER();
            TR(3);
            if (h == 0 && w == 3) {
                u32 lo = lane;
                asm volatile("" : "+v"(lo));
                int sta = *(const int*)(shm + Q_ST + lo * 4), stb = *(const int*)(shm + Q_ST + 256 + lo * 4);
                asm("s_nop 1\n\t" DPP2("v_max_i32", "8") DPP2("v_max_i32", "4") DPP2("v_max_i32", "2") DPP2("v_max_i32", "1") : "+v"(sta), "+v"(stb));
                const int m1 = max(__builtin_amdgcn_readlane(stb, 16), __builtin_amdgcn_readlane(stb, 32));
                const int m2 = max(max(max(__builtin_amdgcn_readlane(sta, 0), __builtin_amdgcn_readlane(sta, 16)), max(__builtin_amdgcn_readlane(sta, 32), __builtin_amdgcn_readlane(sta, 48))),
                                   __builtin_amdgcn_readlane(stb, 0));
                d1 = track(d1, m1); d2 = track(d2, m2);
                u32 keep;
                asm volatile("s_mov_b32 %0, m0\n\ts_mov_b32 m0, %4\n\ts_nop 0\n\tglobal_load_lds_dword %1, %2 sc1\n\ts_add_u32 m0, m0, 256\n\ts_nop 0\n\tglobal_load_lds_dword %1, %3 sc1\n\ts_mov_b32 m0, %0"
                             : "=&s"(keep) : "v"(lo * 128), "s"(mine + T_SLOT * SLOT), "s"(mine + (T_SLOT + 1) * SLOT), "s"(lds0 + Q_ST), "s"(d1), "s"(d2) : "memory", "scc");
            }
            if (w == 2 && s < 2 && layer > 0) wait_all_tokens(p, lane, cap);
            if (w == 2 && s >= 2 && lane < 40) arm16(mine + (P_SLOT0 + ((rot + 3) & 3) * 4 + (h >> 3)) * SLOT + (h & 7) * 640, lane * 16);
            if (s >= 4 && lane < 40) {
                char* o = px + (size_t)(set * 4 + ((rot + 2) & 3)) * PX_BUF + ((w - 2) * 64 + h) * 640;
                arm16(o, lane * 16); arm16(o + 32 * 640, lane * 16);
            }
        }
        {
            int ln = lane;
            asm volatile("" : "+v"(ln));
            PIN(u0); PIN(u1);
            f32x4 u;
#pragma unroll
            for (int i = 0; i < 4; ++i) u[i] = __uint_as_float(__builtin_amdgcn_permlane32_swap(__float_as_uint(u0[i]), __float_as_uint(u1[i]), false, false)[1]);
            *(f32x4*)((float*)(shm + Q_PART) + (w * 40 + 8 + (ln >> 5) * 16 + (ln & 15)) * Q_PS + 32 + ((ln >> 4) & 1) * 4) = u;
        }
        RAW_BARRIER();
        softmax_tok(shm, 8 + 8 * w, lane, px + (size_t)(set * 4 + rot) * PX_BUF + (w * 32 + h) * 640);
        TR(5); TRFLUSH(set * 128 + h);
        if (s + 2 >= NSEG) { TT(set * 128 + h, 0); break; }
        RAW_BARRIER();
        xproj(s + 4, s + 1);
    }
    }
}

constexpr int C_YL = 0, C_RED = 20480, C_EX = 28672, C_XN = 32768;
__device__ __forceinline__ void comb_role(const LaParams& p, const int set, const int c, char* shm) {
    const int lane = threadIdx.x & 63, w = __builtin_amdgcn_readfirstlane(threadIdx.x >> 6);
    const int fr = lane & 15, g = lane >> 4;
    const u32 lds0 = (u32)(uintptr_t)(LDS3 char*)shm;
    const int sidx = (lane & 8) ? 0xEEEE : 0x4444;
    int cap = SPIN_CAP;
    char* mine = p.hop;
    char* other = p.hop;
    int phys = 0;
    char* px = (wsp(p) + O_PX);
    const char* xn = (wsp(p) + O_XN);
    char* yl = shm + C_YL + lane * 16;
    const int cb = (set * 80 + c) % 10, rb = (set * 80 + c) / 10;

    bf16v WA[32], WV[8];
    {
        const char* wb = (p.ws + O_WVO) + ((size_t)(c * 4 + w) * 40) * 2048 + lane * 16;
        dma_kb<10>(xn + (size_t)set * 40960 + w * 10240, lds0 + C_XN + w * 10240, lane * 16);
#pragma unroll
        for (int i = 0; i < 32; ++i) WA[i] = ldfrag(wb + i * 2048);
#pragma unroll
        for (int i = 0; i < 8; ++i) WV[i] = ldfrag(wb + (32 + i) * 2048);
    }
    auto yx = [&](bf8 (&yn)[4], const int sy, auto&& extra) {
        f32x4 acc[4][2];
        const char* xa = shm + C_XN + ((sy >> 1) & 1) * 40960 + lane * 16;
        const char* xs = (sy + 2 < NSEG ? xn + (size_t)(sy + 2) * 40960 : (wsp(p) + O_WO) + cb * 81920) + w * 10240;
        u32 xl = lds0 + C_XN + (~(sy >> 1) & 1) * 40960 + w * 10240;
        asm volatile("" : "+s"(xs), "+s"(xl));
        bf8 x0 = *(const bf8*)(xa), x1 = *(const bf8*)(xa + 1024);
#pragma unroll
        for (int k = 0; k < 20; ++k) {
            bf8 n0, n1;
#pragma unroll
            for (int t = 0; t < 4; ++t) {
                if (k < 19 && t == 0) n0 = *(const bf8*)(xa + (k + 1) * 2048);
                if (k < 19 && t == 1) n1 = *(const bf8*)(xa + (k + 1) * 2048 + 1024);
                if (t >= 2 && k < 5) dma_kb<1>(xs + (2 * k + t - 2) * 1024, xl + (2 * k + t - 2) * 1024, lane * 16);
                if (t >= 2 && k >= 5 && k < 15) extra(2 * k + t - 12);
                SCHEDB();
                const int id = (k >> 1) * 4 + t;
                if (id < 32) {
                    const bf8 wf = (k & 1) ? hi8(WA[id & 31]) : lo8(WA[id & 31]);
                    if (k == 0) { MMA0_VA(acc[t][0], x0, wf); MMA0_VA(acc[t][1], x1, wf); } else { MMA_VA(acc[t][0], x0, wf); MMA_VA(acc[t][1], x1, wf); }
                } else {
                    const bf8 wf = (k & 1) ? hi8(WV[id & 7]) : lo8(WV[id & 7]);
                    MMA_VV(acc[t][0], x0, wf); MMA_VV(acc[t][1], x1, wf);
                }
                SCHEDB();
            }
            if (k < 19) { x0 = n0; x1 = n1; }
        }
        SETTLE();
#pragma unroll
        for (int t = 0; t < 4; ++t) { PIN(acc[t][0]); PIN(acc[t][1]); yn[t] = pack8(acc[t][0], acc[t][1]); }
    };
    const u32 pofs = (fr >> 3) * 640 + ((fr >> 2) & 1) * 256 + g * 64 + (fr & 3) * 16;
    const u32 mofs = (2 * g + (fr >> 3)) * 640 + 512 + (fr & 7) * 16;

    for (int layer = 0; layer < 2; ++layer) {
    u32 peek = ~0u;
    VM0(); RAW_BARRIER();
    {
        bf8 yn[4];
        yx(yn, set, [](int) {});
#pragma unroll
        for (int t = 0; t < 4; ++t) *(bf8*)(yl + (w * 5 + t) * 1024) = yn[t];
    }
    if (layer == 0) { phys = set ^ get_side(p, cap); mine += phys * 8192; other += (phys ^ 1) * 8192; }
    TT(set * 128 + 32 + c, 10);
    for (int s = set; s < NSEG; s += 2) {
        const int rot = (s >> 1) & 3;
        TRDECL; TR(0);
        if (s + 2 >= NSEG) dma_kb<10>((wsp(p) + O_WO) + cb * 81920 + 40960 + w * 10240, lds0 + C_XN + 40960 + w * 10240, lane * 16);
        const char* mb0 = (s > 0 ? mine + (M_SLOT0 + rot * 8) * SLOT : p.hop + M0_OFF + phys * 8192) + 2 * w * SLOT + 2048;
        const char* mb1 = mb0 + SLOT;
        asm volatile("" : "+s"(mb0), "+s"(mb1));
        if (s > 0) wait_rt(sched_get(peek, mine + (S_SLOT + w) * SLOT + (s - 1) * 128 + 4, cap) + CLAG);
        TR(1);
        {
            f32x4 acc[4];
            u32x4 A[10];
            auto gather = [&]() {
                u32 vo = lane * 16;
                asm volatile("" : "+v"(vo) :: "memory");
#pragma unroll
                for (int ks = 0; ks < 10; ++ks) {
                    A[ks] = ld16(ks < 5 ? mb0 : mb1, vo, (ks % 5 - 2) * 1024);
                    SCHEDB();
                    if (ks < 4) ZERO4(acc[ks]);
                    if (ks < 9) asm volatile("s_nop %0" :: "n"(PACE));
                    SCHEDB();
                }
            };
            auto mproj = [&]() {
#pragma unroll
                for (int ks = 0; ks < 10; ++ks)
#pragma unroll
                    for (int t = 0; t < 4; ++t) { if (ks < 8) SMM_A(acc[t], A[ks], WA[(ks * 4 + t) & 31], sidx); else SMM_V(acc[t], A[ks], WV[(ks * 4 + t) & 7], sidx); }
                asm volatile("s_nop 7");
#pragma unroll
                for (int t = 0; t < 4; ++t) PIN(acc[t]);
            };
            gather();
            mproj();
            if (__builtin_expect(__any(__builtin_isunordered(acc[0][0], acc[0][1]) || __builtin_isunordered(acc[0][2], acc[0][3])), 0)) {
                for (int tries = 1;; ++tries) {
                    gather();
                    u32 m = 0;
#pragma unroll
                    for (int ks = 0; ks < 10; ++ks) m = max4(m, A[ks]);
                    if (!__any(m == ~0u)) break;
                    if (tries >= cap) { cap = 1; break; }
                    __builtin_amdgcn_s_sleep(8);
                }
                mproj();
            }
            peek = sched_peek(mine + (S_SLOT + w) * SLOT + s * 128);
#pragma unroll
            for (int pr = 0; pr < 2; ++pr) {
                float v[4];
#pragma unroll
                for (int i = 0; i < 4; ++i) v[i] = halfsum(acc[2 * pr][i], acc[2 * pr + 1][i]);
                *(u32x2*)(shm + C_YL + (w * 5 + 4) * 1024 + ((2 * pr + (g >> 1)) * 16 + fr) * 16 + (g & 1) * 8) = (u32x2){pk2bf(v[0], v[1]), pk2bf(v[2], v[3])};
            }
        }
        RAW_BARRIER();
        TR(2);
        if (s < NSEG - 1) {
            const char* pb[4];
#pragma unroll
            for (int i = 0; i < 4; ++i) { pb[i] = mine + (P_SLOT0 + rot * 4 + ((i + c * ROTC) & 3)) * SLOT; asm volatile("" : "+s"(pb[i])); }
            auto ldy = [&](const int f) { return *(const bf8*)(yl + (((f / 5 + c * ROTC) & 3) * 5 + f % 5) * 1024); };
            auto ldp = [&](const int f, const u32 vo, const u32 vm) { return f % 5 == 4 ? ld16(pb[f / 5], vm, 0) : ld16(pb[f / 5], vo, (f % 5) * 1280); };
            u32 xt;
            auto partial = [&](auto NF, auto F0, f32x4& acc, auto&& late) {
                constexpr int nf = CI(NF), f0 = CI(F0);
                bf8 Y[nf];
#pragma unroll
                for (int i = 0; i < nf; ++i) Y[i] = ldy(f0 + i);
#pragma unroll
                for (int i = 0; i < nf; ++i) PIN(Y[i]);
                u32 vo = pofs, vm = mofs;
                asm volatile("" : "+v"(vo), "+v"(vm));
                xt = sched_get(peek, mine + (S_SLOT + w) * SLOT + s * 128, cap);
                TR(3);
                wait_rt(xt);
                TRV(7, xt); TRC(6);
                asm volatile("" ::: "memory");
                u32x4 P[nf];
#pragma unroll
                for (int i = 0; i < nf; ++i) { P[i] = ldp(f0 + i, vo, vm); SCHEDB(); }
#pragma unroll
                for (int i = 0; i < nf; ++i) {
                    if (i == nf - 2) late();
                    if (i == 0) MMA0_VV(acc, Y[i], P[i]); else MMA_VV(acc, Y[i], P[i]);
                    if (i == 0) TRC(7);
                    SCHEDB();
                }
                TRC(8);
            };
            f32x4 acc;
            if (w > 0) {
                if (w == 1) partial(std::integral_constant<int, 3>(), std::integral_constant<int, 11>(), acc, [] {});
                else if (w == 2) partial(std::integral_constant<int, 3>(), std::integral_constant<int, 14>(), acc, [] {});
                else partial(std::integral_constant<int, 3>(), std::integral_constant<int, 17>(), acc, [] {});
                asm volatile("s_nop 7" : "+v"(acc));
                *(f32x4*)(shm + C_EX + (w - 1) * 1024 + lane * 16) = acc;
                RAW_BARRIER();
            } else {
                const int rn = ((s + 1) >> 1) & 3;
                u32 dst = g * 2 * SLOT + (lane & 7) * 16;
                char* dstb = other + (M_SLOT0 + rn * 8 + c / 40) * SLOT + (c % 40) * 128;
                u32 exa = lds0 + C_EX + lane * 16;
                asm volatile("" : "+s"(dstb), "+v"(dst), "+v"(exa));
                f32x4 h1, h2, h3;
                partial(std::integral_constant<int, 11>(), std::integral_constant<int, 0>(), acc, [&] {
                    __builtin_amdgcn_s_barrier();
                    asm volatile("" ::: "memory");
                    h1 = *(LDS3 const f32x4*)(uintptr_t)exa; h2 = *(LDS3 const f32x4*)(uintptr_t)(exa + 1024); h3 = *(LDS3 const f32x4*)(uintptr_t)(exa + 2048);
                    SCHEDB();
                });
                h1 = (h1 + h2) + h3;
                asm volatile("s_nop 2" : "+v"(acc), "+v"(h1));
                acc += h1;
                float v0, v1;
                auto merge = [&]() {
                    auto a = __builtin_amdgcn_permlane32_swap(__float_as_uint(acc[0]), __float_as_uint(acc[2]), false, false);
                    auto b = __builtin_amdgcn_permlane32_swap(__float_as_uint(acc[1]), __float_as_uint(acc[3]), false, false);
                    v0 = __uint_as_float(a[0]) + fdpp<0x128>(__uint_as_float(a[1]));
                    v1 = __uint_as_float(b[0]) + fdpp<0x128>(__uint_as_float(b[1]));
                };
                merge();
                int tries = 0;
                if (__builtin_expect(__any(__builtin_isunordered(v0, v1)), 0)) {
                    while (true) {
                        u32 vo = pofs, vm = mofs, m = 0;
                        asm volatile("" : "+v"(vo), "+v"(vm) :: "memory");
                        u32x4 P[20];
#pragma unroll
                        for (int i = 0; i < 20; ++i) P[i] = ldp(i, vo, vm);
                        f32x4 hh[4];
#pragma unroll
                        for (int i = 0; i < 20; ++i) {
                            const bf8 y = ldy(i);
                            const int k = i < 11 ? 0 : (i - 8) / 3;
                            if (i == 0 || i == 11 || i == 14 || i == 17) MMA0_VV(hh[k], y, P[i]); else MMA_VV(hh[k], y, P[i]);
                            m = max4(m, P[i]);
                            SCHEDB();
                        }
                        asm volatile("s_nop 7" : "+v"(hh[0]), "+v"(hh[1]), "+v"(hh[2]), "+v"(hh[3]));
                        acc = hh[0] + ((hh[1] + hh[2]) + hh[3]);
                        ++tries;
                        if (!__any(m == ~0u)) break;
                        if (tries >= cap) { cap = 1; break; }
                        __builtin_amdgcn_s_sleep(4);
                    }
                    merge();
                }
                TRV(6, tries + 1);
                const u32 dw = nosent(pk2bf(v0, v1));
                auto e = __builtin_amdgcn_permlane32_swap(dw, dw, false, false);
                auto e0 = __builtin_amdgcn_permlane16_swap(e[0], e[0], false, false);
                auto e1 = __builtin_amdgcn_permlane16_swap(e[1], e[1], false, false);
                st16m(dstb, dst, (u32x4){e0[0], e1[0], e0[1], e1[1]}, 0x00ff00ff00ff00ffull);
                TR(4);
                st4_lane0(mine + (T_SLOT + (c >> 6)) * SLOT + (c & 63) * 128, tries == 0 && s > 0 ? rt32() - xt : 0);
                if (s >= 2 && fr < 8) arm16(dstb + (((rn + 3) & 3) - rn) * 8 * SLOT, dst);
            }
        } else wait_rt(sched_get(peek, mine + (S_SLOT + w) * SLOT + s * 128, cap) + T_PX);
        RAW_BARRIER();
        auto tokens = [&](auto MORE) {
            f32x4 oa[4];
            bf8 ya[4];
            u32x4 P[20];
            u32 vo = pofs, vm = mofs;
            asm volatile("" : "+v"(vo), "+v"(vm));
            const char* pb[4];
#pragma unroll
            for (int i = 0; i < 4; ++i) { pb[i] = px + (size_t)(set * 4 + rot) * PX_BUF + (i * 32 + w * 8) * 640; asm volatile("" : "+s"(pb[i])); }
            auto ldp = [&](const int i) { P[i] = i % 5 == 4 ? ld16(pb[i / 5], vm, 0) : ld16(pb[i / 5], vo, (i % 5) * 1280); };
            int ln = lane;
            asm volatile("" : "+v"(ln));
            const u32x4 rg = ld16((wsp(p) + O_XR) + layer * ACT + (size_t)s * 40960 + ((c >> 2) * 2 + (w >> 1)) * 1024 + (c & 2) * 256 + (w & 1) * 128, (ln & 1) * 256 + ((ln >> 1) & 7) * 16, 0);
            TRC(9);
            if constexpr (CI(MORE)) yx(ya, s + 2, ldp);
            else {
#pragma unroll
                for (int i = 0; i < 20; ++i) ldp(i);
            }
            TRC(10);
            if constexpr (CI(MORE)) peek = sched_peek(mine + (S_SLOT + w) * SLOT + (s + 1) * 128 + 4);
            bf8 y[5];
#pragma unroll
            for (int j = 0; j < 5; ++j) y[j] = *(const bf8*)(yl + (w * 5 + j) * 1024);
            auto outx = [&]() {
#pragma unroll
                for (int i = 0; i < 20; ++i) PIN(P[i]);
                NOPV();
#pragma unroll
                for (int j = 0; j < 5; ++j)
#pragma unroll
                    for (int q = 0; q < 4; ++q) { if (j == 0) MMA0_VV(oa[q], y[j], P[q * 5 + j]); else MMA_VV(oa[q], y[j], P[q * 5 + j]); }
                asm volatile("s_nop 7");
#pragma unroll
                for (int q = 0; q < 4; ++q) PIN(oa[q]);
            };
            outx();
            if (__builtin_expect(__any(__builtin_isunordered(oa[0][0], oa[1][0]) || __builtin_isunordered(oa[2][0], oa[3][0])), 0)) {
                for (int tries = 1;; ++tries) {
                    asm volatile("" : "+v"(vo), "+v"(vm) :: "memory");
                    u32 m = 0;
#pragma unroll
                    for (int i = 0; i < 20; ++i) ldp(i);
#pragma unroll
                    for (int i = 0; i < 20; ++i) m = max4(m, P[i]);
                    if (!__any(m == ~0u)) break;
                    if (tries >= cap) { cap = 1; break; }
                    __builtin_amdgcn_s_sleep(8);
                }
                outx();
            }
            const bool own = (fr < 8) == (g < 2);
#pragma unroll
            for (int q = 0; q < 4; q += 2) {
                f32x4 o;
#pragma unroll
                for (int i = 0; i < 4; ++i) { const float t = fdpp<0x128>(oa[q + 1][i]); o[i] = own ? oa[q][i] : t; }
                *(f32x4*)(shm + C_RED + ((w * 2 + (g >> 1)) * 32 + (q + !own) * 8 + (fr & 7)) * 32 + (g & 1) * 16) = o;
            }
            if constexpr (CI(MORE)) {
#pragma unroll
                for (int t = 0; t < 4; ++t) *(bf8*)(yl + (w * 5 + t) * 1024) = ya[t];
            }
            RAW_BARRIER();
            TR(5); TRFLUSH(set * 128 + 32 + c);
            f32x4 a = *(const f32x4*)(shm + C_RED + (ln >> 4) * 2048 + w * 256 + (ln & 15) * 16) + *(const f32x4*)(shm + C_RED + (ln >> 4) * 2048 + 1024 + w * 256 + (ln & 15) * 16);
#pragma unroll
            for (int i = 0; i < 4; ++i) {
                auto t = __builtin_amdgcn_permlane16_swap(__float_as_uint(a[i]), __float_as_uint(a[i]), false, false);
                const float z = __uint_as_float(t[0]) + __uint_as_float(t[1]);
                a[i] = halfsum(z, z);
            }
            const u32 ra = (c & 1) ? rg[1] : rg[0], rb2 = (c & 1) ? rg[3] : rg[2];
            const u32 d0 = nosent(pk2bf(bflo(ra) + rbf(a[0]), bfhi(ra) + rbf(a[1]))), d1 = nosent(pk2bf(bflo(rb2) + rbf(a[2]), bfhi(rb2) + rbf(a[3])));
            const u32 n0 = __builtin_amdgcn_mov_dpp(d0, 0xB1, 0xf, 0xf, true), n1 = __builtin_amdgcn_mov_dpp(d1, 0xB1, 0xf, 0xf, true);
            st16m((wsp(p) + O_X1) + layer * ACT + (size_t)s * 40960 + c * 512 + w * 128, ln * 8, (u32x4){d0, d1, n0, n1}, 0x5555ull);
        };
        if (s + 2 >= NSEG) { tokens(std::integral_constant<int, 0>()); break; }
        tokens(std::integral_constant<int, 1>());
    }
    TT(set * 128 + 32 + c, 0);
    wait_rt(sched_get(~0u, mine + (S_SLOT + w) * SLOT + 63 * 128, cap) + T_OB + 16 * w);
    for (int pass = 0; pass < 2; ++pass) {
        const int T = rb + 16 * (w + 4 * pass);
        const char* ob = (wsp(p) + O_OB) + layer * ACT + (size_t)T * 20480;
        const char* xb = (wsp(p) + O_X1) + layer * ACT + (size_t)(T >> 1) * 40960 + cb * 4096 + (T & 1) * 256;
        int ln = lane;
        asm volatile("" : "+v"(ln));
        for (int spin = 0;; ++spin) {
            const u32 v = sched_peek(ob + 16384 + (ln & 31) * 128);
            if (__all(v != ~0u)) break;
            if (spin >= cap) { cap = 1; break; }
            __builtin_amdgcn_s_sleep(6);
        }
        u32 dw[8];
        TT(set * 128 + 32 + c, 1 + pass * 4);
        for (int tries = 0;; ++tries) {
            u32 vo = ln * 16, wa0 = lds0 + C_XN + ln * 16, wa1 = wa0 + 40960;
            const char* ob0 = ob + 4096;
            const char* ob1 = ob + 12288;
            asm volatile("" : "+v"(vo), "+v"(wa0), "+v"(wa1), "+s"(ob0), "+s"(ob1) :: "memory");
            u32x4 O[20], R[2];
#pragma unroll
            for (int k = 0; k < 16; ++k) O[k] = ld16(k < 8 ? ob0 : ob1, vo, ((k & 7) - 4) * 1024);
#pragma unroll
            for (int k = 0; k < 4; ++k) {
                const char* a = ob + 16384 + k * 1024 + (ln >> 4) * 256 + (ln & 15) * 8;
                const u64 lo = __hip_atomic_load((const gu64*)a, __ATOMIC_RELAXED, __HIP_MEMORY_SCOPE_AGENT);
                const u64 hi = __hip_atomic_load((const gu64*)(a + 128), __ATOMIC_RELAXED, __HIP_MEMORY_SCOPE_AGENT);
                O[16 + k] = (u32x4){(u32)lo, (u32)(lo >> 32), (u32)hi, (u32)(hi >> 32)};
            }
            R[0] = ld16(xb, (ln >> 4) * 1024 + (ln & 15) * 16, 0); R[1] = ld16(xb, (ln >> 4) * 1024 + (ln & 15) * 16, 512);
            SCHEDB();
            if (tries > 0) {
                u32 m = max4(max4(0, R[0]), R[1]);
#pragma unroll
                for (int k = 0; k < 20; ++k) m = max4(m, O[k]);
                if (__any(m == ~0u)) {
                    if (tries < cap) { __builtin_amdgcn_s_sleep(6); continue; }
                    cap = 1;
                }
            }
            f32x4 acc[4];
            bf8 wf[4];
#pragma unroll
            for (int t = 0; t < 4; ++t) wf[t] = *(LDS3 const bf8*)(uintptr_t)(wa0 + t * 1024);
#pragma unroll
            for (int k = 0; k < 20; ++k) {
                if (k >= 16) { PIN(O[k]); NOPV(); }
#pragma unroll
                for (int t = 0; t < 4; ++t) {
                    if (k == 0) MMA0_VV(acc[t], wf[t], O[k]); else MMA_VV(acc[t], wf[t], O[k]);
                    SCHEDB();
                    if (k < 19) wf[t] = *(LDS3 const bf8*)(uintptr_t)((k < 9 ? wa0 : wa1) + (((k + 1) % 10) * 4 + t) * 1024);
                    SCHEDB();
                }
            }
            asm volatile("s_nop 7" : "+v"(acc[0]), "+v"(acc[1]), "+v"(acc[2]), "+v"(acc[3]));
            float chk = 0.f;
#pragma unroll
            for (int i = 0; i < 8; ++i) {
                const u32 r = R[i >> 2][i & 3];
                const float a = bflo(r) + rbf(acc[i >> 1][(i & 1) * 2]), b = bfhi(r) + rbf(acc[i >> 1][(i & 1) * 2 + 1]);
                dw[i] = pk2bf(a, b);
                chk += a;
            }
            if (tries > 0 || !__any(chk != chk)) break;
        }
        TT(set * 128 + 32 + c, 2 + pass * 4);
        if (layer == 1) {
            u32x4* o = (u32x4*)(p.out + (size_t)(T * 16 + (ln & 15)) * DM + cb * 64 + (ln >> 4) * 16);
            o[0] = (u32x4){dw[0], dw[1], dw[2], dw[3]}; o[1] = (u32x4){dw[4], dw[5], dw[6], dw[7]};
            continue;
        }
        float s1 = 0.f, s2 = 0.f;
#pragma unroll
        for (int i = 0; i < 8; ++i) { const float a = bflo(dw[i]), b = bfhi(dw[i]); s1 += a + b; s2 = fmaf(a, a, fmaf(b, b, s2)); }
        s1 = grp_sum(s1); s2 = grp_sum(s2);
        char* stl = (wsp(p) + O_ST) + (size_t)T * 1280;
        if (ln < 16) __hip_atomic_store((gu64*)(stl + cb * 128 + ln * 8), (u64)nosent(__float_as_uint(s1)) | ((u64)nosent(__float_as_uint(s2)) << 32), __ATOMIC_RELAXED, __HIP_MEMORY_SCOPE_AGENT);
        const size_t fo = ACT + (size_t)(T >> 1) * 40960 + (cb * 4 + (T & 1)) * 1024;
        const u32 lo = (ln >> 5) * 2048 + ((ln >> 4) & 1) * 512 + (ln & 15) * 16;
        st16s((wsp(p) + O_XR) + fo, lo, (u32x4){dw[0], dw[4], dw[1], dw[5]}); st16s((wsp(p) + O_XR) + fo + 256, lo, (u32x4){dw[2], dw[6], dw[3], dw[7]});
        for (int spin = 0;; ++spin) {
            u32 m = 0;
            s1 = 0.f; s2 = 0.f;
#pragma unroll
            for (int i = 0; i < 3; ++i) {
                const int k = (ln >> 4) + 4 * i;
                const u64 v = __hip_atomic_load((const gu64*)(stl + min(k, 9) * 128 + (ln & 15) * 8), __ATOMIC_RELAXED, __HIP_MEMORY_SCOPE_AGENT);
                m = max(m, max((u32)v, (u32)(v >> 32)));
                if (k < 10) { s1 += __uint_as_float((u32)v); s2 += __uint_as_float((u32)(v >> 32)); }
            }
            if (!__any(m == ~0u)) break;
            if (spin >= cap) { cap = 1; break; }
            __builtin_amdgcn_s_sleep(2);
        }
        TT(set * 128 + 32 + c, 3 + pass * 4);
        s1 = grp_sum(s1); s2 = grp_sum(s2);
        const float mu = s1 * (1.0f / DM), rs = __builtin_amdgcn_rsqf(fmaxf(fmaf(-mu, mu, s2 * (1.0f / DM)), 0.f) + 1e-5f);
#pragma unroll
        for (int i = 0; i < 8; ++i) dw[i] = pk2bf((bflo(dw[i]) - mu) * rs, (bfhi(dw[i]) - mu) * rs);
        st16s((wsp(p) + O_XN) + fo, lo, (u32x4){dw[0], dw[4], dw[1], dw[5]}); st16s((wsp(p) + O_XN) + fo + 256, lo, (u32x4){dw[2], dw[6], dw[3], dw[7]});
        TT(set * 128 + 32 + c, 4 + pass * 4);
        if (w == 0 && pass == 0) { VM0(); st4_lane0(wsp(p) + O_FL + (set * 80 + c) * 128, 1u); }
    }
    if (layer == 1) break;
    mine += HOP_BYTES; other += HOP_BYTES; px += PX_BYTES; xn += ACT;
    VM0(); RAW_BARRIER();
    if (w == 0) {
        st4_lane0(wsp(p) + O_FL + (192 + set * 80 + c) * 128, 1u);
        __builtin_amdgcn_s_sleep(6);
        wait_all_tokens(p, lane, cap);
    }
    RAW_BARRIER();
    TT(set * 128 + 32 + c, 9);
    dma_kb<10>(xn + (size_t)set * 40960 + w * 10240, lds0 + C_XN + w * 10240, lane * 16);
    }
}

constexpr int B_PQ = 0, B_IM = 131072, B_CS = 155648, B_IS = 176;
__device__ __forceinline__ void bw_role(const LaParams& p, const int hb, char* shm) {
    const int lane = threadIdx.x & 63, w = __builtin_amdgcn_readfirstlane(threadIdx.x >> 6);
    const int fr = lane & 15, g = lane >> 4;
    int cap = SPIN_CAP;
    bf8 WA[64], WV[16];
    {
        const char* wb = (p.ws + O_WB) + (size_t)hb * 81920 + lane * 16;
#pragma unroll
        for (int i = 0; i < 64; ++i) WA[i] = *(const bf8*)(wb + i * 1024);
#pragma unroll
        for (int i = 0; i < 16; ++i) WV[i] = *(const bf8*)(wb + (64 + i) * 1024);
    }
    auto proj = [&](f32x4 (&acc)[4], f32x4& sq, auto&& fetch) {
        u32x4 X[20];
#pragma unroll
        for (int k = 0; k < 12; ++k) { X[k] = fetch(k); SCHEDB(); }
#pragma unroll
        for (int k = 0; k < 20; ++k) {
#pragma unroll
            for (int t = 0; t < 4; ++t) {
                if (k == 0) MMA0_AV(acc[t], WA[t], X[k]); else if (k < 16) MMA_AV(acc[t], WA[(k * 4 + t) & 63], X[k]); else MMA_VV(acc[t], WV[(k * 4 + t) & 15], X[k]);
            }
            if (k == 0) MMA0_VV(sq, X[k], X[k]); else MMA_VV(sq, X[k], X[k]);
            SCHEDB();
            if (k < 8) X[k + 12] = fetch(k + 12);
            SCHEDB();
        }
        asm volatile("s_nop 7" : "+v"(acc[0]), "+v"(acc[1]), "+v"(acc[2]), "+v"(acc[3]), "+v"(sq));
    };
    {
        f32x4 t, cs[4];
        u32x4 o1 = {0x3f803f80u, 0x3f803f80u, 0x3f803f80u, 0x3f803f80u};
        asm volatile("s_nop 1" : "+v"(o1));
        proj(cs, t, [&](int) { return o1; });
#pragma unroll
        for (int i = 0; i < 4; ++i) *(f32x4*)(shm + B_CS + i * 1024 + lane * 16) = cs[i];
    }
    if (hb == 0 && w == 0) {
        u32 best[2] = {~0u, ~0u};
        for (int i = 0; i < 64; ++i) {
#pragma unroll
            for (int sd = 0; sd < 2; ++sd) {
                u64 t0, t1;
                u32 z = 0, one = 1;
                asm volatile("s_memtime %0\n\ts_mov_b64 exec, 1\n\ts_waitcnt lgkmcnt(0)\n\tglobal_atomic_add %2, %3, %2, %4 sc0 sc1\n\ts_waitcnt vmcnt(0)\n\t"
                             "s_memtime %1\n\ts_mov_b64 exec, -1\n\ts_waitcnt lgkmcnt(0)"
                             : "=&s"(t0), "=&s"(t1), "+v"(one) : "v"(z), "s"(p.hop + M0_OFF + sd * 8192 + 6144) : "memory");
                best[sd] = min(best[sd], (u32)(t1 - t0));
            }
            if (i >= 2 && max(best[0], best[1]) < 1000u && max(best[0], best[1]) - min(best[0], best[1]) > 120u) break;
        }
        u32 xcc; asm volatile("s_getreg_b32 %0, hwreg(HW_REG_XCC_ID)" : "=s"(xcc));
        st4_lane0(wsp(p) + O_FL + 384 * 128, ((xcc >> 2) & 1) ^ (best[1] < best[0] ? 1u : 0u));
    }
    char* im = shm + B_IM + w * (16 * B_IS);
    const u32 imr = (u32)(uintptr_t)(LDS3 char*)im + (4 * g + (fr >> 2)) * B_IS + (fr & 3) * 8;
    typedef __attribute__((ext_vector_type(32))) u32 u32x32;
    for (int layer = 0; layer < 2; ++layer) {
        const char* sch = p.hop + layer * HOP_BYTES + (hb & 1) * 8192 + (S_SLOT + 4) * SLOT;
        if (g == 0) { *(u64*)(im + fr * B_IS + 96 + 24) = 0x3f80ull; *(u64*)(im + fr * B_IS + 96 + 48) = 0; *(u64*)(im + fr * B_IS + 96 + 56) = 0; }
        f32x4 kv[3][2];
#pragma unroll
        for (int i = 0; i < 6; ++i) ZERO4(kv[i >> 1][i & 1]);
        u32x32 q5;
        u32 tn = 0;
        for (int it = 0; it < 32; ++it) {
            const int s = 2 * it + (w >> 1);
            if (s + 2 < NSEG) { wait_rt(tn); tn = sched_get(~0u, sch + (s + 1) * 128 + 4, cap); wait_rt(tn + B_LAG); tn += 250; }
            else wait_rt(sched_get(~0u, sch + s * 128, cap) + T_X1);
            const char* xb = (wsp(p) + O_X1) + layer * ACT + (size_t)s * 40960 + (w & 1) * 256;
            if (it >= 30) TT((hb >> 4) * 128 + 112 + (hb & 15), (it - 30) * 3);
            f32x4 acc[4], sq;
            const char* xk[5];
#pragma unroll
            for (int i = 0; i < 5; ++i) { xk[i] = xb + i * 8192 + 4096; asm volatile("" : "+s"(xk[i])); }
            for (int pass = 0;; ++pass) {
                u32 vo = g * 512 + fr * 16;
                asm volatile("" : "+v"(vo) :: "memory");
                auto fetch = [&](const int k) { return ld16(xk[k >> 2], vo, ((k & 3) - 2) * 2048); };
                proj(acc, sq, fetch);
                if (pass > 0 || !__any(acc[3][3] != acc[3][3])) break;
                for (int tries = 0;; ++tries) {
                    asm volatile("" : "+v"(vo) :: "memory");
                    u32 m = 0;
#pragma unroll
                    for (int k = 0; k < 20; k += 10) {
#pragma unroll
                        for (int i = 0; i < 10; ++i) m = max4(m, fetch(k + i));
                        SCHEDB();
                    }
                    if (!__any(m == ~0u)) break;
                    if (tries >= cap) { cap = 1; break; }
                    __builtin_amdgcn_s_sleep(6);
                }
            }
            if (it >= 30) TT((hb >> 4) * 128 + 112 + (hb & 15), (it - 30) * 3 + 1);
            const float dsel = (fr & 2) ? ((fr & 1) ? sq[3] : sq[2]) : ((fr & 1) ? sq[1] : sq[0]);
            const float s2 = grp_sum(g == (fr >> 2) ? dsel : 0.f);
            const float mu = acc[3][3] * (1.0f / DM), rs = __builtin_amdgcn_rsqf(fmaxf(fmaf(-mu, mu, s2 * (1.0f / DM)), 0.f) + 1e-5f);
            float v[15];
            f32x4 cs[4];
#pragma unroll
            for (int i = 0; i < 4; ++i) cs[i] = *(const f32x4*)(shm + B_CS + i * 1024 + lane * 16);
#pragma unroll
            for (int j = 0; j < 15; ++j) v[j] = fmaf(-mu, cs[j >> 2][j & 3], acc[j >> 2][j & 3]) * rs;
            u32 ph[2][5];
#pragma unroll
            for (int m = 0; m < 2; ++m) {
                const float* x = v + 5 * m;
                const float mx = grp_max(fmaxf(fmaxf(fmaxf(x[0], x[1]), fmaxf(x[2], x[3])), x[4]));
                const float mn = -grp_max(-fminf(fminf(fminf(x[0], x[1]), fminf(x[2], x[3])), x[4]));
                float e[10], a = 0.f, b = 0.f;
#pragma unroll
                for (int j = 0; j < 5; ++j) {
                    e[j] = __builtin_amdgcn_exp2f((x[j] - mx) * 1.4426950408889634f); a += e[j];
                    e[5 + j] = __builtin_amdgcn_exp2f((mn - x[j]) * 1.4426950408889634f); b += e[5 + j];
                }
                a = __builtin_amdgcn_rcpf(grp_sum(a)); b = __builtin_amdgcn_rcpf(grp_sum(b));
#pragma unroll
                for (int j = 0; j < 5; ++j) { e[j] *= a; e[5 + j] *= b; }
#pragma unroll
                for (int j = 0; j < 5; ++j) ph[m][j] = pk2bf(e[2 * j], e[2 * j + 1]);
            }
            *(u32x4*)(shm + B_PQ + (w * 32 + it) * 1024 + lane * 16) = (u32x4){ph[0][0], ph[0][1], ph[0][2], ph[0][3]};
            q5[it] = ph[0][4];
            char* row = im + fr * B_IS;
            *(u32x2*)(row + g * 8) = (u32x2){ph[1][0], ph[1][1]};
            *(u32x2*)(row + 32 + g * 8) = (u32x2){ph[1][2], ph[1][3]};
            *(u32x2*)(row + 64 + g * 8) = (u32x2){ph[1][4], 0u};
            *(u32x2*)(row + 96 + (g & 1) * 32 + (g >> 1) * 8) = (u32x2){pk2bf(v[10], v[11]), pk2bf(v[12], v[13])};
            *(u16*)(row + 96 + 16 + g * 2) = (u16)pk2bf(v[14], 0.f);
            s16x4 tf[5];
#pragma unroll
            for (int i = 0; i < 5; ++i) tf[i] = __builtin_amdgcn_ds_read_tr16_b64_v4i16((LDS3 s16x4*)(uintptr_t)(imr + i * 32));
#pragma unroll
            for (int i = 0; i < 6; ++i) MMA16(kv[i >> 1][i & 1], tf[i >> 1], tf[3 + (i & 1)]);
            if (it >= 30) TT((hb >> 4) * 128 + 112 + (hb & 15), (it - 30) * 3 + 2);
        }
        asm volatile("s_nop 7" : "+v"(kv[0][0]), "+v"(kv[0][1]), "+v"(kv[1][0]), "+v"(kv[1][1]), "+v"(kv[2][0]), "+v"(kv[2][1]));
        RAW_BARRIER();
#pragma unroll
        for (int i = 0; i < 6; ++i) *(f32x4*)(shm + B_IM + (w * 6 + i) * 1024 + lane * 16) = kv[i >> 1][i & 1];
        RAW_BARRIER();
        bf8 ka[3];
        u32x2 kb[3];
#pragma unroll
        for (int dt = 0; dt < 3; ++dt) {
            f32x4 t[3];
#pragma unroll
            for (int ft = 0; ft < 3; ++ft) {
                const char* a = shm + B_IM + (ft * 2 + (dt & 1)) * 1024 + (dt < 2 ? lane : g * 16 + 12) * 16;
                t[ft] = (*(const f32x4*)a + *(const f32x4*)(a + 6144)) + (*(const f32x4*)(a + 12288) + *(const f32x4*)(a + 18432));
            }
            ka[dt] = pack8(t[0], t[1]);
            kb[dt] = (u32x2){pk2bf(t[2][0], t[2][1]), pk2bf(t[2][2], t[2][3])};
            PIN(ka[dt]); PIN(kb[dt]);
        }
        RAW_BARRIER();
        char* ob = (wsp(p) + O_OB) + layer * ACT + (size_t)w * 20480 + hb * 512;
        TT((hb >> 4) * 128 + 112 + (hb & 15), 6);
        const u32 rofs = ((g >> 1) + (g & 1) * 2) * 81920 + 16384 - hb * 384 + fr * 8, mofs = (lane >> 5) * 81920 + (lane & 31) * 16;
        static_for<0, 8>([&](auto IT) {
            constexpr int i0 = CI(IT) * 4;
            f32x4 o[4][3];
            bf8 qa[4];
            u32x2 qb[4];
#pragma unroll
            for (int u = 0; u < 4; ++u) { qa[u] = *(const bf8*)(shm + B_PQ + (w * 32 + i0 + u) * 1024 + lane * 16); qb[u] = (u32x2){q5[i0 + u], 0u}; PIN(qb[u]); }
#pragma unroll
            for (int u = 0; u < 4; ++u)
#pragma unroll
                for (int dt = 0; dt < 3; ++dt) MMA0_VV(o[u][dt], ka[dt], qa[u]);
#pragma unroll
            for (int u = 0; u < 4; ++u)
#pragma unroll
                for (int dt = 0; dt < 3; ++dt) MMA16(o[u][dt], kb[dt], qb[u]);
            asm volatile("s_nop 7");
            u32 d[4][4], r[2][2];
#pragma unroll
            for (int u = 0; u < 4; ++u) {
                PIN(o[u][0]); PIN(o[u][1]); PIN(o[u][2]);
                const float inv = __builtin_amdgcn_rcpf(o[u][2][0] + 1e-6f);
                const f32x4 a = o[u][0] * inv, b = o[u][1] * inv;
                d[u][0] = pk2bf(a[0], a[1]); d[u][1] = pk2bf(a[2], a[3]); d[u][2] = pk2bf(b[0], b[1]); d[u][3] = pk2bf(b[2], b[3]);
                if (u & 1) {
                    u32x4 m;
#pragma unroll
                    for (int i = 0; i < 4; ++i) {
                        auto t = __builtin_amdgcn_permlane32_swap(d[u - 1][i], d[u][i], false, false);
                        m[i] = nosent(t[0]);
                        if (i < 2) r[u >> 1][i] = t[1];
                    }
                    st16s(ob + (u - 1) * 81920, mofs, m);
                }
            }
            const u32x2 rr = {nosent(__builtin_amdgcn_permlane16_swap(r[0][0], r[1][0], false, false)[0]), nosent(__builtin_amdgcn_permlane16_swap(r[0][1], r[1][1], false, false)[0])};
            st8m(ob, rofs, rr, ~0ull);
            ob += 4 * 81920;
            asm volatile("" : "+s"(ob));
        });
        TT((hb >> 4) * 128 + 112 + (hb & 15), 7);
    }
}

__global__ void __launch_bounds__(256, 1) la_kernel(LaParams p) {
    extern __shared__ __attribute__((aligned(16))) char shm[];
    p.hop = align2m(p.ws + O_HOP);
    if (threadIdx.x == 0) {
        u32 xcc; asm volatile("s_getreg_b32 %0, hwreg(HW_REG_XCC_ID)" : "=s"(xcc));
        int role = 255;
        for (int a = 0; a < 8; ++a) {
            const u32 v = (xcc + a) & 7;
            const u32 k = __hip_atomic_fetch_add((gu32*)(((u32*)(p.ws + O_CTL)) + v * 32), 1u, __ATOMIC_RELAXED, __HIP_MEMORY_SCOPE_AGENT);
            if (k < 32) { role = v * 32 + k; break; }
        }
        *(int*)shm = role;
    }
    __syncthreads();
    const int role = __builtin_amdgcn_readfirstlane(*(int*)shm);
    __syncthreads();
    const int set = role >> 7, id = (role >> 5) & 3, k = role & 31;
    if (k < 8) qk_role(p, set, id * 8 + k, shm);
    else if (k < 28) comb_role(p, set, id * 20 + k - 8, shm);
    else bw_role(p, (role >> 5) * 4 + k - 28, shm);
}
__device__ __forceinline__ int kperm(int g, int e) { return 16 * (g >> 1) + 8 * ((e & 3) >> 1) + 4 * (g & 1) + 2 * (e >> 2) + (e & 1); }
__device__ __forceinline__ float row16_sum(float x) { x += fdpp<0x128>(x); x += fdpp<0x124>(x); x += fdpp<0x122>(x); return x + fdpp<0x121>(x); }

struct PrepParams { const u16* x; const u16* mem0; const u16* Wq; const u16* Wk; const u16* Wv; const u16* Wo; const u16* wqkv; const u16* wphi; const u16* wo; char* ws; };
constexpr int N_VO = 320, N_WB = 320, N_LN = 128, N_QK = 640, N_WO = 200, N_FILL = 256;
__global__ void __launch_bounds__(256) prep_kernel(PrepParams q) {
    __shared__ __attribute__((aligned(16))) u16 sm[192 * 72];
    int b = blockIdx.x;
    const int t = threadIdx.x, lane = t & 63, w = t >> 6, fr = lane & 15, g = lane >> 4;
    char* hop = align2m(q.ws + O_HOP);
    if (b < N_VO) {
        const int h = b / 10, db = (b % 10) >> 1, ch0 = (b & 1) * 5, k32 = db * 4 + w;
        u32x4 st[6];
        const u16* src = q.Wo + ((size_t)h * DH + (t >> 3)) * DM + ch0 * 64 + (t & 7) * 8;
#pragma unroll
        for (int i = 0; i < 6; ++i) st[i] = *(const u32x4*)(src + (size_t)i * 32 * DM);
        bf8 A[2][6];
#pragma unroll
        for (int u = 0; u < 2; ++u) {
            const u16* row = q.Wv + (size_t)(k32 * 32 + kperm(fr >> 2, u * 4 + (fr & 3))) * HD + h * DH + g * 8;
#pragma unroll
            for (int k = 0; k < 6; ++k) A[u][k] = *(const bf8*)(row + k * 32);
        }
        for (int cb = ch0; cb < ch0 + 5; ++cb) {
            __syncthreads();
#pragma unroll
            for (int i = 0; i < 6; ++i) *(u32x4*)(sm + (i * 32 + (t >> 3)) * 72 + (t & 7) * 8) = st[i];
            __syncthreads();
            if (cb + 1 < ch0 + 5) {
#pragma unroll
                for (int i = 0; i < 6; ++i) st[i] = *(const u32x4*)(src + (size_t)i * 32 * DM + (cb + 1 - ch0) * 64);
            }
#pragma unroll
            for (int n = 0; n < 4; ++n) {
                f32x4 c0 = {}, c1 = {};
#pragma unroll
                for (int k = 0; k < 6; ++k) {
                    const u16* a = sm + (k * 32 + g * 8 + (fr >> 2)) * 72 + n * 16 + (fr & 3) * 4;
                    s16x4 lo = __builtin_amdgcn_ds_read_tr16_b64_v4i16((LDS3 s16x4*)a);
                    s16x4 hi = __builtin_amdgcn_ds_read_tr16_b64_v4i16((LDS3 s16x4*)(a + 4 * 72));
                    const bf8 B = __builtin_bit_cast(bf8, __builtin_shufflevector(lo, hi, 0, 1, 2, 3, 4, 5, 6, 7));
                    c0 = __builtin_amdgcn_mfma_f32_16x16x32_bf16(A[0][k], B, c0, 0, 0, 0);
                    c1 = __builtin_amdgcn_mfma_f32_16x16x32_bf16(A[1][k], B, c1, 0, 0, 0);
                }
                const int c = cb * 8 + n * 2 + (fr >> 3);
                const size_t frag = (size_t)(c * 4 + (h >> 3)) * 40 + (k32 >> 1) * 4 + ((h & 7) >> 1);
                *(bf8*)(q.ws + O_WVO + frag * 2048 + (k32 & 1) * 1024 + (g * 16 + (h & 1) * 8 + (fr & 7)) * 16) = pack8(c0, c1);
            }
        }
        return;
    }
    b -= N_VO;
    if (b < N_WB) {
        const int h = b / 10, d0 = (b % 10) * 64;
        float* xs = (float*)sm;
        float* ph = xs + 64 * 61;
        for (int i = t; i < 64 * 30; i += 256) {
            const int d = i / 30, m = (i % 30) / 10, e = (i % 10) * 2;
            const u32 v = *(const u32*)(q.wqkv + (size_t)(d0 + d) * 1920 + m * 640 + h * 20 + e);
            xs[d * 61 + m * 20 + e] = bflo(v); xs[d * 61 + m * 20 + e + 1] = bfhi(v);
        }
        for (int i = t; i < 400; i += 256) ph[i] = bf2f(q.wphi[h * 400 + i]);
        __syncthreads();
        const int j = 4 * w + (fr & 3), G = fr >> 2;
#pragma unroll
        for (int kk = 0; kk < 2; ++kk) {
            float v[8];
            const float* xr = xs + (kk * 32 + g * 8) * 61;
            if (j < 10) {
                const int m = j / 5, f = 5 * G + j % 5;
#pragma unroll
                for (int e = 0; e < 8; ++e) v[e] = 0.f;
                for (int i = 0; i < 20; ++i) {
                    const float pw = ph[i * 20 + f];
#pragma unroll
                    for (int e = 0; e < 8; ++e) v[e] = fmaf(xr[e * 61 + m * 20 + i], pw, v[e]);
                }
            } else {
#pragma unroll
                for (int e = 0; e < 8; ++e) v[e] = j < 15 ? xr[e * 61 + 40 + 5 * G + min(j, 14) - 10] : 1.0f;
            }
            *(u32x4*)(q.ws + O_WB + (size_t)h * 81920 + ((d0 / 32 + kk) * 4 + w) * 1024 + lane * 16) =
                (u32x4){pk2bf(v[0], v[1]), pk2bf(v[2], v[3]), pk2bf(v[4], v[5]), pk2bf(v[6], v[7])};
        }
        return;
    }
    b -= N_WB;
    if (b < N_LN) {
        const int tok = b * 16 + w * 4 + g;
        u32x4 d[3][2];
        float s = 0.f;
#pragma unroll
        for (int i = 0; i < 3; ++i) {
            const u32x4* a = (const u32x4*)(q.x + (size_t)tok * DM + min(fr + 16 * i, 39) * 16);
            d[i][0] = a[0]; d[i][1] = a[1];
        }
#pragma unroll
        for (int i = 0; i < 3; ++i)
#pragma unroll
            for (int e = 0; e < 8; ++e) if (fr + 16 * i < 40) s += bflo(d[i][e >> 2][e & 3]) + bfhi(d[i][e >> 2][e & 3]);
        const float mu = row16_sum(s) * (1.0f / DM);
        float v = 0.f;
#pragma unroll
        for (int i = 0; i < 3; ++i)
#pragma unroll
            for (int e = 0; e < 8; ++e) if (fr + 16 * i < 40) { const float a = bflo(d[i][e >> 2][e & 3]) - mu, c = bfhi(d[i][e >> 2][e & 3]) - mu; v = fmaf(a, a, fmaf(c, c, v)); }
        const float rs = __builtin_amdgcn_rsqf(row16_sum(v) * (1.0f / DM) + 1e-5f);
#pragma unroll
        for (int i = 0; i < 3; ++i) {
            const int blk = fr + 16 * i;
            if (blk >= 40) continue;
            const size_t o = (size_t)(tok >> 5) * 40960 + ((blk >> 1) * 2 + ((tok >> 4) & 1)) * 1024 + ((blk & 1) * 32 + (tok & 15)) * 16;
            u32 n[8];
#pragma unroll
            for (int e = 0; e < 8; ++e) n[e] = pk2bf((bflo(d[i][e >> 2][e & 3]) - mu) * rs, (bfhi(d[i][e >> 2][e & 3]) - mu) * rs);
            *(u32x4*)(q.ws + O_XR + o) = (u32x4){d[i][0][0], d[i][1][0], d[i][0][1], d[i][1][1]};
            *(u32x4*)(q.ws + O_XR + o + 256) = (u32x4){d[i][0][2], d[i][1][2], d[i][0][3], d[i][1][3]};
            *(u32x4*)(q.ws + O_XN + o) = (u32x4){n[0], n[4], n[1], n[5]};
            *(u32x4*)(q.ws + O_XN + o + 256) = (u32x4){n[2], n[6], n[3], n[7]};
        }
        return;
    }
    b -= N_LN;
    if (b < N_QK) {
        const int mt = b / 320, h = (b / 10) & 31, ks = b % 10;
        const u16* W = mt ? q.Wk : q.Wq;
#pragma unroll
        for (int i = 0; i < 6; ++i) {
            const int id = t + i * 256, d = id / 24, ch = id % 24;
            *(u32x4*)(sm + d * 200 + ch * 8) = *(const u32x4*)(W + (size_t)(ks * 64 + d) * HD + h * DH + ch * 8);
        }
        __syncthreads();
#pragma unroll
        for (int pr = 0; pr < 3; ++pr)
#pragma unroll
            for (int hf = 0; hf < 2; ++hf) {
                const u16* a = sm + (hf * 32) * 200 + (w * 3 + pr) * 16 + (fr & 3) * 4;
                s16x4 lo = __builtin_amdgcn_ds_read_tr16_b64_v4i16((LDS3 s16x4*)(a + kperm(g, fr >> 2) * 200));
                s16x4 hi = __builtin_amdgcn_ds_read_tr16_b64_v4i16((LDS3 s16x4*)(a + kperm(g, 4 + (fr >> 2)) * 200));
                const size_t frag = (size_t)(h * 4 + w) * 60 + qk_frag(2 * pr + mt, ks);
                *(bf8*)(q.ws + O_WQK + frag * 2048 + hf * 1024 + lane * 16) = __builtin_bit_cast(bf8, __builtin_shufflevector(lo, hi, 0, 1, 2, 3, 4, 5, 6, 7));
            }
        return;
    }
    b -= N_QK;
    if (b < N_WO) {
        const int cb = b / 20, kk = b % 20;
        const u16* col = q.wo + cb * 64 + 16 * (fr >> 2) + 4 * w + (fr & 3);
        u32 v[8];
#pragma unroll
        for (int e = 0; e < 8; ++e) {
            const int row = kk < 16 ? (2 * kk + (g >> 1)) * 20 + 5 * (2 * (g & 1) + (e >> 2)) + (e & 3) : (8 * (kk - 16) + 2 * g + (e >> 2)) * 20 + 5 * (e & 3) + 4;
            v[e] = col[(size_t)row * DM];
        }
        *(u32x4*)(q.ws + O_WO + (size_t)cb * 81920 + (kk * 4 + w) * 1024 + lane * 16) = (u32x4){v[0] | v[1] << 16, v[2] | v[3] << 16, v[4] | v[5] << 16, v[6] | v[7] << 16};
        return;
    }
    b -= N_WO;
    const u32x4 SENT = {~0u, ~0u, ~0u, ~0u};
    const u32 i0 = b * 256 + t;
    if (b == 0) ((u32*)(q.ws + O_CTL))[t] = 0;
    if (i0 < 8 * 2 * 320) {
        const int sl = i0 / 640, e = i0 % 320;
        const u32x4 v = *(const u32x4*)(q.mem0 + (e & 7) * DM + ((sl & 1) * 40 + (e >> 3)) * 8);
        st16(hop + M0_OFF + (i0 / 320) * 8192 + e * 16, (u32x4){nosent(v[0]), nosent(v[1]), nosent(v[2]), nosent(v[3])});
    }
    for (u32 i = i0; i < 2 * HOP_BYTES / 16; i += N_FILL * 256) st16(hop + (size_t)i * 16, SENT);
    for (u32 i = i0; i < (O_XN - O_ST) / 16; i += N_FILL * 256) st16(q.ws + O_ST + (size_t)i * 16, SENT);
}

extern "C" void kernel_launch(void* const* d_in, const int* in_sizes, int n_in,
                              void* d_out, int out_size, void* d_ws, size_t ws_size,
                              hipStream_t stream) {
    PrepParams q;
    q.x = (const u16*)d_in[0]; q.mem0 = (const u16*)d_in[1];
    q.Wq = (const u16*)d_in[2]; q.Wk = (const u16*)d_in[3]; q.Wv = (const u16*)d_in[4]; q.Wo = (const u16*)d_in[5];
    q.wqkv = (const u16*)d_in[8]; q.wphi = (const u16*)d_in[9]; q.wo = (const u16*)d_in[10];
    q.ws = (char*)d_ws;
    prep_kernel<<<N_VO + N_WB + N_LN + N_QK + N_WO + N_FILL, 256, 0, stream>>>(q);
    LaParams p;
    p.ws = (char*)d_ws; p.out = (u16*)d_out; p.hop = (char*)d_ws;
    (void)hipFuncSetAttribute((const void*)la_kernel, hipFuncAttributeMaxDynamicSharedMemorySize, LA_LDS);
    la_kernel<<<256, 256, LA_LDS, stream>>>(p);
}
```

```cpp
#include <hip/hip_runtime.h>
#include <hip/hip_bf16.h>
#include <stdint.h>
#include <type_traits>

typedef unsigned short u16;
typedef unsigned int u32;
typedef unsigned long long u64;
using bf16x8 = __attribute__((ext_vector_type(8))) short;
using f32x4 = __attribute__((ext_vector_type(4))) float;

constexpr int S_TOK = 2048, DM = 640, NH = 32, DH = 192;
constexpr int MEM = 8, SEG = 32, NSEG = S_TOK / SEG;
constexpr int HD = NH * DH;

__device__ __forceinline__ float bf2f(u16 v) { return __uint_as_float(((u32)v) << 16); }
__device__ __forceinline__ u32 pk2bf(float lo, float hi) {
    typedef __attribute__((ext_vector_type(2))) float f32x2_t;
    typedef __attribute__((ext_vector_type(2))) __bf16 bf16x2_t;
    bf16x2_t r = __builtin_convertvector((f32x2_t){lo, hi}, bf16x2_t);
    return __builtin_bit_cast(u32, r);
}
__device__ __forceinline__ float rbf(float f) { return __uint_as_float(pk2bf(f, f) << 16); }
typedef __attribute__((ext_vector_type(8))) __bf16 bf8;
typedef __attribute__((ext_vector_type(16))) __bf16 bf16v;
typedef __attribute__((ext_vector_type(4))) __bf16 bf4;
typedef __attribute__((ext_vector_type(4))) short s16x4;
using u32x4 = __attribute__((ext_vector_type(4))) u32;
using u32x2 = __attribute__((ext_vector_type(2))) u32;
typedef __attribute__((address_space(1))) u32 gu32;
typedef __attribute__((address_space(1))) u64 gu64;
#define LDS3 __attribute__((address_space(3)))

constexpr int SLOT = 16384;
constexpr int P_SLOT0 = 0, M_SLOT0 = 16, S_SLOT = 48, T_SLOT = 53, N_SLOT = 55;
constexpr size_t HOP_BYTES = (size_t)N_SLOT * SLOT, M0_OFF = 2 * HOP_BYTES;
constexpr size_t PX_BUF = 4 * 32 * 640, PX_BYTES = 8 * PX_BUF;
constexpr int LA_LDS = 155648 + 4096 + 256;
constexpr int SPIN_CAP = 1 << 16;
#define D1I 135
#define D2I 68
#define MG1 10
#define MG2 12
#define ROTC 0
#define DUP 4
#define PACE 6
#define CLAG 50
#define T_PX 45
#define T_X1 222
#define T_OB 525
#define T_TOK 760
#define ST_LAG 6
#define OB_STEP 28
#define OB_POLL 0
#define OB_COLD 20
#define B_LAG 30
#define ELATE 30

__device__ __forceinline__ void st16(void* p, u32x4 v) {
    asm volatile("global_store_dwordx4 %0, %1, off sc1\n\ts_nop 2" :: "v"(p), "v"(v) : "memory");
}
typedef __attribute__((address_space(1))) u32x4 gu32x4;
__device__ __forceinline__ u32x4 ld16(const char* base, u32 off, int imm) { return __builtin_amdgcn_global_load_b128((gu32x4*)(base + off + imm), "agent"); }
__device__ __forceinline__ u32 nosent(u32 v) { return min(v, 0xFFFFFFFEu); }
__device__ __forceinline__ u32 rt32() { return (u32)__builtin_amdgcn_s_memrealtime(); }
__device__ __forceinline__ void wait_rt(u32 t) {
    int d = (int)(t - rt32());
    if (d > 4096) return;
    while (d > 10) { if (d > 20) __builtin_amdgcn_s_sleep(2); d = (int)(t - rt32()); }
    asm volatile("s_cmp_lt_i32 %0, 1\n\ts_cbranch_scc1 2f\n1:\n\ts_sub_i32 %0, %0, 1\n\ts_cmp_gt_i32 %0, 0\n\ts_cbranch_scc1 1b\n2:" : "+s"(d) :: "scc");
}
template <int N> __device__ __forceinline__ void dma_kb(const char* src, u32 lds_dst, u32 lane16) {
#pragma unroll
    for (int i = 0; i < N; i += 4) {
        u32 keep;
        if (N - i >= 4)
            asm volatile("s_mov_b32 %0, m0\n\ts_mov_b32 m0, %3\n\ts_nop 2\n\tglobal_load_lds_dwordx4 %1, %2 sc1\n\tglobal_load_lds_dwordx4 %1, %2 offset:1024 sc1\n\t"
                         "global_load_lds_dwordx4 %1, %2 offset:2048 sc1\n\tglobal_load_lds_dwordx4 %1, %2 offset:3072 sc1\n\ts_mov_b32 m0, %0"
                         : "=&s"(keep) : "v"(lane16), "s"(src + i * 1024), "s"(lds_dst + i * 1024) : "memory");
        else if (N - i == 2)
            asm volatile("s_mov_b32 %0, m0\n\ts_mov_b32 m0, %3\n\ts_nop 2\n\tglobal_load_lds_dwordx4 %1, %2 sc1\n\tglobal_load_lds_dwordx4 %1, %2 offset:1024 sc1\n\ts_mov_b32 m0, %0"
                         : "=&s"(keep) : "v"(lane16), "s"(src + i * 1024), "s"(lds_dst + i * 1024) : "memory");
        else
            asm volatile("s_mov_b32 %0, m0\n\ts_mov_b32 m0, %3\n\ts_nop 2\n\tglobal_load_lds_dwordx4 %1, %2 sc1\n\ts_mov_b32 m0, %0"
                         : "=&s"(keep) : "v"(lane16), "s"(src + i * 1024), "s"(lds_dst + i * 1024) : "memory");
    }
}
__device__ __forceinline__ void dma_gather(const char* src, u32 lds_dst, u32 off) {
    u32 keep;
    asm volatile("s_mov_b32 %0, m0\n\ts_mov_b32 m0, %3\n\ts_nop 2\n\tglobal_load_lds_dwordx4 %1, %2\n\ts_mov_b32 m0, %0"
                 : "=&s"(keep) : "v"(off), "s"(src), "s"(lds_dst) : "memory");
}
__device__ __forceinline__ void st16s(char* base, u32 off, u32x4 v) {
    asm volatile("s_nop 4\n\tglobal_store_dwordx4 %0, %1, %2 sc1\n\ts_nop 2" :: "v"(off), "v"(v), "s"(base) : "memory");
}
__device__ __forceinline__ void arm16(char* base, u32 off) {
    u32x4 v;
    asm volatile("v_mov_b64 %0, -1\n\tv_mov_b64 %1, -1" : "=v"(((u64*)&v)[0]), "=v"(((u64*)&v)[1]));
    st16s(base, off, v);
}
template <int B, int E, typename F> __device__ __forceinline__ void static_for(F&& f) {
    if constexpr (B < E) { f(std::integral_constant<int, B>()); static_for<B + 1, E>(f); }
}
#define CI(x) decltype(x)::value
#define RAW_BARRIER() do { asm volatile("s_waitcnt lgkmcnt(0)" ::: "memory"); __builtin_amdgcn_s_barrier(); asm volatile("" ::: "memory"); } while (0)
#define VM0() asm volatile("s_waitcnt vmcnt(0)" ::: "memory")
#define SCHEDB() __builtin_amdgcn_sched_barrier(0)
__device__ __forceinline__ bf8 lo8(const bf16v& v) { return __builtin_shufflevector(v, v, 0, 1, 2, 3, 4, 5, 6, 7); }
__device__ __forceinline__ bf8 hi8(const bf16v& v) { return __builtin_shufflevector(v, v, 8, 9, 10, 11, 12, 13, 14, 15); }
__device__ __forceinline__ bf16v cat8(bf8 a, bf8 b) { return __builtin_shufflevector(a, b, 0, 1, 2, 3, 4, 5, 6, 7, 8, 9, 10, 11, 12, 13, 14, 15); }
#define MMA_VV(c, a, b) asm volatile("v_mfma_f32_16x16x32_bf16 %0, %1, %2, %0" : "+v"(c) : "v"(a), "v"(b))
#define MMA_AV(c, a, b) asm volatile("v_mfma_f32_16x16x32_bf16 %0, %1, %2, %0" : "+v"(c) : "a"(a), "v"(b))
#define MMA_VA(c, a, b) asm volatile("v_mfma_f32_16x16x32_bf16 %0, %1, %2, %0" : "+v"(c) : "v"(a), "a"(b))
#define MMA0_VV(c, a, b) asm volatile("v_mfma_f32_16x16x32_bf16 %0, %1, %2, 0" : "=&v"(c) : "v"(a), "v"(b))
#define MMA0_AV(c, a, b) asm volatile("v_mfma_f32_16x16x32_bf16 %0, %1, %2, 0" : "=&v"(c) : "a"(a), "v"(b))
#define MMA0_VA(c, a, b) asm volatile("v_mfma_f32_16x16x32_bf16 %0, %1, %2, 0" : "=&v"(c) : "v"(a), "a"(b))
#define MMA16(c, a, b) asm volatile("v_mfma_f32_16x16x16_bf16 %0, %1, %2, %0" : "+v"(c) : "v"(a), "v"(b))
#define MMA16_0(c, a, b) asm volatile("v_mfma_f32_16x16x16_bf16 %0, %1, %2, 0" : "=&v"(c) : "v"(a), "v"(b))
#define SMM_V(c, a, b, i) asm volatile("v_smfmac_f32_16x16x64_bf16 %0, %1, %2, %3" : "+v"(c) : "v"(a), "v"(b), "v"(i))
#define SMM_A(c, a, b, i) asm volatile("v_smfmac_f32_16x16x64_bf16 %0, %1, %2, %3" : "+v"(c) : "v"(a), "a"(b), "v"(i))
#define NOPV() asm volatile("s_nop 1")
#define PIN(x) asm volatile("" : "+v"(x))
#define SETTLE() asm volatile("s_nop 7\n\ts_nop 3")
__device__ __forceinline__ bf8 pack8(f32x4 a, f32x4 b) {
    u32x4 r = {pk2bf(a[0], a[1]), pk2bf(a[2], a[3]), pk2bf(b[0], b[1]), pk2bf(b[2], b[3])};
    return __builtin_bit_cast(bf8, r);
}
__device__ __forceinline__ u32 max4(u32 m, u32x4 g) { m = max(max(m, g[0]), g[1]); return max(max(m, g[2]), g[3]); }
#define ZERO4(c) asm volatile("v_mov_b64 %0, 0\n\tv_mov_b64 %1, 0" : "=v"(((u64*)&(c))[0]), "=v"(((u64*)&(c))[1]))
__device__ __forceinline__ bf8 lds8(const char* shm, int off) { return *(const bf8*)(shm + off); }
__device__ __forceinline__ bf16v ldfrag(const char* p) { return cat8(*(const bf8*)p, *(const bf8*)(p + 1024)); }
template <int CTRL> __device__ __forceinline__ float fdpp(float x) {
    return __builtin_bit_cast(float, __builtin_amdgcn_mov_dpp(__builtin_bit_cast(int, x), CTRL, 0xf, 0xf, true));
}
__device__ __forceinline__ float halfsum(float a, float b) {
    auto r = __builtin_amdgcn_permlane32_swap(__float_as_uint(a), __float_as_uint(b), false, false);
    return __uint_as_float(r[0]) + __uint_as_float(r[1]);
}
__device__ __forceinline__ float grp_sum(float x) {
    auto t = __builtin_amdgcn_permlane16_swap(__float_as_uint(x), __float_as_uint(x), false, false);
    x = __uint_as_float(t[0]) + __uint_as_float(t[1]);
    return halfsum(x, x);
}
__device__ __forceinline__ float grp_max(float x) {
    auto t = __builtin_amdgcn_permlane16_swap(__float_as_uint(x), __float_as_uint(x), false, false);
    x = fmaxf(__uint_as_float(t[0]), __uint_as_float(t[1]));
    auto u = __builtin_amdgcn_permlane32_swap(__float_as_uint(x), __float_as_uint(x), false, false);
    return fmaxf(__uint_as_float(u[0]), __uint_as_float(u[1]));
}
__device__ __forceinline__ float bflo(u32 v) { return __uint_as_float(v << 16); }
__device__ __forceinline__ float bfhi(u32 v) { return __uint_as_float(v & 0xffff0000u); }
__device__ __forceinline__ void st4_lane0(char* base, u32 v) {
    u32 z, d;
    asm volatile("s_mov_b64 exec, 1\n\tv_mov_b32 %0, 0\n\tv_mov_b32 %1, %3\n\ts_nop 1\n\tglobal_store_dword %0, %1, %2 sc1\n\ts_mov_b64 exec, -1" : "=&v"(z), "=&v"(d) : "s"(base), "s"(v) : "memory");
}
__device__ __forceinline__ int track(int d, int sample) {
    if (sample <= 0) return d;
    return sample >= d ? min(sample, d + DUP) : (d - sample > 2 ? d - ((d - sample) >> 1) : d);
}
__device__ __forceinline__ u32 sched_peek(const char* p) { return __hip_atomic_load((const gu32*)p, __ATOMIC_RELAXED, __HIP_MEMORY_SCOPE_AGENT); }
__device__ __forceinline__ u32 sched_get(u32 v, const char* p, int& cap) {
    for (int spin = 0; v == ~0u; ++spin) {
        if (spin >= cap) { cap = 1; break; }
        __builtin_amdgcn_s_sleep(3);
        v = sched_peek(p);
    }
    return __builtin_amdgcn_readfirstlane(v);
}

#define TRDECL
#define TR(i)
#define TRV(i, v)
#define TRFLUSH(role)
#define TRC(i)
constexpr size_t ACT = (size_t)S_TOK * DM * 2;
constexpr size_t O_DBG = 0, O_CTL = 2u << 20, O_ST = O_CTL + 4096, O_FL = O_ST + 128 * 1280, O_PX = O_FL + 3 * 192 * 128, O_X1 = O_PX + 2 * PX_BYTES,
                 O_OB = O_X1 + 2 * ACT, O_XN = O_OB + 2 * ACT, O_XR = O_XN + 2 * ACT, O_WO = O_XR + 2 * ACT,
                 O_WVO = O_WO + 10 * 81920, O_HOP = O_WVO + (size_t)80 * 4 * 40 * 2048, WS_BYTES = O_HOP + (4u << 20);
#define TT(role, i)
struct LaParams { char* ws; u16* out; char* hop; const u16* Wq; const u16* Wk; const u16* Wv; const u16* Wo; const u16* wqkv; const u16* wphi; };
__device__ __forceinline__ int kperm(int g, int e) { return 16 * (g >> 1) + 8 * ((e & 3) >> 1) + 4 * (g & 1) + 2 * (e >> 2) + (e & 1); }
__device__ __forceinline__ bf8 tr8(u32 a, u32 b) {
    const s16x4 lo = __builtin_amdgcn_ds_read_tr16_b64_v4i16((LDS3 s16x4*)(uintptr_t)a), hi = __builtin_amdgcn_ds_read_tr16_b64_v4i16((LDS3 s16x4*)(uintptr_t)b);
    return __builtin_bit_cast(bf8, __builtin_shufflevector(lo, hi, 0, 1, 2, 3, 4, 5, 6, 7));
}
__device__ __forceinline__ char* wsp(const LaParams& p) { char* b = p.ws; asm volatile("" : "+s"(b)); return b; }
template <int N> __device__ __forceinline__ void wait_flags(const char* f, const u32 (&off)[N], int& cap) {
    for (int spin = 0;; ++spin) {
        u32 m = 0;
#pragma unroll
        for (int i = 0; i < N; ++i) m = max(m, sched_peek(f + off[i]));
        if (!__any(m == ~0u)) break;
        if (spin >= cap) { cap = 1; break; }
        __builtin_amdgcn_s_sleep(4);
    }
    asm volatile("" ::: "memory");
}
__device__ __forceinline__ void wait_all_tokens(const LaParams& p, u32 lane, int& cap) {
    asm volatile("" : "+v"(lane));
    const u32 off[3] = {lane * 128, lane * 128 + 8192, min(lane + 128, 159u) * 128};
    wait_flags<3>(wsp(p) + O_FL + 192 * 128, off, cap);
}
__device__ __forceinline__ void wait_first_tokens(const LaParams& p, const int set, u32 lane, int& cap) {
    asm volatile("" : "+v"(lane));
    const u32 off[1] = {(set * 20 + min(lane, 39u) + (lane >= 20 ? 20 : 0)) * 128};
    wait_flags<1>(wsp(p) + O_FL, off, cap);
}
__device__ __forceinline__ int get_side(const LaParams& p, int& cap) { return sched_get(sched_peek(wsp(p) + O_FL + 384 * 128), wsp(p) + O_FL + 384 * 128, cap) & 1; }
__device__ __forceinline__ char* align2m(char* p) { return p + ((size_t)(-(intptr_t)p) & ((2u << 20) - 1)); }

constexpr int Q_XA = 0, Q_PART = 20480, Q_TR = 48640, Q_WL = 54784, Q_ST = 153088, Q_PS = 44;

#define DPP1(op, ror) op "_dpp %0, %0, %0 row_ror:" ror " row_mask:0xf bank_mask:0xf\n\t"
#define DPP2(op, ror) DPP1(op, ror) op "_dpp %1, %1, %1 row_ror:" ror " row_mask:0xf bank_mask:0xf\n\ts_nop 0\n\t"
__device__ __forceinline__ void st16m(char* base, u32 off, u32x4 v, u64 mask) {
    asm volatile("s_mov_b64 exec, %3\n\ts_nop 3\n\tglobal_store_dwordx4 %0, %1, %2 sc1\n\ts_mov_b64 exec, -1\n\ts_nop 1" :: "v"(off), "v"(v), "s"(base), "s"(mask) : "memory");
}
__device__ __forceinline__ void st32(char* base, u32 off, u32x4 a, u32x4 b) {
    asm volatile("s_nop 4\n\tglobal_store_dwordx4 %0, %1, %3\n\tglobal_store_dwordx4 %0, %2, %3 offset:16\n\ts_nop 2" :: "v"(off), "v"(a), "v"(b), "s"(base) : "memory");
}
__device__ __forceinline__ void st8m(char* base, u32 off, u32x2 v, u64 mask) {
    asm volatile("s_mov_b64 exec, %3\n\ts_nop 3\n\tglobal_store_dwordx2 %0, %1, %2 sc1\n\ts_mov_b64 exec, -1" :: "v"(off), "v"(v), "s"(base), "s"(mask) : "memory");
}
__device__ __forceinline__ u32 pk2bf_abs(float lo, float hi) {
    u32 r;
    asm("v_cvt_pk_bf16_f32 %0, |%1|, |%2|" : "=v"(r) : "v"(lo), "v"(hi));
    return r;
}
constexpr float SM_C = 0.07216878364870322f * 1.4426950408889634f, NEG_BIG = -0x1p100f;
constexpr u64 SM_MASK = 0x0155015501550155ull;
__device__ __forceinline__ void softmax_mem(const float* p, char* dst, const u32 off) {
    f32x4 a[4];
#pragma unroll
    for (int i = 0; i < 4; ++i) a[i] = *(const f32x4*)(p + i * 40 * Q_PS);
    f32x4 x = (a[0] + a[1]) + (a[2] + a[3]);
    const float ml = fmaxf(fmaxf(x[0], x[1]), fmaxf(x[2], x[3])) * SM_C;
    float mx = ml, e0, e1, e2, e3;
    asm("v_fma_f32 %1, %5, %9, -%0\n\tv_fma_f32 %2, %6, %9, -%0\n\tv_fma_f32 %3, %7, %9, -%0\n\tv_fma_f32 %4, %8, %9, -%0\n\t"
        DPP1("v_max_f32", "8") "v_exp_f32 %1, %1\n\ts_nop 0\n\t" DPP1("v_max_f32", "4") "v_exp_f32 %2, %2\n\ts_nop 0\n\t"
        DPP1("v_max_f32", "2") "v_exp_f32 %3, %3\n\ts_nop 0\n\t" DPP1("v_max_f32", "1") "v_exp_f32 %4, %4\n\ts_nop 0"
        : "+v"(mx), "=&v"(e0), "=&v"(e1), "=&v"(e2), "=&v"(e3) : "v"(x[0]), "v"(x[1]), "v"(x[2]), "v"(x[3]), "s"(SM_C));
    const float f = __builtin_amdgcn_exp2f(ml - mx);
    float sm = ((e0 + e1) + (e2 + e3)) * f;
    asm("s_nop 1\n\t" DPP1("v_add_f32", "8") "v_mul_f32 %1, %1, %5\n\ts_nop 0\n\t" DPP1("v_add_f32", "4") "v_mul_f32 %2, %2, %5\n\ts_nop 0\n\t"
        DPP1("v_add_f32", "2") "v_mul_f32 %3, %3, %5\n\ts_nop 0\n\t" DPP1("v_add_f32", "1") "v_mul_f32 %4, %4, %5"
        : "+v"(sm), "+v"(e0), "+v"(e1), "+v"(e2), "+v"(e3) : "v"(f));
    const float inv = __builtin_amdgcn_rcpf(sm);
    const u32 d0 = pk2bf_abs(e0 * inv, e1 * inv), d1 = pk2bf_abs(e2 * inv, e3 * inv);
    u32 n0, n1;
    asm("s_nop 0\n\tv_mov_b32_dpp %0, %2 quad_perm:[1,0,3,2] row_mask:0xf bank_mask:0xf\n\tv_mov_b32_dpp %1, %3 quad_perm:[1,0,3,2] row_mask:0xf bank_mask:0xf"
        : "=&v"(n0), "=&v"(n1) : "v"(d0), "v"(d1));
    st16m(dst, off, (u32x4){d0, d1, n0, n1}, SM_MASK);
}
__device__ __forceinline__ void softmax_tok(const char* shm, const int q0, int lane, char* dst) {
    asm volatile("" : "+v"(lane));
    const int r = lane >> 4, j = lane & 15;
    const float* p = (const float*)(shm + Q_PART) + (q0 + r) * Q_PS + min(j, 10) * 4;
    f32x4 x[2];
    float mx[2], sm[2];
#pragma unroll
    for (int n = 0; n < 2; ++n) {
        f32x4 a[4];
#pragma unroll
        for (int i = 0; i < 4; ++i) a[i] = *(const f32x4*)(p + (i * 40 + n * 4) * Q_PS);
        x[n] = (a[0] + a[1]) + (a[2] + a[3]);
        mx[n] = fmaxf(fmaxf(x[n][0], x[n][1]), fmaxf(x[n][2], x[n][3])) * SM_C;
    }
    asm("s_nop 1\n\t" DPP2("v_max_f32", "8") DPP2("v_max_f32", "4") DPP2("v_max_f32", "2") DPP2("v_max_f32", "1") : "+v"(mx[0]), "+v"(mx[1]));
#pragma unroll
    for (int n = 0; n < 2; ++n) {
#pragma unroll
        for (int i = 0; i < 4; ++i) x[n][i] = __builtin_amdgcn_exp2f(fmaf(x[n][i], SM_C, -mx[n]));
        sm[n] = (x[n][0] + x[n][1]) + (x[n][2] + x[n][3]);
    }
    asm("s_nop 1\n\t" DPP2("v_add_f32", "8") DPP2("v_add_f32", "4") DPP2("v_add_f32", "2") DPP2("v_add_f32", "1") : "+v"(sm[0]), "+v"(sm[1]));
#pragma unroll
    for (int n = 0; n < 2; ++n) {
        const float inv = __builtin_amdgcn_rcpf(sm[n]);
        const u32 d0 = pk2bf_abs(x[n][0] * inv, x[n][1] * inv), d1 = pk2bf_abs(x[n][2] * inv, x[n][3] * inv);
        u32 n0, n1;
        asm("s_nop 1\n\tv_mov_b32_dpp %0, %2 quad_perm:[1,0,3,2] row_mask:0xf bank_mask:0xf\n\tv_mov_b32_dpp %1, %3 quad_perm:[1,0,3,2] row_mask:0xf bank_mask:0xf"
            : "=&v"(n0), "=&v"(n1) : "v"(d0), "v"(d1));
        st16m(dst, (j < 8 ? n * 256 + j * 32 : 512 + n * 64) + r * 16, (u32x4){d0, d1, n0, n1}, SM_MASK);
    }
}

__host__ __device__ constexpr int qk_slot(int T, int ks) { return ks < 5 ? ks * 6 + T : 30 + (T >> 1) * 10 + (ks - 5) * 2 + (T & 1); }
__host__ __device__ constexpr int lds_slot(int n) { return n < 7 ? 5 * n + 2 : (n == 7 ? 38 : (n == 8 ? 41 : (n == 9 ? 46 : (n == 10 ? 51 : 55)))); }
__host__ __device__ constexpr int slot_lds(int sl) { for (int n = 0; n < 12; ++n) if (lds_slot(n) == sl) return n; return -1; }
__host__ __device__ constexpr int slot_frag(int sl) { if (slot_lds(sl) >= 0) return 48 + slot_lds(sl); int n = 0; for (int i = 0; i < 12; ++i) n += lds_slot(i) < sl; return sl - n; }
__host__ __device__ constexpr int qk_frag(int T, int ks) { return slot_frag(qk_slot(T, ks)); }
__host__ __device__ constexpr int slot_T(int sl) { return sl < 30 ? sl % 6 : ((sl - 30) / 10) * 2 + (sl & 1); }
__host__ __device__ constexpr int slot_ks(int sl) { return sl < 30 ? sl / 6 : 5 + ((sl - 30) % 10) / 2; }
__host__ __device__ constexpr int lds_rank(int T, int ks) { int n = 0; for (int t = 0; t < T; ++t) n += qk_frag(t, ks) >= 48; return n; }
__host__ __device__ constexpr int lds_nth(int ks, int n) { for (int t = 0; t < 6; ++t) if (qk_frag(t, ks) >= 48 && lds_rank(t, ks) == n) return qk_frag(t, ks) - 48; return -1; }

__device__ __forceinline__ void qk_role(const LaParams& p, const int set, const int h, char* shm) {
    const int lane = threadIdx.x & 63, w = __builtin_amdgcn_readfirstlane(threadIdx.x >> 6);
    const int fr = lane & 15, g = lane >> 4;
    const u32 lds0 = (u32)(uintptr_t)(LDS3 char*)shm;
    const int sidx = (lane & 8) ? 0xEEEE : 0x4444;
    int cap = SPIN_CAP;
    char* hop = p.hop;
    char* mine = hop;
    int phys = 0;
    char* px = (wsp(p) + O_PX);
    const char* xn = (wsp(p) + O_XN);
    char* wl = shm + Q_WL + w * 24576 + lane * 16;
    const char* xa = shm + Q_XA + lane * 16;

    bf16v WA[32], WV[16];
    {
        bf16v WT[12];
        u32 vo[6], rd[3];
#pragma unroll
        for (int i = 0; i < 6; ++i) {
            const int id = i * 64 + lane, r = w * 16 + id / 24;
            vo[i] = (32 * (r >> 5) + kperm((r >> 2) & 3, ((r >> 4) & 1) * 4 + (r & 3))) * (HD * 2) + ((id % 24) ^ (r & 6)) * 16;
        }
#pragma unroll
        for (int i = 0; i < 3; ++i) rd[i] = lds0 + (g * 4 + (fr >> 2)) * 384 + ((w * 3 + i) ^ ((g & 1) * 2 + (fr >> 3))) * 32 + (fr & 3) * 8;
        auto issue = [&](const int j) {
            const char* src = (const char*)((j & 1) ? p.Wk : p.Wq) + (size_t)(j >> 1) * 64 * HD * 2 + h * 384;
#pragma unroll
            for (int i = 0; i < 6; ++i) dma_gather(src, lds0 + (j % 6) * 24576 + w * 6144 + i * 1024, vo[i]);
        };
#pragma unroll
        for (int j = 0; j < 5; ++j) issue(j);
        static_for<0, 20>([&](auto J) {
            constexpr int j = CI(J);
            asm volatile("s_waitcnt vmcnt(%0)" :: "n"((19 - j < 4 ? 19 - j : 4) * 6) : "memory");
            RAW_BARRIER();
            if constexpr (j + 5 < 20) issue(j + 5);
            static_for<0, 3>([&](auto PR) {
                constexpr int id = qk_frag(2 * CI(PR) + (j & 1), j >> 1);
                u32 a = rd[CI(PR)] + (j % 6) * 24576;
                asm volatile("" : "+v"(a));
                const bf16v f = cat8(tr8(a, a + 16 * 384), tr8(a + 32 * 384, a + 48 * 384));
                if constexpr (id < 32) WA[id] = f; else if constexpr (id < 48) WV[id - 32] = f; else WT[id - 48] = f;
            });
        });
        RAW_BARRIER();
        dma_kb<5>(xn + (size_t)set * 40960 + w * 5120, lds0 + Q_XA + w * 5120, lane * 16);
#pragma unroll
        for (int i = 0; i < 12; ++i) { *(bf8*)(wl + i * 2048) = lo8(WT[i]); *(bf8*)(wl + i * 2048 + 1024) = hi8(WT[i]); }
    }
    TRDECL;
    bf8 QX[2], KX[2];
    u32x2 QY[2], KY[2];
    u32 peek = ~0u;
    int d1 = D1I, d2 = D2I;
    if (w == 3) *(u64*)(shm + Q_ST + lane * 8) = 0;
    auto xproj = [&](const int snext, const int sprev) {
        f32x4 acc[6][2];
        TR(8);
        const char* xs1 = xn + (size_t)(snext - 2) * 40960 + 20480 + w * 5120;
        const char* xs0 = xn + (size_t)min(snext, NSEG - 1) * 40960 + w * 5120;
        u32 xl = lds0 + Q_XA + w * 5120;
        asm volatile("" : "+s"(xs0), "+s"(xs1), "+s"(xl));
        bf8 x0 = *(const bf8*)(xa), x1 = *(const bf8*)(xa + 1024), lw[2];
        lw[0] = *(const bf8*)(wl);
        static_for<0, 20>([&](auto K) {
            constexpr int k = CI(K), l0 = lds_nth(((k + 1) >> 1) % 10, 0), l1 = lds_nth(((k + 1) >> 1) % 10, 1);
            bf8 n0, n1, nw[2];
            if constexpr (k == 9) {
                TR(9);
                VM0(); RAW_BARRIER();
                TR(10);
            }
            static_for<0, 6>([&](auto TT) {
                constexpr int T = CI(TT), id = qk_frag(T, k >> 1), rk = lds_rank(T, k >> 1);
                if constexpr (k < 19) {
                    if constexpr (T == 0) n0 = *(const bf8*)(xa + (k + 1) * 2048);
                    if constexpr (T == 1) n1 = *(const bf8*)(xa + (k + 1) * 2048 + 1024);
                    if constexpr (T == 2 && l0 >= 0) nw[0] = *(const bf8*)(wl + l0 * 2048 + ((k + 1) & 1) * 1024);
                    if constexpr (T == 3 && l1 >= 0) nw[1] = *(const bf8*)(wl + l1 * 2048 + ((k + 1) & 1) * 1024);
                }
                if constexpr (k < 3 && (T == 3 || T == 5) && 2 * k + (T == 5) < 5)
                    dma_kb<1>(xs1 + (2 * k + (T == 5)) * 1024, xl + 20480 + (2 * k + (T == 5)) * 1024, lane * 16);
                if constexpr (k >= 9 && k < 14 && T == 4)
                    dma_kb<1>(xs0 + (k - 9) * 1024, xl + (k - 9) * 1024, lane * 16);
                if constexpr (k == 18 && T == 4) peek = sched_peek(mine + (S_SLOT + w) * SLOT + max(sprev, 0) * 128 + 4);
                SCHEDB();
                if constexpr (id < 32) {
                    const bf8 wf = (k & 1) ? hi8(WA[id]) : lo8(WA[id]);
                    if constexpr (k == 0) { MMA0_AV(acc[T][0], wf, x0); MMA0_AV(acc[T][1], wf, x1); } else { MMA_AV(acc[T][0], wf, x0); MMA_AV(acc[T][1], wf, x1); }
                } else {
                    bf8 wf;
                    if constexpr (id < 48) wf = (k & 1) ? hi8(WV[id - 32]) : lo8(WV[id - 32]); else wf = lw[rk];
                    if constexpr (k == 0) { MMA0_VV(acc[T][0], wf, x0); MMA0_VV(acc[T][1], wf, x1); } else { MMA_VV(acc[T][0], wf, x0); MMA_VV(acc[T][1], wf, x1); }
                }
                SCHEDB();
            });
            if constexpr (k < 19) { x0 = n0; x1 = n1; if constexpr (l0 >= 0) lw[0] = nw[0]; if constexpr (l1 >= 0) lw[1] = nw[1]; }
        });
        TR(11);
        int ln = lane;
        asm volatile("" : "+v"(ln));
        f32x4 s[3];
#pragma unroll
        for (int i = 0; i < 4; ++i) s[0][i] = (ln >> 4) * 4 + i > (ln & 15) ? NEG_BIG : 0.f;
        s[2] = s[0];
        float* pp = (float*)(shm + Q_PART) + (w * 40 + 8 + (ln & 15)) * Q_PS + (ln >> 4) * 8;
        PIN(s[0]); PIN(s[2]);
        RAW_BARRIER();
#pragma unroll
        for (int T = 0; T < 6; ++T) { PIN(acc[T][0]); PIN(acc[T][1]); }
#pragma unroll
        for (int tt = 0; tt < 2; ++tt) { QX[tt] = pack8(acc[0][tt], acc[2][tt]); KX[tt] = pack8(acc[1][tt], acc[3][tt]); PIN(QX[tt]); PIN(KX[tt]); }
        NOPV();
        MMA_VV(s[0], KX[0], QX[0]); MMA0_VV(s[1], KX[0], QX[1]); MMA_VV(s[2], KX[1], QX[1]);
#pragma unroll
        for (int tt = 0; tt < 2; ++tt) {
            PIN(acc[4][tt]); PIN(acc[5][tt]);
            QY[tt] = (u32x2){pk2bf(acc[4][tt][0], acc[4][tt][1]), pk2bf(acc[4][tt][2], acc[4][tt][3])};
            KY[tt] = (u32x2){pk2bf(acc[5][tt][0], acc[5][tt][1]), pk2bf(acc[5][tt][2], acc[5][tt][3])};
            PIN(QY[tt]); PIN(KY[tt]);
        }
        NOPV();
        MMA16(s[0], KY[0], QY[0]); MMA16(s[1], KY[0], QY[1]); MMA16(s[2], KY[1], QY[1]);
        asm volatile("s_nop 7");
        PIN(s[0]); PIN(s[1]); PIN(s[2]);
        *(f32x4*)(pp) = s[0];
        *(f32x4*)(pp + 16 * Q_PS) = s[1];
        *(f32x4*)(pp + 16 * Q_PS + 4) = s[2];
    };
    {
        const f32x4 neg = {NEG_BIG, NEG_BIG, NEG_BIG, NEG_BIG};
        *(f32x4*)((float*)(shm + Q_PART) + (w * 40 + 8 + fr) * Q_PS + g * 8 + 4) = neg;
        if (lane < 40) *(f32x4*)((float*)(shm + Q_PART) + (w * 40 + lane) * Q_PS + 40) = neg;
    }
    float* pw0 = (float*)(shm + Q_PART) + (w * 40 + (fr & 7)) * Q_PS + g * 8 + (fr >> 3) * 4;
    float* pw2 = fr < 8 && g >= 2 ? (float*)(shm + Q_PART) + (w * 40 + fr) * Q_PS + 24 + g * 4 : (float*)(shm + Q_TR + w * 1536) + lane * 4;
    const float* prd = (const float*)(shm + Q_PART) + ((w & 1) * 4 + g) * Q_PS + min(fr, 10) * 4;
    u32 trw = lds0 + Q_TR + w * 1536 + lane * 8, trr = lds0 + Q_TR + w * 1536 + (fr & 3) * 128 + (4 * g + (fr >> 2)) * 8;
    asm volatile("" : "+v"(trw), "+v"(trr));
    const u32 poff = (fr < 8 ? (w & 1) * 256 + fr * 32 : 512 + (w & 1) * 64) + g * 16;

    for (int layer = 0; layer < 2; ++layer) {
    if (layer > 0) {
        if (w == 0) {
            __builtin_amdgcn_s_sleep(127);
            wait_rt(sched_get(~0u, mine + S_SLOT * SLOT + 63 * 128, cap) + T_TOK);
            wait_first_tokens(p, set, lane, cap);
        }
        RAW_BARRIER();
        hop += HOP_BYTES; mine += HOP_BYTES; px += PX_BYTES; xn += ACT;
        TT(set * 128 + h, 1);
        dma_kb<5>(xn + (size_t)set * 40960 + w * 5120, lds0 + Q_XA + w * 5120, lane * 16);
    }
    VM0(); RAW_BARRIER();
    TT(set * 128 + h, 2);
    xproj(set + 2, set - 1);
    if (layer == 0) { phys = set ^ get_side(p, cap); mine += phys * 8192; }
    TT(set * 128 + h, 3);
    for (int s = set; s < NSEG; s += 2) {
        const int rot = (s >> 1) & 3;
        TR(0);
        u32 ytime = 0;
        const char* mb0 = (s > 0 ? mine + (M_SLOT0 + rot * 8) * SLOT : p.hop + M0_OFF + phys * 8192) + 2 * w * SLOT + 2048;
        const char* mb1 = mb0 + SLOT;
        char* pdst = mine + (P_SLOT0 + rot * 4 + (h >> 3)) * SLOT + (h & 7) * 640;
        asm volatile("" : "+s"(mb0), "+s"(mb1), "+s"(pdst));
        if (s > 0) { ytime = sched_get(peek, mine + (S_SLOT + w) * SLOT + (s - 1) * 128 + 4, cap); wait_rt(ytime); }
        TR(1); TRV(7, ytime);
        f32x4 acc[6];
        u32x4 A[10];
        bf8 Ll[2], Lh[2];
        s16x4 F[3];
        u32 r0[4], r1[4], d[2];
        float v[4];
        int tries = 0;
        auto fin = [&](const int pr, const int i) {
            if (i == 0) { PIN(acc[2 * pr]); PIN(acc[2 * pr + 1]); }
            if (i < 4) { auto r = __builtin_amdgcn_permlane32_swap(__float_as_uint(acc[2 * pr][i]), __float_as_uint(acc[2 * pr + 1][i]), false, false); r0[i] = r[0]; r1[i] = r[1]; }
            if (i >= 1 && i < 5) { asm("v_add_f32 %0, %1, %2" : "=v"(v[i - 1]) : "v"(r0[i - 1]), "v"(r1[i - 1])); }
            if (i == 4) d[0] = pk2bf(v[0], v[1]);
            if (i == 5) {
                d[1] = pk2bf(v[2], v[3]);
                *(LDS3 u32x2*)(uintptr_t)(trw + pr * 512) = (u32x2){d[0], d[1]};
                F[pr] = __builtin_amdgcn_ds_read_tr16_b64_v4i16((LDS3 s16x4*)(uintptr_t)(trr + pr * 512));
            }
        };
        auto slot = [&](auto SL) {
            constexpr int sl = CI(SL), T = slot_T(sl), ks = slot_ks(sl), id = slot_frag(sl);
            if constexpr (id < 32) SMM_A(acc[T], A[ks], WA[id], sidx);
            else if constexpr (id < 48) SMM_V(acc[T], A[ks], WV[id - 32], sidx);
            else { const bf16v L = cat8(Ll[id & 1], Lh[id & 1]); SMM_V(acc[T], A[ks], L, sidx); }
            SCHEDB();
            if constexpr (sl == 0) TRC(6);
            constexpr int n0 = slot_lds(sl), n1 = slot_lds(sl - 1);
            if constexpr (n0 >= 0 && n0 < 10) Ll[n0 & 1] = *(const bf8*)(wl + (n0 + 2) * 2048);
            if constexpr (n1 >= 0 && n1 < 10) Lh[n1 & 1] = *(const bf8*)(wl + (n1 + 2) * 2048 + 1024);
            if constexpr (sl >= 43 && sl < 49) fin(0, sl - 43);
            if constexpr (sl >= 53 && sl < 59) fin(1, sl - 53);
            SCHEDB();
        };
        auto gather = [&]() {
            u32 vo = lane * 16;
            asm volatile("" : "+v"(vo) :: "memory");
#pragma unroll
            for (int ks = 0; ks < 10; ++ks) {
                A[ks] = ld16(ks < 5 ? mb0 : mb1, vo, (ks % 5 - 2) * 1024);
                SCHEDB();
                if (ks < 6) ZERO4(acc[ks]);
                if (ks == 6 || ks == 7) { Ll[ks & 1] = *(const bf8*)(wl + (ks & 1) * 2048); Lh[ks & 1] = *(const bf8*)(wl + (ks & 1) * 2048 + 1024); }
                if (ks < 9) asm volatile("s_nop %0" :: "n"(PACE));
                SCHEDB();
            }
        };
        gather();
        static_for<0, 49>(slot);
        if (__builtin_expect(__any(__builtin_isunordered(v[0], v[1]) || __builtin_isunordered(v[2], v[3])), 0)) {
            SETTLE();
            while (true) {
                gather();
                u32 m = 0;
#pragma unroll
                for (int ks = 0; ks < 10; ++ks) m = max4(m, A[ks]);
                ++tries;
                if (!__any(m == ~0u)) break;
                if (tries >= cap) { cap = 1; break; }
                __builtin_amdgcn_s_sleep(4);
            }
            static_for<0, 49>(slot);
        }
        TR(2); TRV(6, tries + 1);
        static_for<49, 51>(slot);
        if (__builtin_expect(h == 0 && w == 3, 0)) {
            u32 base = (tries == 0 && s > 0) ? ytime : rt32() + ELATE;
            u32 ln8 = lane;
            asm volatile("" : "+v"(ln8));
            ln8 *= 8192;
            const u32x2 rec = {nosent(base + d1 + MG1), nosent(base + d1 + MG1 + d2 + MG2)};
            asm volatile("s_mov_b64 exec, 0x3ff\n\ts_nop 3\n\tglobal_store_dwordx2 %0, %1, %2 sc1\n\ts_mov_b64 exec, -1" :: "v"(ln8), "v"(rec), "s"(hop + S_SLOT * SLOT + s * 128) : "memory");
        }
        static_for<51, 60>(slot);
        TRC(7);
        bf8 F01 = __builtin_bit_cast(bf8, __builtin_shufflevector(F[0], F[1], 0, 1, 2, 3, 4, 5, 6, 7));
        f32x4 s0, s1, s2, u0, u1;
        asm volatile("s_nop 6");
#pragma unroll
        for (int i = 0; i < 6; ++i) fin(2, i);
        PIN(F01); NOPV();
        MMA0_VV(s1, KX[1], F01); MMA0_VV(s0, KX[0], F01); MMA0_VV(s2, F01, F01); MMA0_VV(u0, F01, QX[0]); MMA0_VV(u1, F01, QX[1]);
        asm volatile("s_nop 3");
        MMA16(s1, KY[1], F[2]); MMA16(s0, KY[0], F[2]); MMA16(s2, F[2], F[2]); MMA16(u0, F[2], QY[0]); MMA16(u1, F[2], QY[1]);
        asm volatile("s_nop 4\n\tv_mov_b32_dpp %0, %4 row_ror:8 row_mask:0xf bank_mask:0xc\n\tv_mov_b32_dpp %1, %5 row_ror:8 row_mask:0xf bank_mask:0xc\n\t"
                     "v_mov_b32_dpp %2, %6 row_ror:8 row_mask:0xf bank_mask:0xc\n\tv_mov_b32_dpp %3, %7 row_ror:8 row_mask:0xf bank_mask:0xc"
                     : "+v"(s0[0]), "+v"(s0[1]), "+v"(s0[2]), "+v"(s0[3]) : "v"(s1[0]), "v"(s1[1]), "v"(s1[2]), "v"(s1[3]));
        PIN(s2); *(f32x4*)pw2 = s2; SCHEDB();
        *(f32x4*)pw0 = s0; SCHEDB();
        if (__builtin_expect(w < 2, 1)) {
            RAW_BARRIER();
            TR(3);
            softmax_mem(prd, pdst, poff);
            TR(4);
            if (w == 1) st4_lane0(mine + (T_SLOT + 1) * SLOT + (16 + h) * 128, tries == 0 && s > 0 ? rt32() - ytime : 0);
        } else {
            RAW_BARRIER();
            TR(3);
            if (h == 0 && w == 3) {
                u32 lo = lane;
                asm volatile("" : "+v"(lo));
                int sta = *(const int*)(shm + Q_ST + lo * 4), stb = *(const int*)(shm + Q_ST + 256 + lo * 4);
                asm("s_nop 1\n\t" DPP2("v_max_i32", "8") DPP2("v_max_i32", "4") DPP2("v_max_i32", "2") DPP2("v_max_i32", "1") : "+v"(sta), "+v"(stb));
                const int m1 = max(__builtin_amdgcn_readlane(stb, 16), __builtin_amdgcn_readlane(stb, 32));
                const int m2 = max(max(max(__builtin_amdgcn_readlane(sta, 0), __builtin_amdgcn_readlane(sta, 16)), max(__builtin_amdgcn_readlane(sta, 32), __builtin_amdgcn_readlane(sta, 48))),
                                   __builtin_amdgcn_readlane(stb, 0));
                d1 = track(d1, m1); d2 = track(d2, m2);
                u32 keep;
                asm volatile("s_mov_b32 %0, m0\n\ts_mov_b32 m0, %4\n\ts_nop 2\n\tglobal_load_lds_dword %1, %2 sc1\n\ts_add_u32 m0, m0, 256\n\ts_nop 0\n\tglobal_load_lds_dword %1, %3 sc1\n\ts_mov_b32 m0, %0"
                             : "=&s"(keep) : "v"(lo * 128), "s"(mine + T_SLOT * SLOT), "s"(mine + (T_SLOT + 1) * SLOT), "s"(lds0 + Q_ST), "s"(d1), "s"(d2) : "memory", "scc");
            }
            if (w == 2 && s < 2 && layer > 0) wait_all_tokens(p, lane, cap);
            if (w == 2 && s >= 2 && lane < 40) arm16(mine + (P_SLOT0 + ((rot + 3) & 3) * 4 + (h >> 3)) * SLOT + (h & 7) * 640, lane * 16);
            if (s >= 4 && lane < 40) {
                char* o = px + (size_t)(set * 4 + ((rot + 2) & 3)) * PX_BUF + ((w - 2) * 64 + h) * 640;
                arm16(o, lane * 16); arm16(o + 32 * 640, lane * 16);
            }
        }
        {
            int ln = lane;
            asm volatile("" : "+v"(ln));
            PIN(u0); PIN(u1);
            f32x4 u;
#pragma unroll
            for (int i = 0; i < 4; ++i) u[i] = __uint_as_float(__builtin_amdgcn_permlane32_swap(__float_as_uint(u0[i]), __float_as_uint(u1[i]), false, false)[1]);
            *(f32x4*)((float*)(shm + Q_PART) + (w * 40 + 8 + (ln >> 5) * 16 + (ln & 15)) * Q_PS + 32 + ((ln >> 4) & 1) * 4) = u;
        }
        RAW_BARRIER();
        softmax_tok(shm, 8 + 8 * w, lane, px + (size_t)(set * 4 + rot) * PX_BUF + (w * 32 + h) * 640);
        TR(5); TRFLUSH(set * 128 + h);
        if (s + 2 >= NSEG) { TT(set * 128 + h, 0); break; }
        RAW_BARRIER();
        xproj(s + 4, s + 1);
    }
    }
}

constexpr int C_YL = 0, C_RED = 20480, C_EX = 28672, C_XN = 32768;
__device__ __forceinline__ void comb_role(const LaParams& p, const int set, const int c, char* shm) {
    const int lane = threadIdx.x & 63, w = __builtin_amdgcn_readfirstlane(threadIdx.x >> 6);
    const int fr = lane & 15, g = lane >> 4;
    const u32 lds0 = (u32)(uintptr_t)(LDS3 char*)shm;
    const int sidx = (lane & 8) ? 0xEEEE : 0x4444;
    int cap = SPIN_CAP;
    char* mine = p.hop;
    char* other = p.hop;
    int phys = 0;
    char* px = (wsp(p) + O_PX);
    const char* xn = (wsp(p) + O_XN);
    char* yl = shm + C_YL + lane * 16;
    const int cb = (set * 80 + c) % 10, rb = (set * 80 + c) / 10;

    {
        const int t = threadIdx.x, h = (set * 80 + c) / 5, cu = c % 5;
        u32x4 st[12];
        const u16* src = p.Wo + ((size_t)h * DH + (t >> 4)) * DM + cu * 128 + (t & 15) * 8;
#pragma unroll
        for (int i = 0; i < 12; ++i) asm volatile("global_load_dwordx4 %0, %1, off" : "=v"(st[i]) : "v"(src + (size_t)i * 16 * DM) : "memory");
        u32 vo[12];
#pragma unroll
        for (int i = 0; i < 12; ++i) {
            const int id = i * 64 + lane, r = id / 24;
            vo[i] = kperm((r >> 2) & 3, (r >> 4) * 4 + (r & 3)) * (HD * 2) + ((id % 24) ^ (r & 7)) * 16;
        }
        const u32 ring = lds0 + 57344 + w * 24576;
        auto issue = [&](const int j) {
            const char* a = (const char*)p.Wv + (size_t)(((j + cu) % 5) * 4 + w) * 32 * HD * 2 + h * 384;
#pragma unroll
            for (int i = 0; i < 12; ++i) dma_gather(a, ring + (j & 1) * 12288 + i * 1024, vo[i]);
        };
        SCHEDB();
        issue(0); issue(1);
        bf8 A[5][2][6];
        const u32 ra = ring + fr * 384 + ((g ^ (fr & 3)) + (fr & 4)) * 16;
        auto fetch = [&](const int j) {
            asm volatile("s_waitcnt vmcnt(%0)" :: "n"(12) : "memory");
            if (j == 4) VM0();
#pragma unroll
            for (int u = 0; u < 2; ++u)
#pragma unroll
                for (int k = 0; k < 6; ++k) A[j][u][k] = *(LDS3 const bf8*)(uintptr_t)((ra ^ ((k & 1) * 64)) + (j & 1) * 12288 + u * 6144 + (k >> 1) * 128);
            asm volatile("s_waitcnt lgkmcnt(0)" ::: "memory");
            if (j < 3) issue(j + 2);
        };
        const int e = t >> 4, pr = ((e >> 2) & 1) * 8 + (e >> 3) * 4 + (e & 3);
        asm volatile("s_waitcnt vmcnt(24)" : "+v"(st[0]), "+v"(st[1]), "+v"(st[2]), "+v"(st[3]), "+v"(st[4]), "+v"(st[5]),
                     "+v"(st[6]), "+v"(st[7]), "+v"(st[8]), "+v"(st[9]), "+v"(st[10]), "+v"(st[11]) :: "memory");
#pragma unroll
        for (int i = 0; i < 12; ++i) *(u32x4*)(shm + (i * 16 + pr) * 288 + (t & 15) * 16) = st[i];
        fetch(0);
        RAW_BARRIER();
        { const int layer = 0; TT(set * 128 + 32 + c, 12); }
        bf8 B[8][6];
        const u32 rd = lds0 + ((g >> 1) * 16 + (g & 1) * 4 + (fr >> 2)) * 288 + (fr & 3) * 8;
#pragma unroll
        for (int n = 0; n < 8; ++n)
#pragma unroll
            for (int k = 0; k < 6; ++k) { B[n][k] = tr8(rd + k * 32 * 288 + n * 32, rd + (k * 32 + 8) * 288 + n * 32); PIN(B[n][k]); }
#pragma unroll
        for (int j = 1; j < 5; ++j) fetch(j);
        f32x4 cc[2][2];
        static_for<0, 41>([&](auto N) {
            constexpr int n = CI(N) % 8, j = CI(N) / 8, q = CI(N) - 1;
            u32x4 o;
#pragma unroll
            for (int k = 0; k < 6; ++k) {
                if constexpr (j < 5) {
#pragma unroll
                    for (int u = 0; u < 2; ++u) { if (k == 0) MMA0_AV(cc[n & 1][u], A[j][u][k], B[n][k]); else MMA_AV(cc[n & 1][u], A[j][u][k], B[n][k]); }
                } else if (k == 0) asm volatile("s_nop 7");
                SCHEDB();
                if constexpr (q >= 0) {
                    if (k == 3) { PIN(cc[q & 1][0]); o[0] = pk2bf(cc[q & 1][0][0], cc[q & 1][0][1]); o[1] = pk2bf(cc[q & 1][0][2], cc[q & 1][0][3]); }
                    if (k == 4) { PIN(cc[q & 1][1]); o[2] = pk2bf(cc[q & 1][1][0], cc[q & 1][1][1]); o[3] = pk2bf(cc[q & 1][1][2], cc[q & 1][1][3]); }
                    if (k == 5) {
                        char* dst = wsp(p) + O_WVO + ((size_t)(h * 20 + ((q / 8 + cu) % 5) * 4 + w) * 40 + cu * 8 + q % 8) * 1024;
                        __builtin_amdgcn_global_store_b128((gu32x4*)(dst + (u32)(lane * 16)), o, "agent");
                    }
                }
                SCHEDB();
            }
        });
        { const int layer = 0; TT(set * 128 + 32 + c, 13); }
        VM0(); RAW_BARRIER();
        { const int layer = 0; TT(set * 128 + 32 + c, 14); }
        dma_kb<10>(xn + (size_t)set * 40960 + w * 10240, lds0 + C_XN + w * 10240, lane * 16);
        if (w == 0) {
            st4_lane0(wsp(p) + O_FL + (400 + set * 80 + c) * 128, 1u);
            __builtin_amdgcn_s_sleep(8);
            const u32 off[1] = {(u32)(400 + (lane & 31) * 5 + (c >> 4)) * 128};
            wait_flags<1>(wsp(p) + O_FL, off, cap);
        }
        RAW_BARRIER();
        { const int layer = 0; TT(set * 128 + 32 + c, 15); }
    }
    bf16v WA[32], WV[8];
    {
        u32 vo = (w * 8 + (fr >> 3)) * 819200 + g * 256 + (fr & 7) * 16;
        asm volatile("" : "+v"(vo));
        auto ldw = [&](const int ks, const int t) {
            const char* a = wsp(p) + O_WVO + (size_t)(t * 1600 + ks * 80 + (c >> 1)) * 1024 + (c & 1) * 128;
            const char* b = a + 40960;
            asm volatile("" : "+s"(b));
            return cat8(__builtin_bit_cast(bf8, ld16(a, vo, 0)), __builtin_bit_cast(bf8, ld16(b, vo, 0)));
        };
#pragma unroll
        for (int i = 0; i < 32; ++i) WA[i] = ldw(i >> 2, i & 3);
#pragma unroll
        for (int i = 0; i < 8; ++i) WV[i] = ldw(8 + (i >> 2), i & 3);
    }
    auto yx = [&](bf8 (&yn)[4], const int sy, auto&& extra) {
        f32x4 acc[4][2];
        const char* xa = shm + C_XN + ((sy >> 1) & 1) * 40960 + lane * 16;
        const char* xs = (sy + 2 < NSEG ? xn + (size_t)(sy + 2) * 40960 : (wsp(p) + O_WO) + cb * 81920) + w * 10240;
        u32 xl = lds0 + C_XN + (~(sy >> 1) & 1) * 40960 + w * 10240;
        asm volatile("" : "+s"(xs), "+s"(xl));
        bf8 x0 = *(const bf8*)(xa), x1 = *(const bf8*)(xa + 1024);
#pragma unroll
        for (int k = 0; k < 20; ++k) {
            bf8 n0, n1;
#pragma unroll
            for (int t = 0; t < 4; ++t) {
                if (k < 19 && t == 0) n0 = *(const bf8*)(xa + (k + 1) * 2048);
                if (k < 19 && t == 1) n1 = *(const bf8*)(xa + (k + 1) * 2048 + 1024);
                if (t >= 2 && k < 5) dma_kb<1>(xs + (2 * k + t - 2) * 1024, xl + (2 * k + t - 2) * 1024, lane * 16);
                if (t >= 2 && k >= 5 && k < 15) extra(2 * k + t - 12);
                SCHEDB();
                const int id = (k >> 1) * 4 + t;
                if (id < 32) {
                    const bf8 wf = (k & 1) ? hi8(WA[id & 31]) : lo8(WA[id & 31]);
                    if (k == 0) { MMA0_VA(acc[t][0], x0, wf); MMA0_VA(acc[t][1], x1, wf); } else { MMA_VA(acc[t][0], x0, wf); MMA_VA(acc[t][1], x1, wf); }
                } else {
                    const bf8 wf = (k & 1) ? hi8(WV[id & 7]) : lo8(WV[id & 7]);
                    MMA_VV(acc[t][0], x0, wf); MMA_VV(acc[t][1], x1, wf);
                }
                SCHEDB();
            }
            if (k < 19) { x0 = n0; x1 = n1; }
        }
        SETTLE();
#pragma unroll
        for (int t = 0; t < 4; ++t) { PIN(acc[t][0]); PIN(acc[t][1]); yn[t] = pack8(acc[t][0], acc[t][1]); }
    };
    const u32 pofs = (fr >> 3) * 640 + ((fr >> 2) & 1) * 256 + g * 64 + (fr & 3) * 16;
    const u32 mofs = (2 * g + (fr >> 3)) * 640 + 512 + (fr & 7) * 16;

    for (int layer = 0; layer < 2; ++layer) {
    u32 peek = ~0u;
    VM0(); RAW_BARRIER();
    TT(set * 128 + 32 + c, 11);
    if (layer > 0 && w == 0) st4_lane0(wsp(p) + O_FL + (192 + set * 80 + c) * 128, 1u);
    {
        bf8 yn[4];
        yx(yn, set, [](int) {});
#pragma unroll
        for (int t = 0; t < 4; ++t) *(bf8*)(yl + (w * 5 + t) * 1024) = yn[t];
    }
    if (layer == 0) { phys = set ^ get_side(p, cap); mine += phys * 8192; other += (phys ^ 1) * 8192; }
    else if (w == 3) wait_all_tokens(p, lane, cap);
    TT(set * 128 + 32 + c, 10);
    for (int s = set; s < NSEG; s += 2) {
        const int rot = (s >> 1) & 3;
        TRDECL; TR(0);
        const char* mb0 = (s > 0 ? mine + (M_SLOT0 + rot * 8) * SLOT : p.hop + M0_OFF + phys * 8192) + 2 * w * SLOT + 2048;
        const char* mb1 = mb0 + SLOT;
        asm volatile("" : "+s"(mb0), "+s"(mb1));
        if (s > 0) wait_rt(sched_get(peek, mine + (S_SLOT + w) * SLOT + (s - 1) * 128 + 4, cap) + CLAG);
        TR(1);
        {
            f32x4 acc[4];
            u32x4 A[10];
            auto gather = [&]() {
                u32 vo = lane * 16;
                asm volatile("" : "+v"(vo) :: "memory");
#pragma unroll
                for (int ks = 0; ks < 10; ++ks) {
                    A[ks] = ld16(ks < 5 ? mb0 : mb1, vo, (ks % 5 - 2) * 1024);
                    SCHEDB();
                    if (ks < 4) ZERO4(acc[ks]);
                    if (ks < 9) asm volatile("s_nop %0" :: "n"(PACE));
                    SCHEDB();
                }
            };
            auto mproj = [&]() {
#pragma unroll
                for (int ks = 0; ks < 10; ++ks)
#pragma unroll
                    for (int t = 0; t < 4; ++t) { if (ks < 8) SMM_A(acc[t], A[ks], WA[(ks * 4 + t) & 31], sidx); else SMM_V(acc[t], A[ks], WV[(ks * 4 + t) & 7], sidx); }
                asm volatile("s_nop 7");
#pragma unroll
                for (int t = 0; t < 4; ++t) PIN(acc[t]);
            };
            gather();
            mproj();
            if (__builtin_expect(__any(__builtin_isunordered(acc[0][0], acc[0][1]) || __builtin_isunordered(acc[0][2], acc[0][3])), 0)) {
                for (int tries = 1;; ++tries) {
                    gather();
                    u32 m = 0;
#pragma unroll
                    for (int ks = 0; ks < 10; ++ks) m = max4(m, A[ks]);
                    if (!__any(m == ~0u)) break;
                    if (tries >= cap) { cap = 1; break; }
                    __builtin_amdgcn_s_sleep(8);
                }
                mproj();
            }
            peek = sched_peek(mine + (S_SLOT + w) * SLOT + s * 128);
#pragma unroll
            for (int pr = 0; pr < 2; ++pr) {
                float v[4];
#pragma unroll
                for (int i = 0; i < 4; ++i) v[i] = halfsum(acc[2 * pr][i], acc[2 * pr + 1][i]);
                *(u32x2*)(shm + C_YL + (w * 5 + 4) * 1024 + ((2 * pr + (g >> 1)) * 16 + fr) * 16 + (g & 1) * 8) = (u32x2){pk2bf(v[0], v[1]), pk2bf(v[2], v[3])};
            }
        }
        RAW_BARRIER();
        TR(2);
        if (s < NSEG - 1) {
            const char* pb[4];
#pragma unroll
            for (int i = 0; i < 4; ++i) { pb[i] = mine + (P_SLOT0 + rot * 4 + ((i + c * ROTC) & 3)) * SLOT; asm volatile("" : "+s"(pb[i])); }
            auto ldy = [&](const int f) { return *(const bf8*)(yl + (((f / 5 + c * ROTC) & 3) * 5 + f % 5) * 1024); };
            auto ldp = [&](const int f, const u32 vo, const u32 vm) { return f % 5 == 4 ? ld16(pb[f / 5], vm, 0) : ld16(pb[f / 5], vo, (f % 5) * 1280); };
            u32 xt;
            auto partial = [&](auto NF, auto F0, f32x4& acc, auto&& late) {
                constexpr int nf = CI(NF), f0 = CI(F0);
                bf8 Y[nf];
#pragma unroll
                for (int i = 0; i < nf; ++i) Y[i] = ldy(f0 + i);
#pragma unroll
                for (int i = 0; i < nf; ++i) PIN(Y[i]);
                u32 vo = pofs, vm = mofs;
                asm volatile("" : "+v"(vo), "+v"(vm));
                xt = sched_get(peek, mine + (S_SLOT + w) * SLOT + s * 128, cap);
                TR(3);
                wait_rt(xt);
                TRV(7, xt); TRC(6);
                asm volatile("" ::: "memory");
                u32x4 P[nf];
#pragma unroll
                for (int i = 0; i < nf; ++i) { P[i] = ldp(f0 + i, vo, vm); SCHEDB(); }
#pragma unroll
                for (int i = 0; i < nf; ++i) {
                    if (i == nf - 2) late();
                    if (i == 0) MMA0_VV(acc, Y[i], P[i]); else MMA_VV(acc, Y[i], P[i]);
                    if (i == 0) TRC(7);
                    SCHEDB();
                }
                TRC(8);
            };
            f32x4 acc;
            if (w > 0) {
                if (w == 1) partial(std::integral_constant<int, 3>(), std::integral_constant<int, 11>(), acc, [] {});
                else if (w == 2) partial(std::integral_constant<int, 3>(), std::integral_constant<int, 14>(), acc, [] {});
                else partial(std::integral_constant<int, 3>(), std::integral_constant<int, 17>(), acc, [] {});
                asm volatile("s_nop 7" : "+v"(acc));
                *(f32x4*)(shm + C_EX + (w - 1) * 1024 + lane * 16) = acc;
                RAW_BARRIER();
            } else {
                const int rn = ((s + 1) >> 1) & 3;
                u32 dst = g * 2 * SLOT + (lane & 7) * 16;
                char* dstb = other + (M_SLOT0 + rn * 8 + c / 40) * SLOT + (c % 40) * 128;
                u32 exa = lds0 + C_EX + lane * 16;
                asm volatile("" : "+s"(dstb), "+v"(dst), "+v"(exa));
                f32x4 h1, h2, h3;
                partial(std::integral_constant<int, 11>(), std::integral_constant<int, 0>(), acc, [&] {
                    __builtin_amdgcn_s_barrier();
                    asm volatile("" ::: "memory");
                    h1 = *(LDS3 const f32x4*)(uintptr_t)exa; h2 = *(LDS3 const f32x4*)(uintptr_t)(exa + 1024); h3 = *(LDS3 const f32x4*)(uintptr_t)(exa + 2048);
                    SCHEDB();
                });
                h1 = (h1 + h2) + h3;
                asm volatile("s_nop 2" : "+v"(acc), "+v"(h1));
                acc += h1;
                float v0, v1;
                auto merge = [&]() {
                    auto a = __builtin_amdgcn_permlane32_swap(__float_as_uint(acc[0]), __float_as_uint(acc[2]), false, false);
                    auto b = __builtin_amdgcn_permlane32_swap(__float_as_uint(acc[1]), __float_as_uint(acc[3]), false, false);
                    v0 = __uint_as_float(a[0]) + fdpp<0x128>(__uint_as_float(a[1]));
                    v1 = __uint_as_float(b[0]) + fdpp<0x128>(__uint_as_float(b[1]));
                };
                merge();
                int tries = 0;
                if (__builtin_expect(__any(__builtin_isunordered(v0, v1)), 0)) {
                    while (true) {
                        u32 vo = pofs, vm = mofs, m = 0;
                        asm volatile("" : "+v"(vo), "+v"(vm) :: "memory");
                        u32x4 P[20];
#pragma unroll
                        for (int i = 0; i < 20; ++i) P[i] = ldp(i, vo, vm);
                        f32x4 hh[4];
#pragma unroll
                        for (int i = 0; i < 20; ++i) {
                            const bf8 y = ldy(i);
                            const int k = i < 11 ? 0 : (i - 8) / 3;
                            if (i == 0 || i == 11 || i == 14 || i == 17) MMA0_VV(hh[k], y, P[i]); else MMA_VV(hh[k], y, P[i]);
                            m = max4(m, P[i]);
                            SCHEDB();
                        }
                        asm volatile("s_nop 7" : "+v"(hh[0]), "+v"(hh[1]), "+v"(hh[2]), "+v"(hh[3]));
                        acc = hh[0] + ((hh[1] + hh[2]) + hh[3]);
                        ++tries;
                        if (!__any(m == ~0u)) break;
                        if (tries >= cap) { cap = 1; break; }
                        __builtin_amdgcn_s_sleep(4);
                    }
                    merge();
                }
                TRV(6, tries + 1);
                const u32 dw = nosent(pk2bf(v0, v1));
                auto e = __builtin_amdgcn_permlane32_swap(dw, dw, false, false);
                auto e0 = __builtin_amdgcn_permlane16_swap(e[0], e[0], false, false);
                auto e1 = __builtin_amdgcn_permlane16_swap(e[1], e[1], false, false);
                st16m(dstb, dst, (u32x4){e0[0], e1[0], e0[1], e1[1]}, 0x00ff00ff00ff00ffull);
                TR(4);
                st4_lane0(mine + (T_SLOT + (c >> 6)) * SLOT + (c & 63) * 128, tries == 0 && s > 0 ? rt32() - xt : 0);
                if (s >= 2 && fr < 8) arm16(dstb + (((rn + 3) & 3) - rn) * 8 * SLOT, dst);
            }
        } else wait_rt(sched_get(peek, mine + (S_SLOT + w) * SLOT + s * 128, cap) + T_PX);
        RAW_BARRIER();
        auto tokens = [&](auto MORE) {
            f32x4 oa[4];
            bf8 ya[4];
            u32x4 P[20];
            u32 vo = pofs, vm = mofs;
            asm volatile("" : "+v"(vo), "+v"(vm));
            const char* pb[4];
#pragma unroll
            for (int i = 0; i < 4; ++i) { pb[i] = px + (size_t)(set * 4 + rot) * PX_BUF + (i * 32 + w * 8) * 640; asm volatile("" : "+s"(pb[i])); }
            auto ldp = [&](const int i) { P[i] = i % 5 == 4 ? ld16(pb[i / 5], vm, 0) : ld16(pb[i / 5], vo, (i % 5) * 1280); };
            int ln = lane;
            asm volatile("" : "+v"(ln));
            const u32x4 rg = ld16((wsp(p) + O_XR) + layer * ACT + (size_t)s * 40960 + ((c >> 2) * 2 + (w >> 1)) * 1024 + (c & 2) * 256 + (w & 1) * 128, (ln & 1) * 256 + ((ln >> 1) & 7) * 16, 0);
            TRC(9);
            if constexpr (CI(MORE)) yx(ya, s + 2, ldp);
            else {
#pragma unroll
                for (int i = 0; i < 20; ++i) ldp(i);
            }
            TRC(10);
            if constexpr (CI(MORE)) peek = sched_peek(mine + (S_SLOT + w) * SLOT + (s + 1) * 128 + 4);
            bf8 y[5];
#pragma unroll
            for (int j = 0; j < 5; ++j) y[j] = *(const bf8*)(yl + (w * 5 + j) * 1024);
            auto outx = [&]() {
#pragma unroll
                for (int i = 0; i < 20; ++i) PIN(P[i]);
                NOPV();
#pragma unroll
                for (int j = 0; j < 5; ++j)
#pragma unroll
                    for (int q = 0; q < 4; ++q) { if (j == 0) MMA0_VV(oa[q], y[j], P[q * 5 + j]); else MMA_VV(oa[q], y[j], P[q * 5 + j]); }
                asm volatile("s_nop 7");
#pragma unroll
                for (int q = 0; q < 4; ++q) PIN(oa[q]);
            };
            outx();
            if (__builtin_expect(__any(__builtin_isunordered(oa[0][0], oa[1][0]) || __builtin_isunordered(oa[2][0], oa[3][0])), 0)) {
                for (int tries = 1;; ++tries) {
                    asm volatile("" : "+v"(vo), "+v"(vm) :: "memory");
                    u32 m = 0;
#pragma unroll
                    for (int i = 0; i < 20; ++i) ldp(i);
#pragma unroll
                    for (int i = 0; i < 20; ++i) m = max4(m, P[i]);
                    if (!__any(m == ~0u)) break;
                    if (tries >= cap) { cap = 1; break; }
                    __builtin_amdgcn_s_sleep(8);
                }
                outx();
            }
            const bool own = (fr < 8) == (g < 2);
#pragma unroll
            for (int q = 0; q < 4; q += 2) {
                f32x4 o;
#pragma unroll
                for (int i = 0; i < 4; ++i) { const float t = fdpp<0x128>(oa[q + 1][i]); o[i] = own ? oa[q][i] : t; }
                *(f32x4*)(shm + C_RED + ((w * 2 + (g >> 1)) * 32 + (q + !own) * 8 + (fr & 7)) * 32 + (g & 1) * 16) = o;
            }
            if constexpr (CI(MORE)) {
#pragma unroll
                for (int t = 0; t < 4; ++t) *(bf8*)(yl + (w * 5 + t) * 1024) = ya[t];
            }
            RAW_BARRIER();
            TR(5); TRFLUSH(set * 128 + 32 + c);
            f32x4 a = *(const f32x4*)(shm + C_RED + (ln >> 4) * 2048 + w * 256 + (ln & 15) * 16) + *(const f32x4*)(shm + C_RED + (ln >> 4) * 2048 + 1024 + w * 256 + (ln & 15) * 16);
#pragma unroll
            for (int i = 0; i < 4; ++i) {
                auto t = __builtin_amdgcn_permlane16_swap(__float_as_uint(a[i]), __float_as_uint(a[i]), false, false);
                const float z = __uint_as_float(t[0]) + __uint_as_float(t[1]);
                a[i] = halfsum(z, z);
            }
            const u32 ra = (c & 1) ? rg[1] : rg[0], rb2 = (c & 1) ? rg[3] : rg[2];
            const u32 d0 = nosent(pk2bf(bflo(ra) + rbf(a[0]), bfhi(ra) + rbf(a[1]))), d1 = nosent(pk2bf(bflo(rb2) + rbf(a[2]), bfhi(rb2) + rbf(a[3])));
            const u32 n0 = __builtin_amdgcn_mov_dpp(d0, 0xB1, 0xf, 0xf, true), n1 = __builtin_amdgcn_mov_dpp(d1, 0xB1, 0xf, 0xf, true);
            st16m((wsp(p) + O_X1) + layer * ACT + (size_t)s * 40960 + c * 512 + w * 128, ln * 8, (u32x4){d0, d1, n0, n1}, 0x5555ull);
        };
        if (s + 2 >= NSEG) { tokens(std::integral_constant<int, 0>()); break; }
        tokens(std::integral_constant<int, 1>());
    }
    TT(set * 128 + 32 + c, 0);
    dma_kb<10>((wsp(p) + O_WO) + cb * 81920 + 40960 + w * 10240, lds0 + C_XN + 40960 + w * 10240, lane * 16);
    VM0(); RAW_BARRIER();
    wait_rt(sched_get(~0u, mine + (S_SLOT + w) * SLOT + 63 * 128, cap) + T_OB + OB_STEP * w + (layer == 0 ? OB_COLD + 8 * w : 0));
    {
        int ln = lane;
        asm volatile("" : "+v"(ln));
        u32x4 O[20], R[2];
        u32 dw[8];
        auto ready = [&](const int T) {
            const char* ob = (wsp(p) + O_OB) + layer * ACT + (size_t)T * 20480 + 16384;
            const u32 off[1] = {(u32)(ln & 31) * 128};
            wait_flags<1>(ob, off, cap);
        };
        auto load = [&](const int T) {
            const char* ob0 = (wsp(p) + O_OB) + layer * ACT + (size_t)T * 20480 + 4096;
            const char* ob1 = ob0 + 8192;
            const char* ob2 = ob0 + 12288;
            const char* xb = (wsp(p) + O_X1) + layer * ACT + (size_t)(T >> 1) * 40960 + cb * 4096 + (T & 1) * 256;
            u32 vo = ln * 16, vr = (ln >> 4) * 256 + (ln & 15) * 8, vx = (ln >> 4) * 1024 + (ln & 15) * 16;
            asm volatile("" : "+v"(vo), "+v"(vr), "+v"(vx), "+s"(ob0), "+s"(ob1), "+s"(ob2), "+s"(xb) :: "memory");
#pragma unroll
            for (int k = 0; k < 16; ++k) { O[k] = ld16(k < 8 ? ob0 : ob1, vo, ((k & 7) - 4) * 1024); SCHEDB(); }
#pragma unroll
            for (int k = 0; k < 4; ++k) {
                const u64 lo = __hip_atomic_load((const gu64*)(ob2 + vr + k * 1024), __ATOMIC_RELAXED, __HIP_MEMORY_SCOPE_AGENT);
                const u64 hi = __hip_atomic_load((const gu64*)(ob2 + vr + k * 1024 + 128), __ATOMIC_RELAXED, __HIP_MEMORY_SCOPE_AGENT);
                O[16 + k] = (u32x4){(u32)lo, (u32)(lo >> 32), (u32)hi, (u32)(hi >> 32)};
            }
            R[0] = ld16(xb, vx, 0); R[1] = ld16(xb, vx, 512);
            SCHEDB();
        };
        auto gemm = [&]() {
            u32 wa0 = lds0 + C_XN + ln * 16, wa1 = wa0 + 40960;
            asm volatile("" : "+v"(wa0), "+v"(wa1));
            f32x4 acc[4];
            bf8 wf[8];
#pragma unroll
            for (int t = 0; t < 8; ++t) wf[t] = *(LDS3 const bf8*)(uintptr_t)(wa0 + t * 1024);
#pragma unroll
            for (int k = 0; k < 20; ++k) {
                if (k >= 16) { PIN(O[k]); NOPV(); }
#pragma unroll
                for (int t = 0; t < 4; ++t) {
                    if (k == 0) MMA0_VV(acc[t], wf[t], O[k]); else MMA_VV(acc[t], wf[(k & 1) * 4 + t], O[k]);
                    SCHEDB();
                    if (k < 18) wf[(k & 1) * 4 + t] = *(LDS3 const bf8*)(uintptr_t)((k < 8 ? wa0 : wa1) + (((k + 2) % 10) * 4 + t) * 1024);
                    SCHEDB();
                }
            }
            asm volatile("s_nop 7" : "+v"(acc[0]), "+v"(acc[1]), "+v"(acc[2]), "+v"(acc[3]));
            float chk = 0.f;
#pragma unroll
            for (int i = 0; i < 8; ++i) {
                const u32 r = R[i >> 2][i & 3], y = pk2bf(acc[i >> 1][(i & 1) * 2], acc[i >> 1][(i & 1) * 2 + 1]);
                const float a = bflo(r) + bflo(y), b = bfhi(r) + bfhi(y);
                dw[i] = pk2bf(a, b);
                chk += a;
            }
            return __any(chk != chk);
        };
        const int T0 = rb + 16 * w;
        if (OB_POLL) ready(T0);
        load(T0);
        static_for<0, 2>([&](auto PASS) {
            constexpr int pass = CI(PASS);
            const int T = T0 + 64 * pass;
            TT(set * 128 + 32 + c, 1 + pass * 4);
            if (__builtin_expect(gemm(), 0)) {
                for (int tries = 0;; ++tries) {
                    ready(T); load(T);
                    u32 m = max4(max4(0, R[0]), R[1]);
#pragma unroll
                    for (int k = 0; k < 20; ++k) m = max4(m, O[k]);
                    if (!__any(m == ~0u)) break;
                    if (tries >= cap) { cap = 1; break; }
                    __builtin_amdgcn_s_sleep(6);
                }
                gemm();
            }
            TT(set * 128 + 32 + c, 2 + pass * 4);
            if (layer == 1) {
                st32((char*)p.out + (size_t)T * 16 * DM * 2 + cb * 128, (ln & 15) * (DM * 2) + (ln >> 4) * 32, (u32x4){dw[0], dw[1], dw[2], dw[3]}, (u32x4){dw[4], dw[5], dw[6], dw[7]});
            } else {
            float s1 = 0.f, s2 = 0.f;
#pragma unroll
            for (int i = 0; i < 8; ++i) { const float a = bflo(dw[i]), b = bfhi(dw[i]); s1 += a + b; s2 = fmaf(a, a, fmaf(b, b, s2)); }
            s1 = grp_sum(s1); s2 = grp_sum(s2);
            char* stl = (wsp(p) + O_ST) + (size_t)T * 1280;
            st8m(stl + cb * 128, ln * 8, (u32x2){nosent(__float_as_uint(s1)), nosent(__float_as_uint(s2))}, 0xffffull);
            const size_t fo = ACT + (size_t)(T >> 1) * 40960 + (cb * 4 + (T & 1)) * 1024;
            const u32 lo = (ln >> 5) * 2048 + ((ln >> 4) & 1) * 512 + (ln & 15) * 16;
            __builtin_amdgcn_s_sleep(ST_LAG);
            for (int spin = 0;; ++spin) {
                u32 m = 0;
                s1 = 0.f; s2 = 0.f;
#pragma unroll
                for (int i = 0; i < 3; ++i) {
                    const int k = (ln >> 4) + 4 * i;
                    const u64 v = __hip_atomic_load((const gu64*)(stl + min(k, 9) * 128 + (ln & 15) * 8), __ATOMIC_RELAXED, __HIP_MEMORY_SCOPE_AGENT);
                    m = max(m, max((u32)v, (u32)(v >> 32)));
                    if (k < 10) { s1 += __uint_as_float((u32)v); s2 += __uint_as_float((u32)(v >> 32)); }
                }
                if (!__any(m == ~0u)) break;
                if (spin >= cap) { cap = 1; break; }
                __builtin_amdgcn_s_sleep(2);
            }
            TT(set * 128 + 32 + c, 3 + pass * 4);
            s1 = grp_sum(s1); s2 = grp_sum(s2);
            const float mu = s1 * (1.0f / DM), rs = __builtin_amdgcn_rsqf(fmaxf(fmaf(-mu, mu, s2 * (1.0f / DM)), 0.f) + 1e-5f);
            u32 dn[8];
#pragma unroll
            for (int i = 0; i < 8; ++i) dn[i] = pk2bf((bflo(dw[i]) - mu) * rs, (bfhi(dw[i]) - mu) * rs);
            st16s((wsp(p) + O_XN) + fo, lo, (u32x4){dn[0], dn[4], dn[1], dn[5]}); st16s((wsp(p) + O_XN) + fo + 256, lo, (u32x4){dn[2], dn[6], dn[3], dn[7]});
            st16s((wsp(p) + O_XR) + fo, lo, (u32x4){dw[0], dw[4], dw[1], dw[5]}); st16s((wsp(p) + O_XR) + fo + 256, lo, (u32x4){dw[2], dw[6], dw[3], dw[7]});
            TT(set * 128 + 32 + c, 4 + pass * 4);
            if (pass == 0 && w == 0) { VM0(); st4_lane0(wsp(p) + O_FL + (set * 80 + c) * 128, 1u); }
            }
            if constexpr (pass == 0) load(T + 64);
        });
    }
    if (layer == 1) break;
    mine += HOP_BYTES; other += HOP_BYTES; px += PX_BYTES; xn += ACT;
    if (w == 0) wait_first_tokens(p, set, lane, cap);
    RAW_BARRIER();
    TT(set * 128 + 32 + c, 9);
    dma_kb<10>(xn + (size_t)set * 40960 + w * 10240, lds0 + C_XN + w * 10240, lane * 16);
    }
}

constexpr int B_PQ = 0, B_IM = 131072, B_CS = 155648, B_IS = 176;
__device__ __forceinline__ void bw_role(const LaParams& p, const int hb, char* shm) {
    const int lane = threadIdx.x & 63, w = __builtin_amdgcn_readfirstlane(threadIdx.x >> 6);
    const int fr = lane & 15, g = lane >> 4;
    int cap = SPIN_CAP;
    if (hb == 0 && w == 0) {
        u32 best[2] = {~0u, ~0u};
        for (int i = 0; i < 64; ++i) {
#pragma unroll
            for (int sd = 0; sd < 2; ++sd) {
                u64 t0, t1;
                u32 z = 0, one = 1;
                asm volatile("s_memtime %0\n\ts_mov_b64 exec, 1\n\ts_waitcnt lgkmcnt(0)\n\tglobal_atomic_add %2, %3, %2, %4 sc0 sc1\n\ts_waitcnt vmcnt(0)\n\t"
                             "s_memtime %1\n\ts_mov_b64 exec, -1\n\ts_waitcnt lgkmcnt(0)"
                             : "=&s"(t0), "=&s"(t1), "+v"(one) : "v"(z), "s"(p.hop + M0_OFF + sd * 8192 + 6144) : "memory");
                best[sd] = min(best[sd], (u32)(t1 - t0));
            }
            if (i >= 2 && max(best[0], best[1]) < 1000u && max(best[0], best[1]) - min(best[0], best[1]) > 120u) break;
        }
        u32 xcc; asm volatile("s_getreg_b32 %0, hwreg(HW_REG_XCC_ID)" : "=s"(xcc));
        st4_lane0(wsp(p) + O_FL + 384 * 128, ((xcc >> 2) & 1) ^ (best[1] < best[0] ? 1u : 0u));
    }
    bf8 WA[64], WV[16];
    {
        const int t = threadIdx.x;
        float* xs = (float*)(shm + 81920);
        float* ph = xs + 128 * 61;
        for (int i = t; i < 400; i += 256) ph[i] = bf2f(p.wphi[hb * 400 + i]);
        const int j = 4 * w + (fr & 3), G = fr >> 2;
        for (int d0 = 0; d0 < DM; d0 += 128) {
            u32 v15[15];
#pragma unroll
            for (int k = 0; k < 15; ++k) { const int i = t + k * 256; v15[k] = *(const u32*)(p.wqkv + (size_t)(d0 + i / 30) * 1920 + ((i % 30) / 10) * 640 + hb * 20 + (i % 10) * 2); }
            __syncthreads();
#pragma unroll
            for (int k = 0; k < 15; ++k) { const int i = t + k * 256; float* o = xs + (i / 30) * 61 + (i % 30) * 2; o[0] = bflo(v15[k]); o[1] = bfhi(v15[k]); }
            __syncthreads();
            for (int kk = 0; kk < 4; ++kk) {
                float v[8];
                const float* xr = xs + (kk * 32 + g * 8) * 61;
                if (j < 10) {
                    const int m = j / 5, f = 5 * G + j % 5;
#pragma unroll
                    for (int e = 0; e < 8; ++e) v[e] = 0.f;
#pragma unroll 4
                    for (int i = 0; i < 20; ++i) {
                        const float pw = ph[i * 20 + f];
#pragma unroll
                        for (int e = 0; e < 8; ++e) v[e] = fmaf(xr[e * 61 + m * 20 + i], pw, v[e]);
                    }
                } else {
#pragma unroll
                    for (int e = 0; e < 8; ++e) v[e] = j < 15 ? xr[e * 61 + 40 + 5 * G + min(j, 14) - 10] : 1.0f;
                }
                *(u32x4*)(shm + ((d0 / 32 + kk) * 4 + w) * 1024 + lane * 16) = (u32x4){pk2bf(v[0], v[1]), pk2bf(v[2], v[3]), pk2bf(v[4], v[5]), pk2bf(v[6], v[7])};
            }
        }
        __syncthreads();
#pragma unroll
        for (int i = 0; i < 64; ++i) WA[i] = *(const bf8*)(shm + i * 1024 + lane * 16);
#pragma unroll
        for (int i = 0; i < 16; ++i) WV[i] = *(const bf8*)(shm + (64 + i) * 1024 + lane * 16);
        __syncthreads();
    }
    auto proj = [&](f32x4 (&acc)[4], f32x4& sq, auto&& fetch) {
        u32x4 X[20];
#pragma unroll
        for (int k = 0; k < 12; ++k) { X[k] = fetch(k); SCHEDB(); }
#pragma unroll
        for (int k = 0; k < 20; ++k) {
#pragma unroll
            for (int t = 0; t < 4; ++t) {
                if (k == 0) MMA0_AV(acc[t], WA[t], X[k]); else if (k < 16) MMA_AV(acc[t], WA[(k * 4 + t) & 63], X[k]); else MMA_VV(acc[t], WV[(k * 4 + t) & 15], X[k]);
            }
            if (k == 0) MMA0_VV(sq, X[k], X[k]); else MMA_VV(sq, X[k], X[k]);
            SCHEDB();
            if (k < 8) X[k + 12] = fetch(k + 12);
            SCHEDB();
        }
        asm volatile("s_nop 7" : "+v"(acc[0]), "+v"(acc[1]), "+v"(acc[2]), "+v"(acc[3]), "+v"(sq));
    };
    {
        f32x4 t, cs[4];
        u32x4 o1 = {0x3f803f80u, 0x3f803f80u, 0x3f803f80u, 0x3f803f80u};
        asm volatile("s_nop 1" : "+v"(o1));
        proj(cs, t, [&](int) { return o1; });
#pragma unroll
        for (int i = 0; i < 4; ++i) *(f32x4*)(shm + B_CS + i * 1024 + lane * 16) = cs[i];
    }
    char* im = shm + B_IM + w * 6144;
    const u32 imr = (u32)(uintptr_t)(LDS3 char*)im + (4 * g + (fr >> 2)) * B_IS + (fr & 3) * 8;
    typedef __attribute__((ext_vector_type(32))) u32 u32x32;
    for (int layer = 0; layer < 2; ++layer) {
        const char* sch = p.hop + layer * HOP_BYTES + (hb & 1) * 8192 + (S_SLOT + 4) * SLOT;
        f32x4 kv[3][2];
#pragma unroll
        for (int i = 0; i < 6; ++i) ZERO4(kv[i >> 1][i & 1]);
        u32x32 q5;
        u32 tn = 0;
        for (int it = 0; it < 32; ++it) {
            const int s = 2 * it + (w >> 1);
            if (s + 2 < NSEG) { wait_rt(tn); tn = sched_get(~0u, sch + (s + 1) * 128 + 4, cap); wait_rt(tn + B_LAG); tn += 250; }
            else wait_rt(sched_get(~0u, sch + s * 128, cap) + T_X1);
            const char* xb = (wsp(p) + O_X1) + layer * ACT + (size_t)s * 40960 + (w & 1) * 256;
            if (it >= 30) TT((hb >> 4) * 128 + 112 + (hb & 15), (it - 30) * 3);
            f32x4 acc[4], sq;
            const char* xk[5];
#pragma unroll
            for (int i = 0; i < 5; ++i) { xk[i] = xb + i * 8192 + 4096; asm volatile("" : "+s"(xk[i])); }
            for (int pass = 0;; ++pass) {
                u32 vo = g * 512 + fr * 16;
                asm volatile("" : "+v"(vo) :: "memory");
                auto fetch = [&](const int k) { return ld16(xk[k >> 2], vo, ((k & 3) - 2) * 2048); };
                proj(acc, sq, fetch);
                if (pass > 0 || !__any(acc[3][3] != acc[3][3])) break;
                for (int tries = 0;; ++tries) {
                    asm volatile("" : "+v"(vo) :: "memory");
                    u32 m = 0;
#pragma unroll
                    for (int k = 0; k < 20; k += 10) {
#pragma unroll
                        for (int i = 0; i < 10; ++i) m = max4(m, fetch(k + i));
                        SCHEDB();
                    }
                    if (!__any(m == ~0u)) break;
                    if (tries >= cap) { cap = 1; break; }
                    __builtin_amdgcn_s_sleep(6);
                }
            }
            if (it >= 30) TT((hb >> 4) * 128 + 112 + (hb & 15), (it - 30) * 3 + 1);
            const float dsel = (fr & 2) ? ((fr & 1) ? sq[3] : sq[2]) : ((fr & 1) ? sq[1] : sq[0]);
            const float s2 = grp_sum(g == (fr >> 2) ? dsel : 0.f);
            const float mu = acc[3][3] * (1.0f / DM), rs = __builtin_amdgcn_rsqf(fmaxf(fmaf(-mu, mu, s2 * (1.0f / DM)), 0.f) + 1e-5f);
            float v[15];
            f32x4 cs[4];
#pragma unroll
            for (int i = 0; i < 4; ++i) cs[i] = *(const f32x4*)(shm + B_CS + i * 1024 + lane * 16);
#pragma unroll
            for (int j = 0; j < 15; ++j) v[j] = fmaf(-mu, cs[j >> 2][j & 3], acc[j >> 2][j & 3]) * rs;
            u32 ph[2][5];
#pragma unroll
            for (int m = 0; m < 2; ++m) {
                const float* x = v + 5 * m;
                const float mx = grp_max(fmaxf(fmaxf(fmaxf(x[0], x[1]), fmaxf(x[2], x[3])), x[4]));
                const float mn = -grp_max(-fminf(fminf(fminf(x[0], x[1]), fminf(x[2], x[3])), x[4]));
                float e[10], a = 0.f, b = 0.f;
#pragma unroll
                for (int j = 0; j < 5; ++j) {
                    e[j] = __builtin_amdgcn_exp2f((x[j] - mx) * 1.4426950408889634f); a += e[j];
                    e[5 + j] = __builtin_amdgcn_exp2f((mn - x[j]) * 1.4426950408889634f); b += e[5 + j];
                }
                a = __builtin_amdgcn_rcpf(grp_sum(a)); b = __builtin_amdgcn_rcpf(grp_sum(b));
#pragma unroll
                for (int j = 0; j < 5; ++j) { e[j] *= a; e[5 + j] *= b; }
#pragma unroll
                for (int j = 0; j < 5; ++j) ph[m][j] = pk2bf(e[2 * j], e[2 * j + 1]);
            }
            *(u32x4*)(shm + B_PQ + (w * 32 + it) * 1024 + lane * 16) = (u32x4){ph[0][0], ph[0][1], ph[0][2], ph[0][3]};
            q5[it] = ph[0][4];
            char* row = im + fr * B_IS;
            *(u32x2*)(row + g * 8) = (u32x2){ph[1][0], ph[1][1]};
            *(u32x2*)(row + 32 + g * 8) = (u32x2){ph[1][2], ph[1][3]};
            *(u32x2*)(row + 64 + g * 8) = (u32x2){ph[1][4], 0u};
            *(u32x2*)(row + 96 + g * 8) = (u32x2){pk2bf(v[10], v[11]), pk2bf(v[12], v[13])};
            *(u32x2*)(row + 128 + g * 8) = (u32x2){pk2bf(v[14], 1.0f), 0u};
            s16x4 tf[5];
#pragma unroll
            for (int i = 0; i < 5; ++i) tf[i] = __builtin_amdgcn_ds_read_tr16_b64_v4i16((LDS3 s16x4*)(uintptr_t)(imr + i * 32));
#pragma unroll
            for (int i = 0; i < 6; ++i) MMA16(kv[i >> 1][i & 1], tf[i >> 1], tf[3 + (i & 1)]);
            if (it >= 30) TT((hb >> 4) * 128 + 112 + (hb & 15), (it - 30) * 3 + 2);
        }
        asm volatile("s_nop 7" : "+v"(kv[0][0]), "+v"(kv[0][1]), "+v"(kv[1][0]), "+v"(kv[1][1]), "+v"(kv[2][0]), "+v"(kv[2][1]));
#pragma unroll
        for (int i = 0; i < 6; ++i) *(f32x4*)(im + i * 1024 + lane * 16) = kv[i >> 1][i & 1];
        RAW_BARRIER();
        bf8 ka[2];
        u32x2 kb[2];
#pragma unroll
        for (int dt = 0; dt < 2; ++dt) {
            f32x4 t[3];
#pragma unroll
            for (int ft = 0; ft < 3; ++ft) {
                const char* a = shm + B_IM + (ft * 2 + dt) * 1024 + lane * 16;
                t[ft] = (*(const f32x4*)a + *(const f32x4*)(a + 6144)) + (*(const f32x4*)(a + 12288) + *(const f32x4*)(a + 18432));
            }
            ka[dt] = pack8(t[0], t[1]);
            kb[dt] = (u32x2){pk2bf(t[2][0], t[2][1]), pk2bf(t[2][2], t[2][3])};
            PIN(ka[dt]); PIN(kb[dt]);
        }
        char* ob = (wsp(p) + O_OB) + layer * ACT + (size_t)w * 20480 + hb * 512;
        TT((hb >> 4) * 128 + 112 + (hb & 15), 6);
        const u32 rofs = g * 81920 + 16384 - hb * 384 + fr * 8, mofs = (g & 1) * 81920 + (g >> 1) * 256 + fr * 16;
        f32x4 o[2][4][2];
        bf8 qa[2][4];
        u32x2 qb[4];
        auto ldq = [&](const int gr) {
#pragma unroll
            for (int u = 0; u < 4; ++u) qa[gr & 1][u] = *(const bf8*)(shm + B_PQ + (w * 32 + gr * 4 + u) * 1024 + lane * 16);
        };
        auto mm = [&](const int gr, const int k) {
            SCHEDB();
            if (k < 4) { qb[k] = (u32x2){q5[gr * 4 + k], 0u}; PIN(qb[k]); }
            if (k < 8) MMA0_VV(o[gr & 1][k >> 1][k & 1], ka[k & 1], qa[gr & 1][k >> 1]);
            else MMA16(o[gr & 1][(k - 8) >> 1][k & 1], kb[k & 1], qb[(k - 8) >> 1]);
            SCHEDB();
        };
        ldq(0); ldq(1);
        NOPV();
#pragma unroll
        for (int k = 0; k < 16; ++k) mm(0, k);
        static_for<0, 8>([&](auto IT) {
            constexpr int gr = CI(IT);
            auto nx = [&](const int k) { if constexpr (gr < 7) mm(gr + 1, k); else { SCHEDB(); asm volatile("s_nop 0"); SCHEDB(); } };
            if constexpr (gr == 7) asm volatile("s_nop 7");
            u32 d[4][2];
            float x[4];
            nx(0); nx(1);
#pragma unroll
            for (int u = 0; u < 4; ++u) {
                f32x4& a = o[gr & 1][u][0];
                f32x4& b = o[gr & 1][u][1];
                PIN(a); PIN(b);
                float inv = __builtin_amdgcn_rcpf(b[1] + 1e-6f), y0, y1, y2, y3;
                nx(2 + 3 * u);
                asm("v_mul_f32 %0, %4, %8\n\tv_mul_f32 %1, %5, %8\n\tv_mul_f32 %2, %6, %8" : "=&v"(y0), "=&v"(y1), "=&v"(y2), "=&v"(y3) : "v"(a[0]), "v"(a[1]), "v"(a[2]), "v"(a[3]), "v"(inv));
                nx(3 + 3 * u);
                y3 = a[3] * inv; x[u] = b[0] * inv;
                d[u][0] = pk2bf(y0, y1);
                nx(4 + 3 * u);
                d[u][1] = pk2bf(y2, y3);
                if (u & 1) {
                    auto t0 = __builtin_amdgcn_permlane16_swap(d[u - 1][0], d[u][0], false, false);
                    auto t1 = __builtin_amdgcn_permlane16_swap(d[u - 1][1], d[u][1], false, false);
                    st16s(ob + (u - 1) * 81920, mofs, (u32x4){nosent(t0[0]), nosent(t1[0]), nosent(t0[1]), nosent(t1[1])});
                }
            }
            if constexpr (gr < 6) ldq(gr + 2);
            nx(14);
            auto a0 = __builtin_amdgcn_permlane32_swap(__float_as_uint(x[0]), __float_as_uint(x[2]), false, false);
            auto a1 = __builtin_amdgcn_permlane32_swap(__float_as_uint(x[1]), __float_as_uint(x[3]), false, false);
            nx(15);
            auto b0 = __builtin_amdgcn_permlane16_swap(a0[0], a1[0], false, false);
            auto b2 = __builtin_amdgcn_permlane16_swap(a0[1], a1[1], false, false);
            st8m(ob, rofs, (u32x2){nosent(pk2bf(__uint_as_float(b0[0]), __uint_as_float(b0[1]))), nosent(pk2bf(__uint_as_float(b2[0]), __uint_as_float(b2[1])))}, ~0ull);
            ob += 4 * 81920;
            asm volatile("" : "+s"(ob));
        });
        RAW_BARRIER();
        TT((hb >> 4) * 128 + 112 + (hb & 15), 7);
    }
}

__global__ void __launch_bounds__(256, 1) la_kernel(LaParams p) {
    extern __shared__ __attribute__((aligned(16))) char shm[];
    p.hop = align2m(p.ws + O_HOP);
    if (threadIdx.x == 0) {
        u32 xcc; asm volatile("s_getreg_b32 %0, hwreg(HW_REG_XCC_ID)" : "=s"(xcc));
        int role = 255;
        for (int a = 0; a < 8; ++a) {
            const u32 v = (xcc + a) & 7;
            const u32 k = __hip_atomic_fetch_add((gu32*)(((u32*)(p.ws + O_CTL)) + v * 32), 1u, __ATOMIC_RELAXED, __HIP_MEMORY_SCOPE_AGENT);
            if (k < 32) { role = v * 32 + k; break; }
        }
        *(int*)shm = role;
    }
    __syncthreads();
    const int role = __builtin_amdgcn_readfirstlane(*(int*)shm);
    __syncthreads();
    const int set = role >> 7, id = (role >> 5) & 3, k = role & 31;
    if (k < 8) qk_role(p, set, id * 8 + k, shm);
    else if (k < 28) comb_role(p, set, id * 20 + k - 8, shm);
    else bw_role(p, (role >> 5) * 4 + k - 28, shm);
}
__device__ __forceinline__ float row16_sum(float x) { x += fdpp<0x128>(x); x += fdpp<0x124>(x); x += fdpp<0x122>(x); return x + fdpp<0x121>(x); }

#define PFB 0
struct PrepParams { const u16* W[4]; const u16* x; const u16* mem0; const u16* wo; char* ws; };
constexpr int N_PF = PFB, N_LN = 128, N_WO = 200, N_FILL = 512;
__global__ void __launch_bounds__(256) prep_kernel(PrepParams q) {
    int b = blockIdx.x;
    const int t = threadIdx.x, lane = t & 63, w = t >> 6, fr = lane & 15, g = lane >> 4;
    char* hop = align2m(q.ws + O_HOP);
    if (b < N_LN) {
        const int tok = b * 16 + w * 4 + g;
        u32x4 d[3][2];
        float s = 0.f;
#pragma unroll
        for (int i = 0; i < 3; ++i) {
            const u32x4* a = (const u32x4*)(q.x + (size_t)tok * DM + min(fr + 16 * i, 39) * 16);
            d[i][0] = a[0]; d[i][1] = a[1];
        }
#pragma unroll
        for (int i = 0; i < 3; ++i)
#pragma unroll
            for (int e = 0; e < 8; ++e) if (fr + 16 * i < 40) s += bflo(d[i][e >> 2][e & 3]) + bfhi(d[i][e >> 2][e & 3]);
        const float mu = row16_sum(s) * (1.0f / DM);
        float v = 0.f;
#pragma unroll
        for (int i = 0; i < 3; ++i)
#pragma unroll
            for (int e = 0; e < 8; ++e) if (fr + 16 * i < 40) { const float a = bflo(d[i][e >> 2][e & 3]) - mu, c = bfhi(d[i][e >> 2][e & 3]) - mu; v = fmaf(a, a, fmaf(c, c, v)); }
        const float rs = __builtin_amdgcn_rsqf(row16_sum(v) * (1.0f / DM) + 1e-5f);
#pragma unroll
        for (int i = 0; i < 3; ++i) {
            const int blk = fr + 16 * i;
            if (blk >= 40) continue;
            const size_t o = (size_t)(tok >> 5) * 40960 + ((blk >> 1) * 2 + ((tok >> 4) & 1)) * 1024 + ((blk & 1) * 32 + (tok & 15)) * 16;
            u32 n[8];
#pragma unroll
            for (int e = 0; e < 8; ++e) n[e] = pk2bf((bflo(d[i][e >> 2][e & 3]) - mu) * rs, (bfhi(d[i][e >> 2][e & 3]) - mu) * rs);
            *(u32x4*)(q.ws + O_XR + o) = (u32x4){d[i][0][0], d[i][1][0], d[i][0][1], d[i][1][1]};
            *(u32x4*)(q.ws + O_XR + o + 256) = (u32x4){d[i][0][2], d[i][1][2], d[i][0][3], d[i][1][3]};
            *(u32x4*)(q.ws + O_XN + o) = (u32x4){n[0], n[4], n[1], n[5]};
            *(u32x4*)(q.ws + O_XN + o + 256) = (u32x4){n[2], n[6], n[3], n[7]};
        }
        return;
    }
    b -= N_LN;
    if (b < N_PF) {
        const u32x4* src = (const u32x4*)q.W[b & 3] + (size_t)(b >> 2) * 3840 + t;
        u32x4 v[15];
#pragma unroll
        for (int i = 0; i < 15; ++i) v[i] = src[i * 256];
#pragma unroll
        for (int i = 0; i < 15; ++i) asm volatile("" :: "v"(v[i]));
        return;
    }
    b -= N_PF;
    if (b < N_WO) {
        const int cb = b / 20, kk = b % 20;
        const u16* col = q.wo + cb * 64 + 16 * (fr >> 2) + 4 * w + (fr & 3);
        u32 v[8];
#pragma unroll
        for (int e = 0; e < 8; ++e) {
            const int row = kk < 16 ? (2 * kk + (g >> 1)) * 20 + 5 * (2 * (g & 1) + (e >> 2)) + (e & 3) : (8 * (kk - 16) + 2 * g + (e >> 2)) * 20 + 5 * (e & 3) + 4;
            v[e] = col[(size_t)row * DM];
        }
        *(u32x4*)(q.ws + O_WO + (size_t)cb * 81920 + (kk * 4 + w) * 1024 + lane * 16) = (u32x4){v[0] | v[1] << 16, v[2] | v[3] << 16, v[4] | v[5] << 16, v[6] | v[7] << 16};
        return;
    }
    b -= N_WO;
    const u32x4 SENT = {~0u, ~0u, ~0u, ~0u};
    const u32 i0 = b * 256 + t;
    if (b == 0) ((u32*)(q.ws + O_CTL))[t] = 0;
    if (i0 < 8 * 2 * 320) {
        const int sl = i0 / 640, e = i0 % 320;
        const u32x4 v = *(const u32x4*)(q.mem0 + (e & 7) * DM + ((sl & 1) * 40 + (e >> 3)) * 8);
        st16(hop + M0_OFF + (i0 / 320) * 8192 + e * 16, (u32x4){nosent(v[0]), nosent(v[1]), nosent(v[2]), nosent(v[3])});
    }
    for (u32 i = i0; i < 2 * HOP_BYTES / 16; i += N_FILL * 256) st16(hop + (size_t)i * 16, SENT);
    for (u32 i = i0; i < (O_XN - O_ST) / 16; i += N_FILL * 256) st16(q.ws + O_ST + (size_t)i * 16, SENT);
}

extern "C" void kernel_launch(void* const* d_in, const int* in_sizes, int n_in,
                              void* d_out, int out_size, void* d_ws, size_t ws_size,
                              hipStream_t stream) {
    PrepParams q;
    q.x = (const u16*)d_in[0]; q.mem0 = (const u16*)d_in[1];
    q.wo = (const u16*)d_in[10];
    for (int i = 0; i < 4; ++i) q.W[i] = (const u16*)d_in[2 + i];
    q.ws = (char*)d_ws;
    prep_kernel<<<N_PF + N_LN + N_WO + N_FILL, 256, 0, stream>>>(q);
    LaParams p;
    p.ws = (char*)d_ws; p.out = (u16*)d_out; p.hop = (char*)d_ws;
    p.Wq = (const u16*)d_in[2]; p.Wk = (const u16*)d_in[3]; p.Wv = (const u16*)d_in[4]; p.Wo = (const u16*)d_in[5];
    p.wqkv = (const u16*)d_in[8]; p.wphi = (const u16*)d_in[9];
    (void)hipFuncSetAttribute((const void*)la_kernel, hipFuncAttributeMaxDynamicSharedMemorySize, LA_LDS);
    la_kernel<<<256, 256, LA_LDS, stream>>>(p);
}
```

```cpp
#include <hip/hip_runtime.h>
#include <hip/hip_bf16.h>
#include <stdint.h>
#include <type_traits>

typedef unsigned short u16;
typedef unsigned int u32;
typedef unsigned long long u64;
using bf16x8 = __attribute__((ext_vector_type(8))) short;
using f32x4 = __attribute__((ext_vector_type(4))) float;

constexpr int S_TOK = 2048, DM = 640, NH = 32, DH = 192;
constexpr int MEM = 8, SEG = 32, NSEG = S_TOK / SEG;
constexpr int HD = NH * DH;

__device__ __forceinline__ float bf2f(u16 v) { return __uint_as_float(((u32)v) << 16); }
__device__ __forceinline__ u32 pk2bf(float lo, float hi) {
    typedef __attribute__((ext_vector_type(2))) float f32x2_t;
    typedef __attribute__((ext_vector_type(2))) __bf16 bf16x2_t;
    bf16x2_t r = __builtin_convertvector((f32x2_t){lo, hi}, bf16x2_t);
    return __builtin_bit_cast(u32, r);
}
__device__ __forceinline__ float rbf(float f) { return __uint_as_float(pk2bf(f, f) << 16); }
typedef __attribute__((ext_vector_type(8))) __bf16 bf8;
typedef __attribute__((ext_vector_type(16))) __bf16 bf16v;
typedef __attribute__((ext_vector_type(4))) __bf16 bf4;
typedef __attribute__((ext_vector_type(4))) short s16x4;
using u32x4 = __attribute__((ext_vector_type(4))) u32;
using u32x2 = __attribute__((ext_vector_type(2))) u32;
typedef __attribute__((address_space(1))) u32 gu32;
typedef __attribute__((address_space(1))) u64 gu64;
#define LDS3 __attribute__((address_space(3)))

constexpr int SLOT = 16384;
constexpr int P_SLOT0 = 0, M_SLOT0 = 16, S_SLOT = 48, T_SLOT = 53, N_SLOT = 55;
constexpr size_t HOP_BYTES = (size_t)N_SLOT * SLOT, M0_OFF = 2 * HOP_BYTES;
constexpr size_t PX_BUF = 4 * 32 * 640, PX_BYTES = 8 * PX_BUF;
constexpr int LA_LDS = 155648 + 4096 + 256;
constexpr int SPIN_CAP = 1 << 16;
#define D1I 135
#define D2I 68
#define MG1 10
#define MG2 12
#define ROTC 0
#define DUP 4
#define PACE 6
#define CLAG 50
#define T_PX 45
#define T_X1 222
#define T_OB 525
#define T_TOK 760
#define ST_LAG 6
#define OB_STEP 28
#define OB_POLL 0
#define OB_COLD 20
#define B_LAG 30
#define ELATE 30

__device__ __forceinline__ void st16(void* p, u32x4 v) {
    asm volatile("global_store_dwordx4 %0, %1, off sc1\n\ts_nop 2" :: "v"(p), "v"(v) : "memory");
}
typedef __attribute__((address_space(1))) u32x4 gu32x4;
__device__ __forceinline__ u32x4 ld16(const char* base, u32 off, int imm) { return __builtin_amdgcn_global_load_b128((gu32x4*)(base + off + imm), "agent"); }
__device__ __forceinline__ u32 nosent(u32 v) { return min(v, 0xFFFFFFFEu); }
__device__ __forceinline__ u32 rt32() { return (u32)__builtin_amdgcn_s_memrealtime(); }
__device__ __forceinline__ void wait_rt(u32 t) {
    int d = (int)(t - rt32());
    if (d > 4096) return;
    while (d > 10) { if (d > 20) __builtin_amdgcn_s_sleep(2); d = (int)(t - rt32()); }
    asm volatile("s_cmp_lt_i32 %0, 1\n\ts_cbranch_scc1 2f\n1:\n\ts_sub_i32 %0, %0, 1\n\ts_cmp_gt_i32 %0, 0\n\ts_cbranch_scc1 1b\n2:" : "+s"(d) :: "scc");
}
template <int N> __device__ __forceinline__ void dma_kb(const char* src, u32 lds_dst, u32 lane16) {
#pragma unroll
    for (int i = 0; i < N; i += 4) {
        u32 keep;
        if (N - i >= 4)
            asm volatile("s_mov_b32 %0, m0\n\ts_mov_b32 m0, %3\n\ts_nop 2\n\tglobal_load_lds_dwordx4 %1, %2 sc1\n\tglobal_load_lds_dwordx4 %1, %2 offset:1024 sc1\n\t"
                         "global_load_lds_dwordx4 %1, %2 offset:2048 sc1\n\tglobal_load_lds_dwordx4 %1, %2 offset:3072 sc1\n\ts_mov_b32 m0, %0"
                         : "=&s"(keep) : "v"(lane16), "s"(src + i * 1024), "s"(lds_dst + i * 1024) : "memory");
        else if (N - i == 2)
            asm volatile("s_mov_b32 %0, m0\n\ts_mov_b32 m0, %3\n\ts_nop 2\n\tglobal_load_lds_dwordx4 %1, %2 sc1\n\tglobal_load_lds_dwordx4 %1, %2 offset:1024 sc1\n\ts_mov_b32 m0, %0"
                         : "=&s"(keep) : "v"(lane16), "s"(src + i * 1024), "s"(lds_dst + i * 1024) : "memory");
        else
            asm volatile("s_mov_b32 %0, m0\n\ts_mov_b32 m0, %3\n\ts_nop 2\n\tglobal_load_lds_dwordx4 %1, %2 sc1\n\ts_mov_b32 m0, %0"
                         : "=&s"(keep) : "v"(lane16), "s"(src + i * 1024), "s"(lds_dst + i * 1024) : "memory");
    }
}
__device__ __forceinline__ void dma_gather(const char* src, u32 lds_dst, u32 off) {
    u32 keep;
    asm volatile("s_mov_b32 %0, m0\n\ts_mov_b32 m0, %3\n\ts_nop 2\n\tglobal_load_lds_dwordx4 %1, %2\n\ts_mov_b32 m0, %0"
                 : "=&s"(keep) : "v"(off), "s"(src), "s"(lds_dst) : "memory");
}
__device__ __forceinline__ void st16s(char* base, u32 off, u32x4 v) {
    asm volatile("s_nop 4\n\tglobal_store_dwordx4 %0, %1, %2 sc1\n\ts_nop 2" :: "v"(off), "v"(v), "s"(base) : "memory");
}
__device__ __forceinline__ void arm16(char* base, u32 off) {
    u32x4 v;
    asm volatile("v_mov_b64 %0, -1\n\tv_mov_b64 %1, -1" : "=v"(((u64*)&v)[0]), "=v"(((u64*)&v)[1]));
    st16s(base, off, v);
}
template <int B, int E, typename F> __device__ __forceinline__ void static_for(F&& f) {
    if constexpr (B < E) { f(std::integral_constant<int, B>()); static_for<B + 1, E>(f); }
}
#define CI(x) decltype(x)::value
#define RAW_BARRIER() do { asm volatile("s_waitcnt lgkmcnt(0)" ::: "memory"); __builtin_amdgcn_s_barrier(); asm volatile("" ::: "memory"); } while (0)
#define VM0() asm volatile("s_waitcnt vmcnt(0)" ::: "memory")
#define SCHEDB() __builtin_amdgcn_sched_barrier(0)
__device__ __forceinline__ bf8 lo8(const bf16v& v) { return __builtin_shufflevector(v, v, 0, 1, 2, 3, 4, 5, 6, 7); }
__device__ __forceinline__ bf8 hi8(const bf16v& v) { return __builtin_shufflevector(v, v, 8, 9, 10, 11, 12, 13, 14, 15); }
__device__ __forceinline__ bf16v cat8(bf8 a, bf8 b) { return __builtin_shufflevector(a, b, 0, 1, 2, 3, 4, 5, 6, 7, 8, 9, 10, 11, 12, 13, 14, 15); }
#define MMA_VV(c, a, b) asm volatile("v_mfma_f32_16x16x32_bf16 %0, %1, %2, %0" : "+v"(c) : "v"(a), "v"(b))
#define MMA_AV(c, a, b) asm volatile("v_mfma_f32_16x16x32_bf16 %0, %1, %2, %0" : "+v"(c) : "a"(a), "v"(b))
#define MMA_VA(c, a, b) asm volatile("v_mfma_f32_16x16x32_bf16 %0, %1, %2, %0" : "+v"(c) : "v"(a), "a"(b))
#define MMA0_VV(c, a, b) asm volatile("v_mfma_f32_16x16x32_bf16 %0, %1, %2, 0" : "=&v"(c) : "v"(a), "v"(b))
#define MMA0_AV(c, a, b) asm volatile("v_mfma_f32_16x16x32_bf16 %0, %1, %2, 0" : "=&v"(c) : "a"(a), "v"(b))
#define MMA0_VA(c, a, b) asm volatile("v_mfma_f32_16x16x32_bf16 %0, %1, %2, 0" : "=&v"(c) : "v"(a), "a"(b))
#define MMA16(c, a, b) asm volatile("v_mfma_f32_16x16x16_bf16 %0, %1, %2, %0" : "+v"(c) : "v"(a), "v"(b))
#define MMA16_0(c, a, b) asm volatile("v_mfma_f32_16x16x16_bf16 %0, %1, %2, 0" : "=&v"(c) : "v"(a), "v"(b))
#define SMM_V(c, a, b, i) asm volatile("v_smfmac_f32_16x16x64_bf16 %0, %1, %2, %3" : "+v"(c) : "v"(a), "v"(b), "v"(i))
#define SMM_A(c, a, b, i) asm volatile("v_smfmac_f32_16x16x64_bf16 %0, %1, %2, %3" : "+v"(c) : "v"(a), "a"(b), "v"(i))
#define NOPV() asm volatile("s_nop 1")
#define PIN(x) asm volatile("" : "+v"(x))
#define SETTLE() asm volatile("s_nop 7\n\ts_nop 3")
__device__ __forceinline__ bf8 pack8(f32x4 a, f32x4 b) {
    u32x4 r = {pk2bf(a[0], a[1]), pk2bf(a[2], a[3]), pk2bf(b[0], b[1]), pk2bf(b[2], b[3])};
    return __builtin_bit_cast(bf8, r);
}
__device__ __forceinline__ u32 max4(u32 m, u32x4 g) { m = max(max(m, g[0]), g[1]); return max(max(m, g[2]), g[3]); }
#define ZERO4(c) asm volatile("v_mov_b64 %0, 0\n\tv_mov_b64 %1, 0" : "=v"(((u64*)&(c))[0]), "=v"(((u64*)&(c))[1]))
__device__ __forceinline__ bf8 lds8(const char* shm, int off) { return *(const bf8*)(shm + off); }
__device__ __forceinline__ bf16v ldfrag(const char* p) { return cat8(*(const bf8*)p, *(const bf8*)(p + 1024)); }
template <int CTRL> __device__ __forceinline__ float fdpp(float x) {
    return __builtin_bit_cast(float, __builtin_amdgcn_mov_dpp(__builtin_bit_cast(int, x), CTRL, 0xf, 0xf, true));
}
__device__ __forceinline__ float halfsum(float a, float b) {
    auto r = __builtin_amdgcn_permlane32_swap(__float_as_uint(a), __float_as_uint(b), false, false);
    return __uint_as_float(r[0]) + __uint_as_float(r[1]);
}
__device__ __forceinline__ float grp_sum(float x) {
    auto t = __builtin_amdgcn_permlane16_swap(__float_as_uint(x), __float_as_uint(x), false, false);
    x = __uint_as_float(t[0]) + __uint_as_float(t[1]);
    return halfsum(x, x);
}
__device__ __forceinline__ float grp_max(float x) {
    auto t = __builtin_amdgcn_permlane16_swap(__float_as_uint(x), __float_as_uint(x), false, false);
    x = fmaxf(__uint_as_float(t[0]), __uint_as_float(t[1]));
    auto u = __builtin_amdgcn_permlane32_swap(__float_as_uint(x), __float_as_uint(x), false, false);
    return fmaxf(__uint_as_float(u[0]), __uint_as_float(u[1]));
}
__device__ __forceinline__ float bflo(u32 v) { return __uint_as_float(v << 16); }
__device__ __forceinline__ float bfhi(u32 v) { return __uint_as_float(v & 0xffff0000u); }
__device__ __forceinline__ void st4_lane0(char* base, u32 v) {
    u32 z, d;
    asm volatile("s_mov_b64 exec, 1\n\tv_mov_b32 %0, 0\n\tv_mov_b32 %1, %3\n\ts_nop 1\n\tglobal_store_dword %0, %1, %2 sc1\n\ts_mov_b64 exec, -1" : "=&v"(z), "=&v"(d) : "s"(base), "s"(v) : "memory");
}
__device__ __forceinline__ int track(int d, int sample) {
    if (sample <= 0) return d;
    return sample >= d ? min(sample, d + DUP) : (d - sample > 2 ? d - ((d - sample) >> 1) : d);
}
__device__ __forceinline__ u32 sched_peek(const char* p) { return __hip_atomic_load((const gu32*)p, __ATOMIC_RELAXED, __HIP_MEMORY_SCOPE_AGENT); }
__device__ __forceinline__ u32 sched_get(u32 v, const char* p, int& cap) {
    for (int spin = 0; v == ~0u; ++spin) {
        if (spin >= cap) { cap = 1; break; }
        __builtin_amdgcn_s_sleep(3);
        v = sched_peek(p);
    }
    return __builtin_amdgcn_readfirstlane(v);
}

#define TRDECL
#define TR(i)
#define TRV(i, v)
#define TRFLUSH(role)
#define TRC(i)
constexpr size_t ACT = (size_t)S_TOK * DM * 2;
constexpr size_t O_DBG = 0, O_CTL = 2u << 20, O_ST = O_CTL + 4096, O_FL = O_ST + 128 * 1280, O_PX = O_FL + 3 * 192 * 128, O_X1 = O_PX + 2 * PX_BYTES,
                 O_OB = O_X1 + 2 * ACT, O_XN = O_OB + 2 * ACT, O_XR = O_XN + 2 * ACT, O_WO = O_XR + 2 * ACT,
                 O_WVO = O_WO + 10 * 81920, O_HOP = O_WVO + (size_t)80 * 4 * 40 * 2048, WS_BYTES = O_HOP + (4u << 20);
#define TT(role, i)
struct LaParams { char* ws; u16* out; char* hop; const u16* Wq; const u16* Wk; const u16* Wv; const u16* Wo; const u16* wqkv; const u16* wphi; };
__device__ __forceinline__ int kperm(int g, int e) { return 16 * (g >> 1) + 8 * ((e & 3) >> 1) + 4 * (g & 1) + 2 * (e >> 2) + (e & 1); }
__device__ __forceinline__ bf8 tr8(u32 a, u32 b) {
    const s16x4 lo = __builtin_amdgcn_ds_read_tr16_b64_v4i16((LDS3 s16x4*)(uintptr_t)a), hi = __builtin_amdgcn_ds_read_tr16_b64_v4i16((LDS3 s16x4*)(uintptr_t)b);
    return __builtin_bit_cast(bf8, __builtin_shufflevector(lo, hi, 0, 1, 2, 3, 4, 5, 6, 7));
}
__device__ __forceinline__ char* wsp(const LaParams& p) { char* b = p.ws; asm volatile("" : "+s"(b)); return b; }
template <int N> __device__ __forceinline__ void wait_flags(const char* f, const u32 (&off)[N], int& cap) {
    for (int spin = 0;; ++spin) {
        u32 m = 0;
#pragma unroll
        for (int i = 0; i < N; ++i) m = max(m, sched_peek(f + off[i]));
        if (!__any(m == ~0u)) break;
        if (spin >= cap) { cap = 1; break; }
        __builtin_amdgcn_s_sleep(4);
    }
    asm volatile("" ::: "memory");
}
__device__ __forceinline__ void wait_all_tokens(const LaParams& p, u32 lane, int& cap) {
    asm volatile("" : "+v"(lane));
    const u32 off[3] = {lane * 128, lane * 128 + 8192, min(lane + 128, 159u) * 128};
    wait_flags<3>(wsp(p) + O_FL + 192 * 128, off, cap);
}
__device__ __forceinline__ void wait_first_tokens(const LaParams& p, const int set, u32 lane, int& cap) {
    asm volatile("" : "+v"(lane));
    const u32 off[1] = {(set * 20 + min(lane, 39u) + (lane >= 20 ? 20 : 0)) * 128};
    wait_flags<1>(wsp(p) + O_FL, off, cap);
}
__device__ __forceinline__ int get_side(const LaParams& p, int& cap) { return sched_get(sched_peek(wsp(p) + O_FL + 384 * 128), wsp(p) + O_FL + 384 * 128, cap) & 1; }
__device__ __forceinline__ char* align2m(char* p) { return p + ((size_t)(-(intptr_t)p) & ((2u << 20) - 1)); }

constexpr int Q_XA = 0, Q_PART = 20480, Q_TR = 48640, Q_WL = 54784, Q_ST = 153088, Q_PS = 44;

#define DPP1(op, ror) op "_dpp %0, %0, %0 row_ror:" ror " row_mask:0xf bank_mask:0xf\n\t"
#define DPP2(op, ror) DPP1(op, ror) op "_dpp %1, %1, %1 row_ror:" ror " row_mask:0xf bank_mask:0xf\n\ts_nop 0\n\t"
__device__ __forceinline__ void st16m(char* base, u32 off, u32x4 v, u64 mask) {
    asm volatile("s_mov_b64 exec, %3\n\ts_nop 3\n\tglobal_store_dwordx4 %0, %1, %2 sc1\n\ts_mov_b64 exec, -1\n\ts_nop 1" :: "v"(off), "v"(v), "s"(base), "s"(mask) : "memory");
}
__device__ __forceinline__ void st32(char* base, u32 off, u32x4 a, u32x4 b) {
    asm volatile("s_nop 4\n\tglobal_store_dwordx4 %0, %1, %3\n\tglobal_store_dwordx4 %0, %2, %3 offset:16\n\ts_nop 2" :: "v"(off), "v"(a), "v"(b), "s"(base) : "memory");
}
__device__ __forceinline__ void st8m(char* base, u32 off, u32x2 v, u64 mask) {
    asm volatile("s_mov_b64 exec, %3\n\ts_nop 3\n\tglobal_store_dwordx2 %0, %1, %2 sc1\n\ts_mov_b64 exec, -1" :: "v"(off), "v"(v), "s"(base), "s"(mask) : "memory");
}
__device__ __forceinline__ u32 pk2bf_abs(float lo, float hi) {
    u32 r;
    asm("v_cvt_pk_bf16_f32 %0, |%1|, |%2|" : "=v"(r) : "v"(lo), "v"(hi));
    return r;
}
constexpr float SM_C = 0.07216878364870322f * 1.4426950408889634f, NEG_BIG = -0x1p100f;
constexpr u64 SM_MASK = 0x0155015501550155ull;
__device__ __forceinline__ void softmax_mem(const float* p, char* dst, const u32 off) {
    f32x4 a[4];
#pragma unroll
    for (int i = 0; i < 4; ++i) a[i] = *(const f32x4*)(p + i * 40 * Q_PS);
    f32x4 x = (a[0] + a[1]) + (a[2] + a[3]);
    const float ml = fmaxf(fmaxf(x[0], x[1]), fmaxf(x[2], x[3])) * SM_C;
    float mx = ml, e0, e1, e2, e3;
    asm("v_fma_f32 %1, %5, %9, -%0\n\tv_fma_f32 %2, %6, %9, -%0\n\tv_fma_f32 %3, %7, %9, -%0\n\tv_fma_f32 %4, %8, %9, -%0\n\t"
        DPP1("v_max_f32", "8") "v_exp_f32 %1, %1\n\ts_nop 0\n\t" DPP1("v_max_f32", "4") "v_exp_f32 %2, %2\n\ts_nop 0\n\t"
        DPP1("v_max_f32", "2") "v_exp_f32 %3, %3\n\ts_nop 0\n\t" DPP1("v_max_f32", "1") "v_exp_f32 %4, %4\n\ts_nop 0"
        : "+v"(mx), "=&v"(e0), "=&v"(e1), "=&v"(e2), "=&v"(e3) : "v"(x[0]), "v"(x[1]), "v"(x[2]), "v"(x[3]), "s"(SM_C));
    const float f = __builtin_amdgcn_exp2f(ml - mx);
    float sm = ((e0 + e1) + (e2 + e3)) * f;
    asm("s_nop 1\n\t" DPP1("v_add_f32", "8") "v_mul_f32 %1, %1, %5\n\ts_nop 0\n\t" DPP1("v_add_f32", "4") "v_mul_f32 %2, %2, %5\n\ts_nop 0\n\t"
        DPP1("v_add_f32", "2") "v_mul_f32 %3, %3, %5\n\ts_nop 0\n\t" DPP1("v_add_f32", "1") "v_mul_f32 %4, %4, %5"
        : "+v"(sm), "+v"(e0), "+v"(e1), "+v"(e2), "+v"(e3) : "v"(f));
    const float inv = __builtin_amdgcn_rcpf(sm);
    const u32 d0 = pk2bf_abs(e0 * inv, e1 * inv), d1 = pk2bf_abs(e2 * inv, e3 * inv);
    u32 n0, n1;
    asm("s_nop 0\n\tv_mov_b32_dpp %0, %2 quad_perm:[1,0,3,2] row_mask:0xf bank_mask:0xf\n\tv_mov_b32_dpp %1, %3 quad_perm:[1,0,3,2] row_mask:0xf bank_mask:0xf"
        : "=&v"(n0), "=&v"(n1) : "v"(d0), "v"(d1));
    st16m(dst, off, (u32x4){d0, d1, n0, n1}, SM_MASK);
}
__device__ __forceinline__ void softmax_tok(const char* shm, const int q0, int lane, char* dst) {
    asm volatile("" : "+v"(lane));
    const int r = lane >> 4, j = lane & 15;
    const float* p = (const float*)(shm + Q_PART) + (q0 + r) * Q_PS + min(j, 10) * 4;
    f32x4 x[2];
    float mx[2], sm[2];
#pragma unroll
    for (int n = 0; n < 2; ++n) {
        f32x4 a[4];
#pragma unroll
        for (int i = 0; i < 4; ++i) a[i] = *(const f32x4*)(p + (i * 40 + n * 4) * Q_PS);
        x[n] = (a[0] + a[1]) + (a[2] + a[3]);
        mx[n] = fmaxf(fmaxf(x[n][0], x[n][1]), fmaxf(x[n][2], x[n][3])) * SM_C;
    }
    asm("s_nop 1\n\t" DPP2("v_max_f32", "8") DPP2("v_max_f32", "4") DPP2("v_max_f32", "2") DPP2("v_max_f32", "1") : "+v"(mx[0]), "+v"(mx[1]));
#pragma unroll
    for (int n = 0; n < 2; ++n) {
#pragma unroll
        for (int i = 0; i < 4; ++i) x[n][i] = __builtin_amdgcn_exp2f(fmaf(x[n][i], SM_C, -mx[n]));
        sm[n] = (x[n][0] + x[n][1]) + (x[n][2] + x[n][3]);
    }
    asm("s_nop 1\n\t" DPP2("v_add_f32", "8") DPP2("v_add_f32", "4") DPP2("v_add_f32", "2") DPP2("v_add_f32", "1") : "+v"(sm[0]), "+v"(sm[1]));
#pragma unroll
    for (int n = 0; n < 2; ++n) {
        const float inv = __builtin_amdgcn_rcpf(sm[n]);
        const u32 d0 = pk2bf_abs(x[n][0] * inv, x[n][1] * inv), d1 = pk2bf_abs(x[n][2] * inv, x[n][3] * inv);
        u32 n0, n1;
        asm("s_nop 1\n\tv_mov_b32_dpp %0, %2 quad_perm:[1,0,3,2] row_mask:0xf bank_mask:0xf\n\tv_mov_b32_dpp %1, %3 quad_perm:[1,0,3,2] row_mask:0xf bank_mask:0xf"
            : "=&v"(n0), "=&v"(n1) : "v"(d0), "v"(d1));
        st16m(dst, (j < 8 ? n * 256 + j * 32 : 512 + n * 64) + r * 16, (u32x4){d0, d1, n0, n1}, SM_MASK);
    }
}

__host__ __device__ constexpr int qk_slot(int T, int ks) { return ks < 5 ? ks * 6 + T : 30 + (T >> 1) * 10 + (ks - 5) * 2 + (T & 1); }
__host__ __device__ constexpr int lds_slot(int n) { return n < 7 ? 5 * n + 2 : (n == 7 ? 38 : (n == 8 ? 41 : (n == 9 ? 46 : (n == 10 ? 51 : 55)))); }
__host__ __device__ constexpr int slot_lds(int sl) { for (int n = 0; n < 12; ++n) if (lds_slot(n) == sl) return n; return -1; }
__host__ __device__ constexpr int slot_frag(int sl) { if (slot_lds(sl) >= 0) return 48 + slot_lds(sl); int n = 0; for (int i = 0; i < 12; ++i) n += lds_slot(i) < sl; return sl - n; }
__host__ __device__ constexpr int qk_frag(int T, int ks) { return slot_frag(qk_slot(T, ks)); }
__host__ __device__ constexpr int slot_T(int sl) { return sl < 30 ? sl % 6 : ((sl - 30) / 10) * 2 + (sl & 1); }
__host__ __device__ constexpr int slot_ks(int sl) { return sl < 30 ? sl / 6 : 5 + ((sl - 30) % 10) / 2; }
__host__ __device__ constexpr int lds_rank(int T, int ks) { int n = 0; for (int t = 0; t < T; ++t) n += qk_frag(t, ks) >= 48; return n; }
__host__ __device__ constexpr int lds_nth(int ks, int n) { for (int t = 0; t < 6; ++t) if (qk_frag(t, ks) >= 48 && lds_rank(t, ks) == n) return qk_frag(t, ks) - 48; return -1; }

__device__ __forceinline__ void qk_role(const LaParams& p, const int set, const int h, char* shm) {
    const int lane = threadIdx.x & 63, w = __builtin_amdgcn_readfirstlane(threadIdx.x >> 6);
    const int fr = lane & 15, g = lane >> 4;
    const u32 lds0 = (u32)(uintptr_t)(LDS3 char*)shm;
    const int sidx = (lane & 8) ? 0xEEEE : 0x4444;
    int cap = SPIN_CAP;
    char* hop = p.hop;
    char* mine = hop;
    int phys = 0;
    char* px = (wsp(p) + O_PX);
    const char* xn = (wsp(p) + O_XN);
    char* wl = shm + Q_WL + w * 24576 + lane * 16;
    const char* xa = shm + Q_XA + lane * 16;

    bf16v WA[32], WV[16];
    {
        bf16v WT[12];
        u32 vo[6], rd[3];
#pragma unroll
        for (int i = 0; i < 6; ++i) {
            const int id = i * 64 + lane, r = w * 16 + id / 24;
            vo[i] = (32 * (r >> 5) + kperm((r >> 2) & 3, ((r >> 4) & 1) * 4 + (r & 3))) * (HD * 2) + ((id % 24) ^ (r & 6)) * 16;
        }
#pragma unroll
        for (int i = 0; i < 3; ++i) rd[i] = lds0 + (g * 4 + (fr >> 2)) * 384 + ((w * 3 + i) ^ ((g & 1) * 2 + (fr >> 3))) * 32 + (fr & 3) * 8;
        auto issue = [&](const int j) {
            const char* src = (const char*)((j & 1) ? p.Wk : p.Wq) + (size_t)(j >> 1) * 64 * HD * 2 + h * 384;
#pragma unroll
            for (int i = 0; i < 6; ++i) dma_gather(src, lds0 + (j % 6) * 24576 + w * 6144 + i * 1024, vo[i]);
        };
#pragma unroll
        for (int j = 0; j < 5; ++j) issue(j);
        static_for<0, 20>([&](auto J) {
            constexpr int j = CI(J);
            asm volatile("s_waitcnt vmcnt(%0)" :: "n"((19 - j < 4 ? 19 - j : 4) * 6) : "memory");
            RAW_BARRIER();
            if constexpr (j + 5 < 20) issue(j + 5);
            static_for<0, 3>([&](auto PR) {
                constexpr int id = qk_frag(2 * CI(PR) + (j & 1), j >> 1);
                u32 a = rd[CI(PR)] + (j % 6) * 24576;
                asm volatile("" : "+v"(a));
                const bf16v f = cat8(tr8(a, a + 16 * 384), tr8(a + 32 * 384, a + 48 * 384));
                if constexpr (id < 32) WA[id] = f; else if constexpr (id < 48) WV[id - 32] = f; else WT[id - 48] = f;
            });
        });
        RAW_BARRIER();
        dma_kb<5>(xn + (size_t)set * 40960 + w * 5120, lds0 + Q_XA + w * 5120, lane * 16);
#pragma unroll
        for (int i = 0; i < 12; ++i) { *(bf8*)(wl + i * 2048) = lo8(WT[i]); *(bf8*)(wl + i * 2048 + 1024) = hi8(WT[i]); }
    }
    TRDECL;
    bf8 QX[2], KX[2];
    u32x2 QY[2], KY[2];
    u32 peek = ~0u;
    int d1 = D1I, d2 = D2I;
    if (w == 3) *(u64*)(shm + Q_ST + lane * 8) = 0;
    auto xproj = [&](const int snext, const int sprev) {
        f32x4 acc[6][2];
        TR(8);
        const char* xs1 = xn + (size_t)(snext - 2) * 40960 + 20480 + w * 5120;
        const char* xs0 = xn + (size_t)min(snext, NSEG - 1) * 40960 + w * 5120;
        u32 xl = lds0 + Q_XA + w * 5120;
        asm volatile("" : "+s"(xs0), "+s"(xs1), "+s"(xl));
        bf8 x0 = *(const bf8*)(xa), x1 = *(const bf8*)(xa + 1024), lw[2];
        lw[0] = *(const bf8*)(wl);
        static_for<0, 20>([&](auto K) {
            constexpr int k = CI(K), l0 = lds_nth(((k + 1) >> 1) % 10, 0), l1 = lds_nth(((k + 1) >> 1) % 10, 1);
            bf8 n0, n1, nw[2];
            if constexpr (k == 9) {
                TR(9);
                VM0(); RAW_BARRIER();
                TR(10);
            }
            static_for<0, 6>([&](auto TT) {
                constexpr int T = CI(TT), id = qk_frag(T, k >> 1), rk = lds_rank(T, k >> 1);
                if constexpr (k < 19) {
                    if constexpr (T == 0) n0 = *(const bf8*)(xa + (k + 1) * 2048);
                    if constexpr (T == 1) n1 = *(const bf8*)(xa + (k + 1) * 2048 + 1024);
                    if constexpr (T == 2 && l0 >= 0) nw[0] = *(const bf8*)(wl + l0 * 2048 + ((k + 1) & 1) * 1024);
                    if constexpr (T == 3 && l1 >= 0) nw[1] = *(const bf8*)(wl + l1 * 2048 + ((k + 1) & 1) * 1024);
                }
                if constexpr (k < 3 && (T == 3 || T == 5) && 2 * k + (T == 5) < 5)
                    dma_kb<1>(xs1 + (2 * k + (T == 5)) * 1024, xl + 20480 + (2 * k + (T == 5)) * 1024, lane * 16);
                if constexpr (k >= 9 && k < 14 && T == 4)
                    dma_kb<1>(xs0 + (k - 9) * 1024, xl + (k - 9) * 1024, lane * 16);
                if constexpr (k == 18 && T == 4) peek = sched_peek(mine + (S_SLOT + w) * SLOT + max(sprev, 0) * 128 + 4);
                SCHEDB();
                if constexpr (id < 32) {
                    const bf8 wf = (k & 1) ? hi8(WA[id]) : lo8(WA[id]);
                    if constexpr (k == 0) { MMA0_AV(acc[T][0], wf, x0); MMA0_AV(acc[T][1], wf, x1); } else { MMA_AV(acc[T][0], wf, x0); MMA_AV(acc[T][1], wf, x1); }
                } else {
                    bf8 wf;
                    if constexpr (id < 48) wf = (k & 1) ? hi8(WV[id - 32]) : lo8(WV[id - 32]); else wf = lw[rk];
                    if constexpr (k == 0) { MMA0_VV(acc[T][0], wf, x0); MMA0_VV(acc[T][1], wf, x1); } else { MMA_VV(acc[T][0], wf, x0); MMA_VV(acc[T][1], wf, x1); }
                }
                SCHEDB();
            });
            if constexpr (k < 19) { x0 = n0; x1 = n1; if constexpr (l0 >= 0) lw[0] = nw[0]; if constexpr (l1 >= 0) lw[1] = nw[1]; }
        });
        TR(11);
        int ln = lane;
        asm volatile("" : "+v"(ln));
        f32x4 s[3];
#pragma unroll
        for (int i = 0; i < 4; ++i) s[0][i] = (ln >> 4) * 4 + i > (ln & 15) ? NEG_BIG : 0.f;
        s[2] = s[0];
        float* pp = (float*)(shm + Q_PART) + (w * 40 + 8 + (ln & 15)) * Q_PS + (ln >> 4) * 8;
        PIN(s[0]); PIN(s[2]);
        RAW_BARRIER();
#pragma unroll
        for (int T = 0; T < 6; ++T) { PIN(acc[T][0]); PIN(acc[T][1]); }
#pragma unroll
        for (int tt = 0; tt < 2; ++tt) { QX[tt] = pack8(acc[0][tt], acc[2][tt]); KX[tt] = pack8(acc[1][tt], acc[3][tt]); PIN(QX[tt]); PIN(KX[tt]); }
        NOPV();
        MMA_VV(s[0], KX[0], QX[0]); MMA0_VV(s[1], KX[0], QX[1]); MMA_VV(s[2], KX[1], QX[1]);
#pragma unroll
        for (int tt = 0; tt < 2; ++tt) {
            PIN(acc[4][tt]); PIN(acc[5][tt]);
            QY[tt] = (u32x2){pk2bf(acc[4][tt][0], acc[4][tt][1]), pk2bf(acc[4][tt][2], acc[4][tt][3])};
            KY[tt] = (u32x2){pk2bf(acc[5][tt][0], acc[5][tt][1]), pk2bf(acc[5][tt][2], acc[5][tt][3])};
            PIN(QY[tt]); PIN(KY[tt]);
        }
        NOPV();
        MMA16(s[0], KY[0], QY[0]); MMA16(s[1], KY[0], QY[1]); MMA16(s[2], KY[1], QY[1]);
        asm volatile("s_nop 7");
        PIN(s[0]); PIN(s[1]); PIN(s[2]);
        *(f32x4*)(pp) = s[0];
        *(f32x4*)(pp + 16 * Q_PS) = s[1];
        *(f32x4*)(pp + 16 * Q_PS + 4) = s[2];
    };
    {
        const f32x4 neg = {NEG_BIG, NEG_BIG, NEG_BIG, NEG_BIG};
        *(f32x4*)((float*)(shm + Q_PART) + (w * 40 + 8 + fr) * Q_PS + g * 8 + 4) = neg;
        if (lane < 40) *(f32x4*)((float*)(shm + Q_PART) + (w * 40 + lane) * Q_PS + 40) = neg;
    }
    float* pw0 = (float*)(shm + Q_PART) + (w * 40 + (fr & 7)) * Q_PS + g * 8 + (fr >> 3) * 4;
    float* pw2 = fr < 8 && g >= 2 ? (float*)(shm + Q_PART) + (w * 40 + fr) * Q_PS + 24 + g * 4 : (float*)(shm + Q_TR + w * 1536) + lane * 4;
    const float* prd = (const float*)(shm + Q_PART) + ((w & 1) * 4 + g) * Q_PS + min(fr, 10) * 4;
    u32 trw = lds0 + Q_TR + w * 1536 + lane * 8, trr = lds0 + Q_TR + w * 1536 + (fr & 3) * 128 + (4 * g + (fr >> 2)) * 8;
    asm volatile("" : "+v"(trw), "+v"(trr));
    const u32 poff = (fr < 8 ? (w & 1) * 256 + fr * 32 : 512 + (w & 1) * 64) + g * 16;

    for (int layer = 0; layer < 2; ++layer) {
    if (layer > 0) {
        if (w == 0) {
            __builtin_amdgcn_s_sleep(127);
            wait_rt(sched_get(~0u, mine + S_SLOT * SLOT + 63 * 128, cap) + T_TOK);
            wait_first_tokens(p, set, lane, cap);
        }
        RAW_BARRIER();
        hop += HOP_BYTES; mine += HOP_BYTES; px += PX_BYTES; xn += ACT;
        TT(set * 128 + h, 1);
        dma_kb<5>(xn + (size_t)set * 40960 + w * 5120, lds0 + Q_XA + w * 5120, lane * 16);
    }
    VM0(); RAW_BARRIER();
    TT(set * 128 + h, 2);
    xproj(set + 2, set - 1);
    if (layer == 0) { phys = set ^ get_side(p, cap); mine += phys * 8192; }
    TT(set * 128 + h, 3);
    for (int s = set; s < NSEG; s += 2) {
        const int rot = (s >> 1) & 3;
        TR(0);
        u32 ytime = 0;
        const char* mb0 = (s > 0 ? mine + (M_SLOT0 + rot * 8) * SLOT : p.hop + M0_OFF + phys * 8192) + 2 * w * SLOT + 2048;
        const char* mb1 = mb0 + SLOT;
        char* pdst = mine + (P_SLOT0 + rot * 4 + (h >> 3)) * SLOT + (h & 7) * 640;
        asm volatile("" : "+s"(mb0), "+s"(mb1), "+s"(pdst));
        if (s > 0) { ytime = sched_get(peek, mine + (S_SLOT + w) * SLOT + (s - 1) * 128 + 4, cap); wait_rt(ytime); }
        TR(1); TRV(7, ytime);
        f32x4 acc[6];
        u32x4 A[10];
        bf8 Ll[2], Lh[2];
        s16x4 F[3];
        u32 r0[4], r1[4], d[2];
        float v[4];
        int tries = 0;
        auto fin = [&](const int pr, const int i) {
            if (i == 0) { PIN(acc[2 * pr]); PIN(acc[2 * pr + 1]); }
            if (i < 4) { auto r = __builtin_amdgcn_permlane32_swap(__float_as_uint(acc[2 * pr][i]), __float_as_uint(acc[2 * pr + 1][i]), false, false); r0[i] = r[0]; r1[i] = r[1]; }
            if (i >= 1 && i < 5) { asm("v_add_f32 %0, %1, %2" : "=v"(v[i - 1]) : "v"(r0[i - 1]), "v"(r1[i - 1])); }
            if (i == 4) d[0] = pk2bf(v[0], v[1]);
            if (i == 5) {
                d[1] = pk2bf(v[2], v[3]);
                *(LDS3 u32x2*)(uintptr_t)(trw + pr * 512) = (u32x2){d[0], d[1]};
                F[pr] = __builtin_amdgcn_ds_read_tr16_b64_v4i16((LDS3 s16x4*)(uintptr_t)(trr + pr * 512));
            }
        };
        auto slot = [&](auto SL) {
            constexpr int sl = CI(SL), T = slot_T(sl), ks = slot_ks(sl), id = slot_frag(sl);
            if constexpr (id < 32) SMM_A(acc[T], A[ks], WA[id], sidx);
            else if constexpr (id < 48) SMM_V(acc[T], A[ks], WV[id - 32], sidx);
            else { const bf16v L = cat8(Ll[id & 1], Lh[id & 1]); SMM_V(acc[T], A[ks], L, sidx); }
            SCHEDB();
            if constexpr (sl == 0) TRC(6);
            constexpr int n0 = slot_lds(sl), n1 = slot_lds(sl - 1);
            if constexpr (n0 >= 0 && n0 < 10) Ll[n0 & 1] = *(const bf8*)(wl + (n0 + 2) * 2048);
            if constexpr (n1 >= 0 && n1 < 10) Lh[n1 & 1] = *(const bf8*)(wl + (n1 + 2) * 2048 + 1024);
            if constexpr (sl >= 43 && sl < 49) fin(0, sl - 43);
            if constexpr (sl >= 53 && sl < 59) fin(1, sl - 53);
            SCHEDB();
        };
        auto gather = [&]() {
            u32 vo = lane * 16;
            asm volatile("" : "+v"(vo) :: "memory");
#pragma unroll
            for (int ks = 0; ks < 10; ++ks) {
                A[ks] = ld16(ks < 5 ? mb0 : mb1, vo, (ks % 5 - 2) * 1024);
                SCHEDB();
                if (ks < 6) ZERO4(acc[ks]);
                if (ks == 6 || ks == 7) { Ll[ks & 1] = *(const bf8*)(wl + (ks & 1) * 2048); Lh[ks & 1] = *(const bf8*)(wl + (ks & 1) * 2048 + 1024); }
                if (ks < 9) asm volatile("s_nop %0" :: "n"(PACE));
                SCHEDB();
            }
        };
        gather();
        static_for<0, 49>(slot);
        if (__builtin_expect(__any(__builtin_isunordered(v[0], v[1]) || __builtin_isunordered(v[2], v[3])), 0)) {
            SETTLE();
            while (true) {
                gather();
                u32 m = 0;
#pragma unroll
                for (int ks = 0; ks < 10; ++ks) m = max4(m, A[ks]);
                ++tries;
                if (!__any(m == ~0u)) break;
                if (tries >= cap) { cap = 1; break; }
                __builtin_amdgcn_s_sleep(4);
            }
            static_for<0, 49>(slot);
        }
        TR(2); TRV(6, tries + 1);
        static_for<49, 51>(slot);
        if (__builtin_expect(h == 0 && w == 3, 0)) {
            u32 base = (tries == 0 && s > 0) ? ytime : rt32() + ELATE;
            u32 ln8 = lane;
            asm volatile("" : "+v"(ln8));
            ln8 *= 8192;
            const u32x2 rec = {nosent(base + d1 + MG1), nosent(base + d1 + MG1 + d2 + MG2)};
            asm volatile("s_mov_b64 exec, 0x3ff\n\ts_nop 3\n\tglobal_store_dwordx2 %0, %1, %2 sc1\n\ts_mov_b64 exec, -1" :: "v"(ln8), "v"(rec), "s"(hop + S_SLOT * SLOT + s * 128) : "memory");
        }
        static_for<51, 60>(slot);
        TRC(7);
        bf8 F01 = __builtin_bit_cast(bf8, __builtin_shufflevector(F[0], F[1], 0, 1, 2, 3, 4, 5, 6, 7));
        f32x4 s0, s1, s2, u0, u1;
        asm volatile("s_nop 6");
#pragma unroll
        for (int i = 0; i < 6; ++i) fin(2, i);
        PIN(F01); NOPV();
        MMA0_VV(s1, KX[1], F01); MMA0_VV(s0, KX[0], F01); MMA0_VV(s2, F01, F01); MMA0_VV(u0, F01, QX[0]); MMA0_VV(u1, F01, QX[1]);
        asm volatile("s_nop 3");
        MMA16(s1, KY[1], F[2]); MMA16(s0, KY[0], F[2]); MMA16(s2, F[2], F[2]); MMA16(u0, F[2], QY[0]); MMA16(u1, F[2], QY[1]);
        asm volatile("s_nop 4\n\tv_mov_b32_dpp %0, %4 row_ror:8 row_mask:0xf bank_mask:0xc\n\tv_mov_b32_dpp %1, %5 row_ror:8 row_mask:0xf bank_mask:0xc\n\t"
                     "v_mov_b32_dpp %2, %6 row_ror:8 row_mask:0xf bank_mask:0xc\n\tv_mov_b32_dpp %3, %7 row_ror:8 row_mask:0xf bank_mask:0xc"
                     : "+v"(s0[0]), "+v"(s0[1]), "+v"(s0[2]), "+v"(s0[3]) : "v"(s1[0]), "v"(s1[1]), "v"(s1[2]), "v"(s1[3]));
        PIN(s2); *(f32x4*)pw2 = s2; SCHEDB();
        *(f32x4*)pw0 = s0; SCHEDB();
        if (__builtin_expect(w < 2, 1)) {
            RAW_BARRIER();
            TR(3);
            softmax_mem(prd, pdst, poff);
            TR(4);
            if (w == 1) st4_lane0(mine + (T_SLOT + 1) * SLOT + (16 + h) * 128, tries == 0 && s > 0 ? rt32() - ytime : 0);
        } else {
            RAW_BARRIER();
            TR(3);
            if (h == 0 && w == 3) {
                u32 lo = lane;
                asm volatile("" : "+v"(lo));
                int sta = *(const int*)(shm + Q_ST + lo * 4), stb = *(const int*)(shm + Q_ST + 256 + lo * 4);
                asm("s_nop 1\n\t" DPP2("v_max_i32", "8") DPP2("v_max_i32", "4") DPP2("v_max_i32", "2") DPP2("v_max_i32", "1") : "+v"(sta), "+v"(stb));
                const int m1 = max(__builtin_amdgcn_readlane(stb, 16), __builtin_amdgcn_readlane(stb, 32));
                const int m2 = max(max(max(__builtin_amdgcn_readlane(sta, 0), __builtin_amdgcn_readlane(sta, 16)), max(__builtin_amdgcn_readlane(sta, 32), __builtin_amdgcn_readlane(sta, 48))),
                                   __builtin_amdgcn_readlane(stb, 0));
                d1 = track(d1, m1); d2 = track(d2, m2);
                u32 keep;
                asm volatile("s_mov_b32 %0, m0\n\ts_mov_b32 m0, %4\n\ts_nop 2\n\tglobal_load_lds_dword %1, %2 sc1\n\ts_add_u32 m0, m0, 256\n\ts_nop 0\n\tglobal_load_lds_dword %1, %3 sc1\n\ts_mov_b32 m0, %0"
                             : "=&s"(keep) : "v"(lo * 128), "s"(mine + T_SLOT * SLOT), "s"(mine + (T_SLOT + 1) * SLOT), "s"(lds0 + Q_ST), "s"(d1), "s"(d2) : "memory", "scc");
            }
            if (w == 2 && s < 2 && layer > 0) wait_all_tokens(p, lane, cap);
            if (w == 2 && s >= 2 && lane < 40) arm16(mine + (P_SLOT0 + ((rot + 3) & 3) * 4 + (h >> 3)) * SLOT + (h & 7) * 640, lane * 16);
            if (s >= 4 && lane < 40) {
                char* o = px + (size_t)(set * 4 + ((rot + 2) & 3)) * PX_BUF + ((w - 2) * 64 + h) * 640;
                arm16(o, lane * 16); arm16(o + 32 * 640, lane * 16);
            }
        }
        {
            int ln = lane;
            asm volatile("" : "+v"(ln));
            PIN(u0); PIN(u1);
            f32x4 u;
#pragma unroll
            for (int i = 0; i < 4; ++i) u[i] = __uint_as_float(__builtin_amdgcn_permlane32_swap(__float_as_uint(u0[i]), __float_as_uint(u1[i]), false, false)[1]);
            *(f32x4*)((float*)(shm + Q_PART) + (w * 40 + 8 + (ln >> 5) * 16 + (ln & 15)) * Q_PS + 32 + ((ln >> 4) & 1) * 4) = u;
        }
        RAW_BARRIER();
        softmax_tok(shm, 8 + 8 * w, lane, px + (size_t)(set * 4 + rot) * PX_BUF + (w * 32 + h) * 640);
        TR(5); TRFLUSH(set * 128 + h);
        if (s + 2 >= NSEG) { TT(set * 128 + h, 0); break; }
        RAW_BARRIER();
        xproj(s + 4, s + 1);
    }
    }
}

constexpr int C_YL = 0, C_RED = 20480, C_EX = 28672, C_XN = 32768;
__device__ __forceinline__ void comb_role(const LaParams& p, const int set, const int c, char* shm) {
    const int lane = threadIdx.x & 63, w = __builtin_amdgcn_readfirstlane(threadIdx.x >> 6);
    const int fr = lane & 15, g = lane >> 4;
    const u32 lds0 = (u32)(uintptr_t)(LDS3 char*)shm;
    const int sidx = (lane & 8) ? 0xEEEE : 0x4444;
    int cap = SPIN_CAP;
    char* mine = p.hop;
    char* other = p.hop;
    int phys = 0;
    char* px = (wsp(p) + O_PX);
    const char* xn = (wsp(p) + O_XN);
    char* yl = shm + C_YL + lane * 16;
    const int cb = (set * 80 + c) % 10, rb = (set * 80 + c) / 10;

    {
        const int t = threadIdx.x, h = (set * 80 + c) / 5, cu = c % 5;
        u32x4 st[12];
        const u16* src = p.Wo + ((size_t)h * DH + (t >> 4)) * DM + cu * 128 + (t & 15) * 8;
#pragma unroll
        for (int i = 0; i < 12; ++i) asm volatile("global_load_dwordx4 %0, %1, off" : "=v"(st[i]) : "v"(src + (size_t)i * 16 * DM) : "memory");
        u32 vo[12];
#pragma unroll
        for (int i = 0; i < 12; ++i) {
            const int id = i * 64 + lane, r = id / 24;
            vo[i] = kperm((r >> 2) & 3, (r >> 4) * 4 + (r & 3)) * (HD * 2) + ((id % 24) ^ (r & 7)) * 16;
        }
        const u32 ring = lds0 + 57344 + w * 24576;
        auto issue = [&](const int j) {
            const char* a = (const char*)p.Wv + (size_t)(((j + cu) % 5) * 4 + w) * 32 * HD * 2 + h * 384;
#pragma unroll
            for (int i = 0; i < 12; ++i) dma_gather(a, ring + (j & 1) * 12288 + i * 1024, vo[i]);
        };
        SCHEDB();
        issue(0); issue(1);
        bf8 A[5][2][6];
        const u32 ra = ring + fr * 384 + ((g ^ (fr & 3)) + (fr & 4)) * 16;
        auto fetch = [&](const int j) {
            if (j < 4) asm volatile("s_waitcnt vmcnt(12)" ::: "memory"); else asm volatile("s_waitcnt vmcnt(8)" ::: "memory");
#pragma unroll
            for (int u = 0; u < 2; ++u)
#pragma unroll
                for (int k = 0; k < 6; ++k) A[j][u][k] = *(LDS3 const bf8*)(uintptr_t)((ra ^ ((k & 1) * 64)) + (j & 1) * 12288 + u * 6144 + (k >> 1) * 128);
            asm volatile("s_waitcnt lgkmcnt(0)" ::: "memory");
            if (j < 3) issue(j + 2);
        };
        const int e = t >> 4, pr = ((e >> 2) & 1) * 8 + (e >> 3) * 4 + (e & 3);
        asm volatile("s_waitcnt vmcnt(24)" : "+v"(st[0]), "+v"(st[1]), "+v"(st[2]), "+v"(st[3]), "+v"(st[4]), "+v"(st[5]),
                     "+v"(st[6]), "+v"(st[7]), "+v"(st[8]), "+v"(st[9]), "+v"(st[10]), "+v"(st[11]) :: "memory");
#pragma unroll
        for (int i = 0; i < 12; ++i) *(u32x4*)(shm + (i * 16 + pr) * 288 + (t & 15) * 16) = st[i];
        fetch(0);
        RAW_BARRIER();
        { const int layer = 0; TT(set * 128 + 32 + c, 12); }
        bf8 B[8][6];
        const u32 rd = lds0 + ((g >> 1) * 16 + (g & 1) * 4 + (fr >> 2)) * 288 + (fr & 3) * 8;
#pragma unroll
        for (int n = 0; n < 8; ++n)
#pragma unroll
            for (int k = 0; k < 6; ++k) { B[n][k] = tr8(rd + k * 32 * 288 + n * 32, rd + (k * 32 + 8) * 288 + n * 32); PIN(B[n][k]); }
        f32x4 cc[2][2];
        static_for<0, 41>([&](auto N) {
            constexpr int n = CI(N) % 8, j = CI(N) / 8, q = CI(N) - 1;
            if constexpr (n == 0 && j < 4) fetch(j + 1);
            u32x4 o;
#pragma unroll
            for (int k = 0; k < 6; ++k) {
                if constexpr (j < 5) {
#pragma unroll
                    for (int u = 0; u < 2; ++u) { if (k == 0) MMA0_AV(cc[n & 1][u], A[j][u][k], B[n][k]); else MMA_AV(cc[n & 1][u], A[j][u][k], B[n][k]); }
                } else if (k == 0) asm volatile("s_nop 7");
                SCHEDB();
                if constexpr (q >= 0) {
                    if (k == 3) { PIN(cc[q & 1][0]); o[0] = pk2bf(cc[q & 1][0][0], cc[q & 1][0][1]); o[1] = pk2bf(cc[q & 1][0][2], cc[q & 1][0][3]); }
                    if (k == 4) { PIN(cc[q & 1][1]); o[2] = pk2bf(cc[q & 1][1][0], cc[q & 1][1][1]); o[3] = pk2bf(cc[q & 1][1][2], cc[q & 1][1][3]); }
                    if (k == 5) {
                        char* dst = wsp(p) + O_WVO + ((size_t)(h * 20 + ((q / 8 + cu) % 5) * 4 + w) * 40 + cu * 8 + q % 8) * 1024;
                        __builtin_amdgcn_global_store_b128((gu32x4*)(dst + (u32)(lane * 16)), o, "agent");
                    }
                }
                SCHEDB();
            }
        });
        { const int layer = 0; TT(set * 128 + 32 + c, 13); }
        VM0(); RAW_BARRIER();
        { const int layer = 0; TT(set * 128 + 32 + c, 14); }
        dma_kb<10>(xn + (size_t)set * 40960 + w * 10240, lds0 + C_XN + w * 10240, lane * 16);
        if (w == 0) st4_lane0(wsp(p) + O_FL + (400 + set * 80 + c) * 128, 1u);
        __builtin_amdgcn_s_sleep(8);
        const u32 off[1] = {(u32)(400 + (w * 8 + (lane & 7)) * 5 + (c >> 4)) * 128};
        wait_flags<1>(wsp(p) + O_FL, off, cap);
        { const int layer = 0; TT(set * 128 + 32 + c, 15); }
    }
    bf16v WA[32], WV[8];
    {
        u32 vo = (w * 8 + (fr >> 3)) * 819200 + g * 256 + (fr & 7) * 16;
        asm volatile("" : "+v"(vo));
        auto ldw = [&](const int ks, const int t) {
            const char* a = wsp(p) + O_WVO + (size_t)(t * 1600 + ks * 80 + (c >> 1)) * 1024 + (c & 1) * 128;
            const char* b = a + 40960;
            asm volatile("" : "+s"(b));
            return cat8(__builtin_bit_cast(bf8, ld16(a, vo, 0)), __builtin_bit_cast(bf8, ld16(b, vo, 0)));
        };
#pragma unroll
        for (int i = 0; i < 32; ++i) WA[i] = ldw(i >> 2, i & 3);
#pragma unroll
        for (int i = 0; i < 8; ++i) WV[i] = ldw(8 + (i >> 2), i & 3);
    }
    auto yx = [&](bf8 (&yn)[4], const int sy, auto&& extra) {
        f32x4 acc[4][2];
        const char* xa = shm + C_XN + ((sy >> 1) & 1) * 40960 + lane * 16;
        const char* xs = (sy + 2 < NSEG ? xn + (size_t)(sy + 2) * 40960 : (wsp(p) + O_WO) + cb * 81920) + w * 10240;
        u32 xl = lds0 + C_XN + (~(sy >> 1) & 1) * 40960 + w * 10240;
        asm volatile("" : "+s"(xs), "+s"(xl));
        bf8 x0 = *(const bf8*)(xa), x1 = *(const bf8*)(xa + 1024);
#pragma unroll
        for (int k = 0; k < 20; ++k) {
            bf8 n0, n1;
#pragma unroll
            for (int t = 0; t < 4; ++t) {
                if (k < 19 && t == 0) n0 = *(const bf8*)(xa + (k + 1) * 2048);
                if (k < 19 && t == 1) n1 = *(const bf8*)(xa + (k + 1) * 2048 + 1024);
                if (t >= 2 && k < 5) dma_kb<1>(xs + (2 * k + t - 2) * 1024, xl + (2 * k + t - 2) * 1024, lane * 16);
                if (t >= 2 && k >= 5 && k < 15) extra(2 * k + t - 12);
                SCHEDB();
                const int id = (k >> 1) * 4 + t;
                if (id < 32) {
                    const bf8 wf = (k & 1) ? hi8(WA[id & 31]) : lo8(WA[id & 31]);
                    if (k == 0) { MMA0_VA(acc[t][0], x0, wf); MMA0_VA(acc[t][1], x1, wf); } else { MMA_VA(acc[t][0], x0, wf); MMA_VA(acc[t][1], x1, wf); }
                } else {
                    const bf8 wf = (k & 1) ? hi8(WV[id & 7]) : lo8(WV[id & 7]);
                    MMA_VV(acc[t][0], x0, wf); MMA_VV(acc[t][1], x1, wf);
                }
                SCHEDB();
            }
            if (k < 19) { x0 = n0; x1 = n1; }
        }
        SETTLE();
#pragma unroll
        for (int t = 0; t < 4; ++t) { PIN(acc[t][0]); PIN(acc[t][1]); yn[t] = pack8(acc[t][0], acc[t][1]); }
    };
    const u32 pofs = (fr >> 3) * 640 + ((fr >> 2) & 1) * 256 + g * 64 + (fr & 3) * 16;
    const u32 mofs = (2 * g + (fr >> 3)) * 640 + 512 + (fr & 7) * 16;

    for (int layer = 0; layer < 2; ++layer) {
    u32 peek = ~0u;
    VM0(); RAW_BARRIER();
    TT(set * 128 + 32 + c, 11);
    if (layer > 0 && w == 0) st4_lane0(wsp(p) + O_FL + (192 + set * 80 + c) * 128, 1u);
    {
        bf8 yn[4];
        yx(yn, set, [](int) {});
#pragma unroll
        for (int t = 0; t < 4; ++t) *(bf8*)(yl + (w * 5 + t) * 1024) = yn[t];
    }
    if (layer == 0) { phys = set ^ get_side(p, cap); mine += phys * 8192; other += (phys ^ 1) * 8192; }
    else if (w == 3) wait_all_tokens(p, lane, cap);
    TT(set * 128 + 32 + c, 10);
    for (int s = set; s < NSEG; s += 2) {
        const int rot = (s >> 1) & 3;
        TRDECL; TR(0);
        const char* mb0 = (s > 0 ? mine + (M_SLOT0 + rot * 8) * SLOT : p.hop + M0_OFF + phys * 8192) + 2 * w * SLOT + 2048;
        const char* mb1 = mb0 + SLOT;
        asm volatile("" : "+s"(mb0), "+s"(mb1));
        if (s > 0) wait_rt(sched_get(peek, mine + (S_SLOT + w) * SLOT + (s - 1) * 128 + 4, cap) + CLAG);
        TR(1);
        {
            f32x4 acc[4];
            u32x4 A[10];
            auto gather = [&]() {
                u32 vo = lane * 16;
                asm volatile("" : "+v"(vo) :: "memory");
#pragma unroll
                for (int ks = 0; ks < 10; ++ks) {
                    A[ks] = ld16(ks < 5 ? mb0 : mb1, vo, (ks % 5 - 2) * 1024);
                    SCHEDB();
                    if (ks < 4) ZERO4(acc[ks]);
                    if (ks < 9) asm volatile("s_nop %0" :: "n"(PACE));
                    SCHEDB();
                }
            };
            auto mproj = [&]() {
#pragma unroll
                for (int ks = 0; ks < 10; ++ks)
#pragma unroll
                    for (int t = 0; t < 4; ++t) { if (ks < 8) SMM_A(acc[t], A[ks], WA[(ks * 4 + t) & 31], sidx); else SMM_V(acc[t], A[ks], WV[(ks * 4 + t) & 7], sidx); }
                asm volatile("s_nop 7");
#pragma unroll
                for (int t = 0; t < 4; ++t) PIN(acc[t]);
            };
            gather();
            mproj();
            if (__builtin_expect(__any(__builtin_isunordered(acc[0][0], acc[0][1]) || __builtin_isunordered(acc[0][2], acc[0][3])), 0)) {
                for (int tries = 1;; ++tries) {
                    gather();
                    u32 m = 0;
#pragma unroll
                    for (int ks = 0; ks < 10; ++ks) m = max4(m, A[ks]);
                    if (!__any(m == ~0u)) break;
                    if (tries >= cap) { cap = 1; break; }
                    __builtin_amdgcn_s_sleep(8);
                }
                mproj();
            }
            peek = sched_peek(mine + (S_SLOT + w) * SLOT + s * 128);
#pragma unroll
            for (int pr = 0; pr < 2; ++pr) {
                float v[4];
#pragma unroll
                for (int i = 0; i < 4; ++i) v[i] = halfsum(acc[2 * pr][i], acc[2 * pr + 1][i]);
                *(u32x2*)(shm + C_YL + (w * 5 + 4) * 1024 + ((2 * pr + (g >> 1)) * 16 + fr) * 16 + (g & 1) * 8) = (u32x2){pk2bf(v[0], v[1]), pk2bf(v[2], v[3])};
            }
        }
        RAW_BARRIER();
        TR(2);
        if (s < NSEG - 1) {
            const char* pb[4];
#pragma unroll
            for (int i = 0; i < 4; ++i) { pb[i] = mine + (P_SLOT0 + rot * 4 + ((i + c * ROTC) & 3)) * SLOT; asm volatile("" : "+s"(pb[i])); }
            auto ldy = [&](const int f) { return *(const bf8*)(yl + (((f / 5 + c * ROTC) & 3) * 5 + f % 5) * 1024); };
            auto ldp = [&](const int f, const u32 vo, const u32 vm) { return f % 5 == 4 ? ld16(pb[f / 5], vm, 0) : ld16(pb[f / 5], vo, (f % 5) * 1280); };
            u32 xt;
            auto partial = [&](auto NF, auto F0, f32x4& acc, auto&& late) {
                constexpr int nf = CI(NF), f0 = CI(F0);
                bf8 Y[nf];
#pragma unroll
                for (int i = 0; i < nf; ++i) Y[i] = ldy(f0 + i);
#pragma unroll
                for (int i = 0; i < nf; ++i) PIN(Y[i]);
                u32 vo = pofs, vm = mofs;
                asm volatile("" : "+v"(vo), "+v"(vm));
                xt = sched_get(peek, mine + (S_SLOT + w) * SLOT + s * 128, cap);
                TR(3);
                wait_rt(xt);
                TRV(7, xt); TRC(6);
                asm volatile("" ::: "memory");
                u32x4 P[nf];
#pragma unroll
                for (int i = 0; i < nf; ++i) { P[i] = ldp(f0 + i, vo, vm); SCHEDB(); }
#pragma unroll
                for (int i = 0; i < nf; ++i) {
                    if (i == nf - 2) late();
                    if (i == 0) MMA0_VV(acc, Y[i], P[i]); else MMA_VV(acc, Y[i], P[i]);
                    if (i == 0) TRC(7);
                    SCHEDB();
                }
                TRC(8);
            };
            f32x4 acc;
            if (w > 0) {
                if (w == 1) partial(std::integral_constant<int, 3>(), std::integral_constant<int, 11>(), acc, [] {});
                else if (w == 2) partial(std::integral_constant<int, 3>(), std::integral_constant<int, 14>(), acc, [] {});
                else partial(std::integral_constant<int, 3>(), std::integral_constant<int, 17>(), acc, [] {});
                asm volatile("s_nop 7" : "+v"(acc));
                *(f32x4*)(shm + C_EX + (w - 1) * 1024 + lane * 16) = acc;
                RAW_BARRIER();
            } else {
                const int rn = ((s + 1) >> 1) & 3;
                u32 dst = g * 2 * SLOT + (lane & 7) * 16;
                char* dstb = other + (M_SLOT0 + rn * 8 + c / 40) * SLOT + (c % 40) * 128;
                u32 exa = lds0 + C_EX + lane * 16;
                asm volatile("" : "+s"(dstb), "+v"(dst), "+v"(exa));
                f32x4 h1, h2, h3;
                partial(std::integral_constant<int, 11>(), std::integral_constant<int, 0>(), acc, [&] {
                    __builtin_amdgcn_s_barrier();
                    asm volatile("" ::: "memory");
                    h1 = *(LDS3 const f32x4*)(uintptr_t)exa; h2 = *(LDS3 const f32x4*)(uintptr_t)(exa + 1024); h3 = *(LDS3 const f32x4*)(uintptr_t)(exa + 2048);
                    SCHEDB();
                });
                h1 = (h1 + h2) + h3;
                asm volatile("s_nop 2" : "+v"(acc), "+v"(h1));
                acc += h1;
                float v0, v1;
                auto merge = [&]() {
                    auto a = __builtin_amdgcn_permlane32_swap(__float_as_uint(acc[0]), __float_as_uint(acc[2]), false, false);
                    auto b = __builtin_amdgcn_permlane32_swap(__float_as_uint(acc[1]), __float_as_uint(acc[3]), false, false);
                    v0 = __uint_as_float(a[0]) + fdpp<0x128>(__uint_as_float(a[1]));
                    v1 = __uint_as_float(b[0]) + fdpp<0x128>(__uint_as_float(b[1]));
                };
                merge();
                int tries = 0;
                if (__builtin_expect(__any(__builtin_isunordered(v0, v1)), 0)) {
                    while (true) {
                        u32 vo = pofs, vm = mofs, m = 0;
                        asm volatile("" : "+v"(vo), "+v"(vm) :: "memory");
                        u32x4 P[20];
#pragma unroll
                        for (int i = 0; i < 20; ++i) P[i] = ldp(i, vo, vm);
                        f32x4 hh[4];
#pragma unroll
                        for (int i = 0; i < 20; ++i) {
                            const bf8 y = ldy(i);
                            const int k = i < 11 ? 0 : (i - 8) / 3;
                            if (i == 0 || i == 11 || i == 14 || i == 17) MMA0_VV(hh[k], y, P[i]); else MMA_VV(hh[k], y, P[i]);
                            m = max4(m, P[i]);
                            SCHEDB();
                        }
                        asm volatile("s_nop 7" : "+v"(hh[0]), "+v"(hh[1]), "+v"(hh[2]), "+v"(hh[3]));
                        acc = hh[0] + ((hh[1] + hh[2]) + hh[3]);
                        ++tries;
                        if (!__any(m == ~0u)) break;
                        if (tries >= cap) { cap = 1; break; }
                        __builtin_amdgcn_s_sleep(4);
                    }
                    merge();
                }
                TRV(6, tries + 1);
                const u32 dw = nosent(pk2bf(v0, v1));
                auto e = __builtin_amdgcn_permlane32_swap(dw, dw, false, false);
                auto e0 = __builtin_amdgcn_permlane16_swap(e[0], e[0], false, false);
                auto e1 = __builtin_amdgcn_permlane16_swap(e[1], e[1], false, false);
                st16m(dstb, dst, (u32x4){e0[0], e1[0], e0[1], e1[1]}, 0x00ff00ff00ff00ffull);
                TR(4);
                st4_lane0(mine + (T_SLOT + (c >> 6)) * SLOT + (c & 63) * 128, tries == 0 && s > 0 ? rt32() - xt : 0);
                if (s >= 2 && fr < 8) arm16(dstb + (((rn + 3) & 3) - rn) * 8 * SLOT, dst);
            }
        } else wait_rt(sched_get(peek, mine + (S_SLOT + w) * SLOT + s * 128, cap) + T_PX);
        RAW_BARRIER();
        auto tokens = [&](auto MORE) {
            f32x4 oa[4];
            bf8 ya[4];
            u32x4 P[20];
            u32 vo = pofs, vm = mofs;
            asm volatile("" : "+v"(vo), "+v"(vm));
            const char* pb[4];
#pragma unroll
            for (int i = 0; i < 4; ++i) { pb[i] = px + (size_t)(set * 4 + rot) * PX_BUF + (i * 32 + w * 8) * 640; asm volatile("" : "+s"(pb[i])); }
            auto ldp = [&](const int i) { P[i] = i % 5 == 4 ? ld16(pb[i / 5], vm, 0) : ld16(pb[i / 5], vo, (i % 5) * 1280); };
            int ln = lane;
            asm volatile("" : "+v"(ln));
            const u32x4 rg = ld16((wsp(p) + O_XR) + layer * ACT + (size_t)s * 40960 + ((c >> 2) * 2 + (w >> 1)) * 1024 + (c & 2) * 256 + (w & 1) * 128, (ln & 1) * 256 + ((ln >> 1) & 7) * 16, 0);
            TRC(9);
            if constexpr (CI(MORE)) yx(ya, s + 2, ldp);
            else {
#pragma unroll
                for (int i = 0; i < 20; ++i) ldp(i);
            }
            TRC(10);
            if constexpr (CI(MORE)) peek = sched_peek(mine + (S_SLOT + w) * SLOT + (s + 1) * 128 + 4);
            bf8 y[5];
#pragma unroll
            for (int j = 0; j < 5; ++j) y[j] = *(const bf8*)(yl + (w * 5 + j) * 1024);
            auto outx = [&]() {
#pragma unroll
                for (int i = 0; i < 20; ++i) PIN(P[i]);
                NOPV();
#pragma unroll
                for (int j = 0; j < 5; ++j)
#pragma unroll
                    for (int q = 0; q < 4; ++q) { if (j == 0) MMA0_VV(oa[q], y[j], P[q * 5 + j]); else MMA_VV(oa[q], y[j], P[q * 5 + j]); }
                asm volatile("s_nop 7");
#pragma unroll
                for (int q = 0; q < 4; ++q) PIN(oa[q]);
            };
            outx();
            if (__builtin_expect(__any(__builtin_isunordered(oa[0][0], oa[1][0]) || __builtin_isunordered(oa[2][0], oa[3][0])), 0)) {
                for (int tries = 1;; ++tries) {
                    asm volatile("" : "+v"(vo), "+v"(vm) :: "memory");
                    u32 m = 0;
#pragma unroll
                    for (int i = 0; i < 20; ++i) ldp(i);
#pragma unroll
                    for (int i = 0; i < 20; ++i) m = max4(m, P[i]);
                    if (!__any(m == ~0u)) break;
                    if (tries >= cap) { cap = 1; break; }
                    __builtin_amdgcn_s_sleep(8);
                }
                outx();
            }
            const bool own = (fr < 8) == (g < 2);
#pragma unroll
            for (int q = 0; q < 4; q += 2) {
                f32x4 o;
#pragma unroll
                for (int i = 0; i < 4; ++i) { const float t = fdpp<0x128>(oa[q + 1][i]); o[i] = own ? oa[q][i] : t; }
                *(f32x4*)(shm + C_RED + ((w * 2 + (g >> 1)) * 32 + (q + !own) * 8 + (fr & 7)) * 32 + (g & 1) * 16) = o;
            }
            if constexpr (CI(MORE)) {
#pragma unroll
                for (int t = 0; t < 4; ++t) *(bf8*)(yl + (w * 5 + t) * 1024) = ya[t];
            }
            RAW_BARRIER();
            TR(5); TRFLUSH(set * 128 + 32 + c);
            f32x4 a = *(const f32x4*)(shm + C_RED + (ln >> 4) * 2048 + w * 256 + (ln & 15) * 16) + *(const f32x4*)(shm + C_RED + (ln >> 4) * 2048 + 1024 + w * 256 + (ln & 15) * 16);
#pragma unroll
            for (int i = 0; i < 4; ++i) {
                auto t = __builtin_amdgcn_permlane16_swap(__float_as_uint(a[i]), __float_as_uint(a[i]), false, false);
                const float z = __uint_as_float(t[0]) + __uint_as_float(t[1]);
                a[i] = halfsum(z, z);
            }
            const u32 ra = (c & 1) ? rg[1] : rg[0], rb2 = (c & 1) ? rg[3] : rg[2];
            const u32 d0 = nosent(pk2bf(bflo(ra) + rbf(a[0]), bfhi(ra) + rbf(a[1]))), d1 = nosent(pk2bf(bflo(rb2) + rbf(a[2]), bfhi(rb2) + rbf(a[3])));
            const u32 n0 = __builtin_amdgcn_mov_dpp(d0, 0xB1, 0xf, 0xf, true), n1 = __builtin_amdgcn_mov_dpp(d1, 0xB1, 0xf, 0xf, true);
            st16m((wsp(p) + O_X1) + layer * ACT + (size_t)s * 40960 + c * 512 + w * 128, ln * 8, (u32x4){d0, d1, n0, n1}, 0x5555ull);
        };
        if (s + 2 >= NSEG) { tokens(std::integral_constant<int, 0>()); break; }
        tokens(std::integral_constant<int, 1>());
    }
    TT(set * 128 + 32 + c, 0);
    dma_kb<10>((wsp(p) + O_WO) + cb * 81920 + 40960 + w * 10240, lds0 + C_XN + 40960 + w * 10240, lane * 16);
    VM0(); RAW_BARRIER();
    wait_rt(sched_get(~0u, mine + (S_SLOT + w) * SLOT + 63 * 128, cap) + T_OB + OB_STEP * w + (layer == 0 ? OB_COLD + 8 * w : 0));
    {
        int ln = lane;
        asm volatile("" : "+v"(ln));
        u32x4 O[20], R[2];
        u32 dw[8];
        auto ready = [&](const int T) {
            const char* ob = (wsp(p) + O_OB) + layer * ACT + (size_t)T * 20480 + 16384;
            const u32 off[1] = {(u32)(ln & 31) * 128};
            wait_flags<1>(ob, off, cap);
        };
        auto load = [&](const int T) {
            const char* ob0 = (wsp(p) + O_OB) + layer * ACT + (size_t)T * 20480 + 4096;
            const char* ob1 = ob0 + 8192;
            const char* ob2 = ob0 + 12288;
            const char* xb = (wsp(p) + O_X1) + layer * ACT + (size_t)(T >> 1) * 40960 + cb * 4096 + (T & 1) * 256;
            u32 vo = ln * 16, vr = (ln >> 4) * 256 + (ln & 15) * 8, vx = (ln >> 4) * 1024 + (ln & 15) * 16;
            asm volatile("" : "+v"(vo), "+v"(vr), "+v"(vx), "+s"(ob0), "+s"(ob1), "+s"(ob2), "+s"(xb) :: "memory");
#pragma unroll
            for (int k = 0; k < 16; ++k) { O[k] = ld16(k < 8 ? ob0 : ob1, vo, ((k & 7) - 4) * 1024); SCHEDB(); }
#pragma unroll
            for (int k = 0; k < 4; ++k) {
                const u64 lo = __hip_atomic_load((const gu64*)(ob2 + vr + k * 1024), __ATOMIC_RELAXED, __HIP_MEMORY_SCOPE_AGENT);
                const u64 hi = __hip_atomic_load((const gu64*)(ob2 + vr + k * 1024 + 128), __ATOMIC_RELAXED, __HIP_MEMORY_SCOPE_AGENT);
                O[16 + k] = (u32x4){(u32)lo, (u32)(lo >> 32), (u32)hi, (u32)(hi >> 32)};
            }
            R[0] = ld16(xb, vx, 0); R[1] = ld16(xb, vx, 512);
            SCHEDB();
        };
        auto gemm = [&]() {
            u32 wa0 = lds0 + C_XN + ln * 16, wa1 = wa0 + 40960;
            asm volatile("" : "+v"(wa0), "+v"(wa1));
            f32x4 acc[4];
            bf8 wf[8];
#pragma unroll
            for (int t = 0; t < 8; ++t) wf[t] = *(LDS3 const bf8*)(uintptr_t)(wa0 + t * 1024);
#pragma unroll
            for (int k = 0; k < 20; ++k) {
                if (k >= 16) { PIN(O[k]); NOPV(); }
#pragma unroll
                for (int t = 0; t < 4; ++t) {
                    if (k == 0) MMA0_VV(acc[t], wf[t], O[k]); else MMA_VV(acc[t], wf[(k & 1) * 4 + t], O[k]);
                    SCHEDB();
                    if (k < 18) wf[(k & 1) * 4 + t] = *(LDS3 const bf8*)(uintptr_t)((k < 8 ? wa0 : wa1) + (((k + 2) % 10) * 4 + t) * 1024);
                    SCHEDB();
                }
            }
            asm volatile("s_nop 7" : "+v"(acc[0]), "+v"(acc[1]), "+v"(acc[2]), "+v"(acc[3]));
            float chk = 0.f;
#pragma unroll
            for (int i = 0; i < 8; ++i) {
                const u32 r = R[i >> 2][i & 3], y = pk2bf(acc[i >> 1][(i & 1) * 2], acc[i >> 1][(i & 1) * 2 + 1]);
                const float a = bflo(r) + bflo(y), b = bfhi(r) + bfhi(y);
                dw[i] = pk2bf(a, b);
                chk += a;
            }
            return __any(chk != chk);
        };
        const int T0 = rb + 16 * w;
        if (OB_POLL) ready(T0);
        load(T0);
        static_for<0, 2>([&](auto PASS) {
            constexpr int pass = CI(PASS);
            const int T = T0 + 64 * pass;
            TT(set * 128 + 32 + c, 1 + pass * 4);
            if (__builtin_expect(gemm(), 0)) {
                for (int tries = 0;; ++tries) {
                    ready(T); load(T);
                    u32 m = max4(max4(0, R[0]), R[1]);
#pragma unroll
                    for (int k = 0; k < 20; ++k) m = max4(m, O[k]);
                    if (!__any(m == ~0u)) break;
                    if (tries >= cap) { cap = 1; break; }
                    __builtin_amdgcn_s_sleep(6);
                }
                gemm();
            }
            TT(set * 128 + 32 + c, 2 + pass * 4);
            if (layer == 1) {
                st32((char*)p.out + (size_t)T * 16 * DM * 2 + cb * 128, (ln & 15) * (DM * 2) + (ln >> 4) * 32, (u32x4){dw[0], dw[1], dw[2], dw[3]}, (u32x4){dw[4], dw[5], dw[6], dw[7]});
            } else {
            float s1 = 0.f, s2 = 0.f;
#pragma unroll
            for (int i = 0; i < 8; ++i) { const float a = bflo(dw[i]), b = bfhi(dw[i]); s1 += a + b; s2 = fmaf(a, a, fmaf(b, b, s2)); }
            s1 = grp_sum(s1); s2 = grp_sum(s2);
            char* stl = (wsp(p) + O_ST) + (size_t)T * 1280;
            st8m(stl + cb * 128, ln * 8, (u32x2){nosent(__float_as_uint(s1)), nosent(__float_as_uint(s2))}, 0xffffull);
            const size_t fo = ACT + (size_t)(T >> 1) * 40960 + (cb * 4 + (T & 1)) * 1024;
            const u32 lo = (ln >> 5) * 2048 + ((ln >> 4) & 1) * 512 + (ln & 15) * 16;
            __builtin_amdgcn_s_sleep(ST_LAG);
            for (int spin = 0;; ++spin) {
                u32 m = 0;
                s1 = 0.f; s2 = 0.f;
#pragma unroll
                for (int i = 0; i < 3; ++i) {
                    const int k = (ln >> 4) + 4 * i;
                    const u64 v = __hip_atomic_load((const gu64*)(stl + min(k, 9) * 128 + (ln & 15) * 8), __ATOMIC_RELAXED, __HIP_MEMORY_SCOPE_AGENT);
                    m = max(m, max((u32)v, (u32)(v >> 32)));
                    if (k < 10) { s1 += __uint_as_float((u32)v); s2 += __uint_as_float((u32)(v >> 32)); }
                }
                if (!__any(m == ~0u)) break;
                if (spin >= cap) { cap = 1; break; }
                __builtin_amdgcn_s_sleep(2);
            }
            TT(set * 128 + 32 + c, 3 + pass * 4);
            s1 = grp_sum(s1); s2 = grp_sum(s2);
            const float mu = s1 * (1.0f / DM), rs = __builtin_amdgcn_rsqf(fmaxf(fmaf(-mu, mu, s2 * (1.0f / DM)), 0.f) + 1e-5f);
            u32 dn[8];
#pragma unroll
            for (int i = 0; i < 8; ++i) dn[i] = pk2bf((bflo(dw[i]) - mu) * rs, (bfhi(dw[i]) - mu) * rs);
            st16s((wsp(p) + O_XN) + fo, lo, (u32x4){dn[0], dn[4], dn[1], dn[5]}); st16s((wsp(p) + O_XN) + fo + 256, lo, (u32x4){dn[2], dn[6], dn[3], dn[7]});
            st16s((wsp(p) + O_XR) + fo, lo, (u32x4){dw[0], dw[4], dw[1], dw[5]}); st16s((wsp(p) + O_XR) + fo + 256, lo, (u32x4){dw[2], dw[6], dw[3], dw[7]});
            TT(set * 128 + 32 + c, 4 + pass * 4);
            if (pass == 0 && w == 0) { VM0(); st4_lane0(wsp(p) + O_FL + (set * 80 + c) * 128, 1u); }
            }
            if constexpr (pass == 0) load(T + 64);
        });
    }
    if (layer == 1) break;
    mine += HOP_BYTES; other += HOP_BYTES; px += PX_BYTES; xn += ACT;
    if (w == 0) wait_first_tokens(p, set, lane, cap);
    RAW_BARRIER();
    TT(set * 128 + 32 + c, 9);
    dma_kb<10>(xn + (size_t)set * 40960 + w * 10240, lds0 + C_XN + w * 10240, lane * 16);
    }
}

constexpr int B_PQ = 0, B_IM = 131072, B_CS = 155648, B_IS = 176;
__device__ __forceinline__ void bw_role(const LaParams& p, const int hb, char* shm) {
    const int lane = threadIdx.x & 63, w = __builtin_amdgcn_readfirstlane(threadIdx.x >> 6);
    const int fr = lane & 15, g = lane >> 4;
    int cap = SPIN_CAP;
    if (hb == 0 && w == 0) {
        u32 best[2] = {~0u, ~0u};
        for (int i = 0; i < 64; ++i) {
#pragma unroll
            for (int sd = 0; sd < 2; ++sd) {
                u64 t0, t1;
                u32 z = 0, one = 1;
                asm volatile("s_memtime %0\n\ts_mov_b64 exec, 1\n\ts_waitcnt lgkmcnt(0)\n\tglobal_atomic_add %2, %3, %2, %4 sc0 sc1\n\ts_waitcnt vmcnt(0)\n\t"
                             "s_memtime %1\n\ts_mov_b64 exec, -1\n\ts_waitcnt lgkmcnt(0)"
                             : "=&s"(t0), "=&s"(t1), "+v"(one) : "v"(z), "s"(p.hop + M0_OFF + sd * 8192 + 6144) : "memory");
                best[sd] = min(best[sd], (u32)(t1 - t0));
            }
            if (i >= 2 && max(best[0], best[1]) < 1000u && max(best[0], best[1]) - min(best[0], best[1]) > 120u) break;
        }
        u32 xcc; asm volatile("s_getreg_b32 %0, hwreg(HW_REG_XCC_ID)" : "=s"(xcc));
        st4_lane0(wsp(p) + O_FL + 384 * 128, ((xcc >> 2) & 1) ^ (best[1] < best[0] ? 1u : 0u));
    }
    bf8 WA[64], WV[16];
    {
        const int t = threadIdx.x;
        float* xs = (float*)(shm + 81920);
        float* ph = xs + 128 * 61;
        for (int i = t; i < 400; i += 256) ph[i] = bf2f(p.wphi[hb * 400 + i]);
        const int j = 4 * w + (fr & 3), G = fr >> 2;
        for (int d0 = 0; d0 < DM; d0 += 128) {
            u32 v15[15];
#pragma unroll
            for (int k = 0; k < 15; ++k) { const int i = t + k * 256; v15[k] = *(const u32*)(p.wqkv + (size_t)(d0 + i / 30) * 1920 + ((i % 30) / 10) * 640 + hb * 20 + (i % 10) * 2); }
            __syncthreads();
#pragma unroll
            for (int k = 0; k < 15; ++k) { const int i = t + k * 256; float* o = xs + (i / 30) * 61 + (i % 30) * 2; o[0] = bflo(v15[k]); o[1] = bfhi(v15[k]); }
            __syncthreads();
            for (int kk = 0; kk < 4; ++kk) {
                float v[8];
                const float* xr = xs + (kk * 32 + g * 8) * 61;
                if (j < 10) {
                    const int m = j / 5, f = 5 * G + j % 5;
#pragma unroll
                    for (int e = 0; e < 8; ++e) v[e] = 0.f;
#pragma unroll 4
                    for (int i = 0; i < 20; ++i) {
                        const float pw = ph[i * 20 + f];
#pragma unroll
                        for (int e = 0; e < 8; ++e) v[e] = fmaf(xr[e * 61 + m * 20 + i], pw, v[e]);
                    }
                } else {
#pragma unroll
                    for (int e = 0; e < 8; ++e) v[e] = j < 15 ? xr[e * 61 + 40 + 5 * G + min(j, 14) - 10] : 1.0f;
                }
                *(u32x4*)(shm + ((d0 / 32 + kk) * 4 + w) * 1024 + lane * 16) = (u32x4){pk2bf(v[0], v[1]), pk2bf(v[2], v[3]), pk2bf(v[4], v[5]), pk2bf(v[6], v[7])};
            }
        }
        __syncthreads();
#pragma unroll
        for (int i = 0; i < 64; ++i) WA[i] = *(const bf8*)(shm + i * 1024 + lane * 16);
#pragma unroll
        for (int i = 0; i < 16; ++i) WV[i] = *(const bf8*)(shm + (64 + i) * 1024 + lane * 16);
        __syncthreads();
    }
    auto proj = [&](f32x4 (&acc)[4], f32x4& sq, auto&& fetch) {
        u32x4 X[20];
#pragma unroll
        for (int k = 0; k < 12; ++k) { X[k] = fetch(k); SCHEDB(); }
#pragma unroll
        for (int k = 0; k < 20; ++k) {
#pragma unroll
            for (int t = 0; t < 4; ++t) {
                if (k == 0) MMA0_AV(acc[t], WA[t], X[k]); else if (k < 16) MMA_AV(acc[t], WA[(k * 4 + t) & 63], X[k]); else MMA_VV(acc[t], WV[(k * 4 + t) & 15], X[k]);
            }
            if (k == 0) MMA0_VV(sq, X[k], X[k]); else MMA_VV(sq, X[k], X[k]);
            SCHEDB();
            if (k < 8) X[k + 12] = fetch(k + 12);
            SCHEDB();
        }
        asm volatile("s_nop 7" : "+v"(acc[0]), "+v"(acc[1]), "+v"(acc[2]), "+v"(acc[3]), "+v"(sq));
    };
    {
        f32x4 t, cs[4];
        u32x4 o1 = {0x3f803f80u, 0x3f803f80u, 0x3f803f80u, 0x3f803f80u};
        asm volatile("s_nop 1" : "+v"(o1));
        proj(cs, t, [&](int) { return o1; });
#pragma unroll
        for (int i = 0; i < 4; ++i) *(f32x4*)(shm + B_CS + i * 1024 + lane * 16) = cs[i];
    }
    char* im = shm + B_IM + w * 6144;
    const u32 imr = (u32)(uintptr_t)(LDS3 char*)im + (4 * g + (fr >> 2)) * B_IS + (fr & 3) * 8;
    typedef __attribute__((ext_vector_type(32))) u32 u32x32;
    for (int layer = 0; layer < 2; ++layer) {
        const char* sch = p.hop + layer * HOP_BYTES + (hb & 1) * 8192 + (S_SLOT + 4) * SLOT;
        f32x4 kv[3][2];
#pragma unroll
        for (int i = 0; i < 6; ++i) ZERO4(kv[i >> 1][i & 1]);
        u32x32 q5;
        u32 tn = 0;
        for (int it = 0; it < 32; ++it) {
            const int s = 2 * it + (w >> 1);
            if (s + 2 < NSEG) { wait_rt(tn); tn = sched_get(~0u, sch + (s + 1) * 128 + 4, cap); wait_rt(tn + B_LAG); tn += 250; }
            else wait_rt(sched_get(~0u, sch + s * 128, cap) + T_X1);
            const char* xb = (wsp(p) + O_X1) + layer * ACT + (size_t)s * 40960 + (w & 1) * 256;
            if (it >= 30) TT((hb >> 4) * 128 + 112 + (hb & 15), (it - 30) * 3);
            f32x4 acc[4], sq;
            const char* xk[5];
#pragma unroll
            for (int i = 0; i < 5; ++i) { xk[i] = xb + i * 8192 + 4096; asm volatile("" : "+s"(xk[i])); }
            for (int pass = 0;; ++pass) {
                u32 vo = g * 512 + fr * 16;
                asm volatile("" : "+v"(vo) :: "memory");
                auto fetch = [&](const int k) { return ld16(xk[k >> 2], vo, ((k & 3) - 2) * 2048); };
                proj(acc, sq, fetch);
                if (pass > 0 || !__any(acc[3][3] != acc[3][3])) break;
                for (int tries = 0;; ++tries) {
                    asm volatile("" : "+v"(vo) :: "memory");
                    u32 m = 0;
#pragma unroll
                    for (int k = 0; k < 20; k += 10) {
#pragma unroll
                        for (int i = 0; i < 10; ++i) m = max4(m, fetch(k + i));
                        SCHEDB();
                    }
                    if (!__any(m == ~0u)) break;
                    if (tries >= cap) { cap = 1; break; }
                    __builtin_amdgcn_s_sleep(6);
                }
            }
            if (it >= 30) TT((hb >> 4) * 128 + 112 + (hb & 15), (it - 30) * 3 + 1);
            const float dsel = (fr & 2) ? ((fr & 1) ? sq[3] : sq[2]) : ((fr & 1) ? sq[1] : sq[0]);
            const float s2 = grp_sum(g == (fr >> 2) ? dsel : 0.f);
            const float mu = acc[3][3] * (1.0f / DM), rs = __builtin_amdgcn_rsqf(fmaxf(fmaf(-mu, mu, s2 * (1.0f / DM)), 0.f) + 1e-5f);
            float v[15];
            f32x4 cs[4];
#pragma unroll
            for (int i = 0; i < 4; ++i) cs[i] = *(const f32x4*)(shm + B_CS + i * 1024 + lane * 16);
#pragma unroll
            for (int j = 0; j < 15; ++j) v[j] = fmaf(-mu, cs[j >> 2][j & 3], acc[j >> 2][j & 3]) * rs;
            u32 ph[2][5];
#pragma unroll
            for (int m = 0; m < 2; ++m) {
                const float* x = v + 5 * m;
                const float mx = grp_max(fmaxf(fmaxf(fmaxf(x[0], x[1]), fmaxf(x[2], x[3])), x[4]));
                const float mn = -grp_max(-fminf(fminf(fminf(x[0], x[1]), fminf(x[2], x[3])), x[4]));
                float e[10], a = 0.f, b = 0.f;
#pragma unroll
                for (int j = 0; j < 5; ++j) {
                    e[j] = __builtin_amdgcn_exp2f((x[j] - mx) * 1.4426950408889634f); a += e[j];
                    e[5 + j] = __builtin_amdgcn_exp2f((mn - x[j]) * 1.4426950408889634f); b += e[5 + j];
                }
                a = __builtin_amdgcn_rcpf(grp_sum(a)); b = __builtin_amdgcn_rcpf(grp_sum(b));
#pragma unroll
                for (int j = 0; j < 5; ++j) { e[j] *= a; e[5 + j] *= b; }
#pragma unroll
                for (int j = 0; j < 5; ++j) ph[m][j] = pk2bf(e[2 * j], e[2 * j + 1]);
            }
            *(u32x4*)(shm + B_PQ + (w * 32 + it) * 1024 + lane * 16) = (u32x4){ph[0][0], ph[0][1], ph[0][2], ph[0][3]};
            q5[it] = ph[0][4];
            char* row = im + fr * B_IS;
            *(u32x2*)(row + g * 8) = (u32x2){ph[1][0], ph[1][1]};
            *(u32x2*)(row + 32 + g * 8) = (u32x2){ph[1][2], ph[1][3]};
            *(u32x2*)(row + 64 + g * 8) = (u32x2){ph[1][4], 0u};
            *(u32x2*)(row + 96 + g * 8) = (u32x2){pk2bf(v[10], v[11]), pk2bf(v[12], v[13])};
            *(u32x2*)(row + 128 + g * 8) = (u32x2){pk2bf(v[14], 1.0f), 0u};
            s16x4 tf[5];
#pragma unroll
            for (int i = 0; i < 5; ++i) tf[i] = __builtin_amdgcn_ds_read_tr16_b64_v4i16((LDS3 s16x4*)(uintptr_t)(imr + i * 32));
#pragma unroll
            for (int i = 0; i < 6; ++i) MMA16(kv[i >> 1][i & 1], tf[i >> 1], tf[3 + (i & 1)]);
            if (it >= 30) TT((hb >> 4) * 128 + 112 + (hb & 15), (it - 30) * 3 + 2);
        }
        asm volatile("s_nop 7" : "+v"(kv[0][0]), "+v"(kv[0][1]), "+v"(kv[1][0]), "+v"(kv[1][1]), "+v"(kv[2][0]), "+v"(kv[2][1]));
#pragma unroll
        for (int i = 0; i < 6; ++i) *(f32x4*)(im + i * 1024 + lane * 16) = kv[i >> 1][i & 1];
        RAW_BARRIER();
        bf8 ka[2];
        u32x2 kb[2];
#pragma unroll
        for (int dt = 0; dt < 2; ++dt) {
            f32x4 t[3];
#pragma unroll
            for (int ft = 0; ft < 3; ++ft) {
                const char* a = shm + B_IM + (ft * 2 + dt) * 1024 + lane * 16;
                t[ft] = (*(const f32x4*)a + *(const f32x4*)(a + 6144)) + (*(const f32x4*)(a + 12288) + *(const f32x4*)(a + 18432));
            }
            ka[dt] = pack8(t[0], t[1]);
            kb[dt] = (u32x2){pk2bf(t[2][0], t[2][1]), pk2bf(t[2][2], t[2][3])};
            PIN(ka[dt]); PIN(kb[dt]);
        }
        char* ob = (wsp(p) + O_OB) + layer * ACT + (size_t)w * 20480 + hb * 512;
        TT((hb >> 4) * 128 + 112 + (hb & 15), 6);
        const u32 rofs = g * 81920 + 16384 - hb * 384 + fr * 8, mofs = (g & 1) * 81920 + (g >> 1) * 256 + fr * 16;
        f32x4 o[2][4][2];
        bf8 qa[2][4];
        u32x2 qb[4];
        auto ldq = [&](const int gr) {
#pragma unroll
            for (int u = 0; u < 4; ++u) qa[gr & 1][u] = *(const bf8*)(shm + B_PQ + (w * 32 + gr * 4 + u) * 1024 + lane * 16);
        };
        auto mm = [&](const int gr, const int k) {
            SCHEDB();
            if (k < 4) { qb[k] = (u32x2){q5[gr * 4 + k], 0u}; PIN(qb[k]); }
            if (k < 8) MMA0_VV(o[gr & 1][k >> 1][k & 1], ka[k & 1], qa[gr & 1][k >> 1]);
            else MMA16(o[gr & 1][(k - 8) >> 1][k & 1], kb[k & 1], qb[(k - 8) >> 1]);
            SCHEDB();
        };
        ldq(0); ldq(1);
        NOPV();
#pragma unroll
        for (int k = 0; k < 16; ++k) mm(0, k);
        static_for<0, 8>([&](auto IT) {
            constexpr int gr = CI(IT);
            auto nx = [&](const int k) { if constexpr (gr < 7) mm(gr + 1, k); else { SCHEDB(); asm volatile("s_nop 0"); SCHEDB(); } };
            if constexpr (gr == 7) asm volatile("s_nop 7");
            u32 d[4][2];
            float x[4];
            nx(0); nx(1);
#pragma unroll
            for (int u = 0; u < 4; ++u) {
                f32x4& a = o[gr & 1][u][0];
                f32x4& b = o[gr & 1][u][1];
                PIN(a); PIN(b);
                float inv = __builtin_amdgcn_rcpf(b[1] + 1e-6f), y0, y1, y2, y3;
                nx(2 + 3 * u);
                asm("v_mul_f32 %0, %4, %8\n\tv_mul_f32 %1, %5, %8\n\tv_mul_f32 %2, %6, %8" : "=&v"(y0), "=&v"(y1), "=&v"(y2), "=&v"(y3) : "v"(a[0]), "v"(a[1]), "v"(a[2]), "v"(a[3]), "v"(inv));
                nx(3 + 3 * u);
                y3 = a[3] * inv; x[u] = b[0] * inv;
                d[u][0] = pk2bf(y0, y1);
                nx(4 + 3 * u);
                d[u][1] = pk2bf(y2, y3);
                if (u & 1) {
                    auto t0 = __builtin_amdgcn_permlane16_swap(d[u - 1][0], d[u][0], false, false);
                    auto t1 = __builtin_amdgcn_permlane16_swap(d[u - 1][1], d[u][1], false, false);
                    st16s(ob + (u - 1) * 81920, mofs, (u32x4){nosent(t0[0]), nosent(t1[0]), nosent(t0[1]), nosent(t1[1])});
                }
            }
            if constexpr (gr < 6) ldq(gr + 2);
            nx(14);
            auto a0 = __builtin_amdgcn_permlane32_swap(__float_as_uint(x[0]), __float_as_uint(x[2]), false, false);
            auto a1 = __builtin_amdgcn_permlane32_swap(__float_as_uint(x[1]), __float_as_uint(x[3]), false, false);
            nx(15);
            auto b0 = __builtin_amdgcn_permlane16_swap(a0[0], a1[0], false, false);
            auto b2 = __builtin_amdgcn_permlane16_swap(a0[1], a1[1], false, false);
            st8m(ob, rofs, (u32x2){nosent(pk2bf(__uint_as_float(b0[0]), __uint_as_float(b0[1]))), nosent(pk2bf(__uint_as_float(b2[0]), __uint_as_float(b2[1])))}, ~0ull);
            ob += 4 * 81920;
            asm volatile("" : "+s"(ob));
        });
        RAW_BARRIER();
        TT((hb >> 4) * 128 + 112 + (hb & 15), 7);
    }
}

__global__ void __launch_bounds__(256, 1) la_kernel(LaParams p) {
    extern __shared__ __attribute__((aligned(16))) char shm[];
    p.hop = align2m(p.ws + O_HOP);
    if (threadIdx.x == 0) {
        u32 xcc; asm volatile("s_getreg_b32 %0, hwreg(HW_REG_XCC_ID)" : "=s"(xcc));
        int role = 255;
        for (int a = 0; a < 8; ++a) {
            const u32 v = (xcc + a) & 7;
            const u32 k = __hip_atomic_fetch_add((gu32*)(((u32*)(p.ws + O_CTL)) + v * 32), 1u, __ATOMIC_RELAXED, __HIP_MEMORY_SCOPE_AGENT);
            if (k < 32) { role = v * 32 + k; break; }
        }
        *(int*)shm = role;
    }
    __syncthreads();
    const int role = __builtin_amdgcn_readfirstlane(*(int*)shm);
    __syncthreads();
    const int set = role >> 7, id = (role >> 5) & 3, k = role & 31;
    if (k < 8) qk_role(p, set, id * 8 + k, shm);
    else if (k < 28) comb_role(p, set, id * 20 + k - 8, shm);
    else bw_role(p, (role >> 5) * 4 + k - 28, shm);
}
__device__ __forceinline__ float row16_sum(float x) { x += fdpp<0x128>(x); x += fdpp<0x124>(x); x += fdpp<0x122>(x); return x + fdpp<0x121>(x); }

#define PFB 0
struct PrepParams { const u16* W[4]; const u16* x; const u16* mem0; const u16* wo; char* ws; };
constexpr int N_PF = PFB, N_LN = 128, N_WO = 200, N_FILL = 512;
__global__ void __launch_bounds__(256) prep_kernel(PrepParams q) {
    int b = blockIdx.x;
    const int t = threadIdx.x, lane = t & 63, w = t >> 6, fr = lane & 15, g = lane >> 4;
    char* hop = align2m(q.ws + O_HOP);
    if (b < N_LN) {
        const int tok = b * 16 + w * 4 + g;
        u32x4 d[3][2];
        float s = 0.f;
#pragma unroll
        for (int i = 0; i < 3; ++i) {
            const u32x4* a = (const u32x4*)(q.x + (size_t)tok * DM + min(fr + 16 * i, 39) * 16);
            d[i][0] = a[0]; d[i][1] = a[1];
        }
#pragma unroll
        for (int i = 0; i < 3; ++i)
#pragma unroll
            for (int e = 0; e < 8; ++e) if (fr + 16 * i < 40) s += bflo(d[i][e >> 2][e & 3]) + bfhi(d[i][e >> 2][e & 3]);
        const float mu = row16_sum(s) * (1.0f / DM);
        float v = 0.f;
#pragma unroll
        for (int i = 0; i < 3; ++i)
#pragma unroll
            for (int e = 0; e < 8; ++e) if (fr + 16 * i < 40) { const float a = bflo(d[i][e >> 2][e & 3]) - mu, c = bfhi(d[i][e >> 2][e & 3]) - mu; v = fmaf(a, a, fmaf(c, c, v)); }
        const float rs = __builtin_amdgcn_rsqf(row16_sum(v) * (1.0f / DM) + 1e-5f);
#pragma unroll
        for (int i = 0; i < 3; ++i) {
            const int blk = fr + 16 * i;
            if (blk >= 40) continue;
            const size_t o = (size_t)(tok >> 5) * 40960 + ((blk >> 1) * 2 + ((tok >> 4) & 1)) * 1024 + ((blk & 1) * 32 + (tok & 15)) * 16;
            u32 n[8];
#pragma unroll
            for (int e = 0; e < 8; ++e) n[e] = pk2bf((bflo(d[i][e >> 2][e & 3]) - mu) * rs, (bfhi(d[i][e >> 2][e & 3]) - mu) * rs);
            *(u32x4*)(q.ws + O_XR + o) = (u32x4){d[i][0][0], d[i][1][0], d[i][0][1], d[i][1][1]};
            *(u32x4*)(q.ws + O_XR + o + 256) = (u32x4){d[i][0][2], d[i][1][2], d[i][0][3], d[i][1][3]};
            *(u32x4*)(q.ws + O_XN + o) = (u32x4){n[0], n[4], n[1], n[5]};
            *(u32x4*)(q.ws + O_XN + o + 256) = (u32x4){n[2], n[6], n[3], n[7]};
        }
        return;
    }
    b -= N_LN;
    if (b < N_PF) {
        const u32x4* src = (const u32x4*)q.W[b & 3] + (size_t)(b >> 2) * 3840 + t;
        u32x4 v[15];
#pragma unroll
        for (int i = 0; i < 15; ++i) v[i] = src[i * 256];
#pragma unroll
        for (int i = 0; i < 15; ++i) asm volatile("" :: "v"(v[i]));
        return;
    }
    b -= N_PF;
    if (b < N_WO) {
        const int cb = b / 20, kk = b % 20;
        const u16* col = q.wo + cb * 64 + 16 * (fr >> 2) + 4 * w + (fr & 3);
        u32 v[8];
#pragma unroll
        for (int e = 0; e < 8; ++e) {
            const int row = kk < 16 ? (2 * kk + (g >> 1)) * 20 + 5 * (2 * (g & 1) + (e >> 2)) + (e & 3) : (8 * (kk - 16) + 2 * g + (e >> 2)) * 20 + 5 * (e & 3) + 4;
            v[e] = col[(size_t)row * DM];
        }
        *(u32x4*)(q.ws + O_WO + (size_t)cb * 81920 + (kk * 4 + w) * 1024 + lane * 16) = (u32x4){v[0] | v[1] << 16, v[2] | v[3] << 16, v[4] | v[5] << 16, v[6] | v[7] << 16};
        return;
    }
    b -= N_WO;
    const u32x4 SENT = {~0u, ~0u, ~0u, ~0u};
    const u32 i0 = b * 256 + t;
    if (b == 0) ((u32*)(q.ws + O_CTL))[t] = 0;
    if (i0 < 8 * 2 * 320) {
        const int sl = i0 / 640, e = i0 % 320;
        const u32x4 v = *(const u32x4*)(q.mem0 + (e & 7) * DM + ((sl & 1) * 40 + (e >> 3)) * 8);
        st16(hop + M0_OFF + (i0 / 320) * 8192 + e * 16, (u32x4){nosent(v[0]), nosent(v[1]), nosent(v[2]), nosent(v[3])});
    }
    for (u32 i = i0; i < 2 * HOP_BYTES / 16; i += N_FILL * 256) st16(hop + (size_t)i * 16, SENT);
    for (u32 i = i0; i < (O_XN - O_ST) / 16; i += N_FILL * 256) st16(q.ws + O_ST + (size_t)i * 16, SENT);
}

extern "C" void kernel_launch(void* const* d_in, const int* in_sizes, int n_in,
                              void* d_out, int out_size, void* d_ws, size_t ws_size,
                              hipStream_t stream) {
    PrepParams q;
    q.x = (const u16*)d_in[0]; q.mem0 = (const u16*)d_in[1];
    q.wo = (const u16*)d_in[10];
    for (int i = 0; i < 4; ++i) q.W[i] = (const u16*)d_in[2 + i];
    q.ws = (char*)d_ws;
    prep_kernel<<<N_PF + N_LN + N_WO + N_FILL, 256, 0, stream>>>(q);
    LaParams p;
    p.ws = (char*)d_ws; p.out = (u16*)d_out; p.hop = (char*)d_ws;
    p.Wq = (const u16*)d_in[2]; p.Wk = (const u16*)d_in[3]; p.Wv = (const u16*)d_in[4]; p.Wo = (const u16*)d_in[5];
    p.wqkv = (const u16*)d_in[8]; p.wphi = (const u16*)d_in[9];
    (void)hipFuncSetAttribute((const void*)la_kernel, hipFuncAttributeMaxDynamicSharedMemorySize, LA_LDS);
    la_kernel<<<256, 256, LA_LDS, stream>>>(p);
}
```

```cpp
#include <hip/hip_runtime.h>
#include <hip/hip_bf16.h>
#include <stdint.h>
#include <type_traits>

typedef unsigned short u16;
typedef unsigned int u32;
typedef unsigned long long u64;
using bf16x8 = __attribute__((ext_vector_type(8))) short;
using f32x4 = __attribute__((ext_vector_type(4))) float;

constexpr int S_TOK = 2048, DM = 640, NH = 32, DH = 192;
constexpr int MEM = 8, SEG = 32, NSEG = S_TOK / SEG;
constexpr int HD = NH * DH;

__device__ __forceinline__ float bf2f(u16 v) { return __uint_as_float(((u32)v) << 16); }
__device__ __forceinline__ u32 pk2bf(float lo, float hi) {
    typedef __attribute__((ext_vector_type(2))) float f32x2_t;
    typedef __attribute__((ext_vector_type(2))) __bf16 bf16x2_t;
    bf16x2_t r = __builtin_convertvector((f32x2_t){lo, hi}, bf16x2_t);
    return __builtin_bit_cast(u32, r);
}
__device__ __forceinline__ float rbf(float f) { return __uint_as_float(pk2bf(f, f) << 16); }
typedef __attribute__((ext_vector_type(8))) __bf16 bf8;
typedef __attribute__((ext_vector_type(16))) __bf16 bf16v;
typedef __attribute__((ext_vector_type(4))) __bf16 bf4;
typedef __attribute__((ext_vector_type(4))) short s16x4;
using u32x4 = __attribute__((ext_vector_type(4))) u32;
using u32x2 = __attribute__((ext_vector_type(2))) u32;
typedef __attribute__((address_space(1))) u32 gu32;
typedef __attribute__((address_space(1))) u64 gu64;
#define LDS3 __attribute__((address_space(3)))

constexpr int SLOT = 16384;
#define PCSV 3
constexpr int PCS = PCSV, P_SLOT0 = 0, M_SLOT0 = 16 * PCS, S_SLOT = M_SLOT0 + 32, T_SLOT = S_SLOT + 10, N_SLOT = T_SLOT + 2;
constexpr size_t HOP_BYTES = (size_t)N_SLOT * SLOT, M0_OFF = HOP_BYTES;
constexpr size_t PX_BUF = 4 * 32 * 640, PX_BYTES = 8 * PX_BUF;
constexpr int LA_LDS = 155648 + 4096 + 256;
constexpr int SPIN_CAP = 1 << 16;
#define D1I 128
#define D2I 55
#define MG1 14
#define MG2 13
#define ROTC 0
#define DUP 4
#define PACE 6
#define CLAG 42
#define T_PX 62
#define T_X1 70
#define T_OB 175
#define T_TOK 400
#define ST_LAG 6
#define OB_STEP 28
#define OB_POLL 0
#define OB_COLD 20
#define NH_ 3
#define HPRIO 0
#define W0D 15
#define LATEP 2
#define SYNCB 1
#define ARMD 0
#define PXK 8
#define PEEKK 6
#define PEEKB 32
#define B_LAG 65
#define ELATE -20

__device__ __forceinline__ void st16(void* p, u32x4 v) {
    asm volatile("global_store_dwordx4 %0, %1, off sc1\n\ts_nop 2" :: "v"(p), "v"(v) : "memory");
}
typedef __attribute__((address_space(1))) u32x4 gu32x4;
__device__ __forceinline__ u32x4 ld16(const char* base, u32 off, int imm) { return __builtin_amdgcn_global_load_b128((gu32x4*)(base + off + imm), "agent"); }
__device__ __forceinline__ u32 nosent(u32 v) { return min(v, 0xFFFFFFFEu); }
__device__ __forceinline__ u32 rt32() { return (u32)__builtin_amdgcn_s_memrealtime(); }
__device__ __forceinline__ void wait_rt(u32 t) {
    int d = (int)(t - rt32()) - 4;
    if (d > 4096 || d < 0) return;
    while (d > 7) { if (d > 18) __builtin_amdgcn_s_sleep(2); d = max((int)(t - rt32()) - 4, 0); }
    asm volatile("s_bitcmp1_b32 %0, 2\n\ts_cbranch_scc0 1f\n\ts_nop 15\n\ts_nop 7\n1:\n\ts_bitcmp1_b32 %0, 1\n\ts_cbranch_scc0 2f\n\ts_nop 12\n2:\n\t"
                 "s_bitcmp1_b32 %0, 0\n\ts_cbranch_scc0 3f\n\ts_nop 7\n3:" :: "s"(d) : "scc");
}
template <int N> __device__ __forceinline__ void dma_kb(const char* src, u32 lds_dst, u32 lane16) {
#pragma unroll
    for (int i = 0; i < N; i += 4) {
        u32 keep;
        if (N - i >= 4)
            asm volatile("s_mov_b32 %0, m0\n\ts_mov_b32 m0, %3\n\ts_nop 2\n\tglobal_load_lds_dwordx4 %1, %2 sc1\n\tglobal_load_lds_dwordx4 %1, %2 offset:1024 sc1\n\t"
                         "global_load_lds_dwordx4 %1, %2 offset:2048 sc1\n\tglobal_load_lds_dwordx4 %1, %2 offset:3072 sc1\n\ts_mov_b32 m0, %0"
                         : "=&s"(keep) : "v"(lane16), "s"(src + i * 1024), "s"(lds_dst + i * 1024) : "memory");
        else if (N - i == 2)
            asm volatile("s_mov_b32 %0, m0\n\ts_mov_b32 m0, %3\n\ts_nop 2\n\tglobal_load_lds_dwordx4 %1, %2 sc1\n\tglobal_load_lds_dwordx4 %1, %2 offset:1024 sc1\n\ts_mov_b32 m0, %0"
                         : "=&s"(keep) : "v"(lane16), "s"(src + i * 1024), "s"(lds_dst + i * 1024) : "memory");
        else
            asm volatile("s_mov_b32 %0, m0\n\ts_mov_b32 m0, %3\n\ts_nop 2\n\tglobal_load_lds_dwordx4 %1, %2 sc1\n\ts_mov_b32 m0, %0"
                         : "=&s"(keep) : "v"(lane16), "s"(src + i * 1024), "s"(lds_dst + i * 1024) : "memory");
    }
}
template <int OFF> __device__ __forceinline__ void dma_one(const char* src, u32 lds_dst, u32 lane16) {
    asm volatile("s_mov_b32 m0, %2\n\ts_nop 0\n\tglobal_load_lds_dwordx4 %0, %1 offset:%3 sc1" :: "v"(lane16), "s"(src), "s"(lds_dst), "n"(OFF) : "memory");
}
__device__ __forceinline__ void dma_gather(const char* src, u32 lds_dst, u32 off) {
    u32 keep;
    asm volatile("s_mov_b32 %0, m0\n\ts_mov_b32 m0, %3\n\ts_nop 2\n\tglobal_load_lds_dwordx4 %1, %2\n\ts_mov_b32 m0, %0"
                 : "=&s"(keep) : "v"(off), "s"(src), "s"(lds_dst) : "memory");
}
__device__ __forceinline__ void st16s(char* base, u32 off, u32x4 v) {
    asm volatile("s_nop 4\n\tglobal_store_dwordx4 %0, %1, %2 sc1\n\ts_nop 2" :: "v"(off), "v"(v), "s"(base) : "memory");
}
__device__ __forceinline__ void arm16(char* base, u32 off) {
    u32x4 v;
    asm volatile("v_mov_b64 %0, -1\n\tv_mov_b64 %1, -1" : "=v"(((u64*)&v)[0]), "=v"(((u64*)&v)[1]));
    st16s(base, off, v);
}
template <int B, int E, typename F> __device__ __forceinline__ void static_for(F&& f) {
    if constexpr (B < E) { f(std::integral_constant<int, B>()); static_for<B + 1, E>(f); }
}
#define CI(x) decltype(x)::value
#define RAW_BARRIER() do { asm volatile("s_waitcnt lgkmcnt(0)" ::: "memory"); __builtin_amdgcn_s_barrier(); asm volatile("" ::: "memory"); } while (0)
#define VM0() asm volatile("s_waitcnt vmcnt(0)" ::: "memory")
#define SCHEDB() __builtin_amdgcn_sched_barrier(0)
__device__ __forceinline__ bf8 lo8(const bf16v& v) { return __builtin_shufflevector(v, v, 0, 1, 2, 3, 4, 5, 6, 7); }
__device__ __forceinline__ bf8 hi8(const bf16v& v) { return __builtin_shufflevector(v, v, 8, 9, 10, 11, 12, 13, 14, 15); }
__device__ __forceinline__ bf16v cat8(bf8 a, bf8 b) { return __builtin_shufflevector(a, b, 0, 1, 2, 3, 4, 5, 6, 7, 8, 9, 10, 11, 12, 13, 14, 15); }
#define MMA_VV(c, a, b) asm volatile("v_mfma_f32_16x16x32_bf16 %0, %1, %2, %0" : "+v"(c) : "v"(a), "v"(b))
#define MMA_AV(c, a, b) asm volatile("v_mfma_f32_16x16x32_bf16 %0, %1, %2, %0" : "+v"(c) : "a"(a), "v"(b))
#define MMA_VA(c, a, b) asm volatile("v_mfma_f32_16x16x32_bf16 %0, %1, %2, %0" : "+v"(c) : "v"(a), "a"(b))
#define MMA0_VV(c, a, b) asm volatile("v_mfma_f32_16x16x32_bf16 %0, %1, %2, 0" : "=&v"(c) : "v"(a), "v"(b))
#define MMA0_AV(c, a, b) asm volatile("v_mfma_f32_16x16x32_bf16 %0, %1, %2, 0" : "=&v"(c) : "a"(a), "v"(b))
#define MMA0_VA(c, a, b) asm volatile("v_mfma_f32_16x16x32_bf16 %0, %1, %2, 0" : "=&v"(c) : "v"(a), "a"(b))
#define MMA16(c, a, b) asm volatile("v_mfma_f32_16x16x16_bf16 %0, %1, %2, %0" : "+v"(c) : "v"(a), "v"(b))
#define MMA16_0(c, a, b) asm volatile("v_mfma_f32_16x16x16_bf16 %0, %1, %2, 0" : "=&v"(c) : "v"(a), "v"(b))
#define SMM_V(c, a, b, i) asm volatile("v_smfmac_f32_16x16x64_bf16 %0, %1, %2, %3" : "+v"(c) : "v"(a), "v"(b), "v"(i))
#define SMM_A(c, a, b, i) asm volatile("v_smfmac_f32_16x16x64_bf16 %0, %1, %2, %3" : "+v"(c) : "v"(a), "a"(b), "v"(i))
#define NOPV() asm volatile("s_nop 1")
#define PIN(x) asm volatile("" : "+v"(x))
#define SETTLE() asm volatile("s_nop 7\n\ts_nop 3")
__device__ __forceinline__ bf8 pack8(f32x4 a, f32x4 b) {
    u32x4 r = {pk2bf(a[0], a[1]), pk2bf(a[2], a[3]), pk2bf(b[0], b[1]), pk2bf(b[2], b[3])};
    return __builtin_bit_cast(bf8, r);
}
__device__ __forceinline__ u32 max4(u32 m, u32x4 g) { m = max(max(m, g[0]), g[1]); return max(max(m, g[2]), g[3]); }
#define ZERO4(c) asm volatile("v_mov_b64 %0, 0\n\tv_mov_b64 %1, 0" : "=v"(((u64*)&(c))[0]), "=v"(((u64*)&(c))[1]))
__device__ __forceinline__ bf8 lds8(const char* shm, int off) { return *(const bf8*)(shm + off); }
__device__ __forceinline__ bf16v ldfrag(const char* p) { return cat8(*(const bf8*)p, *(const bf8*)(p + 1024)); }
template <int CTRL> __device__ __forceinline__ float fdpp(float x) {
    return __builtin_bit_cast(float, __builtin_amdgcn_mov_dpp(__builtin_bit_cast(int, x), CTRL, 0xf, 0xf, true));
}
__device__ __forceinline__ float halfsum(float a, float b) {
    auto r = __builtin_amdgcn_permlane32_swap(__float_as_uint(a), __float_as_uint(b), false, false);
    return __uint_as_float(r[0]) + __uint_as_float(r[1]);
}
__device__ __forceinline__ float grp_sum(float x) {
    auto t = __builtin_amdgcn_permlane16_swap(__float_as_uint(x), __float_as_uint(x), false, false);
    x = __uint_as_float(t[0]) + __uint_as_float(t[1]);
    return halfsum(x, x);
}
__device__ __forceinline__ float grp_max(float x) {
    auto t = __builtin_amdgcn_permlane16_swap(__float_as_uint(x), __float_as_uint(x), false, false);
    x = fmaxf(__uint_as_float(t[0]), __uint_as_float(t[1]));
    auto u = __builtin_amdgcn_permlane32_swap(__float_as_uint(x), __float_as_uint(x), false, false);
    return fmaxf(__uint_as_float(u[0]), __uint_as_float(u[1]));
}
__device__ __forceinline__ float bflo(u32 v) { return __uint_as_float(v << 16); }
__device__ __forceinline__ float bfhi(u32 v) { return __uint_as_float(v & 0xffff0000u); }
__device__ __forceinline__ void st4_lane0(char* base, u32 v) {
    u32 z, d;
    base = (char*)(((u64)(u32)__builtin_amdgcn_readfirstlane((u32)((u64)base >> 32)) << 32) | (u64)(u32)__builtin_amdgcn_readfirstlane((u32)(u64)base));
    asm volatile("s_mov_b64 exec, 1\n\tv_mov_b32 %0, 0\n\tv_mov_b32 %1, %3\n\ts_nop 1\n\tglobal_store_dword %0, %1, %2 sc1\n\ts_mov_b64 exec, -1" : "=&v"(z), "=&v"(d) : "s"(base), "s"(v) : "memory");
}
__device__ __forceinline__ void stamp2(char* hop, const int line) {
    const u32 t = nosent(rt32());
    st4_lane0(hop + (T_SLOT + 1) * SLOT + line * 128, t); st4_lane0(hop + (T_SLOT + 1) * SLOT + 8192 + line * 128, t);
}
__device__ __forceinline__ int track(int d, int sample) {
    if (sample <= 0) return d;
    return sample >= d ? min(sample, d + DUP) : (d - sample > 2 ? d - ((d - sample) >> 1) : d);
}
__device__ __forceinline__ u32 sched_peek(const char* p) { return __hip_atomic_load((const gu32*)p, __ATOMIC_RELAXED, __HIP_MEMORY_SCOPE_AGENT); }
__device__ __forceinline__ u32 sched_get(u32 v, const char* p, int& cap) {
    for (int spin = 0; v == ~0u; ++spin) {
        if (spin >= cap) { cap = 1; break; }
        __builtin_amdgcn_s_sleep(3);
        v = sched_peek(p);
    }
    return __builtin_amdgcn_readfirstlane(v);
}

#define TRDECL
#define TR(i)
#define TRV(i, v)
#define TRFLUSH(role)
#define TRC(i)
constexpr size_t ACT = (size_t)S_TOK * DM * 2;
constexpr size_t O_DBG = 0, O_CTL = 2u << 20, O_ST = O_CTL + 4096, O_FL = O_ST + 128 * 1280, O_PX = O_FL + 3 * 192 * 128, O_X1 = O_PX + PX_BYTES,
                 O_OB = O_X1 + 2 * ACT, O_XN = O_OB + 2 * ACT, O_XR = O_XN + 2 * ACT, O_WO = O_XR + 2 * ACT,
                 O_WVO = O_WO + 10 * 81920, O_HOP = O_WVO + (size_t)80 * 4 * 40 * 2048, WS_BYTES = O_HOP + (4u << 20);
#define TT(role, i)
#define CNT(i)
struct LaParams { char* ws; u16* out; char* hop; const u16* Wq; const u16* Wk; const u16* Wv; const u16* Wo; const u16* wqkv; const u16* wphi; };
__device__ __forceinline__ int kperm(int g, int e) { return 16 * (g >> 1) + 8 * ((e & 3) >> 1) + 4 * (g & 1) + 2 * (e >> 2) + (e & 1); }
__device__ __forceinline__ bf8 tr8(u32 a, u32 b) {
    const s16x4 lo = __builtin_amdgcn_ds_read_tr16_b64_v4i16((LDS3 s16x4*)(uintptr_t)a), hi = __builtin_amdgcn_ds_read_tr16_b64_v4i16((LDS3 s16x4*)(uintptr_t)b);
    return __builtin_bit_cast(bf8, __builtin_shufflevector(lo, hi, 0, 1, 2, 3, 4, 5, 6, 7));
}
__device__ __forceinline__ char* wsp(const LaParams& p) { char* b = p.ws; asm volatile("" : "+s"(b)); return b; }
template <int N> __device__ __forceinline__ void wait_flags(const char* f, const u32 (&off)[N], int& cap) {
    for (int spin = 0;; ++spin) {
        u32 m = 0;
#pragma unroll
        for (int i = 0; i < N; ++i) m = max(m, sched_peek(f + off[i]));
        if (!__any(m == ~0u)) break;
        if (spin >= cap) { cap = 1; break; }
        __builtin_amdgcn_s_sleep(4);
    }
    asm volatile("" ::: "memory");
}
__device__ __forceinline__ void wait_all_tokens(const LaParams& p, u32 lane, int& cap) {
    asm volatile("" : "+v"(lane));
    const u32 off[3] = {lane * 128, lane * 128 + 8192, min(lane + 128, 159u) * 128};
    wait_flags<3>(wsp(p) + O_FL + 192 * 128, off, cap);
}
__device__ __forceinline__ void wait_first_tokens(const LaParams& p, const int set, u32 lane, int& cap) {
    asm volatile("" : "+v"(lane));
    const u32 off[1] = {(set * 20 + min(lane, 39u) + (lane >= 20 ? 20 : 0)) * 128};
    wait_flags<1>(wsp(p) + O_FL, off, cap);
}
__device__ __forceinline__ int get_side(const LaParams& p, int& cap) { return sched_get(sched_peek(wsp(p) + O_FL + 384 * 128), wsp(p) + O_FL + 384 * 128, cap) & 1; }
__device__ __forceinline__ char* align2m(char* p) { return p + ((size_t)(-(intptr_t)p) & ((2u << 20) - 1)); }

constexpr int Q_XA = 0, Q_PART = 20480, Q_TR = 48640, Q_WL = 54784, Q_ST = 153088, Q_PS = 44;

#define DPP1(op, ror) op "_dpp %0, %0, %0 row_ror:" ror " row_mask:0xf bank_mask:0xf\n\t"
#define DPP2(op, ror) DPP1(op, ror) op "_dpp %1, %1, %1 row_ror:" ror " row_mask:0xf bank_mask:0xf\n\ts_nop 0\n\t"
__device__ __forceinline__ void st16m(char* base, u32 off, u32x4 v, u64 mask) {
    asm volatile("s_mov_b64 exec, %3\n\ts_nop 3\n\tglobal_store_dwordx4 %0, %1, %2 sc1\n\ts_mov_b64 exec, -1\n\ts_nop 1" :: "v"(off), "v"(v), "s"(base), "s"(mask) : "memory");
}
__device__ __forceinline__ void st16f(char* base, u32 off, u32x4 v, u64 mask) {
    asm volatile("s_mov_b64 exec, %3\n\tglobal_store_dwordx4 %0, %1, %2 sc1\n\ts_mov_b64 exec, -1\n\ts_nop 1" :: "v"(off), "v"(v), "s"(base), "s"(mask) : "memory");
}
__device__ __forceinline__ void st16f64(char* base, u32 off, u32x4 v, u64 mask) {
    asm volatile("s_mov_b64 exec, %3\n\tglobal_store_dwordx4 %0, %1, %2 offset:64 sc1\n\ts_mov_b64 exec, -1\n\ts_nop 1" :: "v"(off), "v"(v), "s"(base), "s"(mask) : "memory");
}
__device__ __forceinline__ void st32(char* base, u32 off, u32x4 a, u32x4 b) {
    asm volatile("s_nop 4\n\tglobal_store_dwordx4 %0, %1, %3\n\tglobal_store_dwordx4 %0, %2, %3 offset:16\n\ts_nop 2" :: "v"(off), "v"(a), "v"(b), "s"(base) : "memory");
}
__device__ __forceinline__ void st8m(char* base, u32 off, u32x2 v, u64 mask) {
    asm volatile("s_mov_b64 exec, %3\n\ts_nop 3\n\tglobal_store_dwordx2 %0, %1, %2 sc1\n\ts_mov_b64 exec, -1" :: "v"(off), "v"(v), "s"(base), "s"(mask) : "memory");
}
__device__ __forceinline__ u32 pk2bf_abs(float lo, float hi) {
    u32 r;
    asm("v_cvt_pk_bf16_f32 %0, |%1|, |%2|" : "=v"(r) : "v"(lo), "v"(hi));
    return r;
}
__device__ __forceinline__ u32 p_line(const int h, const int piece, const u32 gap) {
    u32 a = (h >> 4) * gap + ((h >> 1) & 7) * 1024 + (h & 1) * 128, b = 2 * gap + h * 128;
    asm volatile("" : "+s"(a), "+s"(b));
    return piece < 4 ? a + piece * 256 : b;
}
constexpr float SM_C = 0.07216878364870322f * 1.4426950408889634f, NEG_BIG = -0x1p100f;
constexpr u64 SM_MASK = 0x0155015501550155ull;
template <typename MID> __device__ __forceinline__ void softmax_mem(const float* p, char* dst, char* dst2, const u32 off, MID&& mid) {
    f32x4 a[4];
#pragma unroll
    for (int i = 0; i < 4; ++i) a[i] = *(const f32x4*)(p + i * 40 * Q_PS);
    SCHEDB(); mid(); SCHEDB();
    f32x4 x = (a[0] + a[1]) + (a[2] + a[3]);
    float ml, mx = fmaxf(fmaxf(x[0], x[1]), fmaxf(x[2], x[3])), e0, e1, e2, e3;
    asm("v_mul_f32 %5, %10, %0\n\ts_nop 0\n\t"
        DPP1("v_max_f32", "8") "v_fma_f32 %1, %6, %10, -%5\n\tv_fma_f32 %2, %7, %10, -%5\n\t"
        DPP1("v_max_f32", "4") "v_fma_f32 %3, %8, %10, -%5\n\tv_fma_f32 %4, %9, %10, -%5\n\t"
        DPP1("v_max_f32", "2") "v_exp_f32 %1, %1\n\tv_exp_f32 %2, %2\n\t"
        DPP1("v_max_f32", "1") "v_exp_f32 %3, %3\n\tv_fma_f32 %5, -%0, %10, %5\n\tv_exp_f32 %4, %4\n\tv_add_f32 %0, %1, %2\n\tv_exp_f32 %5, %5\n\tv_add_f32 %0, %0, %3\n\t"
        "v_add_f32 %0, %0, %4\n\tv_mul_f32 %0, %0, %5\n\tv_mul_f32 %1, %1, %5\n\tv_mul_f32 %2, %2, %5\n\t"
        DPP1("v_add_f32", "8") "v_mul_f32 %3, %3, %5\n\tv_mul_f32 %4, %4, %5\n\t" DPP1("v_add_f32", "4") "s_nop 1\n\t"
        DPP1("v_add_f32", "2") "s_nop 1\n\t" DPP1("v_add_f32", "1") "v_rcp_f32 %0, %0\n\ts_nop 0\n\t"
        "v_mul_f32 %1, %1, %0\n\tv_mul_f32 %2, %2, %0\n\tv_mul_f32 %3, %3, %0\n\tv_mul_f32 %4, %4, %0"
        : "+v"(mx), "=&v"(e0), "=&v"(e1), "=&v"(e2), "=&v"(e3), "=&v"(ml) : "v"(x[0]), "v"(x[1]), "v"(x[2]), "v"(x[3]), "s"(SM_C));
    u32x4 o;
    asm volatile("v_cvt_pk_bf16_f32 %4, |%4|, |%6|\n\tv_cvt_pk_bf16_f32 %5, |%5|, |%7|\n\ts_nop 0\n\t"
                 "v_mov_b32_dpp %0, %4 quad_perm:[0,0,2,2] row_mask:0xf bank_mask:0xf\n\tv_mov_b32_dpp %1, %5 quad_perm:[0,0,2,2] row_mask:0xf bank_mask:0xf\n\t"
                 "v_mov_b32_dpp %2, %4 quad_perm:[1,1,3,3] row_mask:0xf bank_mask:0xf\n\tv_mov_b32_dpp %3, %5 quad_perm:[1,1,3,3] row_mask:0xf bank_mask:0xf"
                 : "=&v"(o[0]), "=&v"(o[1]), "=&v"(o[2]), "=&v"(o[3]), "+v"(e0), "+v"(e2) : "v"(e1), "v"(e3), "s"(dst), "s"(dst2));
    asm volatile("s_mov_b64 exec, %4\n\tglobal_store_dwordx4 %0, %1, %2 sc1\n\tglobal_store_dwordx4 %0, %1, %3 sc1\n\ts_mov_b64 exec, -1\n\ts_nop 1"
                 :: "v"(off), "v"(o), "s"(dst), "s"(dst2), "s"(0x03ff03ff03ff03ffull) : "memory");
}
struct SmTok {
    float x[2][4], mx[2], sm[2], t0, t1;
    u32 off, d00, d01, d10, d11, n0, n1;
    static constexpr int N = 70;
    __device__ __forceinline__ void load(const float* p) {
        f32x4 a[2][4];
#pragma unroll
        for (int n = 0; n < 2; ++n)
#pragma unroll
            for (int i = 0; i < 4; ++i) a[n][i] = *(const f32x4*)(p + (i * 40 + n * 4) * Q_PS);
#pragma unroll
        for (int n = 0; n < 2; ++n) { const f32x4 t = (a[n][0] + a[n][1]) + (a[n][2] + a[n][3]); x[n][0] = t[0]; x[n][1] = t[1]; x[n][2] = t[2]; x[n][3] = t[3]; }
    }
    template <int I> __device__ __forceinline__ void piece(char* dst, int lane, const int h) {
        constexpr int n = I & 1, k = I >> 1;
        float& t = n ? t1 : t0;
#define ROR(op, ror, v) asm volatile(op "_dpp %0, %0, %0 row_ror:" ror " row_mask:0xf bank_mask:0xf" : "+v"(v))
        if constexpr (k == 0) asm volatile("v_max_f32 %0, %1, %2" : "=v"(mx[n]) : "v"(x[n][0]), "v"(x[n][1]));
        else if constexpr (k == 1) asm volatile("v_max3_f32 %0, %0, %1, %2" : "+v"(mx[n]) : "v"(x[n][2]), "v"(x[n][3]));
        else if constexpr (k == 2) ROR("v_max_f32", "8", mx[n]);
        else if constexpr (k == 3) ROR("v_max_f32", "4", mx[n]);
        else if constexpr (k == 4) ROR("v_max_f32", "2", mx[n]);
        else if constexpr (k == 5) ROR("v_max_f32", "1", mx[n]);
        else if constexpr (k == 6) { mx[n] *= SM_C; PIN(mx[n]); }
        else if constexpr (k < 11) { x[n][k - 7] = fmaf(x[n][k - 7], SM_C, -mx[n]); PIN(x[n][k - 7]); }
        else if constexpr (k < 15) asm volatile("v_exp_f32 %0, %0" : "+v"(x[n][k - 11]));
        else if constexpr (k == 15) asm volatile("v_add_f32 %0, %1, %2" : "=v"(sm[n]) : "v"(x[n][0]), "v"(x[n][1]));
        else if constexpr (k == 16) asm volatile("v_add_f32 %0, %1, %2" : "=v"(t) : "v"(x[n][2]), "v"(x[n][3]));
        else if constexpr (k == 17) asm volatile("v_add_f32 %0, %0, %1" : "+v"(sm[n]) : "v"(t));
        else if constexpr (k == 18) ROR("v_add_f32", "8", sm[n]);
        else if constexpr (k == 19) ROR("v_add_f32", "4", sm[n]);
        else if constexpr (k == 20) ROR("v_add_f32", "2", sm[n]);
        else if constexpr (k == 21) ROR("v_add_f32", "1", sm[n]);
        else if constexpr (k == 22) asm volatile("v_rcp_f32 %0, %0" : "+v"(sm[n]));
        else if constexpr (k < 27) asm volatile("v_mul_f32 %0, %0, %1" : "+v"(x[n][k - 23]) : "v"(sm[n]));
        else if constexpr (k == 27) asm volatile("v_cvt_pk_bf16_f32 %0, |%1|, |%2|" : "=v"(n ? d10 : d00) : "v"(x[n][0]), "v"(x[n][1]));
        else if constexpr (k == 28) asm volatile("v_cvt_pk_bf16_f32 %0, |%1|, |%2|" : "=v"(n ? d11 : d01) : "v"(x[n][2]), "v"(x[n][3]));
        else if constexpr (I == 58) { asm volatile("" : "+v"(lane)); off = (lane >> 1) & 7; PIN(off); }
        else if constexpr (I == 60) { off = p_line(h, off, 8192); PIN(off); }
        else if constexpr (I == 62) { asm volatile("" : "+v"(lane)); off += lane & 48; PIN(off); }
        else if constexpr (I == 65 || I == 69) {
            u32 c0 = I == 69 ? d10 : d00, c1 = I == 69 ? d11 : d01;
            asm volatile("s_nop 1\n\tv_mov_b32_dpp %0, %2 quad_perm:[1,0,3,2] row_mask:0xf bank_mask:0xf\n\tv_mov_b32_dpp %1, %3 quad_perm:[1,0,3,2] row_mask:0xf bank_mask:0xf"
                         : "=&v"(n0), "=&v"(n1), "+v"(c0), "+v"(c1) : "s"(dst));
            if constexpr (I == 65) st16f(dst, off, (u32x4){c0, c1, n0, n1}, SM_MASK); else st16f64(dst, off, (u32x4){c0, c1, n0, n1}, SM_MASK);
        }
    }
};

__host__ __device__ constexpr int qk_slot(int T, int ks) { return ks < 5 ? ks * 6 + T : (T < 4 ? 30 + (T >> 1) * 10 + (ks - 5) * 2 + (T & 1) : 25 + T * 5 + ks); }
__host__ __device__ constexpr int lds_slot(int n) { return n < 7 ? 5 * n + 2 : (n == 7 ? 38 : (n == 8 ? 41 : (n == 9 ? 46 : (n == 10 ? 52 : 57)))); }
__host__ __device__ constexpr int slot_lds(int sl) { for (int n = 0; n < 12; ++n) if (lds_slot(n) == sl) return n; return -1; }
__host__ __device__ constexpr int slot_frag(int sl) { if (slot_lds(sl) >= 0) return 48 + slot_lds(sl); int n = 0; for (int i = 0; i < 12; ++i) n += lds_slot(i) < sl; return sl - n; }
__host__ __device__ constexpr int qk_frag(int T, int ks) { return slot_frag(qk_slot(T, ks)); }
__host__ __device__ constexpr int slot_T(int sl) { return sl < 30 ? sl % 6 : (sl < 50 ? ((sl - 30) / 10) * 2 + (sl & 1) : (sl - 30) / 5); }
__host__ __device__ constexpr int slot_ks(int sl) { return sl < 30 ? sl / 6 : (sl < 50 ? 5 + ((sl - 30) % 10) / 2 : 5 + sl % 5); }
__host__ __device__ constexpr int lds_rank(int T, int ks) { int n = 0; for (int t = 0; t < T; ++t) n += qk_frag(t, ks) >= 48; return n; }
__host__ __device__ constexpr int lds_nth(int ks, int n) { for (int t = 0; t < 6; ++t) if (qk_frag(t, ks) >= 48 && lds_rank(t, ks) == n) return qk_frag(t, ks) - 48; return -1; }

__device__ __forceinline__ void qk_role(const LaParams& p, const int set, const int h, char* shm) {
    const int lane = threadIdx.x & 63, w = __builtin_amdgcn_readfirstlane(threadIdx.x >> 6);
    const int fr = lane & 15, g = lane >> 4;
    const u32 lds0 = (u32)(uintptr_t)(LDS3 char*)shm;
    const int sidx = (lane & 8) ? 0xEEEE : 0x4444;
    int cap = SPIN_CAP;
    char* mine = p.hop;
    int sch = (S_SLOT + w) * SLOT;
    int phys = 0;
    char* px = (wsp(p) + O_PX);
    const char* xn = (wsp(p) + O_XN);
    char* wl = shm + Q_WL + w * 24576 + lane * 16;
    const char* xa = shm + Q_XA + lane * 16;

    bf16v WA[32], WV[16];
    {
        bf16v WT[12];
        u32 vo[6], rd[3];
#pragma unroll
        for (int i = 0; i < 6; ++i) {
            const int id = i * 64 + lane, r = w * 16 + id / 24;
            vo[i] = (32 * (r >> 5) + kperm((r >> 2) & 3, ((r >> 4) & 1) * 4 + (r & 3))) * (HD * 2) + ((id % 24) ^ (r & 6)) * 16;
        }
#pragma unroll
        for (int i = 0; i < 3; ++i) rd[i] = lds0 + (g * 4 + (fr >> 2)) * 384 + ((w * 3 + i) ^ ((g & 1) * 2 + (fr >> 3))) * 32 + (fr & 3) * 8;
        auto issue = [&](const int j) {
            const char* src = (const char*)((j & 1) ? p.Wk : p.Wq) + (size_t)(j >> 1) * 64 * HD * 2 + h * 384;
#pragma unroll
            for (int i = 0; i < 6; ++i) dma_gather(src, lds0 + (j % 6) * 24576 + w * 6144 + i * 1024, vo[i]);
        };
#pragma unroll
        for (int j = 0; j < 5; ++j) issue(j);
        static_for<0, 20>([&](auto J) {
            constexpr int j = CI(J);
            asm volatile("s_waitcnt vmcnt(%0)" :: "n"((19 - j < 4 ? 19 - j : 4) * 6) : "memory");
            RAW_BARRIER();
            if constexpr (j + 5 < 20) issue(j + 5);
            static_for<0, 3>([&](auto PR) {
                constexpr int id = qk_frag(2 * CI(PR) + (j & 1), j >> 1);
                u32 a = rd[CI(PR)] + (j % 6) * 24576;
                asm volatile("" : "+v"(a));
                const bf16v f = cat8(tr8(a, a + 16 * 384), tr8(a + 32 * 384, a + 48 * 384));
                if constexpr (id < 32) WA[id] = f; else if constexpr (id < 48) WV[id - 32] = f; else WT[id - 48] = f;
            });
        });
        RAW_BARRIER();
        dma_kb<5>(xn + (size_t)set * 40960 + w * 5120, lds0 + Q_XA + w * 5120, lane * 16);
#pragma unroll
        for (int i = 0; i < 12; ++i) { *(bf8*)(wl + i * 2048) = lo8(WT[i]); *(bf8*)(wl + i * 2048 + 1024) = hi8(WT[i]); }
    }
    TRDECL;
    bf8 QX[2], KX[2];
    u32x2 QY[2], KY[2];
    u32 peek = ~0u;
    int d1 = D1I, d2 = D2I, k1 = D1I + MG1;
    if (w == 3) *(u64*)(shm + Q_ST + lane * 8) = 0;
    SmTok smt;
    auto rearm = [&](const int rot, const int which, const int i, u32& ao, u32x4& v) {
        if (i == 0) { ao = lane; asm volatile("" : "+v"(ao)); ao >>= 3; PIN(ao); }
        if (i == 2) { ao = p_line(h, ao, which ? 8192 : SLOT); PIN(ao); }
        if (i == 4) { u32 la = lane * 16; asm volatile("" : "+v"(la)); ao += la & 127; PIN(ao); }
        if (i == 6) asm volatile("v_mov_b64 %0, -1\n\tv_mov_b64 %1, -1" : "=v"(((u64*)&v)[0]), "=v"(((u64*)&v)[1]));
        if (i == 9) st16m(which ? px + (size_t)(set * 4 + ((rot + 2) & 3)) * PX_BUF + w * 20480 : mine + (P_SLOT0 + (((rot + 3) & 3) * 4 + w) * PCS) * SLOT, ao, v, 0xffffffffffull);
    };
    auto xproj = [&](const int snext, const int sprev, auto SOFT, char* xdst) {
        f32x4 acc[6][2];
        TR(8);
        const char* xs1 = xn + (size_t)(snext - 2) * 40960 + 20480 + w * 5120;
        const char* xs0 = xn + (size_t)min(snext, NSEG - 1) * 40960 + w * 5120;
        u32 xl0 = lds0 + Q_XA + w * 5120, xl1 = xl0 + 20480, xl0b = xl0 + 4096, xl1b = xl1 + 4096;
        const char* xs0b = xs0 + 4096;
        const char* xs1b = xs1 + 4096;
        asm volatile("" : "+s"(xs0), "+s"(xs1), "+s"(xs0b), "+s"(xs1b), "+s"(xl0), "+s"(xl1), "+s"(xl0b), "+s"(xl1b));
        bf8 x0 = *(const bf8*)(xa), x1 = *(const bf8*)(xa + 1024), lw[2];
        lw[0] = *(const bf8*)(wl);
        if constexpr (CI(SOFT)) RAW_BARRIER();
        auto pack = [&](const int T, const int tt) {
            PIN(acc[T][tt]);
            const u32 a = pk2bf(acc[T][tt][0], acc[T][tt][1]), b = pk2bf(acc[T][tt][2], acc[T][tt][3]);
            if (T < 4) { u32* d = (u32*)((T & 1) ? &KX[tt] : &QX[tt]) + (T & 2); d[0] = a; d[1] = b; }
            else if (T == 4) QY[tt] = (u32x2){a, b}; else KY[tt] = (u32x2){a, b};
            SCHEDB();
        };
        u32 ao;
        u32x4 sent;
        static_for<0, 20>([&](auto K) {
            constexpr int k = CI(K), l0 = lds_nth(((k + 1) >> 1) % 10, 0), l1 = lds_nth(((k + 1) >> 1) % 10, 1);
            bf8 n0, n1, nw[2];
            if constexpr (k == 9) {
                TR(9);
                if constexpr (CI(SOFT)) asm volatile("s_waitcnt vmcnt(4)" ::: "memory"); else VM0();
                RAW_BARRIER();
                TR(10);
            }
            static_for<0, 6>([&](auto TT) {
                constexpr int T = CI(TT), id = qk_frag(T, k >> 1), rk = lds_rank(T, k >> 1);
                if constexpr (k < 19) {
                    if constexpr (T == 0) n0 = *(const bf8*)(xa + (k + 1) * 2048);
                    if constexpr (T == 1) n1 = *(const bf8*)(xa + (k + 1) * 2048 + 1024);
                    if constexpr (T == 2 && l0 >= 0) nw[0] = *(const bf8*)(wl + l0 * 2048 + ((k + 1) & 1) * 1024);
                    if constexpr (T == 3 && l1 >= 0) nw[1] = *(const bf8*)(wl + l1 * 2048 + ((k + 1) & 1) * 1024);
                }
                if constexpr (k < 3 && (T == 3 || T == 5) && 2 * k + (T == 5) < 5)
                    dma_one<((2 * k + (T == 5)) & 3) * 1024>(k < 2 ? xs1 : xs1b, k < 2 ? xl1 : xl1b, lane * 16);
                if constexpr (k >= 9 && k < 14 && T == 4)
                    dma_one<((k - 9) & 3) * 1024>(k < 13 ? xs0 : xs0b, k < 13 ? xl0 : xl0b, lane * 16);
                if constexpr (k == 18 && T == 4) peek = sched_peek(mine + sch + max(sprev, 0) * 128 + 4);
                SCHEDB();
                static_for<0, 2>([&](auto U) {
                    constexpr int u = CI(U);
                    const bf8 xx = u ? x1 : x0;
                    if constexpr (id < 32) {
                        const bf8 wf = (k & 1) ? hi8(WA[id]) : lo8(WA[id]);
                        if constexpr (k == 0) MMA0_AV(acc[T][u], wf, xx); else MMA_AV(acc[T][u], wf, xx);
                    } else {
                        bf8 wf;
                        if constexpr (id < 48) wf = (k & 1) ? hi8(WV[id - 32]) : lo8(WV[id - 32]); else wf = lw[rk];
                        if constexpr (k == 0) MMA0_VV(acc[T][u], wf, xx); else MMA_VV(acc[T][u], wf, xx);
                    }
                    SCHEDB();
                    if constexpr (CI(SOFT) && k * 12 + T * 2 + u < SmTok::N) smt.template piece<k * 12 + T * 2 + u>(xdst, lane, h);
                    if constexpr (CI(SOFT) && (k == 6 || k == 7)) rearm((snext >> 1) + 2, k - 6, T * 2 + u, ao, sent);
                    if constexpr (k == 19 && T >= 2) pack(T - 2, u);
                    SCHEDB();
                });
            });
            if constexpr (k < 19) { x0 = n0; x1 = n1; if constexpr (l0 >= 0) lw[0] = nw[0]; if constexpr (l1 >= 0) lw[1] = nw[1]; }
        });
        TR(11);
        pack(4, 0); pack(4, 1);
        asm volatile("s_nop 3");
        pack(5, 0); pack(5, 1);
    };
    {
        const f32x4 neg = {NEG_BIG, NEG_BIG, NEG_BIG, NEG_BIG};
        *(f32x4*)((float*)(shm + Q_PART) + (w * 40 + 8 + fr) * Q_PS + g * 8 + 4) = neg;
        if (lane < 40) *(f32x4*)((float*)(shm + Q_PART) + (w * 40 + lane) * Q_PS + 40) = neg;
    }
    float* pw0 = (float*)(shm + Q_PART) + (w * 40 + (fr & 7)) * Q_PS + g * 8 + (fr >> 3) * 4;
    float* pw2 = fr < 8 && g >= 2 ? (float*)(shm + Q_PART) + (w * 40 + fr) * Q_PS + 24 + g * 4 : (float*)(shm + Q_TR + w * 1536) + lane * 4;
    const float* prd = (const float*)(shm + Q_PART) + ((w & 1) * 4 + g) * Q_PS + min(fr, 10) * 4;
    u32 trw = lds0 + Q_TR + w * 1536 + lane * 8, trr = lds0 + Q_TR + w * 1536 + (fr & 3) * 128 + (4 * g + (fr >> 2)) * 8;
    u32 trh = lds0 + Q_TR + w * 1536 + 1024 + (lane & 31) * 8 + (lane >> 5) * 4;
    u32 pw3 = lds0 + Q_PART + ((w * 40 + 8 + (lane >> 5) * 16 + fr) * Q_PS + 32 + (g & 1) * 4) * 4;
    asm volatile("" : "+v"(trw), "+v"(trr), "+v"(trh), "+v"(pw3));
    const u32 poff = p_line(h, fr >> 1, SLOT) + (w & 1) * 64 + g * 16 + (fr & 1) * (PCS * SLOT);

    for (int layer = 0; layer < 2; ++layer) {
    if (layer > 0) {
        if (w == 0) {
            __builtin_amdgcn_s_sleep(127);
            wait_rt(sched_get(~0u, mine + (T_SLOT + 1) * SLOT + 50 * 128, cap) + T_TOK);
            wait_first_tokens(p, set, lane, cap);
        }
        RAW_BARRIER();
        sch += 5 * SLOT; xn += ACT;
        TT(set * 128 + h, 1);
        dma_kb<5>(xn + (size_t)set * 40960 + w * 5120, lds0 + Q_XA + w * 5120, lane * 16);
    }
    VM0(); RAW_BARRIER();
    TT(set * 128 + h, 2);
    xproj(set + 2, set - 1, std::integral_constant<int, 0>(), nullptr);
    if (layer == 0) { phys = set ^ get_side(p, cap); mine += phys * 8192; }
    TT(set * 128 + h, 3);
    for (int s = set; s < NSEG; s += 2) {
        const int rot = (s >> 1) & 3;
        TR(0);
        u32 ytime = 0;
        const char* mb0 = (s > 0 ? mine + (M_SLOT0 + rot * 8) * SLOT : p.hop + M0_OFF + phys * 8192) + 2 * w * SLOT + 2048;
        const char* mb1 = mb0 + SLOT;
        char* pdst = mine + (P_SLOT0 + rot * 4 * PCS) * SLOT;
        char* pdst2 = pdst + 2 * PCS * SLOT;
        asm volatile("" : "+s"(mb0), "+s"(mb1), "+s"(pdst), "+s"(pdst2));
        if (s > 0) { ytime = sched_get(peek, mine + sch + (s - 1) * 128 + 4, cap);
            if (!SYNCB || w == 0) wait_rt(ytime); }
        __builtin_amdgcn_s_barrier();
        TR(1); TRV(7, ytime);
        f32x4 acc[6];
        u32x4 A[10];
        bf8 Ll[2], Lh[2];
        s16x4 F[4];
        u32 r0[4], r1[4], d[2];
        float v[4];
        int tries = 0;
        auto fin = [&](const int pr, const int i) {
            if (i == 0) { PIN(acc[2 * pr]); PIN(acc[2 * pr + 1]); }
            if (i < 4) { auto r = __builtin_amdgcn_permlane32_swap(__float_as_uint(acc[2 * pr][i]), __float_as_uint(acc[2 * pr + 1][i]), false, false); r0[i] = r[0]; r1[i] = r[1]; }
            const int a0 = pr == 0 ? i - 4 : i - 1;
            if (a0 >= 0 && a0 < 4) { asm("v_add_f32 %0, %1, %2" : "=v"(v[a0]) : "v"(r0[a0]), "v"(r1[a0])); PIN(v[a0]); }
            if (a0 == 3) d[0] = pk2bf(v[0], v[1]);
            if (a0 == 4) {
                d[1] = pk2bf(v[2], v[3]);
                *(LDS3 u32x2*)(uintptr_t)(trw + pr * 512) = (u32x2){d[0], d[1]};
                F[pr] = __builtin_amdgcn_ds_read_tr16_b64_v4i16((LDS3 s16x4*)(uintptr_t)(trr + pr * 512));
            }
        };
        auto fin1 = [&](const int T, const int i) {
            if (i == 0) asm volatile("s_nop 1\n\tv_permlane32_swap_b32 %0, %2\n\tv_permlane32_swap_b32 %1, %3" : "+v"(acc[T][0]), "+v"(acc[T][1]), "+v"(acc[T][2]), "+v"(acc[T][3]));
            else {
                PIN(acc[T]);
                asm volatile("v_add_f32 %0, %2, %4\n\tv_add_f32 %1, %3, %5\n\tv_cvt_pk_bf16_f32 %0, %0, %1" : "=&v"(v[0]), "=&v"(v[1]) : "v"(acc[T][0]), "v"(acc[T][1]), "v"(acc[T][2]), "v"(acc[T][3]));
                *(LDS3 float*)(uintptr_t)(trh + (T - 4) * 256) = v[0];
                F[T - 2] = __builtin_amdgcn_ds_read_tr16_b64_v4i16((LDS3 s16x4*)(uintptr_t)(trr + 1024));
            }
        };
        auto slot = [&](auto SL) {
            constexpr int sl = CI(SL), T = slot_T(sl), ks = slot_ks(sl), id = slot_frag(sl);
            if constexpr (id < 32) SMM_A(acc[T], A[ks], WA[id], sidx);
            else if constexpr (id < 48) SMM_V(acc[T], A[ks], WV[id - 32], sidx);
            else { const bf16v L = cat8(Ll[id & 1], Lh[id & 1]); SMM_V(acc[T], A[ks], L, sidx); }
            SCHEDB();
            if constexpr (sl == 0) TRC(6);
            constexpr int n0 = slot_lds(sl), n1 = slot_lds(sl - 1);
            if constexpr (n0 >= 0 && n0 < 10) Ll[n0 & 1] = *(const bf8*)(wl + (n0 + 2) * 2048);
            if constexpr (n1 >= 0 && n1 < 10) Lh[n1 & 1] = *(const bf8*)(wl + (n1 + 2) * 2048 + 1024);
            if constexpr (sl >= 43 && sl < 52) fin(0, sl - 43);
            if constexpr (sl >= 52 && sl < 58) fin(1, sl - 52);
            if constexpr (sl >= 58) fin1(4, sl - 58);
            SCHEDB();
        };
        auto gather = [&](const bool first) {
            u32 vo = lane * 16;
            asm volatile("" : "+v"(vo) :: "memory");
            f32x4 t[3];
            int df;
            u32 pp;
#pragma unroll
            for (int ks = 0; ks < 10; ++ks) {
                A[ks] = ld16(ks < 5 ? mb0 : mb1, vo, (ks % 5 - 2) * 1024);
                SCHEDB();
                if (first) {
                    if (ks == 0) { df = lane; asm volatile("" : "+v"(df)); df = (df & 15) - (df >> 4) * 4; PIN(df); PIN(QX[0]); PIN(QX[1]); PIN(KX[0]); PIN(KX[1]); PIN(QY[0]); PIN(QY[1]); PIN(KY[0]); PIN(KY[1]); }
                    if (ks == 1) {
#pragma unroll
                        for (int i = 0; i < 4; ++i) t[0][i] = i > df ? NEG_BIG : 0.f;
                        PIN(t[0]);
                    }
                    if (ks == 2) {
                        MMA0_VV(t[1], KX[0], QX[1]);
                        asm volatile("v_mfma_f32_16x16x32_bf16 %0, %1, %2, %3" : "=&v"(t[2]) : "v"(KX[1]), "v"(QX[1]), "v"(t[0]));
                        MMA_VV(t[0], KX[0], QX[0]);
                    }
                    if (ks == 3) { pp = lane; asm volatile("" : "+v"(pp)); pp = lds0 + Q_PART + ((w * 40 + 8 + (pp & 15)) * Q_PS + (pp >> 4) * 8) * 4; asm volatile("s_nop 2" : "+v"(pp)); }
                    if (ks == 4) { MMA16(t[1], KY[0], QY[1]); MMA16(t[2], KY[1], QY[1]); MMA16(t[0], KY[0], QY[0]); }
                    if (ks == 6) { PIN(t[1]); *(LDS3 f32x4*)(uintptr_t)(pp + 16 * Q_PS * 4) = t[1]; }
                    if (ks == 7) { PIN(t[2]); *(LDS3 f32x4*)(uintptr_t)(pp + 16 * Q_PS * 4 + 16) = t[2]; }
                    if (ks == 8) { PIN(t[0]); *(LDS3 f32x4*)(uintptr_t)pp = t[0]; }
                } else if (ks < 9) asm volatile("s_nop %0" :: "n"(PACE));
                if (ks == 5) {
#pragma unroll
                    for (int i = 0; i < 2; ++i) { Ll[i] = *(const bf8*)(wl + i * 2048); Lh[i] = *(const bf8*)(wl + i * 2048 + 1024); }
                    ZERO4(acc[0]); ZERO4(acc[1]);
                }
                if (ks >= 6) ZERO4(acc[ks - 4]);
                SCHEDB();
            }
        };
        gather(true);
        static_for<0, 42>(slot);
        PIN(acc[0]);
        if (__builtin_expect(__any(__builtin_isunordered(acc[0][0], acc[0][1]) || __builtin_isunordered(acc[0][2], acc[0][3])), 0)) {
            SETTLE(); CNT(0);
            while (true) {
                gather(false);
                u32 m = 0;
#pragma unroll
                for (int ks = 0; ks < 10; ++ks) m = max4(m, A[ks]);
                ++tries;
                if (!__any(m == ~0u)) break;
                if (tries >= cap) { cap = 1; break; }
                __builtin_amdgcn_s_sleep(4);
            }
            static_for<0, 42>(slot);
        }
        TR(2); TRV(6, tries + 1);
        static_for<42, 43>(slot);
        if (__builtin_expect(h == 0 && w == 3, 0)) {
            u32 base = (tries == 0 && s > 0) ? ytime : rt32() + ELATE;
            u32 ln8 = lane;
            asm volatile("" : "+v"(ln8));
            ln8 *= 8192;
            const u32x4 rec = {nosent(base + k1), nosent(base + k1 + d2 + MG2), (u32)(d1 + MG1), 0u};
            k1 = d1 + MG1;
            asm volatile("s_mov_b64 exec, 0x3ff\n\ts_nop 3\n\tglobal_store_dwordx4 %0, %1, %2 sc1\n\ts_mov_b64 exec, -1\n\ts_nop 0" :: "v"(ln8), "v"(rec), "s"(p.hop + (S_SLOT + layer * 5) * SLOT + s * 128) : "memory");
        }
        static_for<43, 60>(slot);
        TRC(7);
        bf8 F01 = __builtin_bit_cast(bf8, __builtin_shufflevector(F[0], F[1], 0, 1, 2, 3, 4, 5, 6, 7));
        f32x4 s0, s1, s2, u0, u1;
        PIN(F01);
        MMA0_VV(s1, KX[1], F01); MMA0_VV(s0, KX[0], F01);
        SCHEDB();
        fin1(5, 0); fin1(5, 1);
        SCHEDB();
        MMA16(s1, KY[1], F[2]); MMA16(s0, KY[0], F[2]);
        MMA0_VV(s2, F01, F01); MMA0_VV(u0, F01, QX[0]); MMA0_VV(u1, F01, QX[1]);
        asm volatile("v_mov_b32_dpp %0, %4 row_ror:8 row_mask:0xf bank_mask:0xc\n\tv_mov_b32_dpp %1, %5 row_ror:8 row_mask:0xf bank_mask:0xc\n\t"
                     "v_mov_b32_dpp %2, %6 row_ror:8 row_mask:0xf bank_mask:0xc\n\tv_mov_b32_dpp %3, %7 row_ror:8 row_mask:0xf bank_mask:0xc"
                     : "+v"(s0[0]), "+v"(s0[1]), "+v"(s0[2]), "+v"(s0[3]) : "v"(s1[0]), "v"(s1[1]), "v"(s1[2]), "v"(s1[3]));
        MMA16(s2, F[3], F[3]);
        SCHEDB();
        *(f32x4*)pw0 = s0; SCHEDB();
        MMA16(u0, F[3], QY[0]); MMA16(u1, F[3], QY[1]);
        asm volatile("s_nop 1");
        PIN(s2); *(f32x4*)pw2 = s2; SCHEDB();
        u64 tpub = 0;
        auto uwrite = [&]() {
            PIN(u0); PIN(u1);
            f32x4 u;
#pragma unroll
            for (int i = 0; i < 4; ++i) u[i] = __uint_as_float(__builtin_amdgcn_permlane32_swap(__float_as_uint(u0[i]), __float_as_uint(u1[i]), false, false)[1]);
            *(LDS3 f32x4*)(uintptr_t)pw3 = u;
        };
        if (__builtin_expect(w < 2, 1)) {
            RAW_BARRIER();
            TR(3);
            softmax_mem(prd, pdst, pdst2, poff, uwrite);
            asm volatile("s_memrealtime %0" : "=s"(tpub));
            TR(4);
        } else {
            RAW_BARRIER();
            TR(3);
            uwrite();
            if (h == 0 && w == 3) {
                u32 lo = lane;
                asm volatile("" : "+v"(lo));
                int sta = *(const int*)(shm + Q_ST + lo * 4), stb = *(const int*)(shm + Q_ST + 256 + lo * 4);
                asm("s_nop 1\n\t" DPP2("v_max_i32", "8") DPP2("v_max_i32", "4") DPP2("v_max_i32", "2") DPP2("v_max_i32", "1") : "+v"(sta), "+v"(stb));
                const int m1 = max(__builtin_amdgcn_readlane(stb, 16), __builtin_amdgcn_readlane(stb, 32));
                const int m2 = max(max(max(__builtin_amdgcn_readlane(sta, 0), __builtin_amdgcn_readlane(sta, 16)), max(__builtin_amdgcn_readlane(sta, 32), __builtin_amdgcn_readlane(sta, 48))),
                                   __builtin_amdgcn_readlane(stb, 0));
                d1 = track(d1, m1); d2 = track(d2, m2);
                u32 keep;
                asm volatile("s_mov_b32 %0, m0\n\ts_mov_b32 m0, %4\n\ts_nop 2\n\tglobal_load_lds_dword %1, %2 sc1\n\ts_add_u32 m0, m0, 256\n\ts_nop 0\n\tglobal_load_lds_dword %1, %3 sc1\n\ts_mov_b32 m0, %0"
                             : "=&s"(keep) : "v"(lo * 128), "s"(mine + T_SLOT * SLOT), "s"(mine + (T_SLOT + 1) * SLOT), "s"(lds0 + Q_ST), "s"(d1), "s"(d2) : "memory", "scc");
            }
            if (w == 2 && s < 2 && layer > 0) wait_all_tokens(p, lane, cap);
        }
        RAW_BARRIER();
        asm volatile("" : "+s"(tpub));
        { int ro = (8 + 8 * w - (w & 1) * 4) * Q_PS; asm volatile("" : "+s"(ro)); smt.load(prd + ro); }
        if (w == 1) st4_lane0(mine + (T_SLOT + 1) * SLOT + (16 + h) * 128, tries == 0 && s > 0 ? (u32)tpub - ytime : 0);
        char* xdst = px + (size_t)(set * 4 + rot) * PX_BUF + w * 20480;
        TR(5); TRFLUSH(set * 128 + h);
        if (s + 2 >= NSEG) {
            static_for<0, SmTok::N>([&](auto I) { smt.template piece<CI(I)>(xdst, lane, h); SCHEDB(); if constexpr (CI(I) & 1) asm volatile("s_nop 0"); SCHEDB(); });
            TT(set * 128 + h, 0);
            __builtin_amdgcn_s_sleep(64);
            u32 ao;
            u32x4 sent;
#pragma unroll
            for (int i = 0; i < 20; ++i) rearm(rot, i / 10, i % 10, ao, sent);
            break;
        }
        xproj(s + 4, s + 1, std::integral_constant<int, 1>(), xdst);
    }
    }
}

constexpr int C_YL = 0, C_RED = 40960, C_EX = 49152, C_XN = 53248;
__device__ __forceinline__ void comb_role(const LaParams& p, const int set, const int c, char* shm) {
    const int lane = threadIdx.x & 63, w = __builtin_amdgcn_readfirstlane(threadIdx.x >> 6);
    const int fr = lane & 15, g = lane >> 4;
    const u32 lds0 = (u32)(uintptr_t)(LDS3 char*)shm;
    const int sidx = (lane & 8) ? 0xEEEE : 0x4444;
    int cap = SPIN_CAP;
    char* mine = p.hop;
    char* other = p.hop;
    int phys = 0, sch = (S_SLOT + w) * SLOT;
    u32 k1 = D1I + MG1;
    char* px = (wsp(p) + O_PX);
    const char* xn = (wsp(p) + O_XN);
    char* const yl0 = shm + C_YL + lane * 16;
    int cb = (set * 80 + c) % 10, rb = (set * 80 + c) / 10;

    {
        const int t = threadIdx.x, h = (set * 80 + c) / 5, cu = c % 5;
        u32x4 st[12];
        const u16* src = p.Wo + ((size_t)h * DH + (t >> 4)) * DM + cu * 128 + (t & 15) * 8;
#pragma unroll
        for (int i = 0; i < 12; ++i) asm volatile("global_load_dwordx4 %0, %1, off" : "=v"(st[i]) : "v"(src + (size_t)i * 16 * DM) : "memory");
        u32 vo[12];
#pragma unroll
        for (int i = 0; i < 12; ++i) {
            const int id = i * 64 + lane, r = id / 24;
            vo[i] = kperm((r >> 2) & 3, (r >> 4) * 4 + (r & 3)) * (HD * 2) + ((id % 24) ^ (r & 7)) * 16;
        }
        const u32 ring = lds0 + 57344 + w * 24576;
        auto issue = [&](const int j) {
            const char* a = (const char*)p.Wv + (size_t)(((j + cu) % 5) * 4 + w) * 32 * HD * 2 + h * 384;
#pragma unroll
            for (int i = 0; i < 12; ++i) dma_gather(a, ring + (j & 1) * 12288 + i * 1024, vo[i]);
        };
        SCHEDB();
        issue(0); issue(1);
        const u32 ra = ring + fr * 384 + ((g ^ (fr & 3)) + (fr & 4)) * 16;
        const int e = t >> 4, pr = ((e >> 2) & 1) * 8 + (e >> 3) * 4 + (e & 3);
        asm volatile("s_waitcnt vmcnt(24)" : "+v"(st[0]), "+v"(st[1]), "+v"(st[2]), "+v"(st[3]), "+v"(st[4]), "+v"(st[5]),
                     "+v"(st[6]), "+v"(st[7]), "+v"(st[8]), "+v"(st[9]), "+v"(st[10]), "+v"(st[11]) :: "memory");
#pragma unroll
        for (int i = 0; i < 12; ++i) *(u32x4*)(shm + (i * 16 + pr) * 288 + (t & 15) * 16) = st[i];
        RAW_BARRIER();
        { const int layer = 0; TT(set * 128 + 32 + c, 12); }
        bf8 B[8][6];
        const u32 rd = lds0 + ((g >> 1) * 16 + (g & 1) * 4 + (fr >> 2)) * 288 + (fr & 3) * 8;
#pragma unroll
        for (int n = 0; n < 8; ++n)
#pragma unroll
            for (int k = 0; k < 6; ++k) B[n][k] = tr8(rd + k * 32 * 288 + n * 32, rd + (k * 32 + 8) * 288 + n * 32);
        for (int j = 0; j < 5; ++j) {
            if (j < 4) asm volatile("s_waitcnt vmcnt(12)" ::: "memory"); else asm volatile("s_waitcnt vmcnt(8)" ::: "memory");
            bf8 A[2][6];
            const u32 rj = ra + (j & 1) * 12288;
#pragma unroll
            for (int u = 0; u < 2; ++u)
#pragma unroll
                for (int k = 0; k < 6; ++k) A[u][k] = *(LDS3 const bf8*)(uintptr_t)((rj ^ ((k & 1) * 64)) + u * 6144 + (k >> 1) * 128);
            asm volatile("s_waitcnt lgkmcnt(0)" ::: "memory");
            if (j < 3) issue(j + 2);
            char* dst = wsp(p) + O_WVO + ((size_t)(h * 20 + ((j + cu) % 5) * 4 + w) * 40 + cu * 8 + 4) * 1024;
            asm volatile("" : "+s"(dst));
            f32x4 cc[2][2];
            static_for<0, 9>([&](auto N) {
                constexpr int n = CI(N), q = n - 1;
                u32x4 o;
#pragma unroll
                for (int k = 0; k < 6; ++k) {
                    if constexpr (n < 8) {
#pragma unroll
                        for (int u = 0; u < 2; ++u) { if (k == 0) MMA0_VA(cc[n & 1][u], A[u][k], B[n][k]); else MMA_VA(cc[n & 1][u], A[u][k], B[n][k]); }
                    } else if (k == 0) asm volatile("s_nop 7");
                    SCHEDB();
                    if constexpr (q >= 0) {
                        if (k == 3) { PIN(cc[q & 1][0]); o[0] = pk2bf(cc[q & 1][0][0], cc[q & 1][0][1]); o[1] = pk2bf(cc[q & 1][0][2], cc[q & 1][0][3]); }
                        if (k == 4) { PIN(cc[q & 1][1]); o[2] = pk2bf(cc[q & 1][1][0], cc[q & 1][1][1]); o[3] = pk2bf(cc[q & 1][1][2], cc[q & 1][1][3]); }
                        if (k == 5) __builtin_amdgcn_global_store_b128((gu32x4*)(dst + (u32)(lane * 16) + (q - 4) * 1024), o, "agent");
                    }
                    SCHEDB();
                }
            });
        }
        { const int layer = 0; TT(set * 128 + 32 + c, 13); }
        VM0(); RAW_BARRIER();
        { const int layer = 0; TT(set * 128 + 32 + c, 14); }
        dma_kb<10>(xn + (size_t)set * 40960 + w * 10240, lds0 + C_XN + w * 10240, lane * 16);
        if (w == 0) st4_lane0(wsp(p) + O_FL + (400 + set * 80 + c) * 128, 1u);
        __builtin_amdgcn_s_sleep(8);
        const u32 off[1] = {(u32)(400 + (w * 8 + (lane & 7)) * 5 + (c >> 4)) * 128};
        wait_flags<1>(wsp(p) + O_FL, off, cap);
        { const int layer = 0; TT(set * 128 + 32 + c, 15); }
    }
    bf16v WA[32], WV[8];
    {
        u32 vo = (w * 8 + (fr >> 3)) * 819200 + g * 256 + (fr & 7) * 16;
        asm volatile("" : "+v"(vo));
        auto ldw = [&](const int ks, const int t) {
            const char* a = wsp(p) + O_WVO + (size_t)(t * 1600 + ks * 80 + (c >> 1)) * 1024 + (c & 1) * 128;
            const char* b = a + 40960;
            asm volatile("" : "+s"(b));
            return cat8(__builtin_bit_cast(bf8, ld16(a, vo, 0)), __builtin_bit_cast(bf8, ld16(b, vo, 0)));
        };
#pragma unroll
        for (int i = 0; i < 32; ++i) WA[i] = ldw(i >> 2, i & 3);
#pragma unroll
        for (int i = 0; i < 8; ++i) WV[i] = ldw(8 + (i >> 2), i & 3);
    }
    auto yx = [&](f32x4 (&acc)[4][2], const int sy, auto&& extra) {
        const char* xa = shm + C_XN + ((sy >> 1) & 1) * 40960 + lane * 16;
        const char* xs = (sy + 2 < NSEG ? xn + (size_t)(sy + 2) * 40960 : (wsp(p) + O_WO) + cb * 81920) + w * 10240;
        u32 xl = lds0 + C_XN + (~(sy >> 1) & 1) * 40960 + w * 10240;
        bf8 x0 = *(const bf8*)(xa), x1 = *(const bf8*)(xa + 1024);
        const char* xs3[3] = {xs, xs + 4096, xs + 8192};
        u32 xl3[3] = {xl, xl + 4096, xl + 8192};
        asm volatile("" : "+s"(xs3[0]), "+s"(xs3[1]), "+s"(xs3[2]), "+s"(xl3[0]), "+s"(xl3[1]), "+s"(xl3[2]));
        static_for<0, 20>([&](auto K) {
            constexpr int k = CI(K);
            bf8 n0, n1;
            static_for<0, 4>([&](auto TT_) {
                constexpr int t = CI(TT_), id = (k >> 1) * 4 + t, dn = 2 * (k - 3) + t - 2;
                if constexpr (k < 19 && t == 0) n0 = *(const bf8*)(xa + (k + 1) * 2048);
                if constexpr (k < 19 && t == 1) n1 = *(const bf8*)(xa + (k + 1) * 2048 + 1024);
                if constexpr (t >= 2 && k >= 3 && k < 8) dma_one<(dn & 3) * 1024>(xs3[dn >> 2], xl3[dn >> 2], lane * 16);
                if constexpr (t >= 2) extra(2 * k + t - 2);
                SCHEDB();
                if constexpr (id < 32) {
                    const bf8 wf = (k & 1) ? hi8(WA[id]) : lo8(WA[id]);
                    if constexpr (k == 0) { MMA0_VA(acc[t][0], x0, wf); MMA0_VA(acc[t][1], x1, wf); } else { MMA_VA(acc[t][0], x0, wf); MMA_VA(acc[t][1], x1, wf); }
                } else {
                    const bf8 wf = (k & 1) ? hi8(WV[id - 32]) : lo8(WV[id - 32]);
                    MMA_VV(acc[t][0], x0, wf); MMA_VV(acc[t][1], x1, wf);
                }
                SCHEDB();
            });
            if constexpr (k < 19) { x0 = n0; x1 = n1; }
        });
    };

    for (int layer = 0; layer < 2; ++layer) {
    u32 ynext = ~0u;
    if (layer > 0) k1 = sched_get(~0u, mine + sch - 5 * SLOT + (62 + set) * 128 + 8, cap);
    VM0(); RAW_BARRIER();
    TT(set * 128 + 32 + c, 11);
    if (layer > 0 && w == 0) st4_lane0(wsp(p) + O_FL + (192 + rb * 10 + cb) * 128, 1u);
    {
        f32x4 ya[4][2];
        yx(ya, set, [](int) {});
        asm volatile("s_nop 7");
#pragma unroll
        for (int t = 0; t < 4; ++t) { PIN(ya[t][0]); PIN(ya[t][1]); *(bf8*)(yl0 + (w * 5 + t) * 1024) = pack8(ya[t][0], ya[t][1]); }
        RAW_BARRIER();
    }
    if (layer == 0) { phys = set ^ get_side(p, cap); mine += phys * 8192; other += (phys ^ 1) * 8192; }
    else if (w == 3) wait_all_tokens(p, lane, cap);
    TT(set * 128 + 32 + c, 10);
    for (int s = set; s < NSEG; s += 2) {
        const int rot = (s >> 1) & 3;
        char* yl = yl0 + (rot & 1) * 20480;
        TRDECL; TR(0);
        const char* mb0 = (s > 0 ? mine + (M_SLOT0 + rot * 8) * SLOT : p.hop + M0_OFF + phys * 8192) + 2 * w * SLOT + 2048;
        const char* mb1 = mb0 + SLOT;
        asm volatile("" : "+s"(mb0), "+s"(mb1));
        u32 yt = 0;
        bool late = s == 0;
        if (s > 0) { yt = sched_get(ynext, mine + sch + (s - 1) * 128 + 4, cap); wait_rt(yt + CLAG); }
        auto ldy = [&](const int f) { return *(const bf8*)(yl + (f < 16 ? (f >> 2) * 5 + (f & 3) : (f - 16) * 5 + 4) * 1024); };
        bf8 Y0[20 - 3 * NH_];
        TR(1);
        {
            f32x4 acc[4];
            u32x4 A[10];
            auto gather = [&]() {
                u32 vo = lane * 16;
                asm volatile("" : "+v"(vo) :: "memory");
#pragma unroll
                for (int ks = 0; ks < 10; ++ks) {
                    A[ks] = ld16(ks < 5 ? mb0 : mb1, vo, (ks % 5 - 2) * 1024);
                    SCHEDB();
                    if (ks < 4) ZERO4(acc[ks]);
                    if (ks < 9) asm volatile("s_nop %0" :: "n"(PACE));
                    SCHEDB();
                }
            };
            auto mproj = [&]() {
#pragma unroll
                for (int ks = 0; ks < 10; ++ks) {
#pragma unroll
                    for (int t = 0; t < 4; ++t) { if (ks < 8) SMM_A(acc[t], A[ks], WA[(ks * 4 + t) & 31], sidx); else SMM_V(acc[t], A[ks], WV[(ks * 4 + t) & 7], sidx); }
                }
                asm volatile("s_nop 7");
#pragma unroll
                for (int t = 0; t < 4; ++t) PIN(acc[t]);
            };
            gather();
#pragma unroll
            for (int i = 0; i < 20 - 3 * NH_; ++i) Y0[i] = ldy(i);
            SCHEDB();
            mproj();
            if (__builtin_expect(__any(__builtin_isunordered(acc[0][0], acc[0][1]) || __builtin_isunordered(acc[0][2], acc[0][3])), 0)) {
                CNT(1);
                late = true;
                for (int tries = 1;; ++tries) {
                    gather();
                    u32 m = 0;
#pragma unroll
                    for (int ks = 0; ks < 10; ++ks) m = max4(m, A[ks]);
                    if (!__any(m == ~0u)) break;
                    if (tries >= cap) { cap = 1; break; }
                    __builtin_amdgcn_s_sleep(8);
                }
                mproj();
            }
#pragma unroll
            for (int pr = 0; pr < 2; ++pr) {
                float v[4];
#pragma unroll
                for (int i = 0; i < 4; ++i) v[i] = halfsum(acc[2 * pr][i], acc[2 * pr + 1][i]);
                *(u32x2*)(shm + C_YL + (rot & 1) * 20480 + (w * 5 + 4) * 1024 + ((2 * pr + (g >> 1)) * 16 + fr) * 16 + (g & 1) * 8) = (u32x2){pk2bf(v[0], v[1]), pk2bf(v[2], v[3])};
            }
        }
        RAW_BARRIER();
        TR(2);
        u32 took = 0;
        if (s < NSEG - 1) {
            const char* pb[3];
#pragma unroll
            for (int i = 0; i < 3; ++i) { pb[i] = mine + (P_SLOT0 + (rot * 4 + c / 20) * PCS + i) * SLOT + 4096; asm volatile("" : "+s"(pb[i])); }
            auto ldp = [&](const int f, const u32 vo) { return ld16(pb[f >> 3], vo, ((f & 7) - 4) * 1024); };
            u32 xt;
            auto partial = [&](auto NF, auto F0, f32x4& acc, auto&& lastp) {
                constexpr int nf = CI(NF), f0 = CI(F0);
                bf8 (&Y)[nf] = Y0;
#pragma unroll
                for (int i = 0; i < nf; ++i) PIN(Y[i]);
                u32 vo = lane * 16;
                asm volatile("" : "+v"(vo));
                xt = late ? sched_get(~0u, mine + sch + s * 128, cap) : yt + k1;
                TR(3);
                if (!SYNCB || f0 == 0) wait_rt(xt);
                if (SYNCB) __builtin_amdgcn_s_barrier();
                TRV(7, xt); TRC(6);
                if (W0D > 0 && f0 == 0) asm volatile("s_nop %0" :: "n"(W0D));
                asm volatile("" ::: "memory");
                u32x4 P[nf];
#pragma unroll
                for (int i = 0; i < nf; ++i) { P[i] = ldp(f0 + i, vo); SCHEDB(); }
#pragma unroll
                for (int i = 0; i < nf; ++i) {
                    if (i == nf - LATEP) lastp();
                    if (i == 0) MMA0_VV(acc, Y[i], P[i]); else MMA_VV(acc, Y[i], P[i]);
                    if (i == 0) TRC(7);
                    SCHEDB();
                }
                TRC(8);
            };
            f32x4 acc;
            if (w > 0) {
                bf8 Y[NH_];
                const char* q[NH_];
                u32 vo = lane * 16, ex = lds0 + C_EX + (w - 1) * 1024 + lane * 16;
#pragma unroll
                for (int i = 0; i < NH_; ++i) {
                    const int f = 20 - (4 - w) * NH_ + i;
                    Y[i] = ldy(f);
                    q[i] = pb[0] + (f >> 3) * SLOT + ((f & 7) - 4) * 1024;
                    asm volatile("" : "+s"(q[i]));
                }
#pragma unroll
                for (int i = 0; i < NH_; ++i) PIN(Y[i]);
                asm volatile("" : "+v"(vo), "+v"(ex));
                __builtin_amdgcn_s_barrier();
                TRC(6);
                asm volatile("" ::: "memory");
                u32x4 P[NH_];
#pragma unroll
                for (int i = 0; i < NH_; ++i) { P[i] = ld16(q[i], vo, 0); SCHEDB(); }
#pragma unroll
                for (int i = 0; i < NH_; ++i) { if (i == 0) MMA0_VV(acc, Y[i], P[i]); else MMA_VV(acc, Y[i], P[i]); if (i == 0) TRC(7); }
                TRC(8);
                asm volatile("s_nop 7" : "+v"(acc));
                *(LDS3 f32x4*)(uintptr_t)ex = acc;
                RAW_BARRIER();
            } else {
                const int rn = ((s + 1) >> 1) & 3;
                u32 dst = g * 2 * SLOT + (lane & 7) * 16;
                char* dstb = other + (M_SLOT0 + rn * 8 + c / 40) * SLOT + (c % 40) * 128;
                u32 exa = lds0 + C_EX + lane * 16;
                asm volatile("" : "+s"(dstb), "+v"(dst), "+v"(exa));
                f32x4 h1, h2, h3;
                partial(std::integral_constant<int, 20 - 3 * NH_>(), std::integral_constant<int, 0>(), acc, [&] {
                    __builtin_amdgcn_s_barrier();
                    asm volatile("" ::: "memory");
                    h1 = *(LDS3 const f32x4*)(uintptr_t)exa; h2 = *(LDS3 const f32x4*)(uintptr_t)(exa + 1024); h3 = *(LDS3 const f32x4*)(uintptr_t)(exa + 2048);
                    SCHEDB();
                });
                h1 = (h1 + h2) + h3;
                asm volatile("s_nop 2" : "+v"(acc), "+v"(h1));
                acc += h1;
                float v0, v1;
                auto merge = [&]() {
                    auto a = __builtin_amdgcn_permlane32_swap(__float_as_uint(acc[0]), __float_as_uint(acc[2]), false, false);
                    auto b = __builtin_amdgcn_permlane32_swap(__float_as_uint(acc[1]), __float_as_uint(acc[3]), false, false);
                    v0 = __uint_as_float(a[0]) + fdpp<0x128>(__uint_as_float(a[1]));
                    v1 = __uint_as_float(b[0]) + fdpp<0x128>(__uint_as_float(b[1]));
                };
                merge();
                int tries = 0;
                if (__builtin_expect(__any(__builtin_isunordered(v0, v1)), 0)) {
                    CNT(2);
                    wait_rt(sched_get(~0u, mine + sch + s * 128, cap));
                    while (true) {
                        u32 vo = lane * 16, m = 0;
                        asm volatile("" : "+v"(vo) :: "memory");
                        u32x4 P[20];
#pragma unroll
                        for (int i = 0; i < 20; ++i) P[i] = ldp(i, vo);
                        f32x4 hh[4];
#pragma unroll
                        for (int i = 0; i < 20; ++i) {
                            const bf8 y = ldy(i);
                            const int k = i < 20 - 3 * NH_ ? 0 : (i - 20 + 4 * NH_) / NH_;
                            if (i == 0 || i == 20 - 3 * NH_ || i == 20 - 2 * NH_ || i == 20 - NH_) MMA0_VV(hh[k], y, P[i]); else MMA_VV(hh[k], y, P[i]);
                            m = max4(m, P[i]);
                            SCHEDB();
                        }
                        asm volatile("s_nop 7" : "+v"(hh[0]), "+v"(hh[1]), "+v"(hh[2]), "+v"(hh[3]));
                        acc = hh[0] + ((hh[1] + hh[2]) + hh[3]);
                        ++tries;
                        if (!__any(m == ~0u)) break;
                        if (tries >= cap) { cap = 1; break; }
                        __builtin_amdgcn_s_sleep(4);
                    }
                    merge();
                }
                TRV(6, tries + 1);
                asm volatile("" :: "s"(dstb));
                const u32 dw = nosent(pk2bf(v0, v1));
                auto e = __builtin_amdgcn_permlane32_swap(dw, dw, false, false);
                auto e0 = __builtin_amdgcn_permlane16_swap(e[0], e[0], false, false);
                auto e1 = __builtin_amdgcn_permlane16_swap(e[1], e[1], false, false);
                st16f(dstb, dst, (u32x4){e0[0], e1[0], e0[1], e1[1]}, 0x00ff00ff00ff00ffull);
                TR(4);
                took = tries == 0 && s > 0 ? rt32() - xt : 0;
            }
            if (s == NSEG - 2) RAW_BARRIER();
        } else wait_rt(sched_get(~0u, mine + sch + s * 128, cap) + T_PX);
        auto tokens = [&](auto MORE) {
            f32x4 oa[4], ya[4][2];
            u32x4 P[20];
            u32 vo = lane * 16 + w * 4096, vm = lane * 16 + 16384 + w * 1024;
            asm volatile("" : "+v"(vo), "+v"(vm));
            const char* pb[4];
#pragma unroll
            for (int i = 0; i < 4; ++i) { pb[i] = px + (size_t)(set * 4 + rot) * PX_BUF + i * 20480; asm volatile("" : "+s"(pb[i])); }
            auto ldp = [&](const int i) { P[i] = i % 5 == 4 ? ld16(pb[i / 5], vm, 0) : ld16(pb[i / 5], vo, (i % 5) * 1024); };
            int ln = lane;
            asm volatile("" : "+v"(ln));
            u32 rg, peek, peek2;
            bf8 y[5];
            auto side = [&](const int i) {
                if (i == 4) rg = sched_peek((wsp(p) + O_XR) + layer * ACT + (size_t)s * 40960 + ((c >> 2) * 2 + (w >> 1)) * 1024 + (c & 2) * 256 + (w & 1) * 128 + (c & 1) * 4 + (u32)((ln & 2) * 128 + (ln >> 3) * 16 + (ln & 1) * 8));
                if (i >= 16 && i < 36) ldp(i - 16);
                if (i == 5 && CI(MORE)) peek2 = sched_peek(mine + sch + s * 128 + 8);
                if (i == 36 && CI(MORE)) peek = sched_peek(mine + sch + (s + 1) * 128 + 4);
                if (i >= 35) y[i - 35] = *(const bf8*)(yl + (w * 5 + i - 35) * 1024);
            };
            TRC(9);
            if constexpr (CI(MORE)) yx(ya, s + 2, side);
            else {
#pragma unroll
                for (int i = 0; i < 40; ++i) side(i);
            }
            TRC(10);
            bf8 yn[4];
            auto outx = [&](const bool first) {
                if (!first) {
#pragma unroll
                    for (int i = 0; i < 20; ++i) PIN(P[i]);
                    NOPV();
                }
#pragma unroll
                for (int q = 0; q < 4; ++q)
#pragma unroll
                    for (int j = 0; j < 5; ++j) {
                        if (j == 0) MMA0_VV(oa[q], y[j], P[q * 5 + j]); else MMA_VV(oa[q], y[j], P[q * 5 + j]);
                        if (CI(MORE) && first && q * 5 + j >= 3 && q * 5 + j < 19) {
                            const int e = q * 5 + j - 3;
                            SCHEDB();
                            if ((e & 1) == 0) PIN(ya[e >> 2][(e >> 1) & 1]);
                            ((u32*)&yn[e >> 2])[e & 3] = pk2bf(ya[e >> 2][(e >> 1) & 1][(e & 1) * 2], ya[e >> 2][(e >> 1) & 1][(e & 1) * 2 + 1]);
                            if ((e & 3) == 3) *(bf8*)(yl0 + (~rot & 1) * 20480 + (w * 5 + (e >> 2)) * 1024) = yn[e >> 2];
                            SCHEDB();
                        }
                    }
                asm volatile("s_nop 7");
#pragma unroll
                for (int q = 0; q < 4; ++q) PIN(oa[q]);
            };
            outx(true);
            if (__builtin_expect(__any(__builtin_isunordered(oa[0][0], oa[1][0]) || __builtin_isunordered(oa[2][0], oa[3][0])), 0)) {
                CNT(3);
                for (int tries = 1;; ++tries) {
                    asm volatile("" : "+v"(vo), "+v"(vm) :: "memory");
                    u32 m = 0;
#pragma unroll
                    for (int i = 0; i < 20; ++i) ldp(i);
#pragma unroll
                    for (int i = 0; i < 20; ++i) m = max4(m, P[i]);
                    if (!__any(m == ~0u)) break;
                    if (tries >= cap) { cap = 1; break; }
                    __builtin_amdgcn_s_sleep(8);
                }
                outx(false);
            }
            if constexpr (!CI(MORE)) { if (c == 0 && w == 0) stamp2(p.hop, 48 + layer * 4 + set); }
            const bool own = (fr < 8) == (g < 2);
#pragma unroll
            for (int q = 0; q < 4; q += 2) {
                f32x4 o;
#pragma unroll
                for (int i = 0; i < 4; ++i) { const float t = fdpp<0x128>(oa[q + 1][i]); o[i] = own ? oa[q][i] : t; }
                *(f32x4*)(shm + C_RED + ((w * 2 + (g >> 1)) * 32 + (q + !own) * 8 + (fr & 7)) * 32 + (g & 1) * 16) = o;
            }
            RAW_BARRIER();
            TR(5);
            typedef __attribute__((ext_vector_type(2))) float f32x2;
            const char* rp = shm + C_RED + (ln & 4) * 1024 + w * 256 + (ln >> 3) * 32 + (ln & 3) * 8;
            f32x2 a = (*(const f32x2*)rp + *(const f32x2*)(rp + 1024)) + (*(const f32x2*)(rp + 2048) + *(const f32x2*)(rp + 3072));
            if constexpr (CI(MORE)) { k1 = sched_get(peek2, mine + sch + s * 128 + 8, cap); ynext = __builtin_amdgcn_readfirstlane(peek); asm volatile("" : "+s"(ynext), "+s"(k1)); }
            a[0] += fdpp<0x104>(a[0]); a[1] += fdpp<0x104>(a[1]);
            const u32 y2 = pk2bf(a[0], a[1]), d = nosent(pk2bf(bflo(rg) + bflo(y2), bfhi(rg) + bfhi(y2)));
            const u32x4 o4 = {d, (u32)__builtin_amdgcn_mov_dpp(d, 0x55, 0xf, 0xf, true), (u32)__builtin_amdgcn_mov_dpp(d, 0xaa, 0xf, 0xf, true), (u32)__builtin_amdgcn_mov_dpp(d, 0xff, 0xf, 0xf, true)};
            st16m((wsp(p) + O_X1) + layer * ACT + (size_t)s * 40960 + c * 512 + w * 128, ln * 2, o4, 0x0101010101010101ull);
            if (w == 0) st4_lane0(mine + (T_SLOT + (c >> 6)) * SLOT + (c & 63) * 128, took);
            if (w == 1) {
                u32x4 v;
                asm volatile("v_mov_b64 %0, -1\n\tv_mov_b64 %1, -1" : "=v"(((u64*)&v)[0]), "=v"(((u64*)&v)[1]));
                st16m(other + (M_SLOT0 + (((s + 7) >> 1) & 3) * 8 + c / 40) * SLOT + (c % 40) * 128, (ln >> 4) * 2 * SLOT + (ln & 7) * 16, v, 0x00ff00ff00ff00ffull);
            }
            TRC(11); TRFLUSH(set * 128 + 32 + c);
        };
        if (s + 2 >= NSEG) { tokens(std::integral_constant<int, 0>()); break; }
        tokens(std::integral_constant<int, 1>());
    }
    TT(set * 128 + 32 + c, 0);
    asm volatile("" : "+s"(cb), "+s"(rb));
    dma_kb<10>((wsp(p) + O_WO) + cb * 81920 + 40960 + w * 10240, lds0 + C_XN + 40960 + w * 10240, lane * 16);
    if (w == 0) { const u32 t = sched_get(~0u, mine + (T_SLOT + 1) * SLOT + (50 + layer * 4) * 128, cap); *(u32*)(shm + C_EX) = t; }
    VM0(); RAW_BARRIER();
    wait_rt(__builtin_amdgcn_readfirstlane(*(const u32*)(shm + C_EX)) + T_OB + OB_STEP * w + (layer == 0 ? OB_COLD + 8 * w : 0));
    {
        int ln = lane;
        asm volatile("" : "+v"(ln));
        u32x4 O[20], R[2];
        u32 dw[8];
        auto ready = [&](const int T) {
            const char* ob = (wsp(p) + O_OB) + layer * ACT + (size_t)T * 20480 + 16384;
            const u32 off[1] = {(u32)(ln & 31) * 128};
            wait_flags<1>(ob, off, cap);
        };
        auto load = [&](const int T) {
            const char* ob0 = (wsp(p) + O_OB) + layer * ACT + (size_t)T * 20480 + 4096;
            const char* ob1 = ob0 + 8192;
            const char* ob2 = ob0 + 12288;
            const char* xb = (wsp(p) + O_X1) + layer * ACT + (size_t)(T >> 1) * 40960 + cb * 4096 + (T & 1) * 256;
            u32 vo = ln * 16, vr = (ln >> 4) * 256 + (ln & 15) * 8, vx = (ln >> 4) * 1024 + (ln & 15) * 16;
            asm volatile("" : "+v"(vo), "+v"(vr), "+v"(vx), "+s"(ob0), "+s"(ob1), "+s"(ob2), "+s"(xb) :: "memory");
#pragma unroll
            for (int k = 0; k < 16; ++k) { O[k] = ld16(k < 8 ? ob0 : ob1, vo, ((k & 7) - 4) * 1024); SCHEDB(); }
#pragma unroll
            for (int k = 0; k < 4; ++k) {
                const u64 lo = __hip_atomic_load((const gu64*)(ob2 + vr + k * 1024), __ATOMIC_RELAXED, __HIP_MEMORY_SCOPE_AGENT);
                const u64 hi = __hip_atomic_load((const gu64*)(ob2 + vr + k * 1024 + 128), __ATOMIC_RELAXED, __HIP_MEMORY_SCOPE_AGENT);
                O[16 + k] = (u32x4){(u32)lo, (u32)(lo >> 32), (u32)hi, (u32)(hi >> 32)};
            }
            R[0] = ld16(xb, vx, 0); R[1] = ld16(xb, vx, 512);
            SCHEDB();
        };
        auto gemm = [&]() {
            u32 wa0 = lds0 + C_XN + ln * 16, wa1 = wa0 + 40960;
            asm volatile("" : "+v"(wa0), "+v"(wa1));
            f32x4 acc[4];
            bf8 wf[8];
#pragma unroll
            for (int t = 0; t < 8; ++t) wf[t] = *(LDS3 const bf8*)(uintptr_t)(wa0 + t * 1024);
#pragma unroll
            for (int k = 0; k < 20; ++k) {
                if (k >= 16) { PIN(O[k]); NOPV(); }
#pragma unroll
                for (int t = 0; t < 4; ++t) {
                    if (k == 0) MMA0_VV(acc[t], wf[t], O[k]); else MMA_VV(acc[t], wf[(k & 1) * 4 + t], O[k]);
                    SCHEDB();
                    if (k < 18) wf[(k & 1) * 4 + t] = *(LDS3 const bf8*)(uintptr_t)((k < 8 ? wa0 : wa1) + (((k + 2) % 10) * 4 + t) * 1024);
                    SCHEDB();
                }
            }
            asm volatile("s_nop 7" : "+v"(acc[0]), "+v"(acc[1]), "+v"(acc[2]), "+v"(acc[3]));
            float chk = 0.f;
#pragma unroll
            for (int i = 0; i < 8; ++i) {
                const u32 r = R[i >> 2][i & 3], y = pk2bf(acc[i >> 1][(i & 1) * 2], acc[i >> 1][(i & 1) * 2 + 1]);
                const float a = bflo(r) + bflo(y), b = bfhi(r) + bfhi(y);
                dw[i] = pk2bf(a, b);
                chk += a;
            }
            return __any(chk != chk);
        };
        const int T0 = rb + 16 * w;
        if (OB_POLL) ready(T0);
        load(T0);
        static_for<0, 2>([&](auto PASS) {
            constexpr int pass = CI(PASS);
            const int T = T0 + 64 * pass;
            TT(set * 128 + 32 + c, 1 + pass * 4);
            if (__builtin_expect(gemm(), 0)) {
                for (int tries = 0;; ++tries) {
                    ready(T); load(T);
                    u32 m = max4(max4(0, R[0]), R[1]);
#pragma unroll
                    for (int k = 0; k < 20; ++k) m = max4(m, O[k]);
                    if (!__any(m == ~0u)) break;
                    if (tries >= cap) { cap = 1; break; }
                    __builtin_amdgcn_s_sleep(6);
                }
                gemm();
            }
            TT(set * 128 + 32 + c, 2 + pass * 4);
            if (layer == 1) {
                st32((char*)p.out + (size_t)T * 16 * DM * 2 + cb * 128, (ln & 15) * (DM * 2) + (ln >> 4) * 32, (u32x4){dw[0], dw[1], dw[2], dw[3]}, (u32x4){dw[4], dw[5], dw[6], dw[7]});
            } else {
            float s1 = 0.f, s2 = 0.f;
#pragma unroll
            for (int i = 0; i < 8; ++i) { const float a = bflo(dw[i]), b = bfhi(dw[i]); s1 += a + b; s2 = fmaf(a, a, fmaf(b, b, s2)); }
            s1 = grp_sum(s1); s2 = grp_sum(s2);
            char* stl = (wsp(p) + O_ST) + (size_t)T * 1280;
            st8m(stl + cb * 128, ln * 8, (u32x2){nosent(__float_as_uint(s1)), nosent(__float_as_uint(s2))}, 0xffffull);
            const size_t fo = ACT + (size_t)(T >> 1) * 40960 + (cb * 4 + (T & 1)) * 1024;
            const u32 lo = (ln >> 5) * 2048 + ((ln >> 4) & 1) * 512 + (ln & 15) * 16;
            __builtin_amdgcn_s_sleep(ST_LAG);
            for (int spin = 0;; ++spin) {
                u32 m = 0;
                s1 = 0.f; s2 = 0.f;
#pragma unroll
                for (int i = 0; i < 3; ++i) {
                    const int k = (ln >> 4) + 4 * i;
                    const u64 v = __hip_atomic_load((const gu64*)(stl + min(k, 9) * 128 + (ln & 15) * 8), __ATOMIC_RELAXED, __HIP_MEMORY_SCOPE_AGENT);
                    m = max(m, max((u32)v, (u32)(v >> 32)));
                    if (k < 10) { s1 += __uint_as_float((u32)v); s2 += __uint_as_float((u32)(v >> 32)); }
                }
                if (!__any(m == ~0u)) break;
                if (spin >= cap) { cap = 1; break; }
                __builtin_amdgcn_s_sleep(2);
            }
            TT(set * 128 + 32 + c, 3 + pass * 4);
            s1 = grp_sum(s1); s2 = grp_sum(s2);
            const float mu = s1 * (1.0f / DM), rs = __builtin_amdgcn_rsqf(fmaxf(fmaf(-mu, mu, s2 * (1.0f / DM)), 0.f) + 1e-5f);
            u32 dn[8];
#pragma unroll
            for (int i = 0; i < 8; ++i) dn[i] = pk2bf((bflo(dw[i]) - mu) * rs, (bfhi(dw[i]) - mu) * rs);
            st16s((wsp(p) + O_XN) + fo, lo, (u32x4){dn[0], dn[4], dn[1], dn[5]}); st16s((wsp(p) + O_XN) + fo + 256, lo, (u32x4){dn[2], dn[6], dn[3], dn[7]});
            st16s((wsp(p) + O_XR) + fo, lo, (u32x4){dw[0], dw[4], dw[1], dw[5]}); st16s((wsp(p) + O_XR) + fo + 256, lo, (u32x4){dw[2], dw[6], dw[3], dw[7]});
            TT(set * 128 + 32 + c, 4 + pass * 4);
            if (pass == 0 && w == 0) { VM0(); st4_lane0(wsp(p) + O_FL + (rb * 10 + cb) * 128, 1u); }
            }
            if constexpr (pass == 0) load(T + 64);
        });
    }
    if (layer == 1) break;
    sch += 5 * SLOT; xn += ACT;
    if (w == 0) wait_first_tokens(p, set, lane, cap);
    RAW_BARRIER();
    TT(set * 128 + 32 + c, 9);
    dma_kb<10>(xn + (size_t)set * 40960 + w * 10240, lds0 + C_XN + w * 10240, lane * 16);
    }
}

constexpr int B_PQ = 0, B_IM = 131072, B_CS = 155648, B_IS = 176;
__device__ __forceinline__ void bw_role(const LaParams& p, const int hb, char* shm) {
    const int lane = threadIdx.x & 63, w = __builtin_amdgcn_readfirstlane(threadIdx.x >> 6);
    const int fr = lane & 15, g = lane >> 4;
    int cap = SPIN_CAP;
    if (hb == 0 && w == 0) {
        u32 best[2] = {~0u, ~0u};
        const u32 tc = rt32();
        for (int i = 0; i < 256; ++i) {
#pragma unroll
            for (int sd = 0; sd < 2; ++sd) {
                u64 t0, t1;
                u32 z = 0, one = 1;
                asm volatile("s_memtime %0\n\ts_mov_b64 exec, 1\n\ts_waitcnt lgkmcnt(0)\n\tglobal_atomic_add %2, %3, %2, %4 sc0 sc1\n\ts_waitcnt vmcnt(0)\n\t"
                             "s_memtime %1\n\ts_mov_b64 exec, -1\n\ts_waitcnt lgkmcnt(0)"
                             : "=&s"(t0), "=&s"(t1), "+v"(one) : "v"(z), "s"(p.hop + M0_OFF + sd * 8192 + 6144) : "memory");
                best[sd] = min(best[sd], (u32)(t1 - t0));
            }
            if (rt32() - tc > 1500u) break;
        }
        u32 xcc; asm volatile("s_getreg_b32 %0, hwreg(HW_REG_XCC_ID)" : "=s"(xcc));
        st4_lane0(wsp(p) + O_FL + 384 * 128, ((xcc >> 2) & 1) ^ (best[1] < best[0] ? 1u : 0u));
    }
    bf8 WA[64], WV[16];
    {
        const int t = threadIdx.x;
        float* xs = (float*)(shm + 81920);
        float* ph = xs + 128 * 61;
        for (int i = t; i < 400; i += 256) ph[i] = bf2f(p.wphi[hb * 400 + i]);
        const int j = 4 * w + (fr & 3), G = fr >> 2;
        for (int d0 = 0; d0 < DM; d0 += 128) {
            u32 v15[15];
#pragma unroll
            for (int k = 0; k < 15; ++k) { const int i = t + k * 256; v15[k] = *(const u32*)(p.wqkv + (size_t)(d0 + i / 30) * 1920 + ((i % 30) / 10) * 640 + hb * 20 + (i % 10) * 2); }
            __syncthreads();
#pragma unroll
            for (int k = 0; k < 15; ++k) { const int i = t + k * 256; float* o = xs + (i / 30) * 61 + (i % 30) * 2; o[0] = bflo(v15[k]); o[1] = bfhi(v15[k]); }
            __syncthreads();
            for (int kk = 0; kk < 4; ++kk) {
                float v[8];
                const float* xr = xs + (kk * 32 + g * 8) * 61;
                if (j < 10) {
                    const int m = j / 5, f = 5 * G + j % 5;
#pragma unroll
                    for (int e = 0; e < 8; ++e) v[e] = 0.f;
#pragma unroll 4
                    for (int i = 0; i < 20; ++i) {
                        const float pw = ph[i * 20 + f];
#pragma unroll
                        for (int e = 0; e < 8; ++e) v[e] = fmaf(xr[e * 61 + m * 20 + i], pw, v[e]);
                    }
                } else {
#pragma unroll
                    for (int e = 0; e < 8; ++e) v[e] = j < 15 ? xr[e * 61 + 40 + 5 * G + min(j, 14) - 10] : 1.0f;
                }
                *(u32x4*)(shm + ((d0 / 32 + kk) * 4 + w) * 1024 + lane * 16) = (u32x4){pk2bf(v[0], v[1]), pk2bf(v[2], v[3]), pk2bf(v[4], v[5]), pk2bf(v[6], v[7])};
            }
        }
        __syncthreads();
#pragma unroll
        for (int i = 0; i < 64; ++i) WA[i] = *(const bf8*)(shm + i * 1024 + lane * 16);
#pragma unroll
        for (int i = 0; i < 16; ++i) WV[i] = *(const bf8*)(shm + (64 + i) * 1024 + lane * 16);
        __syncthreads();
    }
    auto proj = [&](f32x4 (&acc)[4], f32x4& sq, auto&& fetch) {
        u32x4 X[20];
#pragma unroll
        for (int k = 0; k < 12; ++k) { X[k] = fetch(k); SCHEDB(); }
#pragma unroll
        for (int k = 0; k < 20; ++k) {
#pragma unroll
            for (int t = 0; t < 4; ++t) {
                if (k == 0) MMA0_AV(acc[t], WA[t], X[k]); else if (k < 16) MMA_AV(acc[t], WA[(k * 4 + t) & 63], X[k]); else MMA_VV(acc[t], WV[(k * 4 + t) & 15], X[k]);
            }
            if (k == 0) MMA0_VV(sq, X[k], X[k]); else MMA_VV(sq, X[k], X[k]);
            SCHEDB();
            if (k < 8) X[k + 12] = fetch(k + 12);
            SCHEDB();
        }
        asm volatile("s_nop 7" : "+v"(acc[0]), "+v"(acc[1]), "+v"(acc[2]), "+v"(acc[3]), "+v"(sq));
    };
    {
        f32x4 t, cs[4];
        u32x4 o1 = {0x3f803f80u, 0x3f803f80u, 0x3f803f80u, 0x3f803f80u};
        asm volatile("s_nop 1" : "+v"(o1));
        proj(cs, t, [&](int) { return o1; });
#pragma unroll
        for (int i = 0; i < 4; ++i) *(f32x4*)(shm + B_CS + i * 1024 + lane * 16) = cs[i];
    }
    char* im = shm + B_IM + w * 6144;
    const u32 imr = (u32)(uintptr_t)(LDS3 char*)im + (4 * g + (fr >> 2)) * B_IS + (fr & 3) * 8;
    typedef __attribute__((ext_vector_type(32))) u32 u32x32;
    for (int layer = 0; layer < 2; ++layer) {
        const char* sch = p.hop + (hb & 1) * 8192 + (S_SLOT + layer * 5 + 4) * SLOT;
        f32x4 kv[3][2];
#pragma unroll
        for (int i = 0; i < 6; ++i) ZERO4(kv[i >> 1][i & 1]);
        u32x32 q5;
        u32 tn = 0;
        for (int it = 0; it < 32; ++it) {
            const int s = 2 * it + (w >> 1);
            if (s + 2 < NSEG) { wait_rt(tn); tn = sched_get(~0u, sch + (s + 1) * 128 + 4, cap); wait_rt(tn + B_LAG); tn += 250; }
            else wait_rt(sched_get(~0u, p.hop + (hb & 1) * 8192 + (T_SLOT + 1) * SLOT + (48 + layer * 4 + (s & 1)) * 128, cap) + T_X1);
            const char* xb = (wsp(p) + O_X1) + layer * ACT + (size_t)s * 40960 + (w & 1) * 256;
            if (it >= 30) TT((hb >> 4) * 128 + 112 + (hb & 15), (it - 30) * 3);
            f32x4 acc[4], sq;
            const char* xk[5];
#pragma unroll
            for (int i = 0; i < 5; ++i) { xk[i] = xb + i * 8192 + 4096; asm volatile("" : "+s"(xk[i])); }
            for (int pass = 0;; ++pass) {
                u32 vo = g * 512 + fr * 16;
                asm volatile("" : "+v"(vo) :: "memory");
                auto fetch = [&](const int k) { return ld16(xk[k >> 2], vo, ((k & 3) - 2) * 2048); };
                proj(acc, sq, fetch);
                if (pass > 0 || !__any(acc[3][3] != acc[3][3])) break;
                CNT(4);
                for (int tries = 0;; ++tries) {
                    asm volatile("" : "+v"(vo) :: "memory");
                    u32 m = 0;
#pragma unroll
                    for (int k = 0; k < 20; k += 10) {
#pragma unroll
                        for (int i = 0; i < 10; ++i) m = max4(m, fetch(k + i));
                        SCHEDB();
                    }
                    if (!__any(m == ~0u)) break;
                    if (tries >= cap) { cap = 1; break; }
                    __builtin_amdgcn_s_sleep(6);
                }
            }
            if (it >= 30) TT((hb >> 4) * 128 + 112 + (hb & 15), (it - 30) * 3 + 1);
            if (s == NSEG - 1 && hb == 5 && w == 3) stamp2(p.hop, 50 + layer * 4);
            const float dsel = (fr & 2) ? ((fr & 1) ? sq[3] : sq[2]) : ((fr & 1) ? sq[1] : sq[0]);
            const float s2 = grp_sum(g == (fr >> 2) ? dsel : 0.f);
            const float mu = acc[3][3] * (1.0f / DM), rs = __builtin_amdgcn_rsqf(fmaxf(fmaf(-mu, mu, s2 * (1.0f / DM)), 0.f) + 1e-5f);
            float v[15];
            f32x4 cs[4];
#pragma unroll
            for (int i = 0; i < 4; ++i) cs[i] = *(const f32x4*)(shm + B_CS + i * 1024 + lane * 16);
#pragma unroll
            for (int j = 0; j < 15; ++j) v[j] = fmaf(-mu, cs[j >> 2][j & 3], acc[j >> 2][j & 3]) * rs;
            u32 ph[2][5];
#pragma unroll
            for (int m = 0; m < 2; ++m) {
                const float* x = v + 5 * m;
                const float mx = grp_max(fmaxf(fmaxf(fmaxf(x[0], x[1]), fmaxf(x[2], x[3])), x[4]));
                const float mn = -grp_max(-fminf(fminf(fminf(x[0], x[1]), fminf(x[2], x[3])), x[4]));
                float e[10], a = 0.f, b = 0.f;
#pragma unroll
                for (int j = 0; j < 5; ++j) {
                    e[j] = __builtin_amdgcn_exp2f((x[j] - mx) * 1.4426950408889634f); a += e[j];
                    e[5 + j] = __builtin_amdgcn_exp2f((mn - x[j]) * 1.4426950408889634f); b += e[5 + j];
                }
                a = __builtin_amdgcn_rcpf(grp_sum(a)); b = __builtin_amdgcn_rcpf(grp_sum(b));
#pragma unroll
                for (int j = 0; j < 5; ++j) { e[j] *= a; e[5 + j] *= b; }
#pragma unroll
                for (int j = 0; j < 5; ++j) ph[m][j] = pk2bf(e[2 * j], e[2 * j + 1]);
            }
            *(u32x4*)(shm + B_PQ + (w * 32 + it) * 1024 + lane * 16) = (u32x4){ph[0][0], ph[0][1], ph[0][2], ph[0][3]};
            q5[it] = ph[0][4];
            char* row = im + fr * B_IS;
            *(u32x2*)(row + g * 8) = (u32x2){ph[1][0], ph[1][1]};
            *(u32x2*)(row + 32 + g * 8) = (u32x2){ph[1][2], ph[1][3]};
            *(u32x2*)(row + 64 + g * 8) = (u32x2){ph[1][4], 0u};
            *(u32x2*)(row + 96 + g * 8) = (u32x2){pk2bf(v[10], v[11]), pk2bf(v[12], v[13])};
            *(u32x2*)(row + 128 + g * 8) = (u32x2){pk2bf(v[14], 1.0f), 0u};
            s16x4 tf[5];
#pragma unroll
            for (int i = 0; i < 5; ++i) tf[i] = __builtin_amdgcn_ds_read_tr16_b64_v4i16((LDS3 s16x4*)(uintptr_t)(imr + i * 32));
#pragma unroll
            for (int i = 0; i < 6; ++i) MMA16(kv[i >> 1][i & 1], tf[i >> 1], tf[3 + (i & 1)]);
            if (it >= 30) TT((hb >> 4) * 128 + 112 + (hb & 15), (it - 30) * 3 + 2);
        }
        asm volatile("s_nop 7" : "+v"(kv[0][0]), "+v"(kv[0][1]), "+v"(kv[1][0]), "+v"(kv[1][1]), "+v"(kv[2][0]), "+v"(kv[2][1]));
#pragma unroll
        for (int i = 0; i < 6; ++i) *(f32x4*)(im + i * 1024 + lane * 16) = kv[i >> 1][i & 1];
        RAW_BARRIER();
        bf8 ka[2];
        u32x2 kb[2];
#pragma unroll
        for (int dt = 0; dt < 2; ++dt) {
            f32x4 t[3];
#pragma unroll
            for (int ft = 0; ft < 3; ++ft) {
                const char* a = shm + B_IM + (ft * 2 + dt) * 1024 + lane * 16;
                t[ft] = (*(const f32x4*)a + *(const f32x4*)(a + 6144)) + (*(const f32x4*)(a + 12288) + *(const f32x4*)(a + 18432));
            }
            ka[dt] = pack8(t[0], t[1]);
            kb[dt] = (u32x2){pk2bf(t[2][0], t[2][1]), pk2bf(t[2][2], t[2][3])};
            PIN(ka[dt]); PIN(kb[dt]);
        }
        char* ob = (wsp(p) + O_OB) + layer * ACT + (size_t)w * 20480 + hb * 512;
        TT((hb >> 4) * 128 + 112 + (hb & 15), 6);
        const u32 rofs = g * 81920 + 16384 - hb * 384 + fr * 8, mofs = (g & 1) * 81920 + (g >> 1) * 256 + fr * 16;
        f32x4 o[2][4][2];
        bf8 qa[2][4];
        u32x2 qb[4];
        auto ldq = [&](const int gr) {
#pragma unroll
            for (int u = 0; u < 4; ++u) qa[gr & 1][u] = *(const bf8*)(shm + B_PQ + (w * 32 + gr * 4 + u) * 1024 + lane * 16);
        };
        auto mm = [&](const int gr, const int k) {
            SCHEDB();
            if (k < 4) { qb[k] = (u32x2){q5[gr * 4 + k], 0u}; PIN(qb[k]); }
            if (k < 8) MMA0_VV(o[gr & 1][k >> 1][k & 1], ka[k & 1], qa[gr & 1][k >> 1]);
            else MMA16(o[gr & 1][(k - 8) >> 1][k & 1], kb[k & 1], qb[(k - 8) >> 1]);
            SCHEDB();
        };
        ldq(0); ldq(1);
        NOPV();
#pragma unroll
        for (int k = 0; k < 16; ++k) mm(0, k);
        static_for<0, 8>([&](auto IT) {
            constexpr int gr = CI(IT);
            auto nx = [&](const int k) { if constexpr (gr < 7) mm(gr + 1, k); else { SCHEDB(); asm volatile("s_nop 0"); SCHEDB(); } };
            if constexpr (gr == 7) asm volatile("s_nop 7");
            u32 d[4][2];
            float x[4];
            nx(0); nx(1);
#pragma unroll
            for (int u = 0; u < 4; ++u) {
                f32x4& a = o[gr & 1][u][0];
                f32x4& b = o[gr & 1][u][1];
                PIN(a); PIN(b);
                float inv = __builtin_amdgcn_rcpf(b[1] + 1e-6f), y0, y1, y2, y3;
                nx(2 + 3 * u);
                asm("v_mul_f32 %0, %4, %8\n\tv_mul_f32 %1, %5, %8\n\tv_mul_f32 %2, %6, %8" : "=&v"(y0), "=&v"(y1), "=&v"(y2), "=&v"(y3) : "v"(a[0]), "v"(a[1]), "v"(a[2]), "v"(a[3]), "v"(inv));
                nx(3 + 3 * u);
                y3 = a[3] * inv; x[u] = b[0] * inv;
                d[u][0] = pk2bf(y0, y1);
                nx(4 + 3 * u);
                d[u][1] = pk2bf(y2, y3);
                if (u & 1) {
                    auto t0 = __builtin_amdgcn_permlane16_swap(d[u - 1][0], d[u][0], false, false);
                    auto t1 = __builtin_amdgcn_permlane16_swap(d[u - 1][1], d[u][1], false, false);
                    st16s(ob + (u - 1) * 81920, mofs, (u32x4){nosent(t0[0]), nosent(t1[0]), nosent(t0[1]), nosent(t1[1])});
                }
            }
            if constexpr (gr < 6) ldq(gr + 2);
            nx(14);
            auto a0 = __builtin_amdgcn_permlane32_swap(__float_as_uint(x[0]), __float_as_uint(x[2]), false, false);
            auto a1 = __builtin_amdgcn_permlane32_swap(__float_as_uint(x[1]), __float_as_uint(x[3]), false, false);
            nx(15);
            auto b0 = __builtin_amdgcn_permlane16_swap(a0[0], a1[0], false, false);
            auto b2 = __builtin_amdgcn_permlane16_swap(a0[1], a1[1], false, false);
            st8m(ob, rofs, (u32x2){nosent(pk2bf(__uint_as_float(b0[0]), __uint_as_float(b0[1]))), nosent(pk2bf(__uint_as_float(b2[0]), __uint_as_float(b2[1])))}, ~0ull);
            ob += 4 * 81920;
            asm volatile("" : "+s"(ob));
        });
        RAW_BARRIER();
        TT((hb >> 4) * 128 + 112 + (hb & 15), 7);
    }
}

__global__ void __launch_bounds__(256, 1) la_kernel(LaParams p) {
    extern __shared__ __attribute__((aligned(16))) char shm[];
    p.hop = align2m(p.ws + O_HOP);
    if (threadIdx.x == 0) {
        u32 xcc; asm volatile("s_getreg_b32 %0, hwreg(HW_REG_XCC_ID)" : "=s"(xcc));
        int role = 255;
        for (int a = 0; a < 8; ++a) {
            const u32 v = (xcc + a) & 7;
            const u32 k = __hip_atomic_fetch_add((gu32*)(((u32*)(p.ws + O_CTL)) + v * 32), 1u, __ATOMIC_RELAXED, __HIP_MEMORY_SCOPE_AGENT);
            if (k < 32) { role = v * 32 + k; break; }
        }
        *(int*)shm = role;
    }
    __syncthreads();
    const int role = __builtin_amdgcn_readfirstlane(*(int*)shm);
    __syncthreads();
    const int set = role >> 7, id = (role >> 5) & 3, k = role & 31;
    if (k < 8) qk_role(p, set, id * 8 + k, shm);
    else if (k < 28) comb_role(p, set, id * 20 + k - 8, shm);
    else bw_role(p, (role >> 5) * 4 + k - 28, shm);
}
__device__ __forceinline__ float row16_sum(float x) { x += fdpp<0x128>(x); x += fdpp<0x124>(x); x += fdpp<0x122>(x); return x + fdpp<0x121>(x); }

#define PFB 0
struct PrepParams { const u16* W[4]; const u16* x; const u16* mem0; const u16* wo; char* ws; };
constexpr int N_PF = PFB, N_LN = 128, N_WO = 200, N_FILL = 512;
__global__ void __launch_bounds__(256) prep_kernel(PrepParams q) {
    int b = blockIdx.x;
    const int t = threadIdx.x, lane = t & 63, w = t >> 6, fr = lane & 15, g = lane >> 4;
    char* hop = align2m(q.ws + O_HOP);
    if (b < N_LN) {
        const int tok = b * 16 + w * 4 + g;
        u32x4 d[3][2];
        float s = 0.f;
#pragma unroll
        for (int i = 0; i < 3; ++i) {
            const u32x4* a = (const u32x4*)(q.x + (size_t)tok * DM + min(fr + 16 * i, 39) * 16);
            d[i][0] = a[0]; d[i][1] = a[1];
        }
#pragma unroll
        for (int i = 0; i < 3; ++i)
#pragma unroll
            for (int e = 0; e < 8; ++e) if (fr + 16 * i < 40) s += bflo(d[i][e >> 2][e & 3]) + bfhi(d[i][e >> 2][e & 3]);
        const float mu = row16_sum(s) * (1.0f / DM);
        float v = 0.f;
#pragma unroll
        for (int i = 0; i < 3; ++i)
#pragma unroll
            for (int e = 0; e < 8; ++e) if (fr + 16 * i < 40) { const float a = bflo(d[i][e >> 2][e & 3]) - mu, c = bfhi(d[i][e >> 2][e & 3]) - mu; v = fmaf(a, a, fmaf(c, c, v)); }
        const float rs = __builtin_amdgcn_rsqf(row16_sum(v) * (1.0f / DM) + 1e-5f);
#pragma unroll
        for (int i = 0; i < 3; ++i) {
            const int blk = fr + 16 * i;
            if (blk >= 40) continue;
            const size_t o = (size_t)(tok >> 5) * 40960 + ((blk >> 1) * 2 + ((tok >> 4) & 1)) * 1024 + ((blk & 1) * 32 + (tok & 15)) * 16;
            u32 n[8];
#pragma unroll
            for (int e = 0; e < 8; ++e) n[e] = pk2bf((bflo(d[i][e >> 2][e & 3]) - mu) * rs, (bfhi(d[i][e >> 2][e & 3]) - mu) * rs);
            *(u32x4*)(q.ws + O_XR + o) = (u32x4){d[i][0][0], d[i][1][0], d[i][0][1], d[i][1][1]};
            *(u32x4*)(q.ws + O_XR + o + 256) = (u32x4){d[i][0][2], d[i][1][2], d[i][0][3], d[i][1][3]};
            *(u32x4*)(q.ws + O_XN + o) = (u32x4){n[0], n[4], n[1], n[5]};
            *(u32x4*)(q.ws + O_XN + o + 256) = (u32x4){n[2], n[6], n[3], n[7]};
        }
        return;
    }
    b -= N_LN;
    if (b < N_PF) {
        const u32x4* src = (const u32x4*)q.W[b & 3] + (size_t)(b >> 2) * 3840 + t;
        u32x4 v[15];
#pragma unroll
        for (int i = 0; i < 15; ++i) v[i] = src[i * 256];
#pragma unroll
        for (int i = 0; i < 15; ++i) asm volatile("" :: "v"(v[i]));
        return;
    }
    b -= N_PF;
    if (b < N_WO) {
        const int cb = b / 20, kk = b % 20;
        const u16* col = q.wo + cb * 64 + 16 * (fr >> 2) + 4 * w + (fr & 3);
        u32 v[8];
#pragma unroll
        for (int e = 0; e < 8; ++e) {
            const int row = kk < 16 ? (2 * kk + (g >> 1)) * 20 + 5 * (2 * (g & 1) + (e >> 2)) + (e & 3) : (8 * (kk - 16) + 2 * g + (e >> 2)) * 20 + 5 * (e & 3) + 4;
            v[e] = col[(size_t)row * DM];
        }
        *(u32x4*)(q.ws + O_WO + (size_t)cb * 81920 + (kk * 4 + w) * 1024 + lane * 16) = (u32x4){v[0] | v[1] << 16, v[2] | v[3] << 16, v[4] | v[5] << 16, v[6] | v[7] << 16};
        return;
    }
    b -= N_WO;
    const u32x4 SENT = {~0u, ~0u, ~0u, ~0u};
    const u32 i0 = b * 256 + t;
    if (b == 0) ((u32*)(q.ws + O_CTL))[t] = 0;
    if (i0 < 8 * 2 * 320) {
        const int sl = i0 / 640, e = i0 % 320;
        const u32x4 v = *(const u32x4*)(q.mem0 + (e & 7) * DM + ((sl & 1) * 40 + (e >> 3)) * 8);
        st16(hop + M0_OFF + (i0 / 320) * 8192 + e * 16, (u32x4){nosent(v[0]), nosent(v[1]), nosent(v[2]), nosent(v[3])});
    }
    for (u32 i = i0; i < HOP_BYTES / 16; i += N_FILL * 256) st16(hop + (size_t)i * 16, SENT);
    for (u32 i = i0; i < (O_XN - O_ST) / 16; i += N_FILL * 256) st16(q.ws + O_ST + (size_t)i * 16, SENT);
}

extern "C" void kernel_launch(void* const* d_in, const int* in_sizes, int n_in,
                              void* d_out, int out_size, void* d_ws, size_t ws_size,
                              hipStream_t stream) {
    PrepParams q;
    q.x = (const u16*)d_in[0]; q.mem0 = (const u16*)d_in[1];
    q.wo = (const u16*)d_in[10];
    for (int i = 0; i < 4; ++i) q.W[i] = (const u16*)d_in[2 + i];
    q.ws = (char*)d_ws;
    prep_kernel<<<N_PF + N_LN + N_WO + N_FILL, 256, 0, stream>>>(q);
    LaParams p;
    p.ws = (char*)d_ws; p.out = (u16*)d_out; p.hop = (char*)d_ws;
    p.Wq = (const u16*)d_in[2]; p.Wk = (const u16*)d_in[3]; p.Wv = (const u16*)d_in[4]; p.Wo = (const u16*)d_in[5];
    p.wqkv = (const u16*)d_in[8]; p.wphi = (const u16*)d_in[9];
    (void)hipFuncSetAttribute((const void*)la_kernel, hipFuncAttributeMaxDynamicSharedMemorySize, LA_LDS);
    la_kernel<<<256, 256, LA_LDS, stream>>>(p);
}
```

```cpp
#include <hip/hip_runtime.h>
#include <hip/hip_bf16.h>
#include <stdint.h>
#include <type_traits>

typedef unsigned short u16;
typedef unsigned int u32;
typedef unsigned long long u64;
using bf16x8 = __attribute__((ext_vector_type(8))) short;
using f32x4 = __attribute__((ext_vector_type(4))) float;

constexpr int S_TOK = 2048, DM = 640, NH = 32, DH = 192;
constexpr int MEM = 8, SEG = 32, NSEG = S_TOK / SEG;
constexpr int HD = NH * DH;

__device__ __forceinline__ float bf2f(u16 v) { return __uint_as_float(((u32)v) << 16); }
__device__ __forceinline__ u32 pk2bf(float lo, float hi) {
    typedef __attribute__((ext_vector_type(2))) float f32x2_t;
    typedef __attribute__((ext_vector_type(2))) __bf16 bf16x2_t;
    bf16x2_t r = __builtin_convertvector((f32x2_t){lo, hi}, bf16x2_t);
    return __builtin_bit_cast(u32, r);
}
__device__ __forceinline__ float rbf(float f) { return __uint_as_float(pk2bf(f, f) << 16); }
typedef __attribute__((ext_vector_type(8))) __bf16 bf8;
typedef __attribute__((ext_vector_type(16))) __bf16 bf16v;
typedef __attribute__((ext_vector_type(4))) __bf16 bf4;
typedef __attribute__((ext_vector_type(4))) short s16x4;
using u32x4 = __attribute__((ext_vector_type(4))) u32;
using u32x2 = __attribute__((ext_vector_type(2))) u32;
typedef __attribute__((address_space(1))) u32 gu32;
typedef __attribute__((address_space(1))) u64 gu64;
#define LDS3 __attribute__((address_space(3)))

constexpr int SLOT = 16384;
#define PCSV 3
constexpr int PCS = PCSV, P_SLOT0 = 0, M_SLOT0 = 16 * PCS, S_SLOT = M_SLOT0 + 32, T_SLOT = S_SLOT + 10, N_SLOT = T_SLOT + 2;
constexpr size_t HOP_BYTES = (size_t)N_SLOT * SLOT, M0_OFF = HOP_BYTES;
constexpr size_t PX_BUF = 4 * 32 * 640, PX_BYTES = 8 * PX_BUF;
constexpr int LA_LDS = 162816 + 256;
constexpr int SPIN_CAP = 1 << 16;
#define D1I 136
#define D2I 58
#define MG1 11
#define MG2 11
#define ROTC 0
#define DUP 5
#define PACE 6
#define CLAG 42
#define T_PX 62
#define T_X1 70
#define T_OB 175
#define T_TOK 400
#define ST_LAG 6
#define OB_STEP 28
#define OB_POLL 0
#define OB_COLD 20
#define NH_ 3
#define HPRIO 0
#define W0D 15
#define LATEP 2
#define SYNCB 1
#define ARMD 0
#define PXK 8
#define PEEKK 6
#define PEEKB 32
#define AN_Q0 385
#define AN_Q1 255
#define AN_C0 285
#define AN_C1 150
#define ANN 0
#define PACE2 5
#define DMASK 3
#define BPEEK 390
#define RECD 0
#define B_LAG 65
#define ELATE 85

__device__ __forceinline__ void st16(void* p, u32x4 v) {
    asm volatile("global_store_dwordx4 %0, %1, off sc1\n\ts_nop 2" :: "v"(p), "v"(v) : "memory");
}
typedef __attribute__((address_space(1))) u32x4 gu32x4;
__device__ __forceinline__ u32x4 ld16(const char* base, u32 off, int imm) { return __builtin_amdgcn_global_load_b128((gu32x4*)(base + off + imm), "agent"); }
__device__ __forceinline__ u32 nosent(u32 v) { return min(v, 0xFFFFFFFEu); }
__device__ __forceinline__ u32 rt32() { return (u32)__builtin_amdgcn_s_memrealtime(); }
__device__ __forceinline__ void wait_rt(u32 t) {
    int d = (int)(t - rt32()) - 4;
    if (d > 4096 || d < 0) return;
    while (d > 7) { if (d > 18) __builtin_amdgcn_s_sleep(2); d = max((int)(t - rt32()) - 4, 0); }
    asm volatile("s_bitcmp1_b32 %0, 2\n\ts_cbranch_scc0 1f\n\ts_nop 15\n\ts_nop 7\n1:\n\ts_bitcmp1_b32 %0, 1\n\ts_cbranch_scc0 2f\n\ts_nop 12\n2:\n\t"
                 "s_bitcmp1_b32 %0, 0\n\ts_cbranch_scc0 3f\n\ts_nop 7\n3:" :: "s"(d) : "scc");
}
template <int N> __device__ __forceinline__ void dma_kb(const char* src, u32 lds_dst, u32 lane16) {
#pragma unroll
    for (int i = 0; i < N; i += 4) {
        u32 keep;
        if (N - i >= 4)
            asm volatile("s_mov_b32 %0, m0\n\ts_mov_b32 m0, %3\n\ts_nop 2\n\tglobal_load_lds_dwordx4 %1, %2 sc1\n\tglobal_load_lds_dwordx4 %1, %2 offset:1024 sc1\n\t"
                         "global_load_lds_dwordx4 %1, %2 offset:2048 sc1\n\tglobal_load_lds_dwordx4 %1, %2 offset:3072 sc1\n\ts_mov_b32 m0, %0"
                         : "=&s"(keep) : "v"(lane16), "s"(src + i * 1024), "s"(lds_dst + i * 1024) : "memory");
        else if (N - i == 2)
            asm volatile("s_mov_b32 %0, m0\n\ts_mov_b32 m0, %3\n\ts_nop 2\n\tglobal_load_lds_dwordx4 %1, %2 sc1\n\tglobal_load_lds_dwordx4 %1, %2 offset:1024 sc1\n\ts_mov_b32 m0, %0"
                         : "=&s"(keep) : "v"(lane16), "s"(src + i * 1024), "s"(lds_dst + i * 1024) : "memory");
        else
            asm volatile("s_mov_b32 %0, m0\n\ts_mov_b32 m0, %3\n\ts_nop 2\n\tglobal_load_lds_dwordx4 %1, %2 sc1\n\ts_mov_b32 m0, %0"
                         : "=&s"(keep) : "v"(lane16), "s"(src + i * 1024), "s"(lds_dst + i * 1024) : "memory");
    }
}
template <int OFF> __device__ __forceinline__ void dma_one(const char* src, u32 lds_dst, u32 lane16) {
    asm volatile("s_mov_b32 m0, %2\n\ts_nop 0\n\tglobal_load_lds_dwordx4 %0, %1 offset:%3 sc1" :: "v"(lane16), "s"(src), "s"(lds_dst), "n"(OFF) : "memory");
}
__device__ __forceinline__ void dma_gather(const char* src, u32 lds_dst, u32 off) {
    u32 keep;
    asm volatile("s_mov_b32 %0, m0\n\ts_mov_b32 m0, %3\n\ts_nop 2\n\tglobal_load_lds_dwordx4 %1, %2\n\ts_mov_b32 m0, %0"
                 : "=&s"(keep) : "v"(off), "s"(src), "s"(lds_dst) : "memory");
}
__device__ __forceinline__ void st16s(char* base, u32 off, u32x4 v) {
    asm volatile("s_nop 4\n\tglobal_store_dwordx4 %0, %1, %2 sc1\n\ts_nop 2" :: "v"(off), "v"(v), "s"(base) : "memory");
}
__device__ __forceinline__ void arm16(char* base, u32 off) {
    u32x4 v;
    asm volatile("v_mov_b64 %0, -1\n\tv_mov_b64 %1, -1" : "=v"(((u64*)&v)[0]), "=v"(((u64*)&v)[1]));
    st16s(base, off, v);
}
template <int B, int E, typename F> __device__ __forceinline__ void static_for(F&& f) {
    if constexpr (B < E) { f(std::integral_constant<int, B>()); static_for<B + 1, E>(f); }
}
#define CI(x) decltype(x)::value
#define RAW_BARRIER() do { asm volatile("s_waitcnt lgkmcnt(0)" ::: "memory"); __builtin_amdgcn_s_barrier(); asm volatile("" ::: "memory"); } while (0)
#define VM0() asm volatile("s_waitcnt vmcnt(0)" ::: "memory")
#define SCHEDB() __builtin_amdgcn_sched_barrier(0)
__device__ __forceinline__ bf8 lo8(const bf16v& v) { return __builtin_shufflevector(v, v, 0, 1, 2, 3, 4, 5, 6, 7); }
__device__ __forceinline__ bf8 hi8(const bf16v& v) { return __builtin_shufflevector(v, v, 8, 9, 10, 11, 12, 13, 14, 15); }
__device__ __forceinline__ bf16v cat8(bf8 a, bf8 b) { return __builtin_shufflevector(a, b, 0, 1, 2, 3, 4, 5, 6, 7, 8, 9, 10, 11, 12, 13, 14, 15); }
#define MMA_VV(c, a, b) asm volatile("v_mfma_f32_16x16x32_bf16 %0, %1, %2, %0" : "+v"(c) : "v"(a), "v"(b))
#define MMA_AV(c, a, b) asm volatile("v_mfma_f32_16x16x32_bf16 %0, %1, %2, %0" : "+v"(c) : "a"(a), "v"(b))
#define MMA_VA(c, a, b) asm volatile("v_mfma_f32_16x16x32_bf16 %0, %1, %2, %0" : "+v"(c) : "v"(a), "a"(b))
#define MMA0_VV(c, a, b) asm volatile("v_mfma_f32_16x16x32_bf16 %0, %1, %2, 0" : "=&v"(c) : "v"(a), "v"(b))
#define MMA0_AV(c, a, b) asm volatile("v_mfma_f32_16x16x32_bf16 %0, %1, %2, 0" : "=&v"(c) : "a"(a), "v"(b))
#define MMA0_VA(c, a, b) asm volatile("v_mfma_f32_16x16x32_bf16 %0, %1, %2, 0" : "=&v"(c) : "v"(a), "a"(b))
#define MMA16(c, a, b) asm volatile("v_mfma_f32_16x16x16_bf16 %0, %1, %2, %0" : "+v"(c) : "v"(a), "v"(b))
#define MMA16_0(c, a, b) asm volatile("v_mfma_f32_16x16x16_bf16 %0, %1, %2, 0" : "=&v"(c) : "v"(a), "v"(b))
#define SMM_V(c, a, b, i) asm volatile("v_smfmac_f32_16x16x64_bf16 %0, %1, %2, %3" : "+v"(c) : "v"(a), "v"(b), "v"(i))
#define SMM_A(c, a, b, i) asm volatile("v_smfmac_f32_16x16x64_bf16 %0, %1, %2, %3" : "+v"(c) : "v"(a), "a"(b), "v"(i))
#define NOPV() asm volatile("s_nop 1")
#define PIN(x) asm volatile("" : "+v"(x))
#define SETTLE() asm volatile("s_nop 7\n\ts_nop 3")
__device__ __forceinline__ bf8 pack8(f32x4 a, f32x4 b) {
    u32x4 r = {pk2bf(a[0], a[1]), pk2bf(a[2], a[3]), pk2bf(b[0], b[1]), pk2bf(b[2], b[3])};
    return __builtin_bit_cast(bf8, r);
}
__device__ __forceinline__ u32 max4(u32 m, u32x4 g) { m = max(max(m, g[0]), g[1]); return max(max(m, g[2]), g[3]); }
#define ZERO4(c) asm volatile("v_mov_b64 %0, 0\n\tv_mov_b64 %1, 0" : "=v"(((u64*)&(c))[0]), "=v"(((u64*)&(c))[1]))
__device__ __forceinline__ bf8 lds8(const char* shm, int off) { return *(const bf8*)(shm + off); }
__device__ __forceinline__ bf16v ldfrag(const char* p) { return cat8(*(const bf8*)p, *(const bf8*)(p + 1024)); }
template <int CTRL> __device__ __forceinline__ float fdpp(float x) {
    return __builtin_bit_cast(float, __builtin_amdgcn_mov_dpp(__builtin_bit_cast(int, x), CTRL, 0xf, 0xf, true));
}
__device__ __forceinline__ float halfsum(float a, float b) {
    auto r = __builtin_amdgcn_permlane32_swap(__float_as_uint(a), __float_as_uint(b), false, false);
    return __uint_as_float(r[0]) + __uint_as_float(r[1]);
}
__device__ __forceinline__ float grp_sum(float x) {
    auto t = __builtin_amdgcn_permlane16_swap(__float_as_uint(x), __float_as_uint(x), false, false);
    x = __uint_as_float(t[0]) + __uint_as_float(t[1]);
    return halfsum(x, x);
}
__device__ __forceinline__ float grp_max(float x) {
    auto t = __builtin_amdgcn_permlane16_swap(__float_as_uint(x), __float_as_uint(x), false, false);
    x = fmaxf(__uint_as_float(t[0]), __uint_as_float(t[1]));
    auto u = __builtin_amdgcn_permlane32_swap(__float_as_uint(x), __float_as_uint(x), false, false);
    return fmaxf(__uint_as_float(u[0]), __uint_as_float(u[1]));
}
__device__ __forceinline__ float bflo(u32 v) { return __uint_as_float(v << 16); }
__device__ __forceinline__ float bfhi(u32 v) { return __uint_as_float(v & 0xffff0000u); }
__device__ __forceinline__ void st4_lane0(char* base, u32 v) {
    u32 z, d;
    base = (char*)(((u64)(u32)__builtin_amdgcn_readfirstlane((u32)((u64)base >> 32)) << 32) | (u64)(u32)__builtin_amdgcn_readfirstlane((u32)(u64)base));
    asm volatile("s_mov_b64 exec, 1\n\tv_mov_b32 %0, 0\n\tv_mov_b32 %1, %3\n\ts_nop 1\n\tglobal_store_dword %0, %1, %2 sc1\n\ts_mov_b64 exec, -1" : "=&v"(z), "=&v"(d) : "s"(base), "s"(v) : "memory");
}
__device__ __forceinline__ void stamp2(char* hop, const int line) {
    const u32 t = nosent(rt32());
    st4_lane0(hop + (T_SLOT + 1) * SLOT + line * 128, t); st4_lane0(hop + (T_SLOT + 1) * SLOT + 8192 + line * 128, t);
}
__device__ __forceinline__ int track(int d, int sample, int down) {
    if (sample <= 0) return d;
    return sample >= d ? min(sample, d + DUP) : d - min((d - sample + 1) >> 1, down);
}
__device__ __forceinline__ u32 sched_peek(const char* p) { return __hip_atomic_load((const gu32*)p, __ATOMIC_RELAXED, __HIP_MEMORY_SCOPE_AGENT); }
__device__ __forceinline__ u32 sched_get(u32 v, const char* p, int& cap) {
    for (int spin = 0; v == ~0u; ++spin) {
        if (spin >= cap) { cap = 1; break; }
        __builtin_amdgcn_s_sleep(3);
        v = sched_peek(p);
    }
    return __builtin_amdgcn_readfirstlane(v);
}

#define TRDECL
#define TR(i)
#define TRV(i, v)
#define TRFLUSH(role)
#define TRC(i)
constexpr size_t ACT = (size_t)S_TOK * DM * 2;
constexpr size_t O_DBG = 0, O_CTL = 2u << 20, O_ST = O_CTL + 4096, O_FL = O_ST + 128 * 1280, O_PX = O_FL + (576 + 512) * 128, O_X1 = O_PX + PX_BYTES,
                 O_OB = O_X1 + 2 * ACT, O_XN = O_OB + 2 * ACT, O_XR = O_XN + 2 * ACT, O_WO = O_XR + 2 * ACT,
                 O_WVO = O_WO + 10 * 81920, O_HOP = O_WVO + (size_t)80 * 4 * 40 * 2048, WS_BYTES = O_HOP + (4u << 20);
#define TT(role, i)
#define CNT(i)
struct LaParams { char* ws; u16* out; char* hop; const u16* Wq; const u16* Wk; const u16* Wv; const u16* Wo; const u16* wqkv; const u16* wphi; };
__device__ __forceinline__ int kperm(int g, int e) { return 16 * (g >> 1) + 8 * ((e & 3) >> 1) + 4 * (g & 1) + 2 * (e >> 2) + (e & 1); }
__device__ __forceinline__ bf8 tr8(u32 a, u32 b) {
    const s16x4 lo = __builtin_amdgcn_ds_read_tr16_b64_v4i16((LDS3 s16x4*)(uintptr_t)a), hi = __builtin_amdgcn_ds_read_tr16_b64_v4i16((LDS3 s16x4*)(uintptr_t)b);
    return __builtin_bit_cast(bf8, __builtin_shufflevector(lo, hi, 0, 1, 2, 3, 4, 5, 6, 7));
}
__device__ __forceinline__ char* wsp(const LaParams& p) { char* b = p.ws; asm volatile("" : "+s"(b)); return b; }
template <int N> __device__ __forceinline__ void wait_flags(const char* f, const u32 (&off)[N], int& cap) {
    for (int spin = 0;; ++spin) {
        u32 m = 0;
#pragma unroll
        for (int i = 0; i < N; ++i) m = max(m, sched_peek(f + off[i]));
        if (!__any(m == ~0u)) break;
        if (spin >= cap) { cap = 1; break; }
        __builtin_amdgcn_s_sleep(4);
    }
    asm volatile("" ::: "memory");
}
__device__ __forceinline__ void wait_all_tokens(const LaParams& p, u32 lane, int& cap) {
    asm volatile("" : "+v"(lane));
    const u32 off[3] = {lane * 128, lane * 128 + 8192, min(lane + 128, 159u) * 128};
    wait_flags<3>(wsp(p) + O_FL + 192 * 128, off, cap);
}
__device__ __forceinline__ void wait_first_tokens(const LaParams& p, const int set, u32 lane, int& cap) {
    asm volatile("" : "+v"(lane));
    const u32 off[1] = {(set * 20 + min(lane, 39u) + (lane >= 20 ? 20 : 0)) * 128};
    wait_flags<1>(wsp(p) + O_FL, off, cap);
}
__device__ __forceinline__ int get_side(const LaParams& p, int& cap) { return sched_get(sched_peek(wsp(p) + O_FL + 384 * 128), wsp(p) + O_FL + 384 * 128, cap) & 1; }
__device__ __forceinline__ char* align2m(char* p) { return p + ((size_t)(-(intptr_t)p) & ((2u << 20) - 1)); }

constexpr int Q_XA = 0, Q_PART = 20480, Q_TR = 48640, Q_MK = 54784, Q_WL = 55808, Q_ST = 162304, Q_PS = 44;

#define DPP1(op, ror) op "_dpp %0, %0, %0 row_ror:" ror " row_mask:0xf bank_mask:0xf\n\t"
#define DPP2(op, ror) DPP1(op, ror) op "_dpp %1, %1, %1 row_ror:" ror " row_mask:0xf bank_mask:0xf\n\ts_nop 0\n\t"
__device__ __forceinline__ void st16m(char* base, u32 off, u32x4 v, u64 mask) {
    asm volatile("s_mov_b64 exec, %3\n\ts_nop 3\n\tglobal_store_dwordx4 %0, %1, %2 sc1\n\ts_mov_b64 exec, -1\n\ts_nop 1" :: "v"(off), "v"(v), "s"(base), "s"(mask) : "memory");
}
__device__ __forceinline__ void st16f(char* base, u32 off, u32x4 v, u64 mask) {
    asm volatile("s_mov_b64 exec, %3\n\tglobal_store_dwordx4 %0, %1, %2 sc1\n\ts_mov_b64 exec, -1\n\ts_nop 1" :: "v"(off), "v"(v), "s"(base), "s"(mask) : "memory");
}
__device__ __forceinline__ void st16f64(char* base, u32 off, u32x4 v, u64 mask) {
    asm volatile("s_mov_b64 exec, %3\n\tglobal_store_dwordx4 %0, %1, %2 offset:64 sc1\n\ts_mov_b64 exec, -1\n\ts_nop 1" :: "v"(off), "v"(v), "s"(base), "s"(mask) : "memory");
}
__device__ __forceinline__ void st32(char* base, u32 off, u32x4 a, u32x4 b) {
    asm volatile("s_nop 4\n\tglobal_store_dwordx4 %0, %1, %3\n\tglobal_store_dwordx4 %0, %2, %3 offset:16\n\ts_nop 2" :: "v"(off), "v"(a), "v"(b), "s"(base) : "memory");
}
__device__ __forceinline__ void st8m(char* base, u32 off, u32x2 v, u64 mask) {
    asm volatile("s_mov_b64 exec, %3\n\ts_nop 3\n\tglobal_store_dwordx2 %0, %1, %2 sc1\n\ts_mov_b64 exec, -1" :: "v"(off), "v"(v), "s"(base), "s"(mask) : "memory");
}
__device__ __forceinline__ u32 pk2bf_abs(float lo, float hi) {
    u32 r;
    asm("v_cvt_pk_bf16_f32 %0, |%1|, |%2|" : "=v"(r) : "v"(lo), "v"(hi));
    return r;
}
__device__ __forceinline__ u32 p_line(const int h, const int piece, const u32 gap) {
    u32 a = (h >> 4) * gap + ((h >> 1) & 7) * 1024 + (h & 1) * 128, b = 2 * gap + h * 128;
    asm volatile("" : "+s"(a), "+s"(b));
    return piece < 4 ? a + piece * 256 : b;
}
constexpr float SM_C = 0.07216878364870322f * 1.4426950408889634f, NEG_BIG = -0x1p100f;
constexpr u64 SM_MASK = 0x0155015501550155ull;
template <typename MID> __device__ __forceinline__ void softmax_mem(const float* p, char* dst, char* dst2, const u32 off, MID&& mid) {
    f32x4 a[4];
#pragma unroll
    for (int i = 0; i < 4; ++i) a[i] = *(const f32x4*)(p + i * 40 * Q_PS);
    SCHEDB(); mid(); SCHEDB();
    f32x4 x = (a[0] + a[1]) + (a[2] + a[3]);
    float ml, mx = fmaxf(fmaxf(x[0], x[1]), fmaxf(x[2], x[3])), e0, e1, e2, e3;
    asm("v_mul_f32 %5, %10, %0\n\ts_nop 0\n\t"
        DPP1("v_max_f32", "8") "v_fma_f32 %1, %6, %10, -%5\n\tv_fma_f32 %2, %7, %10, -%5\n\t"
        DPP1("v_max_f32", "4") "v_fma_f32 %3, %8, %10, -%5\n\tv_fma_f32 %4, %9, %10, -%5\n\t"
        DPP1("v_max_f32", "2") "v_exp_f32 %1, %1\n\tv_exp_f32 %2, %2\n\t"
        DPP1("v_max_f32", "1") "v_exp_f32 %3, %3\n\tv_fma_f32 %5, -%0, %10, %5\n\tv_exp_f32 %4, %4\n\tv_add_f32 %0, %1, %2\n\tv_exp_f32 %5, %5\n\tv_add_f32 %0, %0, %3\n\t"
        "v_add_f32 %0, %0, %4\n\tv_mul_f32 %0, %0, %5\n\tv_mul_f32 %1, %1, %5\n\tv_mul_f32 %2, %2, %5\n\t"
        DPP1("v_add_f32", "8") "v_mul_f32 %3, %3, %5\n\tv_mul_f32 %4, %4, %5\n\t" DPP1("v_add_f32", "4") "s_nop 1\n\t"
        DPP1("v_add_f32", "2") "s_nop 1\n\t" DPP1("v_add_f32", "1") "v_rcp_f32 %0, %0\n\ts_nop 0\n\t"
        "v_mul_f32 %1, %1, %0\n\tv_mul_f32 %2, %2, %0\n\tv_mul_f32 %3, %3, %0\n\tv_mul_f32 %4, %4, %0"
        : "+v"(mx), "=&v"(e0), "=&v"(e1), "=&v"(e2), "=&v"(e3), "=&v"(ml) : "v"(x[0]), "v"(x[1]), "v"(x[2]), "v"(x[3]), "s"(SM_C));
    u32x4 o;
    asm volatile("v_cvt_pk_bf16_f32 %4, |%4|, |%6|\n\tv_cvt_pk_bf16_f32 %5, |%5|, |%7|\n\ts_nop 0\n\t"
                 "v_mov_b32_dpp %0, %4 quad_perm:[0,0,2,2] row_mask:0xf bank_mask:0xf\n\tv_mov_b32_dpp %1, %5 quad_perm:[0,0,2,2] row_mask:0xf bank_mask:0xf\n\t"
                 "v_mov_b32_dpp %2, %4 quad_perm:[1,1,3,3] row_mask:0xf bank_mask:0xf\n\tv_mov_b32_dpp %3, %5 quad_perm:[1,1,3,3] row_mask:0xf bank_mask:0xf"
                 : "=&v"(o[0]), "=&v"(o[1]), "=&v"(o[2]), "=&v"(o[3]), "+v"(e0), "+v"(e2) : "v"(e1), "v"(e3), "s"(dst), "s"(dst2));
    asm volatile("s_mov_b64 exec, %4\n\tglobal_store_dwordx4 %0, %1, %2 sc1\n\tglobal_store_dwordx4 %0, %1, %3 sc1\n\ts_mov_b64 exec, -1\n\ts_nop 1"
                 :: "v"(off), "v"(o), "s"(dst), "s"(dst2), "s"(0x03ff03ff03ff03ffull) : "memory");
}
struct SmTok {
    float x[2][4], b1[2][4], b2[2][4], b3[2][4], mx[2], sm[2], t0, t1;
    u32 d00, d01, d10, d11;
    static constexpr int N = 88;
    __device__ __forceinline__ void load(const float* p) {
#pragma unroll
        for (int n = 0; n < 2; ++n) {
            const f32x4 a0 = *(const f32x4*)(p + (n * 4) * Q_PS), a1 = *(const f32x4*)(p + (40 + n * 4) * Q_PS);
            const f32x4 a2 = *(const f32x4*)(p + (80 + n * 4) * Q_PS), a3 = *(const f32x4*)(p + (120 + n * 4) * Q_PS);
#pragma unroll
            for (int i = 0; i < 4; ++i) { x[n][i] = a0[i]; b1[n][i] = a1[i]; b2[n][i] = a2[i]; b3[n][i] = a3[i]; }
        }
    }
    template <int I> __device__ __forceinline__ void piece(char* dst, const u32 off) {
        constexpr int n = I & 1, k = (I >> 1) - 12;
        float& t = n ? t1 : t0;
#define ROR(op, ror, v) asm volatile(op "_dpp %0, %0, %0 row_ror:" ror " row_mask:0xf bank_mask:0xf" : "+v"(v))
        if constexpr (k < -8) asm volatile("v_add_f32 %0, %0, %1" : "+v"(x[n][k + 12]) : "v"(b1[n][k + 12]));
        else if constexpr (k < -4) asm volatile("v_add_f32 %0, %0, %1" : "+v"(b2[n][k + 8]) : "v"(b3[n][k + 8]));
        else if constexpr (k < 0) asm volatile("v_add_f32 %0, %0, %1" : "+v"(x[n][k + 4]) : "v"(b2[n][k + 4]));
        else if constexpr (k == 0) asm volatile("v_max_f32 %0, %1, %2" : "=v"(mx[n]) : "v"(x[n][0]), "v"(x[n][1]));
        else if constexpr (k == 1) asm volatile("v_max3_f32 %0, %0, %1, %2" : "+v"(mx[n]) : "v"(x[n][2]), "v"(x[n][3]));
        else if constexpr (k == 2) ROR("v_max_f32", "8", mx[n]);
        else if constexpr (k == 3) ROR("v_max_f32", "4", mx[n]);
        else if constexpr (k == 4) ROR("v_max_f32", "2", mx[n]);
        else if constexpr (k == 5) ROR("v_max_f32", "1", mx[n]);
        else if constexpr (k == 6) { mx[n] *= SM_C; PIN(mx[n]); }
        else if constexpr (k < 11) { x[n][k - 7] = fmaf(x[n][k - 7], SM_C, -mx[n]); PIN(x[n][k - 7]); }
        else if constexpr (k < 15) asm volatile("v_exp_f32 %0, %0" : "+v"(x[n][k - 11]));
        else if constexpr (k == 15) asm volatile("v_add_f32 %0, %1, %2" : "=v"(sm[n]) : "v"(x[n][0]), "v"(x[n][1]));
        else if constexpr (k == 16) asm volatile("v_add_f32 %0, %1, %2" : "=v"(t) : "v"(x[n][2]), "v"(x[n][3]));
        else if constexpr (k == 17) asm volatile("v_add_f32 %0, %0, %1" : "+v"(sm[n]) : "v"(t));
        else if constexpr (k == 18) ROR("v_add_f32", "8", sm[n]);
        else if constexpr (k == 19) ROR("v_add_f32", "4", sm[n]);
        else if constexpr (k == 20) ROR("v_add_f32", "2", sm[n]);
        else if constexpr (k == 21) ROR("v_add_f32", "1", sm[n]);
        else if constexpr (k == 22) asm volatile("v_rcp_f32 %0, %0" : "+v"(sm[n]));
        else if constexpr (k < 27) asm volatile("v_mul_f32 %0, %0, %1" : "+v"(x[n][k - 23]) : "v"(sm[n]));
        else if constexpr (k == 27) asm volatile("v_cvt_pk_bf16_f32 %0, |%1|, |%2|" : "=v"(n ? d10 : d00) : "v"(x[n][0]), "v"(x[n][1]));
        else if constexpr (k == 28) asm volatile("v_cvt_pk_bf16_f32 %0, |%1|, |%2|" : "=v"(n ? d11 : d01) : "v"(x[n][2]), "v"(x[n][3]));
        else if constexpr (I == 84 || I == 87) {
            u32x4 o;
            asm volatile("v_mov_b32 %0, %4\n\tv_mov_b32 %1, %5\n\tv_mov_b32_dpp %2, %4 quad_perm:[1,0,3,2] row_mask:0xf bank_mask:0xf\n\tv_mov_b32_dpp %3, %5 quad_perm:[1,0,3,2] row_mask:0xf bank_mask:0xf"
                         : "=&v"(o[0]), "=&v"(o[1]), "=&v"(o[2]), "=&v"(o[3]) : "v"(n ? d10 : d00), "v"(n ? d11 : d01), "s"(dst));
            if constexpr (n == 0) st16f(dst, off, o, SM_MASK); else st16f64(dst, off, o, SM_MASK);
        }
    }
};

__host__ __device__ constexpr int qk_slot(int T, int ks) { return ks < 5 ? ks * 6 + T : (T < 4 ? 30 + (T >> 1) * 10 + (ks - 5) * 2 + (T & 1) : 25 + T * 5 + ks); }
constexpr int NLF = 13;
__host__ __device__ constexpr int lds_slot(int n) { constexpr int t[NLF] = {2, 6, 11, 15, 20, 24, 29, 33, 37, 41, 45, 49, 55}; return t[n]; }
__host__ __device__ constexpr int slot_lds(int sl) { for (int n = 0; n < NLF; ++n) if (lds_slot(n) == sl) return n; return -1; }
__host__ __device__ constexpr int slot_frag(int sl) { if (slot_lds(sl) >= 0) return 48 + slot_lds(sl); int n = 0; for (int i = 0; i < NLF; ++i) n += lds_slot(i) < sl; return sl - n; }
__host__ __device__ constexpr int qk_frag(int T, int ks) { return slot_frag(qk_slot(T, ks)); }
__host__ __device__ constexpr int slot_T(int sl) { return sl < 30 ? sl % 6 : (sl < 50 ? ((sl - 30) / 10) * 2 + (sl & 1) : (sl - 30) / 5); }
__host__ __device__ constexpr int slot_ks(int sl) { return sl < 30 ? sl / 6 : (sl < 50 ? 5 + ((sl - 30) % 10) / 2 : 5 + sl % 5); }
__host__ __device__ constexpr int lds_rank(int T, int ks) { int n = 0; for (int t = 0; t < T; ++t) n += qk_frag(t, ks) >= 48; return n; }
__host__ __device__ constexpr int lds_nth(int ks, int n) { for (int t = 0; t < 6; ++t) if (qk_frag(t, ks) >= 48 && lds_rank(t, ks) == n) return qk_frag(t, ks) - 48; return -1; }

__device__ __forceinline__ void qk_role(const LaParams& p, const int set, const int h, char* shm) {
    const int lane = threadIdx.x & 63, w = __builtin_amdgcn_readfirstlane(threadIdx.x >> 6);
    const int fr = lane & 15, g = lane >> 4;
    const u32 lds0 = (u32)(uintptr_t)(LDS3 char*)shm;
    const int sidx = (lane & 8) ? 0xEEEE : 0x4444;
    int cap = SPIN_CAP;
    char* mine = p.hop;
    int sch = (S_SLOT + w) * SLOT;
    int phys = 0;
    char* px = (wsp(p) + O_PX);
    const char* xn = (wsp(p) + O_XN);
    char* wl = shm + Q_WL + w * (NLF * 2048) + lane * 16;
    const char* xa = shm + Q_XA + lane * 16;

    bf16v WA[32], WV[28 - NLF];
    {
        bf16v WT[NLF];
        u32 vo[6], rd[3];
#pragma unroll
        for (int i = 0; i < 6; ++i) {
            const int id = i * 64 + lane, r = w * 16 + id / 24;
            vo[i] = (32 * (r >> 5) + kperm((r >> 2) & 3, ((r >> 4) & 1) * 4 + (r & 3))) * (HD * 2) + ((id % 24) ^ (r & 6)) * 16;
        }
#pragma unroll
        for (int i = 0; i < 3; ++i) rd[i] = lds0 + (g * 4 + (fr >> 2)) * 384 + ((w * 3 + i) ^ ((g & 1) * 2 + (fr >> 3))) * 32 + (fr & 3) * 8;
        auto issue = [&](const int j) {
            const char* src = (const char*)((j & 1) ? p.Wk : p.Wq) + (size_t)(j >> 1) * 64 * HD * 2 + h * 384;
#pragma unroll
            for (int i = 0; i < 6; ++i) dma_gather(src, lds0 + (j % 6) * 24576 + w * 6144 + i * 1024, vo[i]);
        };
#pragma unroll
        for (int j = 0; j < 5; ++j) issue(j);
        static_for<0, 20>([&](auto J) {
            constexpr int j = CI(J);
            asm volatile("s_waitcnt vmcnt(%0)" :: "n"((19 - j < 4 ? 19 - j : 4) * 6) : "memory");
            RAW_BARRIER();
            if constexpr (j + 5 < 20) issue(j + 5);
            static_for<0, 3>([&](auto PR) {
                constexpr int id = qk_frag(2 * CI(PR) + (j & 1), j >> 1);
                u32 a = rd[CI(PR)] + (j % 6) * 24576;
                asm volatile("" : "+v"(a));
                const bf16v f = cat8(tr8(a, a + 16 * 384), tr8(a + 32 * 384, a + 48 * 384));
                if constexpr (id < 32) WA[id] = f; else if constexpr (id < 48) WV[id - 32] = f; else WT[id - 48] = f;
            });
        });
        RAW_BARRIER();
        dma_kb<5>(xn + (size_t)set * 40960 + w * 5120, lds0 + Q_XA + w * 5120, lane * 16);
#pragma unroll
        for (int i = 0; i < NLF; ++i) { *(bf8*)(wl + i * 2048) = lo8(WT[i]); *(bf8*)(wl + i * 2048 + 1024) = hi8(WT[i]); }
    }
    TRDECL;
    bf8 QX[2], KX[2];
    u32x2 QY[2], KY[2];
    u32 peek = ~0u;
    int d1 = D1I, d2 = D2I, k1 = D1I + MG1;
    if (w == 3) *(u64*)(shm + Q_ST + lane * 8) = 0;
    SmTok smt;
    u32 xoff = p_line(h, fr >> 1, 8192) + g * 16, ao1 = p_line(h, min(lane >> 3, 4), 8192) + (lane & 7) * 16, ao0;
    {
        u32 a = (h >> 4) * SLOT + (h & 15) * 512, b = 2 * SLOT + h * 256;
        asm volatile("" : "+s"(a), "+s"(b));
        ao0 = lane < 32 ? a + lane * 16 : b + (lane & 15) * 16;
    }
    asm volatile("" : "+v"(xoff), "+v"(ao0), "+v"(ao1));
    auto rearm = [&](const int rot, const int i, char*& a, u32x4& v) {
        if (i == 0) asm volatile("v_mov_b64 %0, -1\n\tv_mov_b64 %1, -1" : "=v"(((u64*)&v)[0]), "=v"(((u64*)&v)[1]));
        if (i == 1) { a = mine + (P_SLOT0 + (((rot + 3) & 3) * 4 + w) * PCS) * SLOT; asm volatile("" : "+s"(a)); }
        if (i == 5) { a = px + (size_t)(set * 4 + ((rot + 2) & 3)) * PX_BUF + w * 20480; asm volatile("" : "+s"(a)); }
        if (i == 3 || i == 8) __builtin_amdgcn_global_store_b128((gu32x4*)(a + (i == 3 ? ao0 : ao1)), v, "agent");
    };
    auto fetch_stats = [&]() {
        if (__builtin_expect(h == 0 && w == 3, 0)) {
                u32 lo = lane;
                asm volatile("" : "+v"(lo));
                lo *= 128;
                u32 keep;
                asm volatile("s_mov_b32 %0, m0\n\ts_mov_b32 m0, %4\n\ts_nop 2\n\tglobal_load_lds_dword %1, %2 sc1\n\ts_add_u32 m0, m0, 256\n\ts_nop 0\n\tglobal_load_lds_dword %1, %3 sc1\n\ts_mov_b32 m0, %0"
                             : "=&s"(keep) : "v"(lo), "s"(mine + T_SLOT * SLOT), "s"(mine + (T_SLOT + 1) * SLOT), "s"(lds0 + Q_ST) : "memory", "scc");
        }
    };
    auto xproj = [&](const int snext, const int sprev, auto SOFT, char* xdst) {
        f32x4 acc[6][2];
        TR(8);
        const char* xs;
        u32 xl;
        bf8 x0 = *(const bf8*)(xa), x1 = *(const bf8*)(xa + 1024), lw[2];
        lw[0] = *(const bf8*)(wl);
        if constexpr (CI(SOFT)) RAW_BARRIER();
        auto pack = [&](const int T, const int tt) {
            PIN(acc[T][tt]);
            const u32 a = pk2bf(acc[T][tt][0], acc[T][tt][1]), b = pk2bf(acc[T][tt][2], acc[T][tt][3]);
            if (T < 4) { u32* d = (u32*)((T & 1) ? &KX[tt] : &QX[tt]) + (T & 2); d[0] = a; d[1] = b; }
            else if (T == 4) QY[tt] = (u32x2){a, b}; else KY[tt] = (u32x2){a, b};
            SCHEDB();
        };
        char* ao;
        u32x4 sent;
        static_for<0, 20>([&](auto K) {
            constexpr int k = CI(K), l0 = lds_nth(((k + 1) >> 1) % 10, 0), l1 = lds_nth(((k + 1) >> 1) % 10, 1);
            bf8 n0, n1, nw[2];
            if constexpr (k == 9) {
                TR(9);
                if constexpr (CI(SOFT)) asm volatile("s_waitcnt vmcnt(4)" ::: "memory"); else VM0();
                RAW_BARRIER();
                TR(10);
            }
            static_for<0, 6>([&](auto TT) {
                constexpr int T = CI(TT), id = qk_frag(T, k >> 1), rk = lds_rank(T, k >> 1);
                if constexpr (k < 19) {
                    if constexpr (T == 0) n0 = *(const bf8*)(xa + (k + 1) * 2048);
                    if constexpr (T == 1) n1 = *(const bf8*)(xa + (k + 1) * 2048 + 1024);
                    if constexpr (T == 2 && l0 >= 0) nw[0] = *(const bf8*)(wl + l0 * 2048 + ((k + 1) & 1) * 1024);
                    if constexpr (T == 3 && l1 >= 0) nw[1] = *(const bf8*)(wl + l1 * 2048 + ((k + 1) & 1) * 1024);
                }
                if constexpr (k == 0 && T == 1) { xs = xn + (size_t)(snext - 2) * 40960 + 20480 + w * 5120; xl = lds0 + Q_XA + 20480 + w * 5120; asm volatile("" : "+s"(xs), "+s"(xl)); }
                if constexpr (k == 9 && T == 1) { xs = xn + (size_t)min(snext, NSEG - 1) * 40960 + w * 5120; xl -= 20480 + 4096; asm volatile("" : "+s"(xs), "+s"(xl)); }
                if constexpr ((k == 2 || k == 13) && T == 1) { xs += 4096; xl += 4096; asm volatile("" : "+s"(xs), "+s"(xl)); }
                if constexpr (k < 3 && (T == 3 || T == 5) && 2 * k + (T == 5) < 5) dma_one<((2 * k + (T == 5)) & 3) * 1024>(xs, xl, lane * 16);
                if constexpr (k >= 9 && k < 14 && T == 4) dma_one<((k - 9) & 3) * 1024>(xs, xl, lane * 16);
                if constexpr (CI(SOFT) && k == 16 && T == 4) fetch_stats();
                if constexpr (k == 17 && T == 4) peek = sched_peek(mine + sch + max(sprev, 0) * 128 + 4);
                SCHEDB();
                static_for<0, 2>([&](auto U) {
                    constexpr int u = CI(U);
                    const bf8 xx = u ? x1 : x0;
                    if constexpr (id < 32) {
                        const bf8 wf = (k & 1) ? hi8(WA[id]) : lo8(WA[id]);
                        if constexpr (k == 0) MMA0_AV(acc[T][u], wf, xx); else MMA_AV(acc[T][u], wf, xx);
                    } else {
                        bf8 wf;
                        if constexpr (id < 48) wf = (k & 1) ? hi8(WV[id - 32]) : lo8(WV[id - 32]); else wf = lw[rk];
                        if constexpr (k == 0) MMA0_VV(acc[T][u], wf, xx); else MMA_VV(acc[T][u], wf, xx);
                    }
                    SCHEDB();
                    if constexpr (CI(SOFT) && k * 12 + T * 2 + u < SmTok::N) smt.template piece<k * 12 + T * 2 + u>(xdst, xoff);
                    if constexpr (CI(SOFT) && k == 8) rearm((snext >> 1) + 2, T * 2 + u, ao, sent);
                    if constexpr (k == 19 && T >= 2) pack(T - 2, u);
                    SCHEDB();
                });
            });
            if constexpr (k < 19) { x0 = n0; x1 = n1; if constexpr (l0 >= 0) lw[0] = nw[0]; if constexpr (l1 >= 0) lw[1] = nw[1]; }
        });
        TR(11);
        pack(4, 0); pack(4, 1);
        asm volatile("s_nop 3");
        pack(5, 0); pack(5, 1);
    };
    {
        const f32x4 neg = {NEG_BIG, NEG_BIG, NEG_BIG, NEG_BIG};
        *(f32x4*)((float*)(shm + Q_PART) + (w * 40 + 8 + fr) * Q_PS + g * 8 + 4) = neg;
        if (lane < 40) *(f32x4*)((float*)(shm + Q_PART) + (w * 40 + lane) * Q_PS + 40) = neg;
        *(f32x4*)(shm + Q_MK + lane * 16) = (f32x4){4 * g > fr ? NEG_BIG : 0.f, 4 * g + 1 > fr ? NEG_BIG : 0.f, 4 * g + 2 > fr ? NEG_BIG : 0.f, 4 * g + 3 > fr ? NEG_BIG : 0.f};
    }
    float* pw0 = (float*)(shm + Q_PART) + (w * 40 + (fr & 7)) * Q_PS + g * 8 + (fr >> 3) * 4;
    float* pw2 = fr < 8 && g >= 2 ? (float*)(shm + Q_PART) + (w * 40 + fr) * Q_PS + 24 + g * 4 : (float*)(shm + Q_TR + w * 1536) + lane * 4;
    const float* prd = (const float*)(shm + Q_PART) + ((w & 1) * 4 + g) * Q_PS + min(fr, 10) * 4;
    u32 trw = lds0 + Q_TR + w * 1536 + lane * 8, trr = lds0 + Q_TR + w * 1536 + (fr & 3) * 128 + (4 * g + (fr >> 2)) * 8;
    u32 trh = lds0 + Q_TR + w * 1536 + 1024 + (lane & 31) * 8 + (lane >> 5) * 4;
    u32 pw3 = lds0 + Q_PART + ((w * 40 + 8 + (lane >> 5) * 16 + fr) * Q_PS + 32 + (g & 1) * 4) * 4;
    u32 pp = lds0 + Q_PART + ((w * 40 + 8 + fr) * Q_PS + g * 8) * 4;
    asm volatile("" : "+v"(trw), "+v"(trr), "+v"(trh), "+v"(pw3), "+v"(pp));
    const int lead = __builtin_amdgcn_readfirstlane((u32)(h * 4 + (w ^ 3) - 1) >> 31);
    u32 pa = (h >> 4) * SLOT + (h & 15) * 512 + (w & 1) * 256, pbm = 2 * SLOT + h * 256 + (w & 1) * 128;
    asm volatile("" : "+s"(pa), "+s"(pbm));
    const u32 poff = (fr < 8 ? pa + (fr >> 1) * 64 : pbm) + g * 16 + (fr & 1) * (PCS * SLOT);

    for (int layer = 0; layer < 2; ++layer) {
    if (layer > 0) {
        if (w == 0) {
            __builtin_amdgcn_s_sleep(127);
            wait_rt(sched_get(~0u, mine + (T_SLOT + 1) * SLOT + 50 * 128, cap) + T_TOK);
            wait_first_tokens(p, set, lane, cap);
        }
        RAW_BARRIER();
        sch += 5 * SLOT; xn += ACT;
        TT(set * 128 + h, 1);
        dma_kb<5>(xn + (size_t)set * 40960 + w * 5120, lds0 + Q_XA + w * 5120, lane * 16);
    }
    VM0(); RAW_BARRIER();
    TT(set * 128 + h, 2);
    if (ANN && w == 1) st4_lane0(wsp(p) + O_FL + (576 + layer * 256 + set * 128 + h) * 128, nosent(rt32() + (set ? AN_Q1 : AN_Q0)));
    xproj(set + 2, set - 1, std::integral_constant<int, 0>(), nullptr);
    if (layer == 0) { phys = set ^ get_side(p, cap); mine += phys * 8192; }
    TT(set * 128 + h, 3);
    for (int s = set; s < NSEG; s += 2) {
        const int rot = (s >> 1) & 3;
        TR(0);
        u32 ytime = 0;
        const char* mb0 = (s > 0 ? mine + (M_SLOT0 + rot * 8) * SLOT : p.hop + M0_OFF + phys * 8192) + 2 * w * SLOT + 2048;
        const char* mb1 = mb0 + SLOT;
        char* pdst = mine + (P_SLOT0 + rot * 4 * PCS) * SLOT;
        char* pdst2 = pdst + 2 * PCS * SLOT;
        asm volatile("" : "+s"(mb0), "+s"(mb1), "+s"(pdst), "+s"(pdst2));
        if (s > 0) { ytime = sched_get(peek, mine + sch + (s - 1) * 128 + 4, cap);
            }
        f32x4 t[3];
        t[0] = *(const f32x4*)(xa + Q_MK);

        PIN(t[0]); PIN(QX[0]); PIN(QX[1]); PIN(KX[0]); PIN(KX[1]); PIN(QY[0]); PIN(QY[1]); PIN(KY[0]); PIN(KY[1]);
        if (s > 0 && w == 0) wait_rt(ytime);
        __builtin_amdgcn_s_barrier();
        TR(1); TRV(7, ytime);
        f32x4 acc[6];
        u32x4 A[10];
        bf8 Ll[2], Lh[2];
        s16x4 F[4];
        u32 r0[4], r1[4], d[2];
        float v[4];
        int tries = 0;
        auto fin = [&](const int pr, const int i) {
            if (i == 0) { PIN(acc[2 * pr]); PIN(acc[2 * pr + 1]); }
            if (i < 4) { auto r = __builtin_amdgcn_permlane32_swap(__float_as_uint(acc[2 * pr][i]), __float_as_uint(acc[2 * pr + 1][i]), false, false); r0[i] = r[0]; r1[i] = r[1]; }
            const int a0 = pr == 0 ? i - 4 : i - 1;
            if (a0 >= 0 && a0 < 4) asm volatile("v_add_f32 %0, %1, %2" : "=v"(v[a0]) : "v"(r0[a0]), "v"(r1[a0]));
            if (a0 == 3) d[0] = pk2bf(v[0], v[1]);
            if (a0 == 4) {
                d[1] = pk2bf(v[2], v[3]);
                *(LDS3 u32x2*)(uintptr_t)(trw + pr * 512) = (u32x2){d[0], d[1]};
                F[pr] = __builtin_amdgcn_ds_read_tr16_b64_v4i16((LDS3 s16x4*)(uintptr_t)(trr + pr * 512));
            }
        };
        auto fin1 = [&](const int T, const int i) {
            if (i == 0) asm volatile("s_nop 1\n\tv_permlane32_swap_b32 %0, %2\n\tv_permlane32_swap_b32 %1, %3" : "+v"(acc[T][0]), "+v"(acc[T][1]), "+v"(acc[T][2]), "+v"(acc[T][3]));
            else {
                PIN(acc[T]);
                asm volatile("v_add_f32 %0, %2, %4\n\tv_add_f32 %1, %3, %5\n\tv_cvt_pk_bf16_f32 %0, %0, %1" : "=&v"(v[0]), "=&v"(v[1]) : "v"(acc[T][0]), "v"(acc[T][1]), "v"(acc[T][2]), "v"(acc[T][3]));
                *(LDS3 float*)(uintptr_t)(trh + (T - 4) * 256) = v[0];
                F[T - 2] = __builtin_amdgcn_ds_read_tr16_b64_v4i16((LDS3 s16x4*)(uintptr_t)(trr + 1024));
            }
        };
        auto slot = [&](auto SL) {
            constexpr int sl = CI(SL), T = slot_T(sl), ks = slot_ks(sl), id = slot_frag(sl);
            if constexpr (id < 32) SMM_A(acc[T], A[ks], WA[id], sidx);
            else if constexpr (id < 48) SMM_V(acc[T], A[ks], WV[id - 32], sidx);
            else { const bf16v L = cat8(Ll[id & 1], Lh[id & 1]); SMM_V(acc[T], A[ks], L, sidx); }
            SCHEDB();
            if constexpr (sl == 0) TRC(6);
            constexpr int n0 = slot_lds(sl), n1 = slot_lds(sl - 1);
            if constexpr (n0 >= 0 && n0 < NLF - 2) Ll[n0 & 1] = *(const bf8*)(wl + (n0 + 2) * 2048);
            if constexpr (n1 >= 0 && n1 < NLF - 2) Lh[n1 & 1] = *(const bf8*)(wl + (n1 + 2) * 2048 + 1024);
            if constexpr (sl >= 43 && sl < 52) fin(0, sl - 43);
            if constexpr (sl >= 52 && sl < 58) fin(1, sl - 52);
            if constexpr (sl >= 58) fin1(4, sl - 58);
            SCHEDB();
        };
        auto gather = [&](const bool first) {
            u32 vo = lane * 16;
            asm volatile("" : "+v"(vo) :: "memory");
#pragma unroll
            for (int ks = 0; ks < 10; ++ks) {
                A[ks] = ld16(ks < 5 ? mb0 : mb1, vo, (ks % 5 - 2) * 1024);
                SCHEDB();
                if (first) {
                    if (ks == 0) {
                        MMA0_VV(t[1], KX[0], QX[1]);
                        asm volatile("v_mfma_f32_16x16x32_bf16 %0, %1, %2, %3" : "=&v"(t[2]) : "v"(KX[1]), "v"(QX[1]), "v"(t[0]));
                        MMA_VV(t[0], KX[0], QX[0]);
                    }
                    if (ks == 2) { MMA16(t[1], KY[0], QY[1]); MMA16(t[2], KY[1], QY[1]); MMA16(t[0], KY[0], QY[0]); }
                    if (ks == 4) { PIN(t[1]); *(LDS3 f32x4*)(uintptr_t)(pp + 16 * Q_PS * 4) = t[1]; }
                    if (ks == 5) { PIN(t[2]); *(LDS3 f32x4*)(uintptr_t)(pp + 16 * Q_PS * 4 + 16) = t[2]; }
                    if (ks == 6) { PIN(t[0]); *(LDS3 f32x4*)(uintptr_t)pp = t[0]; }
                    if (ks == 1 || ks == 3 || ks == 7 || ks == 8) asm volatile("s_nop %0" :: "n"(PACE2));
                } else if (ks < 9) asm volatile("s_nop %0" :: "n"(PACE));
                if (ks == 3 || ks == 7) { Ll[ks >> 2] = *(const bf8*)(wl + (ks >> 2) * 2048); Lh[ks >> 2] = *(const bf8*)(wl + (ks >> 2) * 2048 + 1024); }
                if (ks == 1) { ZERO4(acc[0]); ZERO4(acc[1]); }
                if (ks >= 5 && ks < 9) ZERO4(acc[ks - 3]);
                SCHEDB();
            }
        };
        gather(true);
        static_for<0, 42>(slot);
        auto record = [&](const u32 xt, const u32 yt) {
            u32 ln8 = lane;
            asm volatile("" : "+v"(ln8));
            ln8 *= 8192;
            const u32x4 rec = {nosent(xt), nosent(yt), (u32)(d1 + MG1), 0u};
            k1 = d1 + MG1;
            st16m(p.hop + (S_SLOT + layer * 5) * SLOT + s * 128, ln8, rec, 0x3ffull);
        };
        u64 bad, bad2;
        asm volatile("v_cmp_u_f32 %0, %2, %3\n\tv_cmp_u_f32 %1, %4, %5" : "=s"(bad), "=s"(bad2) : "v"(acc[0][0]), "v"(acc[0][1]), "v"(acc[0][2]), "v"(acc[0][3]));
        static_for<42, 43>(slot);
        asm volatile("" : "+s"(bad), "+s"(bad2));
        if (__builtin_expect((bad | bad2) != 0, 0)) {
            {
                SETTLE(); CNT(0);
                while (true) {
                    gather(false);
                    u32 m = 0;
#pragma unroll
                    for (int ks = 0; ks < 10; ++ks) m = max4(m, A[ks]);
                    ++tries;
                    if (!__any(m == ~0u)) break;
                    if (tries >= cap) { cap = 1; break; }
                    __builtin_amdgcn_s_sleep(4);
                }
                static_for<0, 43>(slot);
            }
        }
        TR(2); TRV(6, tries + 1);
        static_for<43, 60>(slot);
        TRC(7);
        bf8 F01 = __builtin_bit_cast(bf8, __builtin_shufflevector(F[0], F[1], 0, 1, 2, 3, 4, 5, 6, 7));
        f32x4 s0, s1, s2, u0, u1;
        PIN(F01);
        MMA0_VV(s1, KX[1], F01); MMA0_VV(s0, KX[0], F01);
        SCHEDB();
        fin1(5, 0); fin1(5, 1);
        SCHEDB();
        MMA16(s1, KY[1], F[2]); MMA16(s0, KY[0], F[2]);
        MMA0_VV(s2, F01, F01); MMA0_VV(u0, F01, QX[0]); MMA0_VV(u1, F01, QX[1]);
        asm volatile("v_mov_b32_dpp %0, %4 row_ror:8 row_mask:0xf bank_mask:0xc\n\tv_mov_b32_dpp %1, %5 row_ror:8 row_mask:0xf bank_mask:0xc\n\t"
                     "v_mov_b32_dpp %2, %6 row_ror:8 row_mask:0xf bank_mask:0xc\n\tv_mov_b32_dpp %3, %7 row_ror:8 row_mask:0xf bank_mask:0xc"
                     : "+v"(s0[0]), "+v"(s0[1]), "+v"(s0[2]), "+v"(s0[3]) : "v"(s1[0]), "v"(s1[1]), "v"(s1[2]), "v"(s1[3]));
        MMA16(s2, F[3], F[3]);
        SCHEDB();
        *(f32x4*)pw0 = s0; SCHEDB();
        MMA16(u0, F[3], QY[0]); MMA16(u1, F[3], QY[1]);
        asm volatile("s_nop 1");
        PIN(s2); *(f32x4*)pw2 = s2; SCHEDB();
        u64 tpub = 0;
        auto uwrite = [&]() {
            PIN(u0); PIN(u1);
            f32x4 u;
#pragma unroll
            for (int i = 0; i < 4; ++i) u[i] = __uint_as_float(__builtin_amdgcn_permlane32_swap(__float_as_uint(u0[i]), __float_as_uint(u1[i]), false, false)[1]);
            *(LDS3 f32x4*)(uintptr_t)pw3 = u;
        };
        if (__builtin_expect(w < 2, 1)) {
            RAW_BARRIER();
            TR(3);
            softmax_mem(prd, pdst, pdst2, poff, uwrite);
            asm volatile("s_memrealtime %0" : "=s"(tpub));
            TR(4);
        } else {
            RAW_BARRIER();
            TR(3);
            uwrite();
            if (lead && s > 1) {
                u32 lo = lane;
                asm volatile("" : "+v"(lo));
                int sta = *(const int*)(shm + Q_ST + lo * 4), stb = *(const int*)(shm + Q_ST + 256 + lo * 4);
                asm("s_nop 1\n\t" DPP2("v_max_i32", "8") DPP2("v_max_i32", "4") DPP2("v_max_i32", "2") DPP2("v_max_i32", "1") : "+v"(sta), "+v"(stb));
                const int m1 = max(__builtin_amdgcn_readlane(stb, 16), __builtin_amdgcn_readlane(stb, 32));
                const int m2 = max(max(max(__builtin_amdgcn_readlane(sta, 0), __builtin_amdgcn_readlane(sta, 16)), max(__builtin_amdgcn_readlane(sta, 32), __builtin_amdgcn_readlane(sta, 48))),
                                   __builtin_amdgcn_readlane(stb, 0));
                const int down = layer == 0 && s < 12 ? 16 : ((s & DMASK) < 2 ? 1 : 0);
                d1 = track(d1, m1, down); d2 = track(d2, m2, down);
            }
            if (lead && (!ANN || s > 0)) {
                const u32 xt = tries == 0 && s > 0 ? ytime + k1 : rt32() + ELATE;
                if (RECD) __builtin_amdgcn_s_sleep(RECD);
                record(xt, xt + d2 + MG2);
            }
            if (ANN && h == 0 && w == 3 && s == 0) {
                u32 ln = lane;
                asm volatile("" : "+v"(ln));
                const char* an = wsp(p) + O_FL + (576 + layer * 256) * 128;
                const u32 off[4] = {ln * 128, min(ln + 64, 111u) * 128, (ln + 128) * 128, min(ln + 192, 239u) * 128};
                wait_flags<4>(an, off, cap);
                const u32 now = rt32() + 40;
                int a = max((int)(sched_peek(an + off[0]) - now), (int)(sched_peek(an + off[1]) - now)), b = max((int)(sched_peek(an + off[2]) - now), (int)(sched_peek(an + off[3]) - now));
                asm("s_nop 1\n\t" DPP2("v_max_i32", "8") DPP2("v_max_i32", "4") DPP2("v_max_i32", "2") DPP2("v_max_i32", "1") : "+v"(a), "+v"(b));
                const int xa_ = max(max(max(__builtin_amdgcn_readlane(a, 0), __builtin_amdgcn_readlane(a, 16)), max(__builtin_amdgcn_readlane(a, 32), __builtin_amdgcn_readlane(a, 48))), 40);
                const int yb_ = max(max(__builtin_amdgcn_readlane(b, 0), __builtin_amdgcn_readlane(b, 16)), max(__builtin_amdgcn_readlane(b, 32), __builtin_amdgcn_readlane(b, 48)));
                record(now + xa_, now + max(xa_ + d2 + MG2 + 10, yb_));
            }
            if (w == 2 && s < 2 && layer > 0) wait_all_tokens(p, lane, cap);
        }
        RAW_BARRIER();
        asm volatile("" : "+s"(tpub));
        { int ro = (8 + 8 * w - (w & 1) * 4) * Q_PS; asm volatile("" : "+s"(ro)); smt.load(prd + ro); }
        if (w == 1) st4_lane0(mine + (T_SLOT + 1) * SLOT + (16 + h) * 128, tries == 0 && s > 0 ? (u32)tpub - ytime : 0);
        char* xdst = px + (size_t)(set * 4 + rot) * PX_BUF + w * 20480;
        TR(5); TRFLUSH(set * 128 + h);
        if (s + 2 >= NSEG) {
            static_for<0, SmTok::N>([&](auto I) { smt.template piece<CI(I)>(xdst, xoff); SCHEDB(); if constexpr (CI(I) & 1) asm volatile("s_nop 0"); SCHEDB(); });
            TT(set * 128 + h, 0);
            __builtin_amdgcn_s_sleep(64);
            char* ao;
            u32x4 sent;
#pragma unroll
            for (int i = 0; i < 10; ++i) rearm(rot, i, ao, sent);
            break;
        }
        xproj(s + 4, s + 1, std::integral_constant<int, 1>(), xdst);
    }
    }
}

constexpr int C_YL = 0, C_RED = 40960, C_EX = 49152, C_XN = 53248;
__device__ __forceinline__ void comb_role(const LaParams& p, const int set, const int c, char* shm) {
    const int lane = threadIdx.x & 63, w = __builtin_amdgcn_readfirstlane(threadIdx.x >> 6);
    const int fr = lane & 15, g = lane >> 4;
    const u32 lds0 = (u32)(uintptr_t)(LDS3 char*)shm;
    const int sidx = (lane & 8) ? 0xEEEE : 0x4444;
    int cap = SPIN_CAP;
    char* mine = p.hop;
    char* other = p.hop;
    int phys = 0, sch = (S_SLOT + w) * SLOT;
    u32 k1 = D1I + MG1;
    char* px = (wsp(p) + O_PX);
    const char* xn = (wsp(p) + O_XN);
    char* const yl0 = shm + C_YL + lane * 16;
    const u32 pvo = (fr >> 2) * 256 + g * 64 + (fr & 3) * 16, pvm = g * 512 + (fr >> 2) * 128 + (fr & 3) * 16;
    int cb = (set * 80 + c) % 10, rb = (set * 80 + c) / 10;

    {
        const int t = threadIdx.x, h = (set * 80 + c) / 5, cu = c % 5;
        u32x4 st[12];
        const u16* src = p.Wo + ((size_t)h * DH + (t >> 4)) * DM + cu * 128 + (t & 15) * 8;
#pragma unroll
        for (int i = 0; i < 12; ++i) asm volatile("global_load_dwordx4 %0, %1, off" : "=v"(st[i]) : "v"(src + (size_t)i * 16 * DM) : "memory");
        u32 vo[12];
#pragma unroll
        for (int i = 0; i < 12; ++i) {
            const int id = i * 64 + lane, r = id / 24;
            vo[i] = kperm((r >> 2) & 3, (r >> 4) * 4 + (r & 3)) * (HD * 2) + ((id % 24) ^ (r & 7)) * 16;
        }
        const u32 ring = lds0 + 57344 + w * 24576;
        auto issue = [&](const int j) {
            const char* a = (const char*)p.Wv + (size_t)(((j + cu) % 5) * 4 + w) * 32 * HD * 2 + h * 384;
#pragma unroll
            for (int i = 0; i < 12; ++i) dma_gather(a, ring + (j & 1) * 12288 + i * 1024, vo[i]);
        };
        SCHEDB();
        issue(0); issue(1);
        const u32 ra = ring + fr * 384 + ((g ^ (fr & 3)) + (fr & 4)) * 16;
        const int e = t >> 4, pr = ((e >> 2) & 1) * 8 + (e >> 3) * 4 + (e & 3);
        asm volatile("s_waitcnt vmcnt(24)" : "+v"(st[0]), "+v"(st[1]), "+v"(st[2]), "+v"(st[3]), "+v"(st[4]), "+v"(st[5]),
                     "+v"(st[6]), "+v"(st[7]), "+v"(st[8]), "+v"(st[9]), "+v"(st[10]), "+v"(st[11]) :: "memory");
#pragma unroll
        for (int i = 0; i < 12; ++i) *(u32x4*)(shm + (i * 16 + pr) * 288 + (t & 15) * 16) = st[i];
        RAW_BARRIER();
        { const int layer = 0; TT(set * 128 + 32 + c, 12); }
        bf8 B[8][6];
        const u32 rd = lds0 + ((g >> 1) * 16 + (g & 1) * 4 + (fr >> 2)) * 288 + (fr & 3) * 8;
#pragma unroll
        for (int n = 0; n < 8; ++n)
#pragma unroll
            for (int k = 0; k < 6; ++k) B[n][k] = tr8(rd + k * 32 * 288 + n * 32, rd + (k * 32 + 8) * 288 + n * 32);
        for (int j = 0; j < 5; ++j) {
            if (j < 4) asm volatile("s_waitcnt vmcnt(12)" ::: "memory"); else asm volatile("s_waitcnt vmcnt(8)" ::: "memory");
            bf8 A[2][6];
            const u32 rj = ra + (j & 1) * 12288;
#pragma unroll
            for (int u = 0; u < 2; ++u)
#pragma unroll
                for (int k = 0; k < 6; ++k) A[u][k] = *(LDS3 const bf8*)(uintptr_t)((rj ^ ((k & 1) * 64)) + u * 6144 + (k >> 1) * 128);
            asm volatile("s_waitcnt lgkmcnt(0)" ::: "memory");
            if (j < 3) issue(j + 2);
            char* dst = wsp(p) + O_WVO + ((size_t)(h * 20 + ((j + cu) % 5) * 4 + w) * 40 + cu * 8 + 4) * 1024;
            asm volatile("" : "+s"(dst));
            f32x4 cc[2][2];
            static_for<0, 9>([&](auto N) {
                constexpr int n = CI(N), q = n - 1;
                u32x4 o;
#pragma unroll
                for (int k = 0; k < 6; ++k) {
                    if constexpr (n < 8) {
#pragma unroll
                        for (int u = 0; u < 2; ++u) { if (k == 0) MMA0_VA(cc[n & 1][u], A[u][k], B[n][k]); else MMA_VA(cc[n & 1][u], A[u][k], B[n][k]); }
                    } else if (k == 0) asm volatile("s_nop 7");
                    SCHEDB();
                    if constexpr (q >= 0) {
                        if (k == 3) { PIN(cc[q & 1][0]); o[0] = pk2bf(cc[q & 1][0][0], cc[q & 1][0][1]); o[1] = pk2bf(cc[q & 1][0][2], cc[q & 1][0][3]); }
                        if (k == 4) { PIN(cc[q & 1][1]); o[2] = pk2bf(cc[q & 1][1][0], cc[q & 1][1][1]); o[3] = pk2bf(cc[q & 1][1][2], cc[q & 1][1][3]); }
                        if (k == 5) __builtin_amdgcn_global_store_b128((gu32x4*)(dst + (u32)(lane * 16) + (q - 4) * 1024), o, "agent");
                    }
                    SCHEDB();
                }
            });
        }
        { const int layer = 0; TT(set * 128 + 32 + c, 13); }
        VM0(); RAW_BARRIER();
        { const int layer = 0; TT(set * 128 + 32 + c, 14); }
        dma_kb<10>(xn + (size_t)set * 40960 + w * 10240, lds0 + C_XN + w * 10240, lane * 16);
        if (w == 0) st4_lane0(wsp(p) + O_FL + (400 + set * 80 + c) * 128, 1u);
        __builtin_amdgcn_s_sleep(8);
        const u32 off[1] = {(u32)(400 + (w * 8 + (lane & 7)) * 5 + (c >> 4)) * 128};
        wait_flags<1>(wsp(p) + O_FL, off, cap);
        { const int layer = 0; TT(set * 128 + 32 + c, 15); }
    }
    bf16v WA[32], WV[8];
    {
        u32 vo = (w * 8 + (fr >> 3)) * 819200 + g * 256 + (fr & 7) * 16;
        asm volatile("" : "+v"(vo));
        auto ldw = [&](const int ks, const int t) {
            const char* a = wsp(p) + O_WVO + (size_t)(t * 1600 + ks * 80 + (c >> 1)) * 1024 + (c & 1) * 128;
            const char* b = a + 40960;
            asm volatile("" : "+s"(b));
            return cat8(__builtin_bit_cast(bf8, ld16(a, vo, 0)), __builtin_bit_cast(bf8, ld16(b, vo, 0)));
        };
#pragma unroll
        for (int i = 0; i < 32; ++i) WA[i] = ldw(i >> 2, i & 3);
#pragma unroll
        for (int i = 0; i < 8; ++i) WV[i] = ldw(8 + (i >> 2), i & 3);
    }
    auto yx = [&](f32x4 (&acc)[4][2], const int sy, auto&& extra) {
        const char* xa = shm + C_XN + ((sy >> 1) & 1) * 40960 + lane * 16;
        const char* xs = (sy + 2 < NSEG ? xn + (size_t)(sy + 2) * 40960 : (wsp(p) + O_WO) + cb * 81920) + w * 10240;
        u32 xl = lds0 + C_XN + (~(sy >> 1) & 1) * 40960 + w * 10240;
        bf8 x0 = *(const bf8*)(xa), x1 = *(const bf8*)(xa + 1024);
        const char* xs3[3] = {xs, xs + 4096, xs + 8192};
        u32 xl3[3] = {xl, xl + 4096, xl + 8192};
        asm volatile("" : "+s"(xs3[0]), "+s"(xs3[1]), "+s"(xs3[2]), "+s"(xl3[0]), "+s"(xl3[1]), "+s"(xl3[2]));
        static_for<0, 20>([&](auto K) {
            constexpr int k = CI(K);
            bf8 n0, n1;
            static_for<0, 4>([&](auto TT_) {
                constexpr int t = CI(TT_), id = (k >> 1) * 4 + t, dn = 2 * (k - 3) + t - 2;
                if constexpr (k < 19 && t == 0) n0 = *(const bf8*)(xa + (k + 1) * 2048);
                if constexpr (k < 19 && t == 1) n1 = *(const bf8*)(xa + (k + 1) * 2048 + 1024);
                if constexpr (t >= 2 && k >= 3 && k < 8) dma_one<(dn & 3) * 1024>(xs3[dn >> 2], xl3[dn >> 2], lane * 16);
                if constexpr (t >= 2) extra(2 * k + t - 2);
                SCHEDB();
                if constexpr (id < 32) {
                    const bf8 wf = (k & 1) ? hi8(WA[id]) : lo8(WA[id]);
                    if constexpr (k == 0) { MMA0_VA(acc[t][0], x0, wf); MMA0_VA(acc[t][1], x1, wf); } else { MMA_VA(acc[t][0], x0, wf); MMA_VA(acc[t][1], x1, wf); }
                } else {
                    const bf8 wf = (k & 1) ? hi8(WV[id - 32]) : lo8(WV[id - 32]);
                    MMA_VV(acc[t][0], x0, wf); MMA_VV(acc[t][1], x1, wf);
                }
                SCHEDB();
            });
            if constexpr (k < 19) { x0 = n0; x1 = n1; }
        });
    };

    for (int layer = 0; layer < 2; ++layer) {
    u32 ynext = ~0u;
    if (layer > 0) k1 = sched_get(~0u, mine + sch - 5 * SLOT + (62 + set) * 128 + 8, cap);
    VM0(); RAW_BARRIER();
    TT(set * 128 + 32 + c, 11);
    if (ANN && w == 1) st4_lane0(wsp(p) + O_FL + (576 + layer * 256 + set * 128 + 32 + c) * 128, nosent(rt32() + (set ? AN_C1 : AN_C0)));
    if (layer > 0 && w == 0) st4_lane0(wsp(p) + O_FL + (192 + rb * 10 + cb) * 128, 1u);
    {
        f32x4 ya[4][2];
        yx(ya, set, [](int) {});
        asm volatile("s_nop 7");
#pragma unroll
        for (int t = 0; t < 4; ++t) { PIN(ya[t][0]); PIN(ya[t][1]); *(bf8*)(yl0 + (w * 5 + t) * 1024) = pack8(ya[t][0], ya[t][1]); }
        RAW_BARRIER();
    }
    if (layer == 0) { phys = set ^ get_side(p, cap); mine += phys * 8192; other += (phys ^ 1) * 8192; }
    else if (w == 3) wait_all_tokens(p, lane, cap);
    TT(set * 128 + 32 + c, 10);
    for (int s = set; s < NSEG; s += 2) {
        const int rot = (s >> 1) & 3;
        char* yl = yl0 + (rot & 1) * 20480;
        TRDECL; TR(0);
        const char* mb0 = (s > 0 ? mine + (M_SLOT0 + rot * 8) * SLOT : p.hop + M0_OFF + phys * 8192) + 2 * w * SLOT + 2048;
        const char* mb1 = mb0 + SLOT;
        asm volatile("" : "+s"(mb0), "+s"(mb1));
        u32 yt = 0;
        bool late = s == 0;
        if (s > 0) { yt = sched_get(ynext, mine + sch + (s - 1) * 128 + 4, cap); wait_rt(yt + CLAG); }
        auto ldy = [&](const int f) { return *(const bf8*)(yl + (f < 16 ? (f >> 2) * 5 + (f & 3) : (f - 16) * 5 + 4) * 1024); };
        bf8 Y0[20 - 3 * NH_];
        TR(1);
        {
            f32x4 acc[4];
            u32x4 A[10];
            auto gather = [&]() {
                u32 vo = lane * 16;
                asm volatile("" : "+v"(vo) :: "memory");
#pragma unroll
                for (int ks = 0; ks < 10; ++ks) {
                    A[ks] = ld16(ks < 5 ? mb0 : mb1, vo, (ks % 5 - 2) * 1024);
                    SCHEDB();
                    if (ks < 4) ZERO4(acc[ks]);
                    if (ks < 9) asm volatile("s_nop %0" :: "n"(PACE));
                    SCHEDB();
                }
            };
            auto mproj = [&]() {
#pragma unroll
                for (int ks = 0; ks < 10; ++ks) {
#pragma unroll
                    for (int t = 0; t < 4; ++t) { if (ks < 8) SMM_A(acc[t], A[ks], WA[(ks * 4 + t) & 31], sidx); else SMM_V(acc[t], A[ks], WV[(ks * 4 + t) & 7], sidx); }
                }
                asm volatile("s_nop 7");
#pragma unroll
                for (int t = 0; t < 4; ++t) PIN(acc[t]);
            };
            gather();
#pragma unroll
            for (int i = 0; i < 20 - 3 * NH_; ++i) Y0[i] = ldy(i);
            SCHEDB();
            mproj();
            if (__builtin_expect(__any(__builtin_isunordered(acc[0][0], acc[0][1]) || __builtin_isunordered(acc[0][2], acc[0][3])), 0)) {
                CNT(1);
                late = true;
                for (int tries = 1;; ++tries) {
                    gather();
                    u32 m = 0;
#pragma unroll
                    for (int ks = 0; ks < 10; ++ks) m = max4(m, A[ks]);
                    if (!__any(m == ~0u)) break;
                    if (tries >= cap) { cap = 1; break; }
                    __builtin_amdgcn_s_sleep(8);
                }
                mproj();
            }
#pragma unroll
            for (int pr = 0; pr < 2; ++pr) {
                float v[4];
#pragma unroll
                for (int i = 0; i < 4; ++i) v[i] = halfsum(acc[2 * pr][i], acc[2 * pr + 1][i]);
                *(u32x2*)(shm + C_YL + (rot & 1) * 20480 + (w * 5 + 4) * 1024 + ((2 * pr + (g >> 1)) * 16 + fr) * 16 + (g & 1) * 8) = (u32x2){pk2bf(v[0], v[1]), pk2bf(v[2], v[3])};
            }
        }
        RAW_BARRIER();
        TR(2);
        u32 took = 0;
        if (s < NSEG - 1) {
            const char* pb[3];
#pragma unroll
            for (int i = 0; i < 3; ++i) { pb[i] = mine + (P_SLOT0 + (rot * 4 + c / 20) * PCS + i) * SLOT + 4096; asm volatile("" : "+s"(pb[i])); }
            auto ldp = [&](const int f, const u32 vo, const u32 vm) { return f < 16 ? ld16(pb[f >> 3], vo, ((f & 7) - 4) * 1024) : ld16(pb[2], vm, (f - 18) * 2048); };
            u32 xt;
            auto partial = [&](auto NF, auto F0, f32x4& acc, auto&& lastp) {
                constexpr int nf = CI(NF), f0 = CI(F0);
                bf8 (&Y)[nf] = Y0;
#pragma unroll
                for (int i = 0; i < nf; ++i) PIN(Y[i]);
                u32 vo = pvo;
                asm volatile("" : "+v"(vo));
                xt = late ? sched_get(~0u, mine + sch + s * 128, cap) : yt + k1;
                TR(3);
                if (!SYNCB || f0 == 0) wait_rt(xt);
                if (SYNCB) __builtin_amdgcn_s_barrier();
                TRV(7, xt); TRC(6);
                if (W0D > 0 && f0 == 0) asm volatile("s_nop %0" :: "n"(W0D));
                asm volatile("" ::: "memory");
                u32x4 P[nf];
#pragma unroll
                for (int i = 0; i < nf; ++i) { P[i] = ldp(f0 + i, vo, vo); SCHEDB(); }
#pragma unroll
                for (int i = 0; i < nf; ++i) {
                    if (i == nf - LATEP) lastp();
                    if (i == 0) MMA0_VV(acc, Y[i], P[i]); else MMA_VV(acc, Y[i], P[i]);
                    if (i == 0) TRC(7);
                    SCHEDB();
                }
                TRC(8);
            };
            f32x4 acc;
            if (w > 0) {
                bf8 Y[NH_];
                const char* q[NH_];
                u32 vo[NH_], ex = lds0 + C_EX + (w - 1) * 1024 + lane * 16;
#pragma unroll
                for (int i = 0; i < NH_; ++i) {
                    const int f = 20 - (4 - w) * NH_ + i;
                    Y[i] = ldy(f);
                    q[i] = pb[0] + (f >> 3) * SLOT + (f & 7) * (f < 16 ? 1024 : 2048) - 4096;
                    vo[i] = f < 16 ? pvo : pvm;
                    asm volatile("" : "+s"(q[i]));
                    PIN(vo[i]);
                }
#pragma unroll
                for (int i = 0; i < NH_; ++i) PIN(Y[i]);
                PIN(ex);
                __builtin_amdgcn_s_barrier();
                TRC(6);
                asm volatile("" ::: "memory");
                u32x4 P[NH_];
#pragma unroll
                for (int i = 0; i < NH_; ++i) { P[i] = ld16(q[i], vo[i], 0); SCHEDB(); }
#pragma unroll
                for (int i = 0; i < NH_; ++i) { if (i == 0) MMA0_VV(acc, Y[i], P[i]); else MMA_VV(acc, Y[i], P[i]); if (i == 0) TRC(7); }
                TRC(8);
                asm volatile("s_nop 7" : "+v"(acc));
                *(LDS3 f32x4*)(uintptr_t)ex = acc;
                RAW_BARRIER();
            } else {
                const int rn = ((s + 1) >> 1) & 3;
                u32 dst = g * 2 * SLOT + (lane & 7) * 16;
                char* dstb = other + (M_SLOT0 + rn * 8 + c / 40) * SLOT + (c % 40) * 128;
                u32 exa = lds0 + C_EX + lane * 16;
                asm volatile("" : "+s"(dstb), "+v"(dst), "+v"(exa));
                f32x4 h1, h2, h3;
                partial(std::integral_constant<int, 20 - 3 * NH_>(), std::integral_constant<int, 0>(), acc, [&] {
                    __builtin_amdgcn_s_barrier();
                    asm volatile("" ::: "memory");
                    h1 = *(LDS3 const f32x4*)(uintptr_t)exa; h2 = *(LDS3 const f32x4*)(uintptr_t)(exa + 1024); h3 = *(LDS3 const f32x4*)(uintptr_t)(exa + 2048);
                    SCHEDB();
                });
                h1 = (h1 + h2) + h3;
                asm volatile("s_nop 2" : "+v"(acc), "+v"(h1));
                acc += h1;
                float v0, v1;
                auto merge = [&]() {
                    auto a = __builtin_amdgcn_permlane32_swap(__float_as_uint(acc[0]), __float_as_uint(acc[2]), false, false);
                    auto b = __builtin_amdgcn_permlane32_swap(__float_as_uint(acc[1]), __float_as_uint(acc[3]), false, false);
                    v0 = __uint_as_float(a[0]) + fdpp<0x128>(__uint_as_float(a[1]));
                    v1 = __uint_as_float(b[0]) + fdpp<0x128>(__uint_as_float(b[1]));
                };
                merge();
                int tries = 0;
                if (__builtin_expect(__any(__builtin_isunordered(v0, v1)), 0)) {
                    CNT(2);
                    wait_rt(sched_get(~0u, mine + sch + s * 128, cap));
                    while (true) {
                        u32 vo = pvo, vm = pvm, m = 0;
                        asm volatile("" : "+v"(vo), "+v"(vm) :: "memory");
                        u32x4 P[20];
#pragma unroll
                        for (int i = 0; i < 20; ++i) P[i] = ldp(i, vo, vm);
                        f32x4 hh[4];
#pragma unroll
                        for (int i = 0; i < 20; ++i) {
                            const bf8 y = ldy(i);
                            const int k = i < 20 - 3 * NH_ ? 0 : (i - 20 + 4 * NH_) / NH_;
                            if (i == 0 || i == 20 - 3 * NH_ || i == 20 - 2 * NH_ || i == 20 - NH_) MMA0_VV(hh[k], y, P[i]); else MMA_VV(hh[k], y, P[i]);
                            m = max4(m, P[i]);
                            SCHEDB();
                        }
                        asm volatile("s_nop 7" : "+v"(hh[0]), "+v"(hh[1]), "+v"(hh[2]), "+v"(hh[3]));
                        acc = hh[0] + ((hh[1] + hh[2]) + hh[3]);
                        ++tries;
                        if (!__any(m == ~0u)) break;
                        if (tries >= cap) { cap = 1; break; }
                        __builtin_amdgcn_s_sleep(4);
                    }
                    merge();
                }
                TRV(6, tries + 1);
                asm volatile("" :: "s"(dstb));
                const u32 dw = nosent(pk2bf(v0, v1));
                auto e = __builtin_amdgcn_permlane32_swap(dw, dw, false, false);
                auto e0 = __builtin_amdgcn_permlane16_swap(e[0], e[0], false, false);
                auto e1 = __builtin_amdgcn_permlane16_swap(e[1], e[1], false, false);
                st16f(dstb, dst, (u32x4){e0[0], e1[0], e0[1], e1[1]}, 0x00ff00ff00ff00ffull);
                TR(4);
                took = tries == 0 && s > 0 ? rt32() - xt : 0;
            }
            if (s == NSEG - 2) RAW_BARRIER();
        } else wait_rt(sched_get(~0u, mine + sch + s * 128, cap) + T_PX);
        auto tokens = [&](auto MORE) {
            f32x4 oa[4], ya[4][2];
            u32x4 P[20];
            u32 vo = lane * 16 + w * 4096, vm = lane * 16 + 16384 + w * 1024;
            asm volatile("" : "+v"(vo), "+v"(vm));
            const char* pb[4];
#pragma unroll
            for (int i = 0; i < 4; ++i) { pb[i] = px + (size_t)(set * 4 + rot) * PX_BUF + i * 20480; asm volatile("" : "+s"(pb[i])); }
            auto ldp = [&](const int i) { P[i] = i % 5 == 4 ? ld16(pb[i / 5], vm, 0) : ld16(pb[i / 5], vo, (i % 5) * 1024); };
            int ln = lane;
            asm volatile("" : "+v"(ln));
            u32 rg, peek, peek2;
            bf8 y[5];
            auto side = [&](const int i) {
                if (i == 4) rg = sched_peek((wsp(p) + O_XR) + layer * ACT + (size_t)s * 40960 + ((c >> 2) * 2 + (w >> 1)) * 1024 + (c & 2) * 256 + (w & 1) * 128 + (c & 1) * 4 + (u32)((ln & 2) * 128 + (ln >> 3) * 16 + (ln & 1) * 8));
                if (i >= 16 && i < 36) ldp(i - 16);
                if (i == 2 && CI(MORE) && w == 0) st4_lane0(mine + (T_SLOT + (c >> 6)) * SLOT + (c & 63) * 128, took);
                if (i == 5 && CI(MORE)) peek2 = sched_peek(mine + sch + s * 128 + 8);
                if (i >= 35) y[i - 35] = *(const bf8*)(yl + (w * 5 + i - 35) * 1024);
            };
            TRC(9);
            if constexpr (CI(MORE)) yx(ya, s + 2, side);
            else {
#pragma unroll
                for (int i = 0; i < 40; ++i) side(i);
            }
            TRC(10);
            bf8 yn[4];
            auto outx = [&](const bool first) {
                if (!first) {
#pragma unroll
                    for (int i = 0; i < 20; ++i) PIN(P[i]);
                    NOPV();
                }
#pragma unroll
                for (int q = 0; q < 4; ++q)
#pragma unroll
                    for (int j = 0; j < 5; ++j) {
                        if (j == 0) MMA0_VV(oa[q], y[j], P[q * 5 + j]); else MMA_VV(oa[q], y[j], P[q * 5 + j]);
                        if (CI(MORE) && first && q * 5 + j >= 3 && q * 5 + j < 19) {
                            const int e = q * 5 + j - 3;
                            SCHEDB();
                            if ((e & 1) == 0) PIN(ya[e >> 2][(e >> 1) & 1]);
                            ((u32*)&yn[e >> 2])[e & 3] = pk2bf(ya[e >> 2][(e >> 1) & 1][(e & 1) * 2], ya[e >> 2][(e >> 1) & 1][(e & 1) * 2 + 1]);
                            if ((e & 3) == 3) *(bf8*)(yl0 + (~rot & 1) * 20480 + (w * 5 + (e >> 2)) * 1024) = yn[e >> 2];
                            SCHEDB();
                        }
                    }
                if (CI(MORE) && first) { SCHEDB(); peek = sched_peek(mine + sch + (s + 1) * 128 + 4); SCHEDB(); }
                asm volatile("s_nop 7");
#pragma unroll
                for (int q = 0; q < 4; ++q) PIN(oa[q]);
            };
            outx(true);
            if (__builtin_expect(__any(__builtin_isunordered(oa[0][0], oa[1][0]) || __builtin_isunordered(oa[2][0], oa[3][0])), 0)) {
                CNT(3);
                for (int tries = 1;; ++tries) {
                    asm volatile("" : "+v"(vo), "+v"(vm) :: "memory");
                    u32 m = 0;
#pragma unroll
                    for (int i = 0; i < 20; ++i) ldp(i);
#pragma unroll
                    for (int i = 0; i < 20; ++i) m = max4(m, P[i]);
                    if (!__any(m == ~0u)) break;
                    if (tries >= cap) { cap = 1; break; }
                    __builtin_amdgcn_s_sleep(8);
                }
                outx(false);
            }
            if constexpr (!CI(MORE)) { if (c == 0 && w == 0) stamp2(p.hop, 48 + layer * 4 + set); }
            const bool own = (fr < 8) == (g < 2);
#pragma unroll
            for (int q = 0; q < 4; q += 2) {
                f32x4 o;
#pragma unroll
                for (int i = 0; i < 4; ++i) { const float t = fdpp<0x128>(oa[q + 1][i]); o[i] = own ? oa[q][i] : t; }
                *(f32x4*)(shm + C_RED + ((w * 2 + (g >> 1)) * 32 + (q + !own) * 8 + (fr & 7)) * 32 + (g & 1) * 16) = o;
            }
            RAW_BARRIER();
            TR(5);
            typedef __attribute__((ext_vector_type(2))) float f32x2;
            const char* rp = shm + C_RED + (ln & 4) * 1024 + w * 256 + (ln >> 3) * 32 + (ln & 3) * 8;
            f32x2 a = (*(const f32x2*)rp + *(const f32x2*)(rp + 1024)) + (*(const f32x2*)(rp + 2048) + *(const f32x2*)(rp + 3072));
            if constexpr (CI(MORE)) { k1 = sched_get(peek2, mine + sch + s * 128 + 8, cap); ynext = __builtin_amdgcn_readfirstlane(peek); asm volatile("" : "+s"(ynext), "+s"(k1)); }
            a[0] += fdpp<0x104>(a[0]); a[1] += fdpp<0x104>(a[1]);
            const u32 y2 = pk2bf(a[0], a[1]), d = nosent(pk2bf(bflo(rg) + bflo(y2), bfhi(rg) + bfhi(y2)));
            const u32x4 o4 = {d, (u32)__builtin_amdgcn_mov_dpp(d, 0x55, 0xf, 0xf, true), (u32)__builtin_amdgcn_mov_dpp(d, 0xaa, 0xf, 0xf, true), (u32)__builtin_amdgcn_mov_dpp(d, 0xff, 0xf, 0xf, true)};
            st16m((wsp(p) + O_X1) + layer * ACT + (size_t)s * 40960 + c * 512 + w * 128, ln * 2, o4, 0x0101010101010101ull);
            if (w == 1) {
                u32x4 v;
                asm volatile("v_mov_b64 %0, -1\n\tv_mov_b64 %1, -1" : "=v"(((u64*)&v)[0]), "=v"(((u64*)&v)[1]));
                st16m(other + (M_SLOT0 + (((s + 7) >> 1) & 3) * 8 + c / 40) * SLOT + (c % 40) * 128, (ln >> 4) * 2 * SLOT + (ln & 7) * 16, v, 0x00ff00ff00ff00ffull);
            }
            TRC(11); TRFLUSH(set * 128 + 32 + c);
        };
        if (s + 2 >= NSEG) { tokens(std::integral_constant<int, 0>()); break; }
        tokens(std::integral_constant<int, 1>());
    }
    TT(set * 128 + 32 + c, 0);
    asm volatile("" : "+s"(cb), "+s"(rb));
    dma_kb<10>((wsp(p) + O_WO) + cb * 81920 + 40960 + w * 10240, lds0 + C_XN + 40960 + w * 10240, lane * 16);
    if (w == 0) { const u32 t = sched_get(~0u, mine + (T_SLOT + 1) * SLOT + (50 + layer * 4) * 128, cap); *(u32*)(shm + C_EX) = t; }
    VM0(); RAW_BARRIER();
    wait_rt(__builtin_amdgcn_readfirstlane(*(const u32*)(shm + C_EX)) + T_OB + OB_STEP * w + (layer == 0 ? OB_COLD + 8 * w : 0));
    {
        int ln = lane;
        asm volatile("" : "+v"(ln));
        u32x4 O[20], R[2];
        u32 dw[8];
        auto ready = [&](const int T) {
            const char* ob = (wsp(p) + O_OB) + layer * ACT + (size_t)T * 20480 + 16384;
            const u32 off[1] = {(u32)(ln & 31) * 128};
            wait_flags<1>(ob, off, cap);
        };
        auto load = [&](const int T) {
            const char* ob0 = (wsp(p) + O_OB) + layer * ACT + (size_t)T * 20480 + 4096;
            const char* ob1 = ob0 + 8192;
            const char* ob2 = ob0 + 12288;
            const char* xb = (wsp(p) + O_X1) + layer * ACT + (size_t)(T >> 1) * 40960 + cb * 4096 + (T & 1) * 256;
            u32 vo = ln * 16, vr = (ln >> 4) * 256 + (ln & 15) * 8, vx = (ln >> 4) * 1024 + (ln & 15) * 16;
            asm volatile("" : "+v"(vo), "+v"(vr), "+v"(vx), "+s"(ob0), "+s"(ob1), "+s"(ob2), "+s"(xb) :: "memory");
#pragma unroll
            for (int k = 0; k < 16; ++k) { O[k] = ld16(k < 8 ? ob0 : ob1, vo, ((k & 7) - 4) * 1024); SCHEDB(); }
#pragma unroll
            for (int k = 0; k < 4; ++k) {
                const u64 lo = __hip_atomic_load((const gu64*)(ob2 + vr + k * 1024), __ATOMIC_RELAXED, __HIP_MEMORY_SCOPE_AGENT);
                const u64 hi = __hip_atomic_load((const gu64*)(ob2 + vr + k * 1024 + 128), __ATOMIC_RELAXED, __HIP_MEMORY_SCOPE_AGENT);
                O[16 + k] = (u32x4){(u32)lo, (u32)(lo >> 32), (u32)hi, (u32)(hi >> 32)};
            }
            R[0] = ld16(xb, vx, 0); R[1] = ld16(xb, vx, 512);
            SCHEDB();
        };
        auto gemm = [&]() {
            u32 wa0 = lds0 + C_XN + ln * 16, wa1 = wa0 + 40960;
            asm volatile("" : "+v"(wa0), "+v"(wa1));
            f32x4 acc[4];
            bf8 wf[8];
#pragma unroll
            for (int t = 0; t < 8; ++t) wf[t] = *(LDS3 const bf8*)(uintptr_t)(wa0 + t * 1024);
#pragma unroll
            for (int k = 0; k < 20; ++k) {
                if (k >= 16) { PIN(O[k]); NOPV(); }
#pragma unroll
                for (int t = 0; t < 4; ++t) {
                    if (k == 0) MMA0_VV(acc[t], wf[t], O[k]); else MMA_VV(acc[t], wf[(k & 1) * 4 + t], O[k]);
                    SCHEDB();
                    if (k < 18) wf[(k & 1) * 4 + t] = *(LDS3 const bf8*)(uintptr_t)((k < 8 ? wa0 : wa1) + (((k + 2) % 10) * 4 + t) * 1024);
                    SCHEDB();
                }
            }
            asm volatile("s_nop 7" : "+v"(acc[0]), "+v"(acc[1]), "+v"(acc[2]), "+v"(acc[3]));
            float chk = 0.f;
#pragma unroll
            for (int i = 0; i < 8; ++i) {
                const u32 r = R[i >> 2][i & 3], y = pk2bf(acc[i >> 1][(i & 1) * 2], acc[i >> 1][(i & 1) * 2 + 1]);
                const float a = bflo(r) + bflo(y), b = bfhi(r) + bfhi(y);
                dw[i] = pk2bf(a, b);
                chk += a;
            }
            return __any(chk != chk);
        };
        const int T0 = rb + 16 * w;
        if (OB_POLL) ready(T0);
        load(T0);
        static_for<0, 2>([&](auto PASS) {
            constexpr int pass = CI(PASS);
            const int T = T0 + 64 * pass;
            TT(set * 128 + 32 + c, 1 + pass * 4);
            if (__builtin_expect(gemm(), 0)) {
                for (int tries = 0;; ++tries) {
                    ready(T); load(T);
                    u32 m = max4(max4(0, R[0]), R[1]);
#pragma unroll
                    for (int k = 0; k < 20; ++k) m = max4(m, O[k]);
                    if (!__any(m == ~0u)) break;
                    if (tries >= cap) { cap = 1; break; }
                    __builtin_amdgcn_s_sleep(6);
                }
                gemm();
            }
            TT(set * 128 + 32 + c, 2 + pass * 4);
            if (layer == 1) {
                st32((char*)p.out + (size_t)T * 16 * DM * 2 + cb * 128, (ln & 15) * (DM * 2) + (ln >> 4) * 32, (u32x4){dw[0], dw[1], dw[2], dw[3]}, (u32x4){dw[4], dw[5], dw[6], dw[7]});
            } else {
            float s1 = 0.f, s2 = 0.f;
#pragma unroll
            for (int i = 0; i < 8; ++i) { const float a = bflo(dw[i]), b = bfhi(dw[i]); s1 += a + b; s2 = fmaf(a, a, fmaf(b, b, s2)); }
            s1 = grp_sum(s1); s2 = grp_sum(s2);
            char* stl = (wsp(p) + O_ST) + (size_t)T * 1280;
            st8m(stl + cb * 128, ln * 8, (u32x2){nosent(__float_as_uint(s1)), nosent(__float_as_uint(s2))}, 0xffffull);
            const size_t fo = ACT + (size_t)(T >> 1) * 40960 + (cb * 4 + (T & 1)) * 1024;
            const u32 lo = (ln >> 5) * 2048 + ((ln >> 4) & 1) * 512 + (ln & 15) * 16;
            __builtin_amdgcn_s_sleep(ST_LAG);
            for (int spin = 0;; ++spin) {
                u32 m = 0;
                s1 = 0.f; s2 = 0.f;
#pragma unroll
                for (int i = 0; i < 3; ++i) {
                    const int k = (ln >> 4) + 4 * i;
                    const u64 v = __hip_atomic_load((const gu64*)(stl + min(k, 9) * 128 + (ln & 15) * 8), __ATOMIC_RELAXED, __HIP_MEMORY_SCOPE_AGENT);
                    m = max(m, max((u32)v, (u32)(v >> 32)));
                    if (k < 10) { s1 += __uint_as_float((u32)v); s2 += __uint_as_float((u32)(v >> 32)); }
                }
                if (!__any(m == ~0u)) break;
                if (spin >= cap) { cap = 1; break; }
                __builtin_amdgcn_s_sleep(2);
            }
            TT(set * 128 + 32 + c, 3 + pass * 4);
            s1 = grp_sum(s1); s2 = grp_sum(s2);
            const float mu = s1 * (1.0f / DM), rs = __builtin_amdgcn_rsqf(fmaxf(fmaf(-mu, mu, s2 * (1.0f / DM)), 0.f) + 1e-5f);
            u32 dn[8];
#pragma unroll
            for (int i = 0; i < 8; ++i) dn[i] = pk2bf((bflo(dw[i]) - mu) * rs, (bfhi(dw[i]) - mu) * rs);
            st16s((wsp(p) + O_XN) + fo, lo, (u32x4){dn[0], dn[4], dn[1], dn[5]}); st16s((wsp(p) + O_XN) + fo + 256, lo, (u32x4){dn[2], dn[6], dn[3], dn[7]});
            st16s((wsp(p) + O_XR) + fo, lo, (u32x4){dw[0], dw[4], dw[1], dw[5]}); st16s((wsp(p) + O_XR) + fo + 256, lo, (u32x4){dw[2], dw[6], dw[3], dw[7]});
            TT(set * 128 + 32 + c, 4 + pass * 4);
            if (pass == 0 && w == 0) { VM0(); st4_lane0(wsp(p) + O_FL + (rb * 10 + cb) * 128, 1u); }
            }
            if constexpr (pass == 0) load(T + 64);
        });
    }
    if (layer == 1) break;
    sch += 5 * SLOT; xn += ACT;
    if (w == 0) wait_first_tokens(p, set, lane, cap);
    RAW_BARRIER();
    TT(set * 128 + 32 + c, 9);
    dma_kb<10>(xn + (size_t)set * 40960 + w * 10240, lds0 + C_XN + w * 10240, lane * 16);
    }
}

constexpr int B_PQ = 0, B_IM = 131072, B_CS = 155648, B_IS = 176;
__device__ __forceinline__ void bw_role(const LaParams& p, const int hb, char* shm) {
    const int lane = threadIdx.x & 63, w = __builtin_amdgcn_readfirstlane(threadIdx.x >> 6);
    const int fr = lane & 15, g = lane >> 4;
    int cap = SPIN_CAP;
    if (hb == 0 && w == 0) {
        u32 best[2] = {~0u, ~0u};
        const u32 tc = rt32();
        for (int i = 0; i < 256; ++i) {
#pragma unroll
            for (int sd = 0; sd < 2; ++sd) {
                u64 t0, t1;
                u32 z = 0, one = 1;
                asm volatile("s_memtime %0\n\ts_mov_b64 exec, 1\n\ts_waitcnt lgkmcnt(0)\n\tglobal_atomic_add %2, %3, %2, %4 sc0 sc1\n\ts_waitcnt vmcnt(0)\n\t"
                             "s_memtime %1\n\ts_mov_b64 exec, -1\n\ts_waitcnt lgkmcnt(0)"
                             : "=&s"(t0), "=&s"(t1), "+v"(one) : "v"(z), "s"(p.hop + M0_OFF + sd * 8192 + 6144) : "memory");
                best[sd] = min(best[sd], (u32)(t1 - t0));
            }
            if (rt32() - tc > 1200u) break;
        }
        u32 xcc; asm volatile("s_getreg_b32 %0, hwreg(HW_REG_XCC_ID)" : "=s"(xcc));
        st4_lane0(wsp(p) + O_FL + 384 * 128, ((xcc >> 2) & 1) ^ (best[1] < best[0] ? 1u : 0u));
    }
    bf8 WA[64], WV[16];
    {
        const int t = threadIdx.x;
        float* xs = (float*)(shm + 81920);
        float* ph = xs + 128 * 61;
        for (int i = t; i < 400; i += 256) ph[i] = bf2f(p.wphi[hb * 400 + i]);
        const int j = 4 * w + (fr & 3), G = fr >> 2;
        for (int d0 = 0; d0 < DM; d0 += 128) {
            u32 v15[15];
#pragma unroll
            for (int k = 0; k < 15; ++k) { const int i = t + k * 256; v15[k] = *(const u32*)(p.wqkv + (size_t)(d0 + i / 30) * 1920 + ((i % 30) / 10) * 640 + hb * 20 + (i % 10) * 2); }
            __syncthreads();
#pragma unroll
            for (int k = 0; k < 15; ++k) { const int i = t + k * 256; float* o = xs + (i / 30) * 61 + (i % 30) * 2; o[0] = bflo(v15[k]); o[1] = bfhi(v15[k]); }
            __syncthreads();
            for (int kk = 0; kk < 4; ++kk) {
                float v[8];
                const float* xr = xs + (kk * 32 + g * 8) * 61;
                if (j < 10) {
                    const int m = j / 5, f = 5 * G + j % 5;
#pragma unroll
                    for (int e = 0; e < 8; ++e) v[e] = 0.f;
#pragma unroll 4
                    for (int i = 0; i < 20; ++i) {
                        const float pw = ph[i * 20 + f];
#pragma unroll
                        for (int e = 0; e < 8; ++e) v[e] = fmaf(xr[e * 61 + m * 20 + i], pw, v[e]);
                    }
                } else {
#pragma unroll
                    for (int e = 0; e < 8; ++e) v[e] = j < 15 ? xr[e * 61 + 40 + 5 * G + min(j, 14) - 10] : 1.0f;
                }
                *(u32x4*)(shm + ((d0 / 32 + kk) * 4 + w) * 1024 + lane * 16) = (u32x4){pk2bf(v[0], v[1]), pk2bf(v[2], v[3]), pk2bf(v[4], v[5]), pk2bf(v[6], v[7])};
            }
        }
        __syncthreads();
#pragma unroll
        for (int i = 0; i < 64; ++i) WA[i] = *(const bf8*)(shm + i * 1024 + lane * 16);
#pragma unroll
        for (int i = 0; i < 16; ++i) WV[i] = *(const bf8*)(shm + (64 + i) * 1024 + lane * 16);
        __syncthreads();
    }
    auto proj = [&](f32x4 (&acc)[4], f32x4& sq, auto&& fetch) {
        u32x4 X[20];
#pragma unroll
        for (int k = 0; k < 12; ++k) { X[k] = fetch(k); SCHEDB(); }
#pragma unroll
        for (int k = 0; k < 20; ++k) {
#pragma unroll
            for (int t = 0; t < 4; ++t) {
                if (k == 0) MMA0_AV(acc[t], WA[t], X[k]); else if (k < 16) MMA_AV(acc[t], WA[(k * 4 + t) & 63], X[k]); else MMA_VV(acc[t], WV[(k * 4 + t) & 15], X[k]);
            }
            if (k == 0) MMA0_VV(sq, X[k], X[k]); else MMA_VV(sq, X[k], X[k]);
            SCHEDB();
            if (k < 8) X[k + 12] = fetch(k + 12);
            SCHEDB();
        }
        asm volatile("s_nop 7" : "+v"(acc[0]), "+v"(acc[1]), "+v"(acc[2]), "+v"(acc[3]), "+v"(sq));
    };
    {
        f32x4 t, cs[4];
        u32x4 o1 = {0x3f803f80u, 0x3f803f80u, 0x3f803f80u, 0x3f803f80u};
        asm volatile("s_nop 1" : "+v"(o1));
        proj(cs, t, [&](int) { return o1; });
#pragma unroll
        for (int i = 0; i < 4; ++i) *(f32x4*)(shm + B_CS + i * 1024 + lane * 16) = cs[i];
    }
    char* im = shm + B_IM + w * 6144;
    const u32 imr = (u32)(uintptr_t)(LDS3 char*)im + (4 * g + (fr >> 2)) * B_IS + (fr & 3) * 8;
    typedef __attribute__((ext_vector_type(32))) u32 u32x32;
    for (int layer = 0; layer < 2; ++layer) {
        const char* sch = p.hop + (hb & 1) * 8192 + (S_SLOT + layer * 5 + 4) * SLOT;
        f32x4 kv[3][2];
#pragma unroll
        for (int i = 0; i < 6; ++i) ZERO4(kv[i >> 1][i & 1]);
        u32x32 q5;
        u32 tn = 0;
        for (int it = 0; it < 32; ++it) {
            const int s = 2 * it + (w >> 1);
            if (s + 2 < NSEG) { wait_rt(tn); tn = sched_get(~0u, sch + (s + 1) * 128 + 4, cap); wait_rt(tn + B_LAG); tn += BPEEK; }
            else wait_rt(sched_get(~0u, p.hop + (hb & 1) * 8192 + (T_SLOT + 1) * SLOT + (48 + layer * 4 + (s & 1)) * 128, cap) + T_X1);
            const char* xb = (wsp(p) + O_X1) + layer * ACT + (size_t)s * 40960 + (w & 1) * 256;
            if (it >= 30) TT((hb >> 4) * 128 + 112 + (hb & 15), (it - 30) * 3);
            f32x4 acc[4], sq;
            const char* xk[5];
#pragma unroll
            for (int i = 0; i < 5; ++i) { xk[i] = xb + i * 8192 + 4096; asm volatile("" : "+s"(xk[i])); }
            for (int pass = 0;; ++pass) {
                u32 vo = g * 512 + fr * 16;
                asm volatile("" : "+v"(vo) :: "memory");
                auto fetch = [&](const int k) { return ld16(xk[k >> 2], vo, ((k & 3) - 2) * 2048); };
                proj(acc, sq, fetch);
                if (pass > 0 || !__any(acc[3][3] != acc[3][3])) break;
                CNT(4);
                for (int tries = 0;; ++tries) {
                    asm volatile("" : "+v"(vo) :: "memory");
                    u32 m = 0;
#pragma unroll
                    for (int k = 0; k < 20; k += 10) {
#pragma unroll
                        for (int i = 0; i < 10; ++i) m = max4(m, fetch(k + i));
                        SCHEDB();
                    }
                    if (!__any(m == ~0u)) break;
                    if (tries >= cap) { cap = 1; break; }
                    __builtin_amdgcn_s_sleep(6);
                }
            }
            if (it >= 30) TT((hb >> 4) * 128 + 112 + (hb & 15), (it - 30) * 3 + 1);
            if (s == NSEG - 1 && hb == 5 && w == 3) stamp2(p.hop, 50 + layer * 4);
            const float dsel = (fr & 2) ? ((fr & 1) ? sq[3] : sq[2]) : ((fr & 1) ? sq[1] : sq[0]);
            const float s2 = grp_sum(g == (fr >> 2) ? dsel : 0.f);
            const float mu = acc[3][3] * (1.0f / DM), rs = __builtin_amdgcn_rsqf(fmaxf(fmaf(-mu, mu, s2 * (1.0f / DM)), 0.f) + 1e-5f);
            float v[15];
            f32x4 cs[4];
#pragma unroll
            for (int i = 0; i < 4; ++i) cs[i] = *(const f32x4*)(shm + B_CS + i * 1024 + lane * 16);
#pragma unroll
            for (int j = 0; j < 15; ++j) v[j] = fmaf(-mu, cs[j >> 2][j & 3], acc[j >> 2][j & 3]) * rs;
            u32 ph[2][5];
#pragma unroll
            for (int m = 0; m < 2; ++m) {
                const float* x = v + 5 * m;
                const float mx = grp_max(fmaxf(fmaxf(fmaxf(x[0], x[1]), fmaxf(x[2], x[3])), x[4]));
                const float mn = -grp_max(-fminf(fminf(fminf(x[0], x[1]), fminf(x[2], x[3])), x[4]));
                float e[10], a = 0.f, b = 0.f;
#pragma unroll
                for (int j = 0; j < 5; ++j) {
                    e[j] = __builtin_amdgcn_exp2f((x[j] - mx) * 1.4426950408889634f); a += e[j];
                    e[5 + j] = __builtin_amdgcn_exp2f((mn - x[j]) * 1.4426950408889634f); b += e[5 + j];
                }
                a = __builtin_amdgcn_rcpf(grp_sum(a)); b = __builtin_amdgcn_rcpf(grp_sum(b));
#pragma unroll
                for (int j = 0; j < 5; ++j) { e[j] *= a; e[5 + j] *= b; }
#pragma unroll
                for (int j = 0; j < 5; ++j) ph[m][j] = pk2bf(e[2 * j], e[2 * j + 1]);
            }
            *(u32x4*)(shm + B_PQ + (w * 32 + it) * 1024 + lane * 16) = (u32x4){ph[0][0], ph[0][1], ph[0][2], ph[0][3]};
            q5[it] = ph[0][4];
            char* row = im + fr * B_IS;
            *(u32x2*)(row + g * 8) = (u32x2){ph[1][0], ph[1][1]};
            *(u32x2*)(row + 32 + g * 8) = (u32x2){ph[1][2], ph[1][3]};
            *(u32x2*)(row + 64 + g * 8) = (u32x2){ph[1][4], 0u};
            *(u32x2*)(row + 96 + g * 8) = (u32x2){pk2bf(v[10], v[11]), pk2bf(v[12], v[13])};
            *(u32x2*)(row + 128 + g * 8) = (u32x2){pk2bf(v[14], 1.0f), 0u};
            s16x4 tf[5];
#pragma unroll
            for (int i = 0; i < 5; ++i) tf[i] = __builtin_amdgcn_ds_read_tr16_b64_v4i16((LDS3 s16x4*)(uintptr_t)(imr + i * 32));
#pragma unroll
            for (int i = 0; i < 6; ++i) MMA16(kv[i >> 1][i & 1], tf[i >> 1], tf[3 + (i & 1)]);
            if (it >= 30) TT((hb >> 4) * 128 + 112 + (hb & 15), (it - 30) * 3 + 2);
        }
        asm volatile("s_nop 7" : "+v"(kv[0][0]), "+v"(kv[0][1]), "+v"(kv[1][0]), "+v"(kv[1][1]), "+v"(kv[2][0]), "+v"(kv[2][1]));
#pragma unroll
        for (int i = 0; i < 6; ++i) *(f32x4*)(im + i * 1024 + lane * 16) = kv[i >> 1][i & 1];
        RAW_BARRIER();
        bf8 ka[2];
        u32x2 kb[2];
#pragma unroll
        for (int dt = 0; dt < 2; ++dt) {
            f32x4 t[3];
#pragma unroll
            for (int ft = 0; ft < 3; ++ft) {
                const char* a = shm + B_IM + (ft * 2 + dt) * 1024 + lane * 16;
                t[ft] = (*(const f32x4*)a + *(const f32x4*)(a + 6144)) + (*(const f32x4*)(a + 12288) + *(const f32x4*)(a + 18432));
            }
            ka[dt] = pack8(t[0], t[1]);
            kb[dt] = (u32x2){pk2bf(t[2][0], t[2][1]), pk2bf(t[2][2], t[2][3])};
            PIN(ka[dt]); PIN(kb[dt]);
        }
        char* ob = (wsp(p) + O_OB) + layer * ACT + (size_t)w * 20480 + hb * 512;
        TT((hb >> 4) * 128 + 112 + (hb & 15), 6);
        const u32 rofs = g * 81920 + 16384 - hb * 384 + fr * 8, mofs = (g & 1) * 81920 + (g >> 1) * 256 + fr * 16;
        f32x4 o[2][4][2];
        bf8 qa[2][4];
        u32x2 qb[4];
        auto ldq = [&](const int gr) {
#pragma unroll
            for (int u = 0; u < 4; ++u) qa[gr & 1][u] = *(const bf8*)(shm + B_PQ + (w * 32 + gr * 4 + u) * 1024 + lane * 16);
        };
        auto mm = [&](const int gr, const int k) {
            SCHEDB();
            if (k < 4) { qb[k] = (u32x2){q5[gr * 4 + k], 0u}; PIN(qb[k]); }
            if (k < 8) MMA0_VV(o[gr & 1][k >> 1][k & 1], ka[k & 1], qa[gr & 1][k >> 1]);
            else MMA16(o[gr & 1][(k - 8) >> 1][k & 1], kb[k & 1], qb[(k - 8) >> 1]);
            SCHEDB();
        };
        ldq(0); ldq(1);
        NOPV();
#pragma unroll
        for (int k = 0; k < 16; ++k) mm(0, k);
        static_for<0, 8>([&](auto IT) {
            constexpr int gr = CI(IT);
            auto nx = [&](const int k) { if constexpr (gr < 7) mm(gr + 1, k); else { SCHEDB(); asm volatile("s_nop 0"); SCHEDB(); } };
            if constexpr (gr == 7) asm volatile("s_nop 7");
            u32 d[4][2];
            float x[4];
            nx(0); nx(1);
#pragma unroll
            for (int u = 0; u < 4; ++u) {
                f32x4& a = o[gr & 1][u][0];
                f32x4& b = o[gr & 1][u][1];
                PIN(a); PIN(b);
                float inv = __builtin_amdgcn_rcpf(b[1] + 1e-6f), y0, y1, y2, y3;
                nx(2 + 3 * u);
                asm("v_mul_f32 %0, %4, %8\n\tv_mul_f32 %1, %5, %8\n\tv_mul_f32 %2, %6, %8" : "=&v"(y0), "=&v"(y1), "=&v"(y2), "=&v"(y3) : "v"(a[0]), "v"(a[1]), "v"(a[2]), "v"(a[3]), "v"(inv));
                nx(3 + 3 * u);
                y3 = a[3] * inv; x[u] = b[0] * inv;
                d[u][0] = pk2bf(y0, y1);
                nx(4 + 3 * u);
                d[u][1] = pk2bf(y2, y3);
                if (u & 1) {
                    auto t0 = __builtin_amdgcn_permlane16_swap(d[u - 1][0], d[u][0], false, false);
                    auto t1 = __builtin_amdgcn_permlane16_swap(d[u - 1][1], d[u][1], false, false);
                    st16s(ob + (u - 1) * 81920, mofs, (u32x4){nosent(t0[0]), nosent(t1[0]), nosent(t0[1]), nosent(t1[1])});
                }
            }
            if constexpr (gr < 6) ldq(gr + 2);
            nx(14);
            auto a0 = __builtin_amdgcn_permlane32_swap(__float_as_uint(x[0]), __float_as_uint(x[2]), false, false);
            auto a1 = __builtin_amdgcn_permlane32_swap(__float_as_uint(x[1]), __float_as_uint(x[3]), false, false);
            nx(15);
            auto b0 = __builtin_amdgcn_permlane16_swap(a0[0], a1[0], false, false);
            auto b2 = __builtin_amdgcn_permlane16_swap(a0[1], a1[1], false, false);
            st8m(ob, rofs, (u32x2){nosent(pk2bf(__uint_as_float(b0[0]), __uint_as_float(b0[1]))), nosent(pk2bf(__uint_as_float(b2[0]), __uint_as_float(b2[1])))}, ~0ull);
            ob += 4 * 81920;
            asm volatile("" : "+s"(ob));
        });
        RAW_BARRIER();
        TT((hb >> 4) * 128 + 112 + (hb & 15), 7);
    }
}

__global__ void __launch_bounds__(256, 1) la_kernel(LaParams p) {
    extern __shared__ __attribute__((aligned(16))) char shm[];
    p.hop = align2m(p.ws + O_HOP);
    if (threadIdx.x == 0) {
        u32 xcc; asm volatile("s_getreg_b32 %0, hwreg(HW_REG_XCC_ID)" : "=s"(xcc));
        int role = 255;
        for (int a = 0; a < 8; ++a) {
            const u32 v = (xcc + a) & 7;
            const u32 k = __hip_atomic_fetch_add((gu32*)(((u32*)(p.ws + O_CTL)) + v * 32), 1u, __ATOMIC_RELAXED, __HIP_MEMORY_SCOPE_AGENT);
            if (k < 32) { role = v * 32 + k; break; }
        }
        *(int*)shm = role;
    }
    __syncthreads();
    const int role = __builtin_amdgcn_readfirstlane(*(int*)shm);
    __syncthreads();
    const int set = role >> 7, id = (role >> 5) & 3, k = role & 31;
    if (k < 8) qk_role(p, set, id * 8 + k, shm);
    else if (k < 28) comb_role(p, set, id * 20 + k - 8, shm);
    else bw_role(p, (role >> 5) * 4 + k - 28, shm);
}
__device__ __forceinline__ float row16_sum(float x) { x += fdpp<0x128>(x); x += fdpp<0x124>(x); x += fdpp<0x122>(x); return x + fdpp<0x121>(x); }

#define PFB 0
struct PrepParams { const u16* W[4]; const u16* x; const u16* mem0; const u16* wo; char* ws; };
constexpr int N_PF = PFB, N_LN = 128, N_WO = 200, N_FILL = 512;
__global__ void __launch_bounds__(256) prep_kernel(PrepParams q) {
    int b = blockIdx.x;
    const int t = threadIdx.x, lane = t & 63, w = t >> 6, fr = lane & 15, g = lane >> 4;
    char* hop = align2m(q.ws + O_HOP);
    if (b < N_LN) {
        const int tok = b * 16 + w * 4 + g;
        u32x4 d[3][2];
        float s = 0.f;
#pragma unroll
        for (int i = 0; i < 3; ++i) {
            const u32x4* a = (const u32x4*)(q.x + (size_t)tok * DM + min(fr + 16 * i, 39) * 16);
            d[i][0] = a[0]; d[i][1] = a[1];
        }
#pragma unroll
        for (int i = 0; i < 3; ++i)
#pragma unroll
            for (int e = 0; e < 8; ++e) if (fr + 16 * i < 40) s += bflo(d[i][e >> 2][e & 3]) + bfhi(d[i][e >> 2][e & 3]);
        const float mu = row16_sum(s) * (1.0f / DM);
        float v = 0.f;
#pragma unroll
        for (int i = 0; i < 3; ++i)
#pragma unroll
            for (int e = 0; e < 8; ++e) if (fr + 16 * i < 40) { const float a = bflo(d[i][e >> 2][e & 3]) - mu, c = bfhi(d[i][e >> 2][e & 3]) - mu; v = fmaf(a, a, fmaf(c, c, v)); }
        const float rs = __builtin_amdgcn_rsqf(row16_sum(v) * (1.0f / DM) + 1e-5f);
#pragma unroll
        for (int i = 0; i < 3; ++i) {
            const int blk = fr + 16 * i;
            if (blk >= 40) continue;
            const size_t o = (size_t)(tok >> 5) * 40960 + ((blk >> 1) * 2 + ((tok >> 4) & 1)) * 1024 + ((blk & 1) * 32 + (tok & 15)) * 16;
            u32 n[8];
#pragma unroll
            for (int e = 0; e < 8; ++e) n[e] = pk2bf((bflo(d[i][e >> 2][e & 3]) - mu) * rs, (bfhi(d[i][e >> 2][e & 3]) - mu) * rs);
            *(u32x4*)(q.ws + O_XR + o) = (u32x4){d[i][0][0], d[i][1][0], d[i][0][1], d[i][1][1]};
            *(u32x4*)(q.ws + O_XR + o + 256) = (u32x4){d[i][0][2], d[i][1][2], d[i][0][3], d[i][1][3]};
            *(u32x4*)(q.ws + O_XN + o) = (u32x4){n[0], n[4], n[1], n[5]};
            *(u32x4*)(q.ws + O_XN + o + 256) = (u32x4){n[2], n[6], n[3], n[7]};
        }
        return;
    }
    b -= N_LN;
    if (b < N_PF) {
        const u32x4* src = (const u32x4*)q.W[b & 3] + (size_t)(b >> 2) * 3840 + t;
        u32x4 v[15];
#pragma unroll
        for (int i = 0; i < 15; ++i) v[i] = src[i * 256];
#pragma unroll
        for (int i = 0; i < 15; ++i) asm volatile("" :: "v"(v[i]));
        return;
    }
    b -= N_PF;
    if (b < N_WO) {
        const int cb = b / 20, kk = b % 20;
        const u16* col = q.wo + cb * 64 + 16 * (fr >> 2) + 4 * w + (fr & 3);
        u32 v[8];
#pragma unroll
        for (int e = 0; e < 8; ++e) {
            const int row = kk < 16 ? (2 * kk + (g >> 1)) * 20 + 5 * (2 * (g & 1) + (e >> 2)) + (e & 3) : (8 * (kk - 16) + 2 * g + (e >> 2)) * 20 + 5 * (e & 3) + 4;
            v[e] = col[(size_t)row * DM];
        }
        *(u32x4*)(q.ws + O_WO + (size_t)cb * 81920 + (kk * 4 + w) * 1024 + lane * 16) = (u32x4){v[0] | v[1] << 16, v[2] | v[3] << 16, v[4] | v[5] << 16, v[6] | v[7] << 16};
        return;
    }
    b -= N_WO;
    const u32x4 SENT = {~0u, ~0u, ~0u, ~0u};
    const u32 i0 = b * 256 + t;
    if (b == 0) ((u32*)(q.ws + O_CTL))[t] = 0;
    if (i0 < 8 * 2 * 320) {
        const int sl = i0 / 640, e = i0 % 320;
        const u32x4 v = *(const u32x4*)(q.mem0 + (e & 7) * DM + ((sl & 1) * 40 + (e >> 3)) * 8);
        st16(hop + M0_OFF + (i0 / 320) * 8192 + e * 16, (u32x4){nosent(v[0]), nosent(v[1]), nosent(v[2]), nosent(v[3])});
    }
    for (u32 i = i0; i < HOP_BYTES / 16; i += N_FILL * 256) st16(hop + (size_t)i * 16, SENT);
    for (u32 i = i0; i < (O_XN - O_ST) / 16; i += N_FILL * 256) st16(q.ws + O_ST + (size_t)i * 16, SENT);
}

extern "C" void kernel_launch(void* const* d_in, const int* in_sizes, int n_in,
                              void* d_out, int out_size, void* d_ws, size_t ws_size,
                              hipStream_t stream) {
    PrepParams q;
    q.x = (const u16*)d_in[0]; q.mem0 = (const u16*)d_in[1];
    q.wo = (const u16*)d_in[10];
    for (int i = 0; i < 4; ++i) q.W[i] = (const u16*)d_in[2 + i];
    q.ws = (char*)d_ws;
    prep_kernel<<<N_PF + N_LN + N_WO + N_FILL, 256, 0, stream>>>(q);
    LaParams p;
    p.ws = (char*)d_ws; p.out = (u16*)d_out; p.hop = (char*)d_ws;
    p.Wq = (const u16*)d_in[2]; p.Wk = (const u16*)d_in[3]; p.Wv = (const u16*)d_in[4]; p.Wo = (const u16*)d_in[5];
    p.wqkv = (const u16*)d_in[8]; p.wphi = (const u16*)d_in[9];
    (void)hipFuncSetAttribute((const void*)la_kernel, hipFuncAttributeMaxDynamicSharedMemorySize, LA_LDS);
    la_kernel<<<256, 256, LA_LDS, stream>>>(p);
}
```

```cpp
#include <hip/hip_runtime.h>
#include <hip/hip_bf16.h>
#include <stdint.h>
#include <type_traits>

typedef unsigned short u16;
typedef unsigned int u32;
typedef unsigned long long u64;
using bf16x8 = __attribute__((ext_vector_type(8))) short;
using f32x4 = __attribute__((ext_vector_type(4))) float;

constexpr int S_TOK = 2048, DM = 640, NH = 32, DH = 192;
constexpr int MEM = 8, SEG = 32, NSEG = S_TOK / SEG;
constexpr int HD = NH * DH;

__device__ __forceinline__ float bf2f(u16 v) { return __uint_as_float(((u32)v) << 16); }
__device__ __forceinline__ u32 pk2bf(float lo, float hi) {
    typedef __attribute__((ext_vector_type(2))) float f32x2_t;
    typedef __attribute__((ext_vector_type(2))) __bf16 bf16x2_t;
    bf16x2_t r = __builtin_convertvector((f32x2_t){lo, hi}, bf16x2_t);
    return __builtin_bit_cast(u32, r);
}
__device__ __forceinline__ float rbf(float f) { return __uint_as_float(pk2bf(f, f) << 16); }
typedef __attribute__((ext_vector_type(8))) __bf16 bf8;
typedef __attribute__((ext_vector_type(16))) __bf16 bf16v;
typedef __attribute__((ext_vector_type(4))) __bf16 bf4;
typedef __attribute__((ext_vector_type(4))) short s16x4;
using u32x4 = __attribute__((ext_vector_type(4))) u32;
using u32x2 = __attribute__((ext_vector_type(2))) u32;
typedef __attribute__((address_space(1))) u32 gu32;
typedef __attribute__((address_space(1))) u64 gu64;
#define LDS3 __attribute__((address_space(3)))

constexpr int SLOT = 16384;
#define PCSV 3
constexpr int PCS = PCSV, P_SLOT0 = 0, M_SLOT0 = 16 * PCS, S_SLOT = M_SLOT0 + 32, T_SLOT = S_SLOT + 10, N_SLOT = T_SLOT + 2;
constexpr size_t HOP_BYTES = (size_t)N_SLOT * SLOT, M0_OFF = HOP_BYTES;
constexpr size_t PX_BUF = 4 * 32 * 640, PX_BYTES = 8 * PX_BUF;
constexpr int LA_LDS = 162816 + 256;
constexpr int SPIN_CAP = 1 << 16;
#define D1I 136
#define D2I 58
#define MG1 11
#define MG2 11
#define ROTC 0
#define DUP 5
#define PACE 6
#define CLAG 42
#define T_PX 62
#define T_X1 70
#define T_OB 175
#define T_TOK 400
#define ST_LAG 6
#define OB_STEP 28
#define OB_POLL 0
#define OB_COLD 20
#define NH_ 2
#define HPRIO 0
#define W0D 6
#define LATEP 2
#define SYNCB 1
#define ARMD 0
#define PXK 8
#define PEEKK 6
#define PEEKB 32
#define AN_Q0 385
#define AN_Q1 255
#define AN_C0 285
#define AN_C1 150
#define ANN 0
#define PACE2 5
#define DMASK 1
#define BPEEK 390
#define RECD 0
#define PLIN 1
#define SLK 6
#define RECP 122
#define QDELAY 0
#define FSL 8
#define GATE 0
#define PKK 18
#define B_LAG 65
#define ELATE 85

__device__ __forceinline__ void st16(void* p, u32x4 v) {
    asm volatile("global_store_dwordx4 %0, %1, off sc1\n\ts_nop 2" :: "v"(p), "v"(v) : "memory");
}
typedef __attribute__((address_space(1))) u32x4 gu32x4;
__device__ __forceinline__ u32x4 ld16(const char* base, u32 off, int imm) { return __builtin_amdgcn_global_load_b128((gu32x4*)(base + off + imm), "agent"); }
__device__ __forceinline__ u32 nosent(u32 v) { return min(v, 0xFFFFFFFEu); }
__device__ __forceinline__ u32 rt32() { return (u32)__builtin_amdgcn_s_memrealtime(); }
__device__ __forceinline__ void wait_rt(u32 t) {
    int d = (int)(t - rt32()) - 4;
    if (d > 4096 || d < 0) return;
    while (d > 7) { if (d > 18) __builtin_amdgcn_s_sleep(2); d = max((int)(t - rt32()) - 4, 0); }
    asm volatile("s_bitcmp1_b32 %0, 2\n\ts_cbranch_scc0 1f\n\ts_nop 15\n\ts_nop 7\n1:\n\ts_bitcmp1_b32 %0, 1\n\ts_cbranch_scc0 2f\n\ts_nop 12\n2:\n\t"
                 "s_bitcmp1_b32 %0, 0\n\ts_cbranch_scc0 3f\n\ts_nop 7\n3:" :: "s"(d) : "scc");
}
template <int N> __device__ __forceinline__ void dma_kb(const char* src, u32 lds_dst, u32 lane16) {
#pragma unroll
    for (int i = 0; i < N; i += 4) {
        u32 keep;
        if (N - i >= 4)
            asm volatile("s_mov_b32 %0, m0\n\ts_mov_b32 m0, %3\n\ts_nop 2\n\tglobal_load_lds_dwordx4 %1, %2 sc1\n\tglobal_load_lds_dwordx4 %1, %2 offset:1024 sc1\n\t"
                         "global_load_lds_dwordx4 %1, %2 offset:2048 sc1\n\tglobal_load_lds_dwordx4 %1, %2 offset:3072 sc1\n\ts_mov_b32 m0, %0"
                         : "=&s"(keep) : "v"(lane16), "s"(src + i * 1024), "s"(lds_dst + i * 1024) : "memory");
        else if (N - i == 2)
            asm volatile("s_mov_b32 %0, m0\n\ts_mov_b32 m0, %3\n\ts_nop 2\n\tglobal_load_lds_dwordx4 %1, %2 sc1\n\tglobal_load_lds_dwordx4 %1, %2 offset:1024 sc1\n\ts_mov_b32 m0, %0"
                         : "=&s"(keep) : "v"(lane16), "s"(src + i * 1024), "s"(lds_dst + i * 1024) : "memory");
        else
            asm volatile("s_mov_b32 %0, m0\n\ts_mov_b32 m0, %3\n\ts_nop 2\n\tglobal_load_lds_dwordx4 %1, %2 sc1\n\ts_mov_b32 m0, %0"
                         : "=&s"(keep) : "v"(lane16), "s"(src + i * 1024), "s"(lds_dst + i * 1024) : "memory");
    }
}
template <int OFF> __device__ __forceinline__ void dma_one(const char* src, u32 lds_dst, u32 lane16) {
    asm volatile("s_mov_b32 m0, %2\n\ts_nop 0\n\tglobal_load_lds_dwordx4 %0, %1 offset:%3 sc1" :: "v"(lane16), "s"(src), "s"(lds_dst), "n"(OFF) : "memory");
}
__device__ __forceinline__ u32 ringp(u32 a, u32 b) { return a + b; }
__device__ __forceinline__ void dma_gather(const char* src, u32 lds_dst, u32 off) {
    u32 keep;
    asm volatile("s_mov_b32 %0, m0\n\ts_mov_b32 m0, %3\n\ts_nop 2\n\tglobal_load_lds_dwordx4 %1, %2\n\ts_mov_b32 m0, %0"
                 : "=&s"(keep) : "v"(off), "s"(src), "s"(lds_dst) : "memory");
}
__device__ __forceinline__ void st16s(char* base, u32 off, u32x4 v) {
    asm volatile("s_nop 4\n\tglobal_store_dwordx4 %0, %1, %2 sc1\n\ts_nop 2" :: "v"(off), "v"(v), "s"(base) : "memory");
}
__device__ __forceinline__ void arm16(char* base, u32 off) {
    u32x4 v;
    asm volatile("v_mov_b64 %0, -1\n\tv_mov_b64 %1, -1" : "=v"(((u64*)&v)[0]), "=v"(((u64*)&v)[1]));
    st16s(base, off, v);
}
template <int B, int E, typename F> __device__ __forceinline__ void static_for(F&& f) {
    if constexpr (B < E) { f(std::integral_constant<int, B>()); static_for<B + 1, E>(f); }
}
#define CI(x) decltype(x)::value
#define RAW_BARRIER() do { asm volatile("s_waitcnt lgkmcnt(0)" ::: "memory"); __builtin_amdgcn_s_barrier(); asm volatile("" ::: "memory"); } while (0)
#define VM0() asm volatile("s_waitcnt vmcnt(0)" ::: "memory")
#define SCHEDB() __builtin_amdgcn_sched_barrier(0)
__device__ __forceinline__ bf8 lo8(const bf16v& v) { return __builtin_shufflevector(v, v, 0, 1, 2, 3, 4, 5, 6, 7); }
__device__ __forceinline__ bf8 hi8(const bf16v& v) { return __builtin_shufflevector(v, v, 8, 9, 10, 11, 12, 13, 14, 15); }
__device__ __forceinline__ bf16v cat8(bf8 a, bf8 b) { return __builtin_shufflevector(a, b, 0, 1, 2, 3, 4, 5, 6, 7, 8, 9, 10, 11, 12, 13, 14, 15); }
#define MMA_VV(c, a, b) asm volatile("v_mfma_f32_16x16x32_bf16 %0, %1, %2, %0" : "+v"(c) : "v"(a), "v"(b))
#define MMA_AV(c, a, b) asm volatile("v_mfma_f32_16x16x32_bf16 %0, %1, %2, %0" : "+v"(c) : "a"(a), "v"(b))
#define MMA_VA(c, a, b) asm volatile("v_mfma_f32_16x16x32_bf16 %0, %1, %2, %0" : "+v"(c) : "v"(a), "a"(b))
#define MMA0_VV(c, a, b) asm volatile("v_mfma_f32_16x16x32_bf16 %0, %1, %2, 0" : "=&v"(c) : "v"(a), "v"(b))
#define MMA0_AV(c, a, b) asm volatile("v_mfma_f32_16x16x32_bf16 %0, %1, %2, 0" : "=&v"(c) : "a"(a), "v"(b))
#define MMA0_VA(c, a, b) asm volatile("v_mfma_f32_16x16x32_bf16 %0, %1, %2, 0" : "=&v"(c) : "v"(a), "a"(b))
#define MMA16(c, a, b) asm volatile("v_mfma_f32_16x16x16_bf16 %0, %1, %2, %0" : "+v"(c) : "v"(a), "v"(b))
#define MMA16_0(c, a, b) asm volatile("v_mfma_f32_16x16x16_bf16 %0, %1, %2, 0" : "=&v"(c) : "v"(a), "v"(b))
#define SMM_V(c, a, b, i) asm volatile("v_smfmac_f32_16x16x64_bf16 %0, %1, %2, %3" : "+v"(c) : "v"(a), "v"(b), "v"(i))
#define SMM_A(c, a, b, i) asm volatile("v_smfmac_f32_16x16x64_bf16 %0, %1, %2, %3" : "+v"(c) : "v"(a), "a"(b), "v"(i))
#define NOPV() asm volatile("s_nop 1")
#define PIN(x) asm volatile("" : "+v"(x))
#define SETTLE() asm volatile("s_nop 7\n\ts_nop 3")
__device__ __forceinline__ bf8 pack8(f32x4 a, f32x4 b) {
    u32x4 r = {pk2bf(a[0], a[1]), pk2bf(a[2], a[3]), pk2bf(b[0], b[1]), pk2bf(b[2], b[3])};
    return __builtin_bit_cast(bf8, r);
}
__device__ __forceinline__ u32 max4(u32 m, u32x4 g) { m = max(max(m, g[0]), g[1]); return max(max(m, g[2]), g[3]); }
#define ZERO4(c) asm volatile("v_mov_b64 %0, 0\n\tv_mov_b64 %1, 0" : "=v"(((u64*)&(c))[0]), "=v"(((u64*)&(c))[1]))
__device__ __forceinline__ bf8 lds8(const char* shm, int off) { return *(const bf8*)(shm + off); }
__device__ __forceinline__ bf16v ldfrag(const char* p) { return cat8(*(const bf8*)p, *(const bf8*)(p + 1024)); }
template <int CTRL> __device__ __forceinline__ float fdpp(float x) {
    return __builtin_bit_cast(float, __builtin_amdgcn_mov_dpp(__builtin_bit_cast(int, x), CTRL, 0xf, 0xf, true));
}
__device__ __forceinline__ float halfsum(float a, float b) {
    auto r = __builtin_amdgcn_permlane32_swap(__float_as_uint(a), __float_as_uint(b), false, false);
    return __uint_as_float(r[0]) + __uint_as_float(r[1]);
}
__device__ __forceinline__ float grp_sum(float x) {
    auto t = __builtin_amdgcn_permlane16_swap(__float_as_uint(x), __float_as_uint(x), false, false);
    x = __uint_as_float(t[0]) + __uint_as_float(t[1]);
    return halfsum(x, x);
}
__device__ __forceinline__ float grp_max(float x) {
    auto t = __builtin_amdgcn_permlane16_swap(__float_as_uint(x), __float_as_uint(x), false, false);
    x = fmaxf(__uint_as_float(t[0]), __uint_as_float(t[1]));
    auto u = __builtin_amdgcn_permlane32_swap(__float_as_uint(x), __float_as_uint(x), false, false);
    return fmaxf(__uint_as_float(u[0]), __uint_as_float(u[1]));
}
__device__ __forceinline__ float bflo(u32 v) { return __uint_as_float(v << 16); }
__device__ __forceinline__ float bfhi(u32 v) { return __uint_as_float(v & 0xffff0000u); }
__device__ __forceinline__ void st4_lane0(char* base, u32 v) {
    u32 z, d;
    base = (char*)(((u64)(u32)__builtin_amdgcn_readfirstlane((u32)((u64)base >> 32)) << 32) | (u64)(u32)__builtin_amdgcn_readfirstlane((u32)(u64)base));
    asm volatile("s_mov_b64 exec, 1\n\tv_mov_b32 %0, 0\n\tv_mov_b32 %1, %3\n\ts_nop 1\n\tglobal_store_dword %0, %1, %2 sc1\n\ts_mov_b64 exec, -1" : "=&v"(z), "=&v"(d) : "s"(base), "s"(v) : "memory");
}
__device__ __forceinline__ void stamp2(char* hop, const int line) {
    const u32 t = nosent(rt32());
    st4_lane0(hop + (T_SLOT + 1) * SLOT + line * 128, t); st4_lane0(hop + (T_SLOT + 1) * SLOT + 8192 + line * 128, t);
}
__device__ __forceinline__ int smax(int a, int b) { int r; asm volatile("s_max_i32 %0, %1, %2" : "=s"(r) : "s"(a), "s"(b) : "scc"); return r; }
__device__ __forceinline__ int track(int d, int sample, int down) {
    int e, a, b;
    asm("s_sub_i32 %0, %3, %4\n\ts_cmp_gt_i32 %3, 0\n\ts_cselect_b32 %0, %0, 0\n\ts_min_i32 %1, %0, %6\n\ts_ashr_i32 %2, %0, 1\n\ts_max_i32 %2, %2, %5\n\t"
        "s_min_i32 %2, %2, 0\n\ts_max_i32 %1, %1, %2" : "=&s"(e), "=&s"(a), "=&s"(b) : "s"(sample), "s"(d), "s"(-down), "n"(DUP) : "scc");
    return d + a;
}
__device__ __forceinline__ u32 sched_peek(const char* p) { return __hip_atomic_load((const gu32*)p, __ATOMIC_RELAXED, __HIP_MEMORY_SCOPE_AGENT); }
__device__ __forceinline__ u32 sched_get(u32 v, const char* p, int& cap) {
    for (int spin = 0; v == ~0u; ++spin) {
        if (spin >= cap) { cap = 1; break; }
        __builtin_amdgcn_s_sleep(3);
        v = sched_peek(p);
    }
    return __builtin_amdgcn_readfirstlane(v);
}

#define TRDECL
#define TR(i)
#define TRV(i, v)
#define TRFLUSH(role)
#define TRC(i)
constexpr size_t ACT = (size_t)S_TOK * DM * 2;
constexpr size_t O_DBG = 0, O_CTL = 2u << 20, O_ST = O_CTL + 4096, O_FL = O_ST + 128 * 1280, O_PX = O_FL + (576 + 512) * 128, O_X1 = O_PX + PX_BYTES,
                 O_OB = O_X1 + 2 * ACT, O_XN = O_OB + 2 * ACT, O_XR = O_XN + 2 * ACT, O_WO = O_XR + 2 * ACT,
                 O_WVO = O_WO + 10 * 81920, O_HOP = O_WVO + (size_t)80 * 4 * 40 * 2048, WS_BYTES = O_HOP + (4u << 20);
#define TT(role, i)
#define CNT(i)
struct LaParams { char* ws; u16* out; char* hop; const u16* Wq; const u16* Wk; const u16* Wv; const u16* Wo; const u16* wqkv; const u16* wphi; };
__device__ __forceinline__ int kperm(int g, int e) { return 16 * (g >> 1) + 8 * ((e & 3) >> 1) + 4 * (g & 1) + 2 * (e >> 2) + (e & 1); }
__device__ __forceinline__ bf8 tr8(u32 a, u32 b) {
    const s16x4 lo = __builtin_amdgcn_ds_read_tr16_b64_v4i16((LDS3 s16x4*)(uintptr_t)a), hi = __builtin_amdgcn_ds_read_tr16_b64_v4i16((LDS3 s16x4*)(uintptr_t)b);
    return __builtin_bit_cast(bf8, __builtin_shufflevector(lo, hi, 0, 1, 2, 3, 4, 5, 6, 7));
}
__device__ __forceinline__ char* wsp(const LaParams& p) { char* b = p.ws; asm volatile("" : "+s"(b)); return b; }
template <int N, int SL = 4> __device__ __forceinline__ void wait_flags(const char* f, const u32 (&off)[N], int& cap) {
    for (int spin = 0;; ++spin) {
        u32 m = 0;
#pragma unroll
        for (int i = 0; i < N; ++i) m = max(m, sched_peek(f + off[i]));
        if (!__any(m == ~0u)) break;
        if (spin >= cap) { cap = 1; break; }
        __builtin_amdgcn_s_sleep(SL);
    }
    asm volatile("" ::: "memory");
}
__device__ __forceinline__ void wait_all_tokens(const LaParams& p, u32 lane, int& cap) {
    asm volatile("" : "+v"(lane));
    const u32 off[3] = {lane * 128, lane * 128 + 8192, min(lane + 128, 159u) * 128};
    wait_flags<3>(wsp(p) + O_FL + 192 * 128, off, cap);
}
__device__ __forceinline__ void wait_first_tokens(const LaParams& p, const int set, u32 lane, int& cap) {
    asm volatile("" : "+v"(lane));
    const u32 off[1] = {(set * 20 + min(lane, 39u) + (lane >= 20 ? 20 : 0)) * 128};
    wait_flags<1>(wsp(p) + O_FL, off, cap);
}
__device__ __forceinline__ int get_side(const LaParams& p, int& cap) { return sched_get(sched_peek(wsp(p) + O_FL + 384 * 128), wsp(p) + O_FL + 384 * 128, cap) & 1; }
__device__ __forceinline__ char* align2m(char* p) { return p + ((size_t)(-(intptr_t)p) & ((2u << 20) - 1)); }

constexpr int Q_XA = 0, Q_PART = 20480, Q_TR = 48640, Q_MK = 54784, Q_WL = 55808, Q_ST = 162304, Q_PS = 44;

#define DPP1(op, ror) op "_dpp %0, %0, %0 row_ror:" ror " row_mask:0xf bank_mask:0xf\n\t"
#define DPP2(op, ror) DPP1(op, ror) op "_dpp %1, %1, %1 row_ror:" ror " row_mask:0xf bank_mask:0xf\n\ts_nop 0\n\t"
__device__ __forceinline__ void st16m(char* base, u32 off, u32x4 v, u64 mask) {
    asm volatile("s_mov_b64 exec, %3\n\ts_nop 3\n\tglobal_store_dwordx4 %0, %1, %2 sc1\n\ts_mov_b64 exec, -1\n\ts_nop 1" :: "v"(off), "v"(v), "s"(base), "s"(mask) : "memory");
}
__device__ __forceinline__ void st16f(char* base, u32 off, u32x4 v, u64 mask) {
    asm volatile("s_mov_b64 exec, %3\n\tglobal_store_dwordx4 %0, %1, %2 sc1\n\ts_mov_b64 exec, -1\n\ts_nop 1" :: "v"(off), "v"(v), "s"(base), "s"(mask) : "memory");
}
__device__ __forceinline__ void st16f64(char* base, u32 off, u32x4 v, u64 mask) {
    asm volatile("s_mov_b64 exec, %3\n\tglobal_store_dwordx4 %0, %1, %2 offset:64 sc1\n\ts_mov_b64 exec, -1\n\ts_nop 1" :: "v"(off), "v"(v), "s"(base), "s"(mask) : "memory");
}
__device__ __forceinline__ void st32(char* base, u32 off, u32x4 a, u32x4 b) {
    asm volatile("s_nop 4\n\tglobal_store_dwordx4 %0, %1, %3\n\tglobal_store_dwordx4 %0, %2, %3 offset:16\n\ts_nop 2" :: "v"(off), "v"(a), "v"(b), "s"(base) : "memory");
}
__device__ __forceinline__ void st8m(char* base, u32 off, u32x2 v, u64 mask) {
    asm volatile("s_mov_b64 exec, %3\n\ts_nop 3\n\tglobal_store_dwordx2 %0, %1, %2 sc1\n\ts_mov_b64 exec, -1" :: "v"(off), "v"(v), "s"(base), "s"(mask) : "memory");
}
__device__ __forceinline__ u32 pk2bf_abs(float lo, float hi) {
    u32 r;
    asm("v_cvt_pk_bf16_f32 %0, |%1|, |%2|" : "=v"(r) : "v"(lo), "v"(hi));
    return r;
}
__device__ __forceinline__ u32 p_line(const int h, const int piece, const u32 gap) {
    u32 a = (h >> 4) * gap + ((h >> 1) & 7) * 1024 + (h & 1) * 128, b = 2 * gap + h * 128;
    asm volatile("" : "+s"(a), "+s"(b));
    return piece < 4 ? a + piece * 256 : b;
}
constexpr float SM_C = 0.07216878364870322f * 1.4426950408889634f, NEG_BIG = -0x1p100f;
constexpr u64 SM_MASK = 0x0155015501550155ull;
template <typename MID> __device__ __forceinline__ void softmax_mem(const float* p, char* dst, char* dst2, const u32 off, MID&& mid) {
    f32x4 a[4];
#pragma unroll
    for (int i = 0; i < 4; ++i) a[i] = *(const f32x4*)(p + i * 40 * Q_PS);
    SCHEDB(); mid(); SCHEDB();
    f32x4 x = (a[0] + a[1]) + (a[2] + a[3]);
    float ml, mx = fmaxf(fmaxf(x[0], x[1]), fmaxf(x[2], x[3])), e0, e1, e2, e3;
    asm volatile("v_mul_f32 %5, %10, %0\n\ts_nop 0\n\t"
        DPP1("v_max_f32", "8") "v_fma_f32 %1, %6, %10, -%5\n\tv_fma_f32 %2, %7, %10, -%5\n\t"
        DPP1("v_max_f32", "4") "v_fma_f32 %3, %8, %10, -%5\n\tv_fma_f32 %4, %9, %10, -%5\n\t"
        DPP1("v_max_f32", "2") "v_exp_f32 %1, %1\n\tv_exp_f32 %2, %2\n\t"
        DPP1("v_max_f32", "1") "v_exp_f32 %3, %3\n\tv_fma_f32 %5, -%0, %10, %5\n\tv_exp_f32 %4, %4\n\tv_add_f32 %0, %1, %2\n\tv_exp_f32 %5, %5\n\tv_add_f32 %0, %0, %3\n\t"
        "v_add_f32 %0, %0, %4\n\tv_mul_f32 %0, %0, %5\n\tv_mul_f32 %1, %1, %5\n\tv_mul_f32 %2, %2, %5\n\t"
        DPP1("v_add_f32", "8") "v_mul_f32 %3, %3, %5\n\tv_mul_f32 %4, %4, %5\n\t" DPP1("v_add_f32", "4") "s_nop 1\n\t"
        DPP1("v_add_f32", "2") "s_nop 1\n\t" DPP1("v_add_f32", "1") "v_rcp_f32 %0, %0\n\ts_nop 0\n\t"
        "v_mul_f32 %1, %1, %0\n\tv_mul_f32 %2, %2, %0\n\tv_mul_f32 %3, %3, %0\n\tv_mul_f32 %4, %4, %0"
        : "+v"(mx), "=&v"(e0), "=&v"(e1), "=&v"(e2), "=&v"(e3), "=&v"(ml) : "v"(x[0]), "v"(x[1]), "v"(x[2]), "v"(x[3]), "s"(SM_C) : "memory");
    u32x4 o;
    asm volatile("v_cvt_pk_bf16_f32 %4, |%4|, |%6|\n\tv_cvt_pk_bf16_f32 %5, |%5|, |%7|\n\ts_nop 0\n\t"
                 "v_mov_b32_dpp %0, %4 quad_perm:[0,0,2,2] row_mask:0xf bank_mask:0xf\n\tv_mov_b32_dpp %1, %5 quad_perm:[0,0,2,2] row_mask:0xf bank_mask:0xf\n\t"
                 "v_mov_b32_dpp %2, %4 quad_perm:[1,1,3,3] row_mask:0xf bank_mask:0xf\n\tv_mov_b32_dpp %3, %5 quad_perm:[1,1,3,3] row_mask:0xf bank_mask:0xf"
                 : "=&v"(o[0]), "=&v"(o[1]), "=&v"(o[2]), "=&v"(o[3]), "+v"(e0), "+v"(e2) : "v"(e1), "v"(e3), "s"(dst), "s"(dst2));
    asm volatile("s_mov_b64 exec, %4\n\tglobal_store_dwordx4 %0, %1, %2 sc1\n\tglobal_store_dwordx4 %0, %1, %3 sc1\n\ts_mov_b64 exec, -1\n\ts_nop 1"
                 :: "v"(off), "v"(o), "s"(dst), "s"(dst2), "s"(0x03ff03ff03ff03ffull) : "memory");
}
struct SmTok {
    float x[2][4], b1[2][4], b2[2][4], b3[2][4], mx[2], sm[2], t0, t1;
    u32 d00, d01, d10, d11;
    static constexpr int N = 88;
    __device__ __forceinline__ void load(const float* p, const int j) {
        const int n = j & 1, k = j >> 1;
        const f32x4 a = *(const f32x4*)(p + (k * 40 + n * 4) * Q_PS);
#pragma unroll
        for (int i = 0; i < 4; ++i) (k == 0 ? x : (k == 1 ? b1 : (k == 2 ? b2 : b3)))[n][i] = a[i];
    }
    template <int I> __device__ __forceinline__ void piece(char* dst, const u32 off) {
        constexpr int n = I & 1, k = (I >> 1) - 12;
        float& t = n ? t1 : t0;
#define ROR(op, ror, v) asm volatile(op "_dpp %0, %0, %0 row_ror:" ror " row_mask:0xf bank_mask:0xf" : "+v"(v))
        if constexpr (k < -8) asm volatile("v_add_f32 %0, %0, %1" : "+v"(x[n][k + 12]) : "v"(b1[n][k + 12]));
        else if constexpr (k < -4) asm volatile("v_add_f32 %0, %0, %1" : "+v"(b2[n][k + 8]) : "v"(b3[n][k + 8]));
        else if constexpr (k < 0) asm volatile("v_add_f32 %0, %0, %1" : "+v"(x[n][k + 4]) : "v"(b2[n][k + 4]));
        else if constexpr (k == 0) asm volatile("v_max_f32 %0, %1, %2" : "=v"(mx[n]) : "v"(x[n][0]), "v"(x[n][1]));
        else if constexpr (k == 1) asm volatile("v_max3_f32 %0, %0, %1, %2" : "+v"(mx[n]) : "v"(x[n][2]), "v"(x[n][3]));
        else if constexpr (k == 2) ROR("v_max_f32", "8", mx[n]);
        else if constexpr (k == 3) ROR("v_max_f32", "4", mx[n]);
        else if constexpr (k == 4) ROR("v_max_f32", "2", mx[n]);
        else if constexpr (k == 5) ROR("v_max_f32", "1", mx[n]);
        else if constexpr (k == 6) { mx[n] *= SM_C; PIN(mx[n]); }
        else if constexpr (k < 11) { x[n][k - 7] = fmaf(x[n][k - 7], SM_C, -mx[n]); PIN(x[n][k - 7]); }
        else if constexpr (k < 15) asm volatile("v_exp_f32 %0, %0" : "+v"(x[n][k - 11]));
        else if constexpr (k == 15) asm volatile("v_add_f32 %0, %1, %2" : "=v"(sm[n]) : "v"(x[n][0]), "v"(x[n][1]));
        else if constexpr (k == 16) asm volatile("v_add_f32 %0, %1, %2" : "=v"(t) : "v"(x[n][2]), "v"(x[n][3]));
        else if constexpr (k == 17) asm volatile("v_add_f32 %0, %0, %1" : "+v"(sm[n]) : "v"(t));
        else if constexpr (k == 18) ROR("v_add_f32", "8", sm[n]);
        else if constexpr (k == 19) ROR("v_add_f32", "4", sm[n]);
        else if constexpr (k == 20) ROR("v_add_f32", "2", sm[n]);
        else if constexpr (k == 21) ROR("v_add_f32", "1", sm[n]);
        else if constexpr (k == 22) asm volatile("v_rcp_f32 %0, %0" : "+v"(sm[n]));
        else if constexpr (k < 27) asm volatile("v_mul_f32 %0, %0, %1" : "+v"(x[n][k - 23]) : "v"(sm[n]));
        else if constexpr (k == 27) asm volatile("v_cvt_pk_bf16_f32 %0, |%1|, |%2|" : "=v"(n ? d10 : d00) : "v"(x[n][0]), "v"(x[n][1]));
        else if constexpr (k == 28) asm volatile("v_cvt_pk_bf16_f32 %0, |%1|, |%2|" : "=v"(n ? d11 : d01) : "v"(x[n][2]), "v"(x[n][3]));
        else if constexpr (I == 84 || I == 87) {
            u32x4 o;
            asm volatile("v_mov_b32 %0, %4\n\tv_mov_b32 %1, %5\n\tv_mov_b32_dpp %2, %4 quad_perm:[1,0,3,2] row_mask:0xf bank_mask:0xf\n\tv_mov_b32_dpp %3, %5 quad_perm:[1,0,3,2] row_mask:0xf bank_mask:0xf"
                         : "=&v"(o[0]), "=&v"(o[1]), "=&v"(o[2]), "=&v"(o[3]) : "v"(n ? d10 : d00), "v"(n ? d11 : d01), "s"(dst));
            if constexpr (n == 0) st16f(dst, off, o, SM_MASK); else st16f64(dst, off, o, SM_MASK);
        }
    }
};

__host__ __device__ constexpr int qk_slot(int T, int ks) { return ks < 5 ? ks * 6 + T : (T < 4 ? 30 + (T >> 1) * 10 + (ks - 5) * 2 + (T & 1) : 25 + T * 5 + ks); }
constexpr int NLF = 13;
__host__ __device__ constexpr int lds_slot(int n) { constexpr int t[NLF] = {2, 6, 11, 15, 20, 24, 29, 33, 37, 41, 45, 49, 55}; return t[n]; }
__host__ __device__ constexpr int slot_lds(int sl) { for (int n = 0; n < NLF; ++n) if (lds_slot(n) == sl) return n; return -1; }
__host__ __device__ constexpr int slot_frag(int sl) { if (slot_lds(sl) >= 0) return 48 + slot_lds(sl); int n = 0; for (int i = 0; i < NLF; ++i) n += lds_slot(i) < sl; return sl - n; }
__host__ __device__ constexpr int qk_frag(int T, int ks) { return slot_frag(qk_slot(T, ks)); }
__host__ __device__ constexpr int slot_T(int sl) { return sl < 30 ? sl % 6 : (sl < 50 ? ((sl - 30) / 10) * 2 + (sl & 1) : (sl - 30) / 5); }
__host__ __device__ constexpr int slot_ks(int sl) { return sl < 30 ? sl / 6 : (sl < 50 ? 5 + ((sl - 30) % 10) / 2 : 5 + sl % 5); }
__host__ __device__ constexpr int lds_rank(int T, int ks) { int n = 0; for (int t = 0; t < T; ++t) n += qk_frag(t, ks) >= 48; return n; }
__host__ __device__ constexpr int lds_nth(int ks, int n) { for (int t = 0; t < 6; ++t) if (qk_frag(t, ks) >= 48 && lds_rank(t, ks) == n) return qk_frag(t, ks) - 48; return -1; }

__device__ __forceinline__ void qk_role(const LaParams& p, const int set, const int h, char* shm) {
    const int lane = threadIdx.x & 63, w = __builtin_amdgcn_readfirstlane(threadIdx.x >> 6);
    const int fr = lane & 15, g = lane >> 4;
    const u32 lds0 = (u32)(uintptr_t)(LDS3 char*)shm;
    const int sidx = (lane & 8) ? 0xEEEE : 0x4444;
    int cap = SPIN_CAP;
    char* mine = p.hop;
    int sch = (S_SLOT + w) * SLOT;
    int phys = 0;
    char* px = (wsp(p) + O_PX);
    const char* xn = (wsp(p) + O_XN);
    char* wl = shm + Q_WL + w * (NLF * 2048) + lane * 16;
    const char* xa = shm + Q_XA + lane * 16;

    bf16v WA[32], WV[28 - NLF];
    {
        bf16v WT[NLF];
        u32 vo[6], rd[3];
#pragma unroll
        for (int i = 0; i < 6; ++i) {
            const int id = i * 64 + lane, r = w * 16 + id / 24;
            vo[i] = (32 * (r >> 5) + kperm((r >> 2) & 3, ((r >> 4) & 1) * 4 + (r & 3))) * (HD * 2) + ((id % 24) ^ (r & 6)) * 16;
        }
#pragma unroll
        for (int i = 0; i < 3; ++i) rd[i] = lds0 + (g * 4 + (fr >> 2)) * 384 + ((w * 3 + i) ^ ((g & 1) * 2 + (fr >> 3))) * 32 + (fr & 3) * 8;
        auto issue = [&](const int j) {
            const char* src = (const char*)((j & 1) ? p.Wk : p.Wq) + (size_t)(j >> 1) * 64 * HD * 2 + h * 384;
#pragma unroll
            for (int i = 0; i < 6; ++i) dma_gather(src, lds0 + (j % 6) * 24576 + w * 6144 + i * 1024, vo[i]);
        };
        if (QDELAY) wait_rt(rt32() + QDELAY);
#pragma unroll
        for (int j = 0; j < 5; ++j) issue(j);
        static_for<0, 20>([&](auto J) {
            constexpr int j = CI(J);
            asm volatile("s_waitcnt vmcnt(%0)" :: "n"((19 - j < 4 ? 19 - j : 4) * 6) : "memory");
            RAW_BARRIER();
            if constexpr (j + 5 < 20) issue(j + 5);
            static_for<0, 3>([&](auto PR) {
                constexpr int id = qk_frag(2 * CI(PR) + (j & 1), j >> 1);
                u32 a = rd[CI(PR)] + (j % 6) * 24576;
                asm volatile("" : "+v"(a));
                const bf16v f = cat8(tr8(a, a + 16 * 384), tr8(a + 32 * 384, a + 48 * 384));
                if constexpr (id < 32) WA[id] = f; else if constexpr (id < 48) WV[id - 32] = f; else WT[id - 48] = f;
            });
        });
        RAW_BARRIER();
        dma_kb<5>(xn + (size_t)set * 40960 + w * 5120, lds0 + Q_XA + w * 5120, lane * 16);
#pragma unroll
        for (int i = 0; i < NLF; ++i) { *(bf8*)(wl + i * 2048) = lo8(WT[i]); *(bf8*)(wl + i * 2048 + 1024) = hi8(WT[i]); }
    }
    TRDECL;
    bf8 QX[2], KX[2];
    u32x2 QY[2], KY[2];
    u32 peek = ~0u;
    int d1 = D1I, d2 = D2I, k1 = D1I + MG1;
    if (w == 3) *(u64*)(shm + Q_ST + lane * 8) = 0;
    SmTok smt;
    u64 tpub = 0, tnow = 0;
    u32 tref = 0, tpost = 0;
    u32 xoff = p_line(h, fr >> 1, 8192) + g * 16, ao1 = p_line(h, min(lane >> 3, 4), 8192) + (lane & 7) * 16, ao0;
    {
        u32 a = (h >> 4) * SLOT + (h & 15) * 512, b = 2 * SLOT + h * 256;
        asm volatile("" : "+s"(a), "+s"(b));
        ao0 = PLIN ? p_line(h, min(lane >> 3, 4), SLOT) + (lane & 7) * 16 : (lane < 32 ? a + lane * 16 : b + (lane & 15) * 16);
    }
    asm volatile("" : "+v"(xoff), "+v"(ao0), "+v"(ao1));
    auto rearm = [&](const int rot, const int i, char*& a, u32x4& v) {
        if (i == 0) asm volatile("v_mov_b64 %0, -1\n\tv_mov_b64 %1, -1" : "=v"(((u64*)&v)[0]), "=v"(((u64*)&v)[1]));
        if (i == 1) { a = mine + (P_SLOT0 + (((rot + 3) & 3) * 4 + w) * PCS) * SLOT; asm volatile("" : "+s"(a)); }
        if (i == 5) { a = px + (size_t)(set * 4 + ((rot + 2) & 3)) * PX_BUF + w * 20480; asm volatile("" : "+s"(a)); }
        if (i == 3 || i == 8) __builtin_amdgcn_global_store_b128((gu32x4*)(a + (i == 3 ? ao0 : ao1)), v, "agent");
    };
    auto fetch_stats = [&]() {
        if (__builtin_expect(h == 0 && w == 3, 0)) {
                u32 lo = lane;
                asm volatile("" : "+v"(lo));
                lo *= 128;
                u32 keep;
                asm volatile("s_mov_b32 %0, m0\n\ts_mov_b32 m0, %4\n\ts_nop 2\n\tglobal_load_lds_dword %1, %2 sc1\n\ts_add_u32 m0, m0, 256\n\ts_lshr_b64 exec, exec, 16\n\tglobal_load_lds_dword %1, %3 sc1\n\ts_mov_b64 exec, -1\n\ts_mov_b32 m0, %0"
                             : "=&s"(keep) : "v"(lo), "s"(mine + T_SLOT * SLOT), "s"(mine + (T_SLOT + 1) * SLOT), "s"(lds0 + Q_ST) : "memory", "scc");
        }
    };
    int down = 16;
    auto xproj = [&](const int snext, const int sprev, auto SOFT, char* xdst) {
        f32x4 acc[6][2];
        TR(8);
        const char* xs;
        u32 xl;
        bf8 x0 = *(const bf8*)(xa), x1 = *(const bf8*)(xa + 1024), lw[2];
        lw[0] = *(const bf8*)(wl);
        if constexpr (CI(SOFT)) RAW_BARRIER();
        auto pack = [&](const int T, const int tt) {
            PIN(acc[T][tt]);
            const u32 a = pk2bf(acc[T][tt][0], acc[T][tt][1]), b = pk2bf(acc[T][tt][2], acc[T][tt][3]);
            if (T < 4) { u32* d = (u32*)((T & 1) ? &KX[tt] : &QX[tt]) + (T & 2); d[0] = a; d[1] = b; }
            else if (T == 4) QY[tt] = (u32x2){a, b}; else KY[tt] = (u32x2){a, b};
            SCHEDB();
        };
        char* ao;
        u32x4 sent;
        static_for<0, 20>([&](auto K) {
            constexpr int k = CI(K), l0 = lds_nth(((k + 1) >> 1) % 10, 0), l1 = lds_nth(((k + 1) >> 1) % 10, 1);
            bf8 n0, n1, nw[2];
            if constexpr (k == 9) {
                TR(9);
                if constexpr (CI(SOFT)) asm volatile("s_waitcnt vmcnt(4)" ::: "memory"); else VM0();
                RAW_BARRIER();
                TR(10);
            }
            static_for<0, 6>([&](auto TT) {
                constexpr int T = CI(TT), id = qk_frag(T, k >> 1), rk = lds_rank(T, k >> 1);
                if constexpr (k < 19) {
                    if constexpr (T == 0) n0 = *(const bf8*)(xa + (k + 1) * 2048);
                    if constexpr (T == 1) n1 = *(const bf8*)(xa + (k + 1) * 2048 + 1024);
                    if constexpr (T == 2 && l0 >= 0) nw[0] = *(const bf8*)(wl + l0 * 2048 + ((k + 1) & 1) * 1024);
                    if constexpr (T == 3 && l1 >= 0) nw[1] = *(const bf8*)(wl + l1 * 2048 + ((k + 1) & 1) * 1024);
                }
                if constexpr (k == 0 && T == 1) { xs = xn + (size_t)(snext - 2) * 40960 + 20480 + w * 5120; xl = lds0 + Q_XA + 20480 + w * 5120; asm volatile("" : "+s"(xs), "+s"(xl)); }
                if constexpr (k == 9 && T == 1) { xs = xn + (size_t)min(snext, NSEG - 1) * 40960 + w * 5120; xl -= 20480 + 4096; asm volatile("" : "+s"(xs), "+s"(xl)); }
                if constexpr ((k == 2 || k == 13) && T == 1) { xs += 4096; xl += 4096; asm volatile("" : "+s"(xs), "+s"(xl)); }
                if constexpr (k < 3 && (T == 3 || T == 5) && 2 * k + (T == 5) < 5) dma_one<((2 * k + (T == 5)) & 3) * 1024>(xs, xl, lane * 16);
                if constexpr (k >= 9 && k < 14 && T == 4) dma_one<((k - 9) & 3) * 1024>(xs, xl, lane * 16);
                if constexpr (CI(SOFT) && k == 10 && T == 1) {
                    asm volatile("" : "+s"(tpub));
                    if (w == 1) st4_lane0(mine + (T_SLOT + 1) * SLOT + (16 + h) * 128, tref ? (u32)tpub - tref : 0);
                }
                if constexpr (CI(SOFT) && k == 16 && T == 4) fetch_stats();
                if constexpr (CI(SOFT) && !GATE && k == PKK && T == 4) peek = sched_peek(mine + sch + sprev * 128 + 4);
                if constexpr (CI(SOFT) && GATE && k == 12 && T == 1) peek = sched_peek(mine + sch + (sprev - 1) * 128 + 4);
                if constexpr (CI(SOFT) && GATE && k == PKK && T == 0) asm volatile("s_memrealtime %0" : "=s"(tnow));
                if constexpr (CI(SOFT) && GATE && k == PKK && T == 5) {
                    tpost = __builtin_amdgcn_readfirstlane(peek) + RECP;
                    asm volatile("s_waitcnt lgkmcnt(0)" : "+s"(tnow), "+s"(tpost) :: "memory");
                    peek = ~0u;
                    if ((int)((u32)tnow - tpost) >= 0) peek = sched_peek(mine + sch + sprev * 128 + 4);
                }
                SCHEDB();
                static_for<0, 2>([&](auto U) {
                    constexpr int u = CI(U);
                    const bf8 xx = u ? x1 : x0;
                    if constexpr (id < 32) {
                        const bf8 wf = (k & 1) ? hi8(WA[id]) : lo8(WA[id]);
                        if constexpr (k == 0) MMA0_AV(acc[T][u], wf, xx); else MMA_AV(acc[T][u], wf, xx);
                    } else {
                        bf8 wf;
                        if constexpr (id < 48) wf = (k & 1) ? hi8(WV[id - 32]) : lo8(WV[id - 32]); else wf = lw[rk];
                        if constexpr (k == 0) MMA0_VV(acc[T][u], wf, xx); else MMA_VV(acc[T][u], wf, xx);
                    }
                    SCHEDB();
                    if constexpr (CI(SOFT) && k * 12 + T * 2 + u < SmTok::N) smt.template piece<k * 12 + T * 2 + u>(xdst, xoff);
                    if constexpr (CI(SOFT) && k == 8) rearm((snext >> 1) + 2, T * 2 + u, ao, sent);
                    if constexpr (k == 19 && T >= 2) pack(T - 2, u);
                    SCHEDB();
                });
            });
            if constexpr (k < 19) { x0 = n0; x1 = n1; if constexpr (l0 >= 0) lw[0] = nw[0]; if constexpr (l1 >= 0) lw[1] = nw[1]; }
        });
        TR(11);
        pack(4, 0); pack(4, 1);
        asm volatile("s_nop 3");
        pack(5, 0); pack(5, 1);
    };
    {
        const f32x4 neg = {NEG_BIG, NEG_BIG, NEG_BIG, NEG_BIG};
        *(f32x4*)((float*)(shm + Q_PART) + (w * 40 + 8 + fr) * Q_PS + g * 8 + 4) = neg;
        if (lane < 40) *(f32x4*)((float*)(shm + Q_PART) + (w * 40 + lane) * Q_PS + 40) = neg;
        *(f32x4*)(shm + Q_MK + lane * 16) = (f32x4){4 * g > fr ? NEG_BIG : 0.f, 4 * g + 1 > fr ? NEG_BIG : 0.f, 4 * g + 2 > fr ? NEG_BIG : 0.f, 4 * g + 3 > fr ? NEG_BIG : 0.f};
    }
    float* pw0 = (float*)(shm + Q_PART) + (w * 40 + (fr & 7)) * Q_PS + g * 8 + (fr >> 3) * 4;
    float* pw2 = fr < 8 && g >= 2 ? (float*)(shm + Q_PART) + (w * 40 + fr) * Q_PS + 24 + g * 4 : (float*)(shm + Q_TR + w * 1536) + lane * 4;
    const float* prd = (const float*)(shm + Q_PART) + ((w & 1) * 4 + g) * Q_PS + min(fr, 10) * 4;
    const float* prt = (const float*)(shm + Q_PART) + (8 + 8 * w + g) * Q_PS + min(fr, 10) * 4;
    u32 trw = lds0 + Q_TR + w * 1536 + lane * 8, trr = lds0 + Q_TR + w * 1536 + (fr & 3) * 128 + (4 * g + (fr >> 2)) * 8;
    u32 trh = lds0 + Q_TR + w * 1536 + 1024 + (lane & 31) * 8 + (lane >> 5) * 4;
    u32 pw3 = lds0 + Q_PART + ((w * 40 + 8 + (lane >> 5) * 16 + fr) * Q_PS + 32 + (g & 1) * 4) * 4;
    u32 pp = lds0 + Q_PART + ((w * 40 + 8 + fr) * Q_PS + g * 8) * 4;
    asm volatile("" : "+v"(trw), "+v"(trr), "+v"(trh), "+v"(pw3), "+v"(pp));
    const int lead = __builtin_amdgcn_readfirstlane((u32)(h * 4 + (w ^ 3) - 1) >> 31);
    u32 pa = (h >> 4) * SLOT + (h & 15) * 512 + (w & 1) * 256, pbm = 2 * SLOT + h * 256 + (w & 1) * 128;
    asm volatile("" : "+s"(pa), "+s"(pbm));
    const u32 poff = (PLIN ? p_line(h, fr >> 1, SLOT) + (w & 1) * 64 : (fr < 8 ? pa + (fr >> 1) * 64 : pbm)) + g * 16 + (fr & 1) * (PCS * SLOT);

    for (int layer = 0; layer < 2; ++layer) {
    if (layer > 0) {
        if (w == 0) {
            __builtin_amdgcn_s_sleep(127);
            wait_rt(sched_get(~0u, mine + (T_SLOT + 1) * SLOT + 50 * 128, cap) + T_TOK);
            wait_first_tokens(p, set, lane, cap);
        }
        RAW_BARRIER();
        sch += 5 * SLOT; xn += ACT;
        TT(set * 128 + h, 1);
        dma_kb<5>(xn + (size_t)set * 40960 + w * 5120, lds0 + Q_XA + w * 5120, lane * 16);
    }
    VM0(); RAW_BARRIER();
    TT(set * 128 + h, 2);
    if (ANN && w == 1) st4_lane0(wsp(p) + O_FL + (576 + layer * 256 + set * 128 + h) * 128, nosent(rt32() + (set ? AN_Q1 : AN_Q0)));
    xproj(set + 2, set - 1, std::integral_constant<int, 0>(), nullptr);
    peek = ~0u;
    if (layer == 0) { phys = set ^ get_side(p, cap); mine += phys * 8192; }
    TT(set * 128 + h, 3);
    for (int s = set; s < NSEG; s += 2) {
        const int rot = (s >> 1) & 3;
        TR(0);
        u32 ytime = 0;
        const char* mb0 = (s > 0 ? mine + (M_SLOT0 + rot * 8) * SLOT : p.hop + M0_OFF + phys * 8192) + 2 * w * SLOT + 2048;
        const char* mb1 = mb0 + SLOT;
        char* pdst = mine + (P_SLOT0 + rot * 4 * PCS) * SLOT;
        char* pdst2 = pdst + 2 * PCS * SLOT;
        asm volatile("" : "+s"(mb0), "+s"(mb1), "+s"(pdst), "+s"(pdst2));
        if (s > 0) {
            if (GATE && __builtin_amdgcn_readfirstlane(peek) == ~0u) { wait_rt(tpost); peek = sched_peek(mine + sch + (s - 1) * 128 + 4); }
            ytime = sched_get(peek, mine + sch + (s - 1) * 128 + 4, cap);
            }
        f32x4 t[3];
        t[0] = *(const f32x4*)(xa + Q_MK);

        PIN(t[0]); PIN(QX[0]); PIN(QX[1]); PIN(KX[0]); PIN(KX[1]); PIN(QY[0]); PIN(QY[1]); PIN(KY[0]); PIN(KY[1]);
        u32 tight = 0;
        if (s > 0 && w == 0) wait_rt(ytime);
        if (w == 1) {
            const int sl = (int)(ytime - rt32());
            tight = min(max(SLK - sl, 0), SLK);
        }
        __builtin_amdgcn_s_barrier();
        TR(1); TRV(7, ytime);
        f32x4 acc[6];
        u32x4 A[10];
        bf8 Ll[2], Lh[2];
        s16x4 F[4];
        u32 r0[4], r1[4], d[2];
        float v[4];
        int tries = 0;
        auto fin = [&](const int pr, const int i) {
            if (i == 0) { PIN(acc[2 * pr]); PIN(acc[2 * pr + 1]); }
            if (i < 4) { auto r = __builtin_amdgcn_permlane32_swap(__float_as_uint(acc[2 * pr][i]), __float_as_uint(acc[2 * pr + 1][i]), false, false); r0[i] = r[0]; r1[i] = r[1]; }
            const int a0 = pr == 0 ? i - 4 : i - 1;
            if (a0 >= 0 && a0 < 4) asm volatile("v_add_f32 %0, %1, %2" : "=v"(v[a0]) : "v"(r0[a0]), "v"(r1[a0]));
            if (a0 == 3) d[0] = pk2bf(v[0], v[1]);
            if (a0 == 4) {
                d[1] = pk2bf(v[2], v[3]);
                *(LDS3 u32x2*)(uintptr_t)(trw + pr * 512) = (u32x2){d[0], d[1]};
                F[pr] = __builtin_amdgcn_ds_read_tr16_b64_v4i16((LDS3 s16x4*)(uintptr_t)(trr + pr * 512));
            }
        };
        auto fin1 = [&](const int T, const int i) {
            if (i == 0) asm volatile("s_nop 1\n\tv_permlane32_swap_b32 %0, %2\n\tv_permlane32_swap_b32 %1, %3" : "+v"(acc[T][0]), "+v"(acc[T][1]), "+v"(acc[T][2]), "+v"(acc[T][3]));
            else {
                PIN(acc[T]);
                asm volatile("v_add_f32 %0, %2, %4\n\tv_add_f32 %1, %3, %5\n\tv_cvt_pk_bf16_f32 %0, %0, %1" : "=&v"(v[0]), "=&v"(v[1]) : "v"(acc[T][0]), "v"(acc[T][1]), "v"(acc[T][2]), "v"(acc[T][3]));
                *(LDS3 float*)(uintptr_t)(trh + (T - 4) * 256) = v[0];
                F[T - 2] = __builtin_amdgcn_ds_read_tr16_b64_v4i16((LDS3 s16x4*)(uintptr_t)(trr + 1024));
            }
        };
        auto slot = [&](auto SL) {
            constexpr int sl = CI(SL), T = slot_T(sl), ks = slot_ks(sl), id = slot_frag(sl);
            if constexpr (id < 32) SMM_A(acc[T], A[ks], WA[id], sidx);
            else if constexpr (id < 48) SMM_V(acc[T], A[ks], WV[id - 32], sidx);
            else { const bf16v L = cat8(Ll[id & 1], Lh[id & 1]); SMM_V(acc[T], A[ks], L, sidx); }
            SCHEDB();
            if constexpr (sl == 0) TRC(6);
            constexpr int n0 = slot_lds(sl), n1 = slot_lds(sl - 1);
            if constexpr (n0 >= 0 && n0 < NLF - 2) Ll[n0 & 1] = *(const bf8*)(wl + (n0 + 2) * 2048);
            if constexpr (n1 >= 0 && n1 < NLF - 2) Lh[n1 & 1] = *(const bf8*)(wl + (n1 + 2) * 2048 + 1024);
            if constexpr (sl >= 43 && sl < 52) fin(0, sl - 43);
            if constexpr (sl >= 52 && sl < 58) fin(1, sl - 52);
            if constexpr (sl >= 58) fin1(4, sl - 58);
            SCHEDB();
        };
        auto gather = [&](const bool first) {
            u32 vo = lane * 16;
            asm volatile("" : "+v"(vo) :: "memory");
#pragma unroll
            for (int ks = 0; ks < 10; ++ks) {
                A[ks] = ld16(ks < 5 ? mb0 : mb1, vo, (ks % 5 - 2) * 1024);
                SCHEDB();
                if (first) {
                    if (ks == 0) {
                        MMA0_VV(t[1], KX[0], QX[1]);
                        asm volatile("v_mfma_f32_16x16x32_bf16 %0, %1, %2, %3" : "=&v"(t[2]) : "v"(KX[1]), "v"(QX[1]), "v"(t[0]));
                        MMA_VV(t[0], KX[0], QX[0]);
                    }
                    if (ks == 2) { MMA16(t[1], KY[0], QY[1]); MMA16(t[2], KY[1], QY[1]); MMA16(t[0], KY[0], QY[0]); }
                    if (ks == 4) { PIN(t[1]); *(LDS3 f32x4*)(uintptr_t)(pp + 16 * Q_PS * 4) = t[1]; }
                    if (ks == 5) { PIN(t[2]); *(LDS3 f32x4*)(uintptr_t)(pp + 16 * Q_PS * 4 + 16) = t[2]; }
                    if (ks == 6) { PIN(t[0]); *(LDS3 f32x4*)(uintptr_t)pp = t[0]; }
                    if (ks == 1 || ks == 3 || ks == 7 || ks == 8) asm volatile("s_nop %0" :: "n"(PACE2));
                } else if (ks < 9) asm volatile("s_nop %0" :: "n"(PACE));
                if (ks == 3 || ks == 7) { Ll[ks >> 2] = *(const bf8*)(wl + (ks >> 2) * 2048); Lh[ks >> 2] = *(const bf8*)(wl + (ks >> 2) * 2048 + 1024); }
                if (ks == 1) { ZERO4(acc[0]); ZERO4(acc[1]); }
                if (ks >= 5 && ks < 9) ZERO4(acc[ks - 3]);
                SCHEDB();
            }
        };
        gather(true);
        static_for<0, 42>(slot);
        auto record = [&](const u32 xt, const u32 yt) {
            u32 ln8 = lane;
            asm volatile("" : "+v"(ln8));
            ln8 *= 8192;
            const u32x4 rec = {nosent(xt), nosent(yt), (u32)(d1 + MG1), 0u};
            k1 = d1 + MG1;
            st16m(p.hop + (S_SLOT + layer * 5) * SLOT + s * 128, ln8, rec, 0x3ffull);
        };
        u64 bad, bad2;
        asm volatile("v_cmp_u_f32 %0, %2, %3\n\tv_cmp_u_f32 %1, %4, %5" : "=s"(bad), "=s"(bad2) : "v"(acc[0][0]), "v"(acc[0][1]), "v"(acc[0][2]), "v"(acc[0][3]));
        static_for<42, 43>(slot);
        asm volatile("" : "+s"(bad), "+s"(bad2));
        if (__builtin_expect((bad | bad2) != 0, 0)) {
            {
                SETTLE(); CNT(0);
                while (true) {
                    gather(false);
                    u32 m = 0;
#pragma unroll
                    for (int ks = 0; ks < 10; ++ks) m = max4(m, A[ks]);
                    ++tries;
                    if (!__any(m == ~0u)) break;
                    if (tries >= cap) { cap = 1; break; }
                    __builtin_amdgcn_s_sleep(4);
                }
                static_for<0, 43>(slot);
            }
        }
        TR(2); TRV(6, tries + 1);
        static_for<43, 60>(slot);
        TRC(7);
        bf8 F01 = __builtin_bit_cast(bf8, __builtin_shufflevector(F[0], F[1], 0, 1, 2, 3, 4, 5, 6, 7));
        f32x4 s0, s1, s2, u0, u1;
        PIN(F01);
        MMA0_VV(s1, KX[1], F01); MMA0_VV(s0, KX[0], F01);
        SCHEDB();
        fin1(5, 0); fin1(5, 1);
        SCHEDB();
        MMA16(s1, KY[1], F[2]); MMA16(s0, KY[0], F[2]);
        MMA0_VV(s2, F01, F01); MMA0_VV(u0, F01, QX[0]); MMA0_VV(u1, F01, QX[1]);
        asm volatile("v_mov_b32_dpp %0, %4 row_ror:8 row_mask:0xf bank_mask:0xc\n\tv_mov_b32_dpp %1, %5 row_ror:8 row_mask:0xf bank_mask:0xc\n\t"
                     "v_mov_b32_dpp %2, %6 row_ror:8 row_mask:0xf bank_mask:0xc\n\tv_mov_b32_dpp %3, %7 row_ror:8 row_mask:0xf bank_mask:0xc"
                     : "+v"(s0[0]), "+v"(s0[1]), "+v"(s0[2]), "+v"(s0[3]) : "v"(s1[0]), "v"(s1[1]), "v"(s1[2]), "v"(s1[3]));
        MMA16(s2, F[3], F[3]);
        SCHEDB();
        *(f32x4*)pw0 = s0; SCHEDB();
        MMA16(u0, F[3], QY[0]); MMA16(u1, F[3], QY[1]);
        asm volatile("s_nop 1");
        PIN(s2); *(f32x4*)pw2 = s2; SCHEDB();
        auto uwrite = [&]() {
            PIN(u0); PIN(u1);
            f32x4 u;
#pragma unroll
            for (int i = 0; i < 4; ++i) u[i] = __uint_as_float(__builtin_amdgcn_permlane32_swap(__float_as_uint(u0[i]), __float_as_uint(u1[i]), false, false)[1]);
            *(LDS3 f32x4*)(uintptr_t)pw3 = u;
        };
        if (__builtin_expect(w < 2, 1)) {
            RAW_BARRIER();
            TR(3);
            softmax_mem(prd, pdst, pdst2, poff, uwrite);
            asm volatile("s_memrealtime %0" : "=s"(tpub));
            TR(4);
        } else {
            RAW_BARRIER();
            TR(3);
            uwrite();
            if (lead && (!ANN || s > 0)) {
                const u32 xt = tries == 0 && s > 0 ? ytime + k1 : rt32() + ELATE;
                if (RECD) __builtin_amdgcn_s_sleep(RECD);
                u32 lo = lane * 4;
                asm volatile("" : "+v"(lo));
                int sta = *(const int*)(shm + Q_ST + lo), stb = *(const int*)(shm + Q_ST + 256 + lo);
                record(xt, xt + d2 + MG2);
                asm volatile("s_nop 1\n\t" DPP2("v_max_i32", "8") DPP2("v_max_i32", "4") DPP2("v_max_i32", "2") DPP2("v_max_i32", "1") : "+v"(sta), "+v"(stb));
                const int a0 = __builtin_amdgcn_readlane(sta, 0), a1 = __builtin_amdgcn_readlane(sta, 16), a2 = __builtin_amdgcn_readlane(sta, 32), a3 = __builtin_amdgcn_readlane(sta, 48);
                const int b0 = __builtin_amdgcn_readlane(stb, 0), b1 = __builtin_amdgcn_readlane(stb, 16), b2 = __builtin_amdgcn_readlane(stb, 32);
                SCHEDB();
                d1 = track(d1, smax(b1, b2), down); d2 = track(d2, smax(smax(smax(a0, a1), smax(a2, a3)), b0), down);
                down = s > 10 ? 1 : down;
            }
            if (ANN && h == 0 && w == 3 && s == 0) {
                u32 ln = lane;
                asm volatile("" : "+v"(ln));
                const char* an = wsp(p) + O_FL + (576 + layer * 256) * 128;
                const u32 off[4] = {ln * 128, min(ln + 64, 111u) * 128, (ln + 128) * 128, min(ln + 192, 239u) * 128};
                wait_flags<4>(an, off, cap);
                const u32 now = rt32() + 40;
                int a = max((int)(sched_peek(an + off[0]) - now), (int)(sched_peek(an + off[1]) - now)), b = max((int)(sched_peek(an + off[2]) - now), (int)(sched_peek(an + off[3]) - now));
                asm("s_nop 1\n\t" DPP2("v_max_i32", "8") DPP2("v_max_i32", "4") DPP2("v_max_i32", "2") DPP2("v_max_i32", "1") : "+v"(a), "+v"(b));
                const int xa_ = max(max(max(__builtin_amdgcn_readlane(a, 0), __builtin_amdgcn_readlane(a, 16)), max(__builtin_amdgcn_readlane(a, 32), __builtin_amdgcn_readlane(a, 48))), 40);
                const int yb_ = max(max(__builtin_amdgcn_readlane(b, 0), __builtin_amdgcn_readlane(b, 16)), max(__builtin_amdgcn_readlane(b, 32), __builtin_amdgcn_readlane(b, 48)));
                record(now + xa_, now + max(xa_ + d2 + MG2 + 10, yb_));
            }
            if (w == 2 && s < 2 && layer > 0) wait_all_tokens(p, lane, cap);
        }
        RAW_BARRIER();
#pragma unroll
        for (int j = 0; j < 8; ++j) smt.load(prt, j);

        tref = tries == 0 && s > 0 ? ytime - tight : 0;
        char* xdst = px + (size_t)(set * 4 + rot) * PX_BUF + w * 20480;
        TR(5); TRFLUSH(set * 128 + h);
        if (s + 2 >= NSEG) {
            static_for<0, SmTok::N>([&](auto I) { smt.template piece<CI(I)>(xdst, xoff); SCHEDB(); if constexpr (CI(I) & 1) asm volatile("s_nop 0"); SCHEDB(); });
            TT(set * 128 + h, 0);
            __builtin_amdgcn_s_sleep(64);
            char* ao;
            u32x4 sent;
#pragma unroll
            for (int i = 0; i < 10; ++i) rearm(rot, i, ao, sent);
            break;
        }
        xproj(s + 4, s + 1, std::integral_constant<int, 1>(), xdst);
    }
    }
}

constexpr int C_YL = 0, C_RED = 40960, C_EX = 49152, C_XN = 53248;
__device__ __forceinline__ void comb_role(const LaParams& p, const int set, const int c, char* shm) {
    const int lane = threadIdx.x & 63, w = __builtin_amdgcn_readfirstlane(threadIdx.x >> 6);
    const int fr = lane & 15, g = lane >> 4;
    const u32 lds0 = (u32)(uintptr_t)(LDS3 char*)shm;
    const int sidx = (lane & 8) ? 0xEEEE : 0x4444;
    int cap = SPIN_CAP;
    char* mine = p.hop;
    char* other = p.hop;
    int phys = 0, sch = (S_SLOT + w) * SLOT;
    u32 k1 = D1I + MG1;
    char* px = (wsp(p) + O_PX);
    const char* xn = (wsp(p) + O_XN);
    char* const yl0 = shm + C_YL + lane * 16;
    const u32 pvo = PLIN ? lane * 16 : (fr >> 2) * 256 + g * 64 + (fr & 3) * 16, pvm = PLIN ? lane * 16 : g * 512 + (fr >> 2) * 128 + (fr & 3) * 16;
    int cb = (set * 80 + c) % 10, rb = (set * 80 + c) / 10;

    {
        const int t = threadIdx.x, h = (set * 80 + c) / 5, cu = c % 5;
        u32x4 st[12];
        const u16* src = p.Wo + ((size_t)h * DH + (t >> 4)) * DM + cu * 128 + (t & 15) * 8;
#pragma unroll
        for (int i = 0; i < 12; ++i) asm volatile("global_load_dwordx4 %0, %1, off" : "=v"(st[i]) : "v"(src + (size_t)i * 16 * DM) : "memory");
        u32 vo[12];
#pragma unroll
        for (int i = 0; i < 12; ++i) {
            const int id = i * 64 + lane, r = id / 24;
            vo[i] = kperm((r >> 2) & 3, (r >> 4) * 4 + (r & 3)) * (HD * 2) + ((id % 24) ^ (r & 7)) * 16;
        }
        const u32 ring = lds0 + 57344 + w * 24576;
        auto issue = [&](const int j) {
            const char* a = (const char*)p.Wv + (size_t)(((j + cu) % 5) * 4 + w) * 32 * HD * 2 + h * 384;
#pragma unroll
            for (int i = 0; i < 12; ++i) dma_gather(a, ring + (j & 1) * 12288 + i * 1024, vo[i]);
        };
        SCHEDB();
        issue(0);
        const u32 ra = ring + fr * 384 + ((g ^ (fr & 3)) + (fr & 4)) * 16;
        const int e = t >> 4, pr = ((e >> 2) & 1) * 8 + (e >> 3) * 4 + (e & 3);
        asm volatile("s_waitcnt vmcnt(12)" : "+v"(st[0]), "+v"(st[1]), "+v"(st[2]), "+v"(st[3]), "+v"(st[4]), "+v"(st[5]),
                     "+v"(st[6]), "+v"(st[7]), "+v"(st[8]), "+v"(st[9]), "+v"(st[10]), "+v"(st[11]) :: "memory");
#pragma unroll
        for (int i = 0; i < 12; ++i) *(u32x4*)(shm + (i * 16 + pr) * 288 + (t & 15) * 16) = st[i];
        issue(1);
        RAW_BARRIER();
        { const int layer = 0; TT(set * 128 + 32 + c, 12); }
        bf8 B[8][6], A[2][2][6];
        const u32 rd = lds0 + ((g >> 1) * 16 + (g & 1) * 4 + (fr >> 2)) * 288 + (fr & 3) * 8;
#pragma unroll
        for (int n = 0; n < 8; ++n)
#pragma unroll
            for (int k = 0; k < 6; ++k) B[n][k] = tr8(rd + k * 32 * 288 + n * 32, rd + (k * 32 + 8) * 288 + n * 32);
        auto lda = [&](const int j, const int k) {
#pragma unroll
            for (int u = 0; u < 2; ++u) A[j & 1][u][k] = *(LDS3 const bf8*)(uintptr_t)((ra ^ ((k & 1) * 64)) + (j & 1) * 12288 + u * 6144 + (k >> 1) * 128);
        };
        asm volatile("s_waitcnt vmcnt(12)" ::: "memory");
#pragma unroll
        for (int k = 0; k < 6; ++k) lda(0, k);
        f32x4 cc[2][2];
        char* dst;
        const char* nsrc;
        static_for<0, 41>([&](auto N) {
            constexpr int n = CI(N) % 8, j = CI(N) / 8, q = CI(N) - 1;
            if constexpr (n == 7 && j < 4) asm volatile("s_waitcnt vmcnt(%0)" :: "n"(j == 0 ? 18 : (j < 3 ? 21 : 9)) : "memory");
            if constexpr (n == 1) { dst = wsp(p) + O_WVO + ((size_t)(h * 20 + ((j + cu) % 5) * 4 + w) * 40 + cu * 8 + 4) * 1024; asm volatile("" : "+s"(dst)); }
            if constexpr (n == 0 && j < 3) { nsrc = (const char*)p.Wv + (size_t)(((j + 2 + cu) % 5) * 4 + w) * 32 * HD * 2 + h * 384; asm volatile("" : "+s"(nsrc)); }
            u32x4 o;
#pragma unroll
            for (int k = 0; k < 6; ++k) {
                if constexpr (j < 5) {
#pragma unroll
                    for (int u = 0; u < 2; ++u) { if (k == 0) MMA0_VA(cc[n & 1][u], A[j & 1][u][k], B[n][k]); else MMA_VA(cc[n & 1][u], A[j & 1][u][k], B[n][k]); }
                } else if (k == 0) asm volatile("s_nop 7");
                SCHEDB();
                if constexpr (j < 3 && n >= 1 && n < 7) {
                    if (k == 0 || k == 2) asm volatile("s_mov_b32 m0, %2\n\ts_nop 0\n\tglobal_load_lds_dwordx4 %0, %1" :: "v"(vo[2 * (n - 1) + k / 2]), "s"(nsrc), "s"(ringp(ring, (j & 1) * 12288 + (2 * (n - 1) + k / 2) * 1024)) : "memory");
                }
                if constexpr (n == 7 && j < 4) lda(j + 1, k);
                if constexpr (q >= 0) {
                    if (k == 3) { PIN(cc[q & 1][0]); o[0] = pk2bf(cc[q & 1][0][0], cc[q & 1][0][1]); o[1] = pk2bf(cc[q & 1][0][2], cc[q & 1][0][3]); }
                    if (k == 4) { PIN(cc[q & 1][1]); o[2] = pk2bf(cc[q & 1][1][0], cc[q & 1][1][1]); o[3] = pk2bf(cc[q & 1][1][2], cc[q & 1][1][3]); }
                    if (k == 5) __builtin_amdgcn_global_store_b128((gu32x4*)(dst + (u32)(lane * 16) + (q % 8 - 4) * 1024), o, "agent");
                }
                SCHEDB();
            }
        });
        { const int layer = 0; TT(set * 128 + 32 + c, 13); }
        VM0(); RAW_BARRIER();
        { const int layer = 0; TT(set * 128 + 32 + c, 14); }
        if (w == 0) st4_lane0(wsp(p) + O_FL + (400 + set * 80 + c) * 128, 1u);
        dma_kb<10>(xn + (size_t)set * 40960 + w * 10240, lds0 + C_XN + w * 10240, lane * 16);
        if (w == 0) {
            __builtin_amdgcn_s_sleep(8);
            const u32 off[1] = {(u32)(400 + (lane & 31) * 5 + (c >> 4)) * 128};
            wait_flags<1, FSL>(wsp(p) + O_FL, off, cap);
        }
        RAW_BARRIER();
        { const int layer = 0; TT(set * 128 + 32 + c, 15); }
    }
    bf16v WA[32], WV[8];
    {
        u32 vo = (w * 8 + (fr >> 3)) * 819200 + g * 256 + (fr & 7) * 16;
        asm volatile("" : "+v"(vo));
        auto ldw = [&](const int ks, const int t) {
            const char* a = wsp(p) + O_WVO + (size_t)(t * 1600 + ks * 80 + (c >> 1)) * 1024 + (c & 1) * 128;
            const char* b = a + 40960;
            asm volatile("" : "+s"(b));
            return cat8(__builtin_bit_cast(bf8, ld16(a, vo, 0)), __builtin_bit_cast(bf8, ld16(b, vo, 0)));
        };
#pragma unroll
        for (int i = 0; i < 32; ++i) WA[i] = ldw(i >> 2, i & 3);
#pragma unroll
        for (int i = 0; i < 8; ++i) WV[i] = ldw(8 + (i >> 2), i & 3);
    }
    auto yx = [&](f32x4 (&acc)[4][2], const int sy, auto&& extra) {
        const char* xa = shm + C_XN + ((sy >> 1) & 1) * 40960 + lane * 16;
        const char* xs = (sy + 2 < NSEG ? xn + (size_t)(sy + 2) * 40960 : (wsp(p) + O_WO) + cb * 81920) + w * 10240;
        u32 xl = lds0 + C_XN + (~(sy >> 1) & 1) * 40960 + w * 10240;
        bf8 x0 = *(const bf8*)(xa), x1 = *(const bf8*)(xa + 1024);
        const char* xs3[3] = {xs, xs + 4096, xs + 8192};
        u32 xl3[3] = {xl, xl + 4096, xl + 8192};
        asm volatile("" : "+s"(xs3[0]), "+s"(xs3[1]), "+s"(xs3[2]), "+s"(xl3[0]), "+s"(xl3[1]), "+s"(xl3[2]));
        static_for<0, 20>([&](auto K) {
            constexpr int k = CI(K);
            bf8 n0, n1;
            static_for<0, 4>([&](auto TT_) {
                constexpr int t = CI(TT_), id = (k >> 1) * 4 + t, dn = 2 * (k - 3) + t - 2;
                if constexpr (k < 19 && t == 0) n0 = *(const bf8*)(xa + (k + 1) * 2048);
                if constexpr (k < 19 && t == 1) n1 = *(const bf8*)(xa + (k + 1) * 2048 + 1024);
                if constexpr (t >= 2 && k >= 3 && k < 8) dma_one<(dn & 3) * 1024>(xs3[dn >> 2], xl3[dn >> 2], lane * 16);
                if constexpr (t >= 2) extra(2 * k + t - 2);
                SCHEDB();
                if constexpr (id < 32) {
                    const bf8 wf = (k & 1) ? hi8(WA[id]) : lo8(WA[id]);
                    if constexpr (k == 0) { MMA0_VA(acc[t][0], x0, wf); MMA0_VA(acc[t][1], x1, wf); } else { MMA_VA(acc[t][0], x0, wf); MMA_VA(acc[t][1], x1, wf); }
                } else {
                    const bf8 wf = (k & 1) ? hi8(WV[id - 32]) : lo8(WV[id - 32]);
                    MMA_VV(acc[t][0], x0, wf); MMA_VV(acc[t][1], x1, wf);
                }
                SCHEDB();
            });
            if constexpr (k < 19) { x0 = n0; x1 = n1; }
        });
    };

    for (int layer = 0; layer < 2; ++layer) {
    u32 ynext = ~0u;
    if (layer > 0) k1 = sched_get(~0u, mine + sch - 5 * SLOT + (62 + set) * 128 + 8, cap);
    VM0(); RAW_BARRIER();
    TT(set * 128 + 32 + c, 11);
    if (ANN && w == 1) st4_lane0(wsp(p) + O_FL + (576 + layer * 256 + set * 128 + 32 + c) * 128, nosent(rt32() + (set ? AN_C1 : AN_C0)));
    if (layer > 0 && w == 0) st4_lane0(wsp(p) + O_FL + (192 + rb * 10 + cb) * 128, 1u);
    {
        f32x4 ya[4][2];
        yx(ya, set, [](int) {});
        asm volatile("s_nop 7");
#pragma unroll
        for (int t = 0; t < 4; ++t) { PIN(ya[t][0]); PIN(ya[t][1]); *(bf8*)(yl0 + (w * 5 + t) * 1024) = pack8(ya[t][0], ya[t][1]); }
        RAW_BARRIER();
    }
    if (layer == 0) { phys = set ^ get_side(p, cap); mine += phys * 8192; other += (phys ^ 1) * 8192; }
    else if (w == 3) wait_all_tokens(p, lane, cap);
    TT(set * 128 + 32 + c, 10);
    for (int s = set; s < NSEG; s += 2) {
        const int rot = (s >> 1) & 3;
        char* yl = yl0 + (rot & 1) * 20480;
        TRDECL; TR(0);
        const char* mb0 = (s > 0 ? mine + (M_SLOT0 + rot * 8) * SLOT : p.hop + M0_OFF + phys * 8192) + 2 * w * SLOT + 2048;
        const char* mb1 = mb0 + SLOT;
        asm volatile("" : "+s"(mb0), "+s"(mb1));
        u32 yt = 0;
        bool late = s == 0;
        if (s > 0) { yt = sched_get(ynext, mine + sch + (s - 1) * 128 + 4, cap); wait_rt(yt + CLAG); }
        auto ldy = [&](const int f) { return *(const bf8*)(yl + (f < 16 ? (f >> 2) * 5 + (f & 3) : (f - 16) * 5 + 4) * 1024); };
        bf8 Y0[20 - 3 * NH_];
        TR(1);
        {
            f32x4 acc[4];
            u32x4 A[10];
            auto gather = [&]() {
                u32 vo = lane * 16;
                asm volatile("" : "+v"(vo) :: "memory");
#pragma unroll
                for (int ks = 0; ks < 10; ++ks) {
                    A[ks] = ld16(ks < 5 ? mb0 : mb1, vo, (ks % 5 - 2) * 1024);
                    SCHEDB();
                    if (ks < 4) ZERO4(acc[ks]);
                    if (ks < 9) asm volatile("s_nop %0" :: "n"(PACE));
                    SCHEDB();
                }
            };
            auto mproj = [&]() {
#pragma unroll
                for (int ks = 0; ks < 10; ++ks) {
#pragma unroll
                    for (int t = 0; t < 4; ++t) { if (ks < 8) SMM_A(acc[t], A[ks], WA[(ks * 4 + t) & 31], sidx); else SMM_V(acc[t], A[ks], WV[(ks * 4 + t) & 7], sidx); }
                }
                asm volatile("s_nop 7");
#pragma unroll
                for (int t = 0; t < 4; ++t) PIN(acc[t]);
            };
            gather();
#pragma unroll
            for (int i = 0; i < 20 - 3 * NH_; ++i) Y0[i] = ldy(i);
            SCHEDB();
            mproj();
            if (__builtin_expect(__any(__builtin_isunordered(acc[0][0], acc[0][1]) || __builtin_isunordered(acc[0][2], acc[0][3])), 0)) {
                CNT(1);
                late = true;
                for (int tries = 1;; ++tries) {
                    gather();
                    u32 m = 0;
#pragma unroll
                    for (int ks = 0; ks < 10; ++ks) m = max4(m, A[ks]);
                    if (!__any(m == ~0u)) break;
                    if (tries >= cap) { cap = 1; break; }
                    __builtin_amdgcn_s_sleep(8);
                }
                mproj();
            }
#pragma unroll
            for (int pr = 0; pr < 2; ++pr) {
                float v[4];
#pragma unroll
                for (int i = 0; i < 4; ++i) v[i] = halfsum(acc[2 * pr][i], acc[2 * pr + 1][i]);
                *(u32x2*)(shm + C_YL + (rot & 1) * 20480 + (w * 5 + 4) * 1024 + ((2 * pr + (g >> 1)) * 16 + fr) * 16 + (g & 1) * 8) = (u32x2){pk2bf(v[0], v[1]), pk2bf(v[2], v[3])};
            }
        }
        RAW_BARRIER();
        TR(2);
        u32 took = 0;
        if (s < NSEG - 1) {
            const char* pb[3];
#pragma unroll
            for (int i = 0; i < 3; ++i) { pb[i] = mine + (P_SLOT0 + (rot * 4 + c / 20) * PCS + i) * SLOT + 4096; asm volatile("" : "+s"(pb[i])); }
            auto ldp = [&](const int f, const u32 vo, const u32 vm) { return f < 16 ? ld16(pb[f >> 3], vo, ((f & 7) - 4) * 1024) : ld16(pb[2], vm, PLIN ? (f - 20) * 1024 : (f - 18) * 2048); };
            u32 xt;
            auto partial = [&](auto NF, auto F0, f32x4& acc, auto&& lastp) {
                constexpr int nf = CI(NF), f0 = CI(F0);
                bf8 (&Y)[nf] = Y0;
#pragma unroll
                for (int i = 0; i < nf; ++i) PIN(Y[i]);
                u32 vo = pvo;
                asm volatile("" : "+v"(vo));
                xt = late ? sched_get(~0u, mine + sch + s * 128, cap) : yt + k1;
                TR(3);
                if (!SYNCB || f0 == 0) wait_rt(xt);
                if (SYNCB) __builtin_amdgcn_s_barrier();
                TRV(7, xt); TRC(6);
                if (W0D > 0 && f0 == 0) asm volatile("s_nop %0" :: "n"(W0D));
                asm volatile("" ::: "memory");
                u32x4 P[nf];
#pragma unroll
                for (int i = 0; i < nf; ++i) { P[i] = ldp(f0 + i, vo, vo); SCHEDB(); }
#pragma unroll
                for (int i = 0; i < nf; ++i) {
                    if (i == nf - LATEP) lastp();
                    if (i == 0) MMA0_VV(acc, Y[i], P[i]); else MMA_VV(acc, Y[i], P[i]);
                    if (i == 0) TRC(7);
                    SCHEDB();
                }
                TRC(8);
            };
            f32x4 acc;
            if (w > 0) {
                bf8 Y[NH_];
                const char* q[NH_];
                u32 vo[NH_], ex = lds0 + C_EX + (w - 1) * 1024 + lane * 16;
#pragma unroll
                for (int i = 0; i < NH_; ++i) {
                    const int f = 20 - (4 - w) * NH_ + i;
                    Y[i] = ldy(f);
                    q[i] = pb[0] + (f >> 3) * SLOT + (f & 7) * (PLIN || f < 16 ? 1024 : 2048) - 4096;
                    vo[i] = f < 16 ? pvo : pvm;
                    asm volatile("" : "+s"(q[i]));
                    PIN(vo[i]);
                }
#pragma unroll
                for (int i = 0; i < NH_; ++i) PIN(Y[i]);
                PIN(ex);
                __builtin_amdgcn_s_barrier();
                TRC(6);
                asm volatile("" ::: "memory");
                u32x4 P[NH_];
#pragma unroll
                for (int i = 0; i < NH_; ++i) { P[i] = ld16(q[i], vo[i], 0); SCHEDB(); }
#pragma unroll
                for (int i = 0; i < NH_; ++i) { if (i == 0) MMA0_VV(acc, Y[i], P[i]); else MMA_VV(acc, Y[i], P[i]); if (i == 0) TRC(7); }
                TRC(8);
                asm volatile("s_nop 7" : "+v"(acc));
                *(LDS3 f32x4*)(uintptr_t)ex = acc;
                RAW_BARRIER();
            } else {
                const int rn = ((s + 1) >> 1) & 3;
                u32 dst = g * 2 * SLOT + (lane & 7) * 16;
                char* dstb = other + (M_SLOT0 + rn * 8 + c / 40) * SLOT + (c % 40) * 128;
                u32 exa = lds0 + C_EX + lane * 16;
                asm volatile("" : "+s"(dstb), "+v"(dst), "+v"(exa));
                f32x4 h1, h2, h3;
                partial(std::integral_constant<int, 20 - 3 * NH_>(), std::integral_constant<int, 0>(), acc, [&] {
                    __builtin_amdgcn_s_barrier();
                    asm volatile("" ::: "memory");
                    h1 = *(LDS3 const f32x4*)(uintptr_t)exa; h2 = *(LDS3 const f32x4*)(uintptr_t)(exa + 1024); h3 = *(LDS3 const f32x4*)(uintptr_t)(exa + 2048);
                    SCHEDB();
                });
                h1 = (h1 + h2) + h3;
                asm volatile("s_nop 2" : "+v"(acc), "+v"(h1));
                acc += h1;
                float v0, v1;
                auto merge = [&]() {
                    auto a = __builtin_amdgcn_permlane32_swap(__float_as_uint(acc[0]), __float_as_uint(acc[2]), false, false);
                    auto b = __builtin_amdgcn_permlane32_swap(__float_as_uint(acc[1]), __float_as_uint(acc[3]), false, false);
                    v0 = __uint_as_float(a[0]) + fdpp<0x128>(__uint_as_float(a[1]));
                    v1 = __uint_as_float(b[0]) + fdpp<0x128>(__uint_as_float(b[1]));
                };
                merge();
                int tries = 0;
                if (__builtin_expect(__any(__builtin_isunordered(v0, v1)), 0)) {
                    CNT(2);
                    wait_rt(sched_get(~0u, mine + sch + s * 128, cap));
                    while (true) {
                        u32 vo = pvo, vm = pvm, m = 0;
                        asm volatile("" : "+v"(vo), "+v"(vm) :: "memory");
                        u32x4 P[20];
#pragma unroll
                        for (int i = 0; i < 20; ++i) P[i] = ldp(i, vo, vm);
                        f32x4 hh[4];
#pragma unroll
                        for (int i = 0; i < 20; ++i) {
                            const bf8 y = ldy(i);
                            const int k = i < 20 - 3 * NH_ ? 0 : (i - 20 + 4 * NH_) / NH_;
                            if (i == 0 || i == 20 - 3 * NH_ || i == 20 - 2 * NH_ || i == 20 - NH_) MMA0_VV(hh[k], y, P[i]); else MMA_VV(hh[k], y, P[i]);
                            m = max4(m, P[i]);
                            SCHEDB();
                        }
                        asm volatile("s_nop 7" : "+v"(hh[0]), "+v"(hh[1]), "+v"(hh[2]), "+v"(hh[3]));
                        acc = hh[0] + ((hh[1] + hh[2]) + hh[3]);
                        ++tries;
                        if (!__any(m == ~0u)) break;
                        if (tries >= cap) { cap = 1; break; }
                        __builtin_amdgcn_s_sleep(4);
                    }
                    merge();
                }
                TRV(6, tries + 1);
                asm volatile("" :: "s"(dstb));
                const u32 dw = nosent(pk2bf(v0, v1));
                auto e = __builtin_amdgcn_permlane32_swap(dw, dw, false, false);
                auto e0 = __builtin_amdgcn_permlane16_swap(e[0], e[0], false, false);
                auto e1 = __builtin_amdgcn_permlane16_swap(e[1], e[1], false, false);
                st16f(dstb, dst, (u32x4){e0[0], e1[0], e0[1], e1[1]}, 0x00ff00ff00ff00ffull);
                TR(4);
                took = tries == 0 && s > 0 ? rt32() - xt : 0;
            }
            if (s == NSEG - 2) RAW_BARRIER();
        } else wait_rt(sched_get(~0u, mine + sch + s * 128, cap) + T_PX);
        auto tokens = [&](auto MORE) {
            f32x4 oa[4], ya[4][2];
            u32x4 P[20];
            u32 vo = lane * 16 + w * 4096, vm = lane * 16 + 16384 + w * 1024;
            asm volatile("" : "+v"(vo), "+v"(vm));
            const char* pb[4];
#pragma unroll
            for (int i = 0; i < 4; ++i) { pb[i] = px + (size_t)(set * 4 + rot) * PX_BUF + i * 20480; asm volatile("" : "+s"(pb[i])); }
            auto ldp = [&](const int i) { P[i] = i % 5 == 4 ? ld16(pb[i / 5], vm, 0) : ld16(pb[i / 5], vo, (i % 5) * 1024); };
            int ln = lane;
            asm volatile("" : "+v"(ln));
            u32 rg, peek, peek2;
            bf8 y[5];
            auto side = [&](const int i) {
                if (i == 4) rg = sched_peek((wsp(p) + O_XR) + layer * ACT + (size_t)s * 40960 + ((c >> 2) * 2 + (w >> 1)) * 1024 + (c & 2) * 256 + (w & 1) * 128 + (c & 1) * 4 + (u32)((ln & 2) * 128 + (ln >> 3) * 16 + (ln & 1) * 8));
                if (i >= 16 && i < 36) ldp(i - 16);
                if (i == 2 && CI(MORE) && w == 0) st4_lane0(mine + (T_SLOT + (c >> 6)) * SLOT + (c & 63) * 128, took);
                if (i == 5 && CI(MORE)) peek2 = sched_peek(mine + sch + s * 128 + 8);
                if (i >= 35) y[i - 35] = *(const bf8*)(yl + (w * 5 + i - 35) * 1024);
            };
            TRC(9);
            if constexpr (CI(MORE)) yx(ya, s + 2, side);
            else {
#pragma unroll
                for (int i = 0; i < 40; ++i) side(i);
            }
            TRC(10);
            bf8 yn[4];
            auto outx = [&](const bool first) {
                if (!first) {
#pragma unroll
                    for (int i = 0; i < 20; ++i) PIN(P[i]);
                    NOPV();
                }
#pragma unroll
                for (int q = 0; q < 4; ++q)
#pragma unroll
                    for (int j = 0; j < 5; ++j) {
                        if (j == 0) MMA0_VV(oa[q], y[j], P[q * 5 + j]); else MMA_VV(oa[q], y[j], P[q * 5 + j]);
                        if (CI(MORE) && first && q * 5 + j >= 3 && q * 5 + j < 19) {
                            const int e = q * 5 + j - 3;
                            SCHEDB();
                            if ((e & 1) == 0) PIN(ya[e >> 2][(e >> 1) & 1]);
                            ((u32*)&yn[e >> 2])[e & 3] = pk2bf(ya[e >> 2][(e >> 1) & 1][(e & 1) * 2], ya[e >> 2][(e >> 1) & 1][(e & 1) * 2 + 1]);
                            if ((e & 3) == 3) *(bf8*)(yl0 + (~rot & 1) * 20480 + (w * 5 + (e >> 2)) * 1024) = yn[e >> 2];
                            SCHEDB();
                        }
                    }
                if (CI(MORE) && first) { SCHEDB(); peek = sched_peek(mine + sch + (w == 0 ? s + 1 : s) * 128 + 4); SCHEDB(); }
                asm volatile("s_nop 7");
#pragma unroll
                for (int q = 0; q < 4; ++q) PIN(oa[q]);
            };
            outx(true);
            if (__builtin_expect(__any(__builtin_isunordered(oa[0][0], oa[1][0]) || __builtin_isunordered(oa[2][0], oa[3][0])), 0)) {
                CNT(3);
                for (int tries = 1;; ++tries) {
                    asm volatile("" : "+v"(vo), "+v"(vm) :: "memory");
                    u32 m = 0;
#pragma unroll
                    for (int i = 0; i < 20; ++i) ldp(i);
#pragma unroll
                    for (int i = 0; i < 20; ++i) m = max4(m, P[i]);
                    if (!__any(m == ~0u)) break;
                    if (tries >= cap) { cap = 1; break; }
                    __builtin_amdgcn_s_sleep(8);
                }
                outx(false);
            }
            if constexpr (!CI(MORE)) { if (c == 0 && w == 0) stamp2(p.hop, 48 + layer * 4 + set); }
            const bool own = (fr < 8) == (g < 2);
#pragma unroll
            for (int q = 0; q < 4; q += 2) {
                f32x4 o;
#pragma unroll
                for (int i = 0; i < 4; ++i) { const float t = fdpp<0x128>(oa[q + 1][i]); o[i] = own ? oa[q][i] : t; }
                *(f32x4*)(shm + C_RED + ((w * 2 + (g >> 1)) * 32 + (q + !own) * 8 + (fr & 7)) * 32 + (g & 1) * 16) = o;
            }
            if constexpr (CI(MORE)) { if (w == 0) *(u32*)(shm + C_EX + 3072) = peek; else asm volatile("" :: "v"(peek)); }
            RAW_BARRIER();
            TR(5);
            typedef __attribute__((ext_vector_type(2))) float f32x2;
            const char* rp = shm + C_RED + (ln & 4) * 1024 + w * 256 + (ln >> 3) * 32 + (ln & 3) * 8;
            f32x2 a = (*(const f32x2*)rp + *(const f32x2*)(rp + 1024)) + (*(const f32x2*)(rp + 2048) + *(const f32x2*)(rp + 3072));
            if constexpr (CI(MORE)) { k1 = sched_get(peek2, mine + sch + s * 128 + 8, cap); ynext = __builtin_amdgcn_readfirstlane(*(const u32*)(shm + C_EX + 3072)); asm volatile("" : "+s"(ynext), "+s"(k1)); }
            a[0] += fdpp<0x104>(a[0]); a[1] += fdpp<0x104>(a[1]);
            const u32 y2 = pk2bf(a[0], a[1]), d = nosent(pk2bf(bflo(rg) + bflo(y2), bfhi(rg) + bfhi(y2)));
            const u32x4 o4 = {d, (u32)__builtin_amdgcn_mov_dpp(d, 0x55, 0xf, 0xf, true), (u32)__builtin_amdgcn_mov_dpp(d, 0xaa, 0xf, 0xf, true), (u32)__builtin_amdgcn_mov_dpp(d, 0xff, 0xf, 0xf, true)};
            st16m((wsp(p) + O_X1) + layer * ACT + (size_t)s * 40960 + c * 512 + w * 128, ln * 2, o4, 0x0101010101010101ull);
            if (w == 1) {
                u32x4 v;
                asm volatile("v_mov_b64 %0, -1\n\tv_mov_b64 %1, -1" : "=v"(((u64*)&v)[0]), "=v"(((u64*)&v)[1]));
                st16m(other + (M_SLOT0 + (((s + 7) >> 1) & 3) * 8 + c / 40) * SLOT + (c % 40) * 128, (ln >> 4) * 2 * SLOT + (ln & 7) * 16, v, 0x00ff00ff00ff00ffull);
            }
            TRC(11); TRFLUSH(set * 128 + 32 + c);
        };
        if (s + 2 >= NSEG) { tokens(std::integral_constant<int, 0>()); break; }
        tokens(std::integral_constant<int, 1>());
    }
    TT(set * 128 + 32 + c, 0);
    asm volatile("" : "+s"(cb), "+s"(rb));
    dma_kb<10>((wsp(p) + O_WO) + cb * 81920 + 40960 + w * 10240, lds0 + C_XN + 40960 + w * 10240, lane * 16);
    if (w == 0) { const u32 t = sched_get(~0u, mine + (T_SLOT + 1) * SLOT + (50 + layer * 4) * 128, cap); *(u32*)(shm + C_EX) = t; }
    VM0(); RAW_BARRIER();
    wait_rt(__builtin_amdgcn_readfirstlane(*(const u32*)(shm + C_EX)) + T_OB + OB_STEP * w + (layer == 0 ? OB_COLD + 8 * w : 0));
    {
        int ln = lane;
        asm volatile("" : "+v"(ln));
        u32x4 O[20], R[2];
        u32 dw[8];
        auto ready = [&](const int T) {
            const char* ob = (wsp(p) + O_OB) + layer * ACT + (size_t)T * 20480 + 16384;
            const u32 off[1] = {(u32)(ln & 31) * 128};
            wait_flags<1>(ob, off, cap);
        };
        auto load = [&](const int T) {
            const char* ob0 = (wsp(p) + O_OB) + layer * ACT + (size_t)T * 20480 + 4096;
            const char* ob1 = ob0 + 8192;
            const char* ob2 = ob0 + 12288;
            const char* xb = (wsp(p) + O_X1) + layer * ACT + (size_t)(T >> 1) * 40960 + cb * 4096 + (T & 1) * 256;
            u32 vo = ln * 16, vr = (ln >> 4) * 256 + (ln & 15) * 8, vx = (ln >> 4) * 1024 + (ln & 15) * 16;
            asm volatile("" : "+v"(vo), "+v"(vr), "+v"(vx), "+s"(ob0), "+s"(ob1), "+s"(ob2), "+s"(xb) :: "memory");
#pragma unroll
            for (int k = 0; k < 16; ++k) { O[k] = ld16(k < 8 ? ob0 : ob1, vo, ((k & 7) - 4) * 1024); SCHEDB(); }
#pragma unroll
            for (int k = 0; k < 4; ++k) {
                const u64 lo = __hip_atomic_load((const gu64*)(ob2 + vr + k * 1024), __ATOMIC_RELAXED, __HIP_MEMORY_SCOPE_AGENT);
                const u64 hi = __hip_atomic_load((const gu64*)(ob2 + vr + k * 1024 + 128), __ATOMIC_RELAXED, __HIP_MEMORY_SCOPE_AGENT);
                O[16 + k] = (u32x4){(u32)lo, (u32)(lo >> 32), (u32)hi, (u32)(hi >> 32)};
            }
            R[0] = ld16(xb, vx, 0); R[1] = ld16(xb, vx, 512);
            SCHEDB();
        };
        auto gemm = [&]() {
            u32 wa0 = lds0 + C_XN + ln * 16, wa1 = wa0 + 40960;
            asm volatile("" : "+v"(wa0), "+v"(wa1));
            f32x4 acc[4];
            bf8 wf[8];
#pragma unroll
            for (int t = 0; t < 8; ++t) wf[t] = *(LDS3 const bf8*)(uintptr_t)(wa0 + t * 1024);
#pragma unroll
            for (int k = 0; k < 20; ++k) {
                if (k >= 16) { PIN(O[k]); NOPV(); }
#pragma unroll
                for (int t = 0; t < 4; ++t) {
                    if (k == 0) MMA0_VV(acc[t], wf[t], O[k]); else MMA_VV(acc[t], wf[(k & 1) * 4 + t], O[k]);
                    SCHEDB();
                    if (k < 18) wf[(k & 1) * 4 + t] = *(LDS3 const bf8*)(uintptr_t)((k < 8 ? wa0 : wa1) + (((k + 2) % 10) * 4 + t) * 1024);
                    SCHEDB();
                }
            }
            asm volatile("s_nop 7" : "+v"(acc[0]), "+v"(acc[1]), "+v"(acc[2]), "+v"(acc[3]));
            float chk = 0.f;
#pragma unroll
            for (int i = 0; i < 8; ++i) {
                const u32 r = R[i >> 2][i & 3], y = pk2bf(acc[i >> 1][(i & 1) * 2], acc[i >> 1][(i & 1) * 2 + 1]);
                const float a = bflo(r) + bflo(y), b = bfhi(r) + bfhi(y);
                dw[i] = pk2bf(a, b);
                chk += a;
            }
            return __any(chk != chk);
        };
        const int T0 = rb + 16 * w;
        if (OB_POLL) ready(T0);
        load(T0);
        static_for<0, 2>([&](auto PASS) {
            constexpr int pass = CI(PASS);
            const int T = T0 + 64 * pass;
            TT(set * 128 + 32 + c, 1 + pass * 4);
            if (__builtin_expect(gemm(), 0)) {
                for (int tries = 0;; ++tries) {
                    ready(T); load(T);
                    u32 m = max4(max4(0, R[0]), R[1]);
#pragma unroll
                    for (int k = 0; k < 20; ++k) m = max4(m, O[k]);
                    if (!__any(m == ~0u)) break;
                    if (tries >= cap) { cap = 1; break; }
                    __builtin_amdgcn_s_sleep(6);
                }
                gemm();
            }
            TT(set * 128 + 32 + c, 2 + pass * 4);
            if (layer == 1) {
                st32((char*)p.out + (size_t)T * 16 * DM * 2 + cb * 128, (ln & 15) * (DM * 2) + (ln >> 4) * 32, (u32x4){dw[0], dw[1], dw[2], dw[3]}, (u32x4){dw[4], dw[5], dw[6], dw[7]});
            } else {
            float s1 = 0.f, s2 = 0.f;
#pragma unroll
            for (int i = 0; i < 8; ++i) { const float a = bflo(dw[i]), b = bfhi(dw[i]); s1 += a + b; s2 = fmaf(a, a, fmaf(b, b, s2)); }
            s1 = grp_sum(s1); s2 = grp_sum(s2);
            char* stl = (wsp(p) + O_ST) + (size_t)T * 1280;
            st8m(stl + cb * 128, ln * 8, (u32x2){nosent(__float_as_uint(s1)), nosent(__float_as_uint(s2))}, 0xffffull);
            const size_t fo = ACT + (size_t)(T >> 1) * 40960 + (cb * 4 + (T & 1)) * 1024;
            const u32 lo = (ln >> 5) * 2048 + ((ln >> 4) & 1) * 512 + (ln & 15) * 16;
            __builtin_amdgcn_s_sleep(ST_LAG);
            for (int spin = 0;; ++spin) {
                u32 m = 0;
                s1 = 0.f; s2 = 0.f;
#pragma unroll
                for (int i = 0; i < 3; ++i) {
                    const int k = (ln >> 4) + 4 * i;
                    const u64 v = __hip_atomic_load((const gu64*)(stl + min(k, 9) * 128 + (ln & 15) * 8), __ATOMIC_RELAXED, __HIP_MEMORY_SCOPE_AGENT);
                    m = max(m, max((u32)v, (u32)(v >> 32)));
                    if (k < 10) { s1 += __uint_as_float((u32)v); s2 += __uint_as_float((u32)(v >> 32)); }
                }
                if (!__any(m == ~0u)) break;
                if (spin >= cap) { cap = 1; break; }
                __builtin_amdgcn_s_sleep(2);
            }
            TT(set * 128 + 32 + c, 3 + pass * 4);
            s1 = grp_sum(s1); s2 = grp_sum(s2);
            const float mu = s1 * (1.0f / DM), rs = __builtin_amdgcn_rsqf(fmaxf(fmaf(-mu, mu, s2 * (1.0f / DM)), 0.f) + 1e-5f);
            u32 dn[8];
#pragma unroll
            for (int i = 0; i < 8; ++i) dn[i] = pk2bf((bflo(dw[i]) - mu) * rs, (bfhi(dw[i]) - mu) * rs);
            st16s((wsp(p) + O_XN) + fo, lo, (u32x4){dn[0], dn[4], dn[1], dn[5]}); st16s((wsp(p) + O_XN) + fo + 256, lo, (u32x4){dn[2], dn[6], dn[3], dn[7]});
            st16s((wsp(p) + O_XR) + fo, lo, (u32x4){dw[0], dw[4], dw[1], dw[5]}); st16s((wsp(p) + O_XR) + fo + 256, lo, (u32x4){dw[2], dw[6], dw[3], dw[7]});
            TT(set * 128 + 32 + c, 4 + pass * 4);
            if (pass == 0 && w == 0) { VM0(); st4_lane0(wsp(p) + O_FL + (rb * 10 + cb) * 128, 1u); }
            }
            if constexpr (pass == 0) load(T + 64);
        });
    }
    if (layer == 1) break;
    sch += 5 * SLOT; xn += ACT;
    if (w == 0) wait_first_tokens(p, set, lane, cap);
    RAW_BARRIER();
    TT(set * 128 + 32 + c, 9);
    dma_kb<10>(xn + (size_t)set * 40960 + w * 10240, lds0 + C_XN + w * 10240, lane * 16);
    }
}

constexpr int B_PQ = 0, B_IM = 131072, B_CS = 155648, B_IS = 176;
__device__ __forceinline__ void bw_role(const LaParams& p, const int hb, char* shm) {
    const int lane = threadIdx.x & 63, w = __builtin_amdgcn_readfirstlane(threadIdx.x >> 6);
    const int fr = lane & 15, g = lane >> 4;
    int cap = SPIN_CAP;
    if (hb == 0 && w == 0) {
        u32 best[2] = {~0u, ~0u};
        const u32 tc = rt32();
        for (int i = 0; i < 256; ++i) {
#pragma unroll
            for (int sd = 0; sd < 2; ++sd) {
                u64 t0, t1;
                u32 z = 0, one = 1;
                asm volatile("s_memtime %0\n\ts_mov_b64 exec, 1\n\ts_waitcnt lgkmcnt(0)\n\tglobal_atomic_add %2, %3, %2, %4 sc0 sc1\n\ts_waitcnt vmcnt(0)\n\t"
                             "s_memtime %1\n\ts_mov_b64 exec, -1\n\ts_waitcnt lgkmcnt(0)"
                             : "=&s"(t0), "=&s"(t1), "+v"(one) : "v"(z), "s"(p.hop + M0_OFF + sd * 8192 + 6144 + (i & 3) * 256) : "memory");
                best[sd] = min(best[sd], (u32)(t1 - t0));
            }
            const u32 el = rt32() - tc, lo = min(best[0], best[1]), hi = max(best[0], best[1]);
            if (el > 1900u || (el > 600u && lo < 700u && hi - lo > 130u && hi - lo < 420u)) break;
        }
        u32 xcc; asm volatile("s_getreg_b32 %0, hwreg(HW_REG_XCC_ID)" : "=s"(xcc));
        st4_lane0(wsp(p) + O_FL + 384 * 128, ((xcc >> 2) & 1) ^ (best[1] < best[0] ? 1u : 0u));
    }
    bf8 WA[64], WV[16];
    {
        const int t = threadIdx.x;
        float* xs = (float*)(shm + 81920);
        float* ph = xs + 128 * 61;
        for (int i = t; i < 400; i += 256) ph[i] = bf2f(p.wphi[hb * 400 + i]);
        const int j = 4 * w + (fr & 3), G = fr >> 2;
        for (int d0 = 0; d0 < DM; d0 += 128) {
            u32 v15[15];
#pragma unroll
            for (int k = 0; k < 15; ++k) { const int i = t + k * 256; v15[k] = *(const u32*)(p.wqkv + (size_t)(d0 + i / 30) * 1920 + ((i % 30) / 10) * 640 + hb * 20 + (i % 10) * 2); }
            __syncthreads();
#pragma unroll
            for (int k = 0; k < 15; ++k) { const int i = t + k * 256; float* o = xs + (i / 30) * 61 + (i % 30) * 2; o[0] = bflo(v15[k]); o[1] = bfhi(v15[k]); }
            __syncthreads();
            for (int kk = 0; kk < 4; ++kk) {
                float v[8];
                const float* xr = xs + (kk * 32 + g * 8) * 61;
                if (j < 10) {
                    const int m = j / 5, f = 5 * G + j % 5;
#pragma unroll
                    for (int e = 0; e < 8; ++e) v[e] = 0.f;
#pragma unroll 4
                    for (int i = 0; i < 20; ++i) {
                        const float pw = ph[i * 20 + f];
#pragma unroll
                        for (int e = 0; e < 8; ++e) v[e] = fmaf(xr[e * 61 + m * 20 + i], pw, v[e]);
                    }
                } else {
#pragma unroll
                    for (int e = 0; e < 8; ++e) v[e] = j < 15 ? xr[e * 61 + 40 + 5 * G + min(j, 14) - 10] : 1.0f;
                }
                *(u32x4*)(shm + ((d0 / 32 + kk) * 4 + w) * 1024 + lane * 16) = (u32x4){pk2bf(v[0], v[1]), pk2bf(v[2], v[3]), pk2bf(v[4], v[5]), pk2bf(v[6], v[7])};
            }
        }
        __syncthreads();
#pragma unroll
        for (int i = 0; i < 64; ++i) WA[i] = *(const bf8*)(shm + i * 1024 + lane * 16);
#pragma unroll
        for (int i = 0; i < 16; ++i) WV[i] = *(const bf8*)(shm + (64 + i) * 1024 + lane * 16);
        __syncthreads();
    }
    auto proj = [&](f32x4 (&acc)[4], f32x4& sq, auto&& fetch) {
        u32x4 X[20];
#pragma unroll
        for (int k = 0; k < 12; ++k) { X[k] = fetch(k); SCHEDB(); }
#pragma unroll
        for (int k = 0; k < 20; ++k) {
#pragma unroll
            for (int t = 0; t < 4; ++t) {
                if (k == 0) MMA0_AV(acc[t], WA[t], X[k]); else if (k < 16) MMA_AV(acc[t], WA[(k * 4 + t) & 63], X[k]); else MMA_VV(acc[t], WV[(k * 4 + t) & 15], X[k]);
            }
            if (k == 0) MMA0_VV(sq, X[k], X[k]); else MMA_VV(sq, X[k], X[k]);
            SCHEDB();
            if (k < 8) X[k + 12] = fetch(k + 12);
            SCHEDB();
        }
        asm volatile("s_nop 7" : "+v"(acc[0]), "+v"(acc[1]), "+v"(acc[2]), "+v"(acc[3]), "+v"(sq));
    };
    {
        f32x4 t, cs[4];
        u32x4 o1 = {0x3f803f80u, 0x3f803f80u, 0x3f803f80u, 0x3f803f80u};
        asm volatile("s_nop 1" : "+v"(o1));
        proj(cs, t, [&](int) { return o1; });
#pragma unroll
        for (int i = 0; i < 4; ++i) *(f32x4*)(shm + B_CS + i * 1024 + lane * 16) = cs[i];
    }
    char* im = shm + B_IM + w * 6144;
    const u32 imr = (u32)(uintptr_t)(LDS3 char*)im + (4 * g + (fr >> 2)) * B_IS + (fr & 3) * 8;
    typedef __attribute__((ext_vector_type(32))) u32 u32x32;
    for (int layer = 0; layer < 2; ++layer) {
        const char* sch = p.hop + (hb & 1) * 8192 + (S_SLOT + layer * 5 + 4) * SLOT;
        f32x4 kv[3][2];
#pragma unroll
        for (int i = 0; i < 6; ++i) ZERO4(kv[i >> 1][i & 1]);
        u32x32 q5;
        u32 tn = 0;
        for (int it = 0; it < 32; ++it) {
            const int s = 2 * it + (w >> 1);
            if (s + 2 < NSEG) { wait_rt(tn); tn = sched_get(~0u, sch + (s + 1) * 128 + 4, cap); wait_rt(tn + B_LAG); tn += BPEEK; }
            else wait_rt(sched_get(~0u, p.hop + (hb & 1) * 8192 + (T_SLOT + 1) * SLOT + (48 + layer * 4 + (s & 1)) * 128, cap) + T_X1);
            const char* xb = (wsp(p) + O_X1) + layer * ACT + (size_t)s * 40960 + (w & 1) * 256;
            if (it >= 30) TT((hb >> 4) * 128 + 112 + (hb & 15), (it - 30) * 3);
            f32x4 acc[4], sq;
            const char* xk[5];
#pragma unroll
            for (int i = 0; i < 5; ++i) { xk[i] = xb + i * 8192 + 4096; asm volatile("" : "+s"(xk[i])); }
            for (int pass = 0;; ++pass) {
                u32 vo = g * 512 + fr * 16;
                asm volatile("" : "+v"(vo) :: "memory");
                auto fetch = [&](const int k) { return ld16(xk[k >> 2], vo, ((k & 3) - 2) * 2048); };
                proj(acc, sq, fetch);
                if (pass > 0 || !__any(acc[3][3] != acc[3][3])) break;
                CNT(4);
                for (int tries = 0;; ++tries) {
                    asm volatile("" : "+v"(vo) :: "memory");
                    u32 m = 0;
#pragma unroll
                    for (int k = 0; k < 20; k += 10) {
#pragma unroll
                        for (int i = 0; i < 10; ++i) m = max4(m, fetch(k + i));
                        SCHEDB();
                    }
                    if (!__any(m == ~0u)) break;
                    if (tries >= cap) { cap = 1; break; }
                    __builtin_amdgcn_s_sleep(6);
                }
            }
            if (it >= 30) TT((hb >> 4) * 128 + 112 + (hb & 15), (it - 30) * 3 + 1);
            if (s == NSEG - 1 && hb == 5 && w == 3) stamp2(p.hop, 50 + layer * 4);
            const float dsel = (fr & 2) ? ((fr & 1) ? sq[3] : sq[2]) : ((fr & 1) ? sq[1] : sq[0]);
            const float s2 = grp_sum(g == (fr >> 2) ? dsel : 0.f);
            const float mu = acc[3][3] * (1.0f / DM), rs = __builtin_amdgcn_rsqf(fmaxf(fmaf(-mu, mu, s2 * (1.0f / DM)), 0.f) + 1e-5f);
            float v[15];
            f32x4 cs[4];
#pragma unroll
            for (int i = 0; i < 4; ++i) cs[i] = *(const f32x4*)(shm + B_CS + i * 1024 + lane * 16);
#pragma unroll
            for (int j = 0; j < 15; ++j) v[j] = fmaf(-mu, cs[j >> 2][j & 3], acc[j >> 2][j & 3]) * rs;
            u32 ph[2][5];
#pragma unroll
            for (int m = 0; m < 2; ++m) {
                const float* x = v + 5 * m;
                const float mx = grp_max(fmaxf(fmaxf(fmaxf(x[0], x[1]), fmaxf(x[2], x[3])), x[4]));
                const float mn = -grp_max(-fminf(fminf(fminf(x[0], x[1]), fminf(x[2], x[3])), x[4]));
                float e[10], a = 0.f, b = 0.f;
#pragma unroll
                for (int j = 0; j < 5; ++j) {
                    e[j] = __builtin_amdgcn_exp2f((x[j] - mx) * 1.4426950408889634f); a += e[j];
                    e[5 + j] = __builtin_amdgcn_exp2f((mn - x[j]) * 1.4426950408889634f); b += e[5 + j];
                }
                a = __builtin_amdgcn_rcpf(grp_sum(a)); b = __builtin_amdgcn_rcpf(grp_sum(b));
#pragma unroll
                for (int j = 0; j < 5; ++j) { e[j] *= a; e[5 + j] *= b; }
#pragma unroll
                for (int j = 0; j < 5; ++j) ph[m][j] = pk2bf(e[2 * j], e[2 * j + 1]);
            }
            *(u32x4*)(shm + B_PQ + (w * 32 + it) * 1024 + lane * 16) = (u32x4){ph[0][0], ph[0][1], ph[0][2], ph[0][3]};
            q5[it] = ph[0][4];
            char* row = im + fr * B_IS;
            *(u32x2*)(row + g * 8) = (u32x2){ph[1][0], ph[1][1]};
            *(u32x2*)(row + 32 + g * 8) = (u32x2){ph[1][2], ph[1][3]};
            *(u32x2*)(row + 64 + g * 8) = (u32x2){ph[1][4], 0u};
            *(u32x2*)(row + 96 + g * 8) = (u32x2){pk2bf(v[10], v[11]), pk2bf(v[12], v[13])};
            *(u32x2*)(row + 128 + g * 8) = (u32x2){pk2bf(v[14], 1.0f), 0u};
            s16x4 tf[5];
#pragma unroll
            for (int i = 0; i < 5; ++i) tf[i] = __builtin_amdgcn_ds_read_tr16_b64_v4i16((LDS3 s16x4*)(uintptr_t)(imr + i * 32));
#pragma unroll
            for (int i = 0; i < 6; ++i) MMA16(kv[i >> 1][i & 1], tf[i >> 1], tf[3 + (i & 1)]);
            if (it >= 30) TT((hb >> 4) * 128 + 112 + (hb & 15), (it - 30) * 3 + 2);
        }
        asm volatile("s_nop 7" : "+v"(kv[0][0]), "+v"(kv[0][1]), "+v"(kv[1][0]), "+v"(kv[1][1]), "+v"(kv[2][0]), "+v"(kv[2][1]));
#pragma unroll
        for (int i = 0; i < 6; ++i) *(f32x4*)(im + i * 1024 + lane * 16) = kv[i >> 1][i & 1];
        RAW_BARRIER();
        bf8 ka[2];
        u32x2 kb[2];
#pragma unroll
        for (int dt = 0; dt < 2; ++dt) {
            f32x4 t[3];
#pragma unroll
            for (int ft = 0; ft < 3; ++ft) {
                const char* a = shm + B_IM + (ft * 2 + dt) * 1024 + lane * 16;
                t[ft] = (*(const f32x4*)a + *(const f32x4*)(a + 6144)) + (*(const f32x4*)(a + 12288) + *(const f32x4*)(a + 18432));
            }
            ka[dt] = pack8(t[0], t[1]);
            kb[dt] = (u32x2){pk2bf(t[2][0], t[2][1]), pk2bf(t[2][2], t[2][3])};
            PIN(ka[dt]); PIN(kb[dt]);
        }
        char* ob = (wsp(p) + O_OB) + layer * ACT + (size_t)w * 20480 + hb * 512;
        TT((hb >> 4) * 128 + 112 + (hb & 15), 6);
        const u32 rofs = g * 81920 + 16384 - hb * 384 + fr * 8, mofs = (g & 1) * 81920 + (g >> 1) * 256 + fr * 16;
        f32x4 o[2][4][2];
        bf8 qa[2][4];
        u32x2 qb[4];
        auto ldq = [&](const int gr) {
#pragma unroll
            for (int u = 0; u < 4; ++u) qa[gr & 1][u] = *(const bf8*)(shm + B_PQ + (w * 32 + gr * 4 + u) * 1024 + lane * 16);
        };
        auto mm = [&](const int gr, const int k) {
            SCHEDB();
            if (k < 4) { qb[k] = (u32x2){q5[gr * 4 + k], 0u}; PIN(qb[k]); }
            if (k < 8) MMA0_VV(o[gr & 1][k >> 1][k & 1], ka[k & 1], qa[gr & 1][k >> 1]);
            else MMA16(o[gr & 1][(k - 8) >> 1][k & 1], kb[k & 1], qb[(k - 8) >> 1]);
            SCHEDB();
        };
        ldq(0); ldq(1);
        NOPV();
#pragma unroll
        for (int k = 0; k < 16; ++k) mm(0, k);
        static_for<0, 8>([&](auto IT) {
            constexpr int gr = CI(IT);
            auto nx = [&](const int k) { if constexpr (gr < 7) mm(gr + 1, k); else { SCHEDB(); asm volatile("s_nop 0"); SCHEDB(); } };
            if constexpr (gr == 7) asm volatile("s_nop 7");
            u32 d[4][2];
            float x[4];
            nx(0); nx(1);
#pragma unroll
            for (int u = 0; u < 4; ++u) {
                f32x4& a = o[gr & 1][u][0];
                f32x4& b = o[gr & 1][u][1];
                PIN(a); PIN(b);
                float inv = __builtin_amdgcn_rcpf(b[1] + 1e-6f), y0, y1, y2, y3;
                nx(2 + 3 * u);
                asm("v_mul_f32 %0, %4, %8\n\tv_mul_f32 %1, %5, %8\n\tv_mul_f32 %2, %6, %8" : "=&v"(y0), "=&v"(y1), "=&v"(y2), "=&v"(y3) : "v"(a[0]), "v"(a[1]), "v"(a[2]), "v"(a[3]), "v"(inv));
                nx(3 + 3 * u);
                y3 = a[3] * inv; x[u] = b[0] * inv;
                d[u][0] = pk2bf(y0, y1);
                nx(4 + 3 * u);
                d[u][1] = pk2bf(y2, y3);
                if (u & 1) {
                    auto t0 = __builtin_amdgcn_permlane16_swap(d[u - 1][0], d[u][0], false, false);
                    auto t1 = __builtin_amdgcn_permlane16_swap(d[u - 1][1], d[u][1], false, false);
                    st16s(ob + (u - 1) * 81920, mofs, (u32x4){nosent(t0[0]), nosent(t1[0]), nosent(t0[1]), nosent(t1[1])});
                }
            }
            if constexpr (gr < 6) ldq(gr + 2);
            nx(14);
            auto a0 = __builtin_amdgcn_permlane32_swap(__float_as_uint(x[0]), __float_as_uint(x[2]), false, false);
            auto a1 = __builtin_amdgcn_permlane32_swap(__float_as_uint(x[1]), __float_as_uint(x[3]), false, false);
            nx(15);
            auto b0 = __builtin_amdgcn_permlane16_swap(a0[0], a1[0], false, false);
            auto b2 = __builtin_amdgcn_permlane16_swap(a0[1], a1[1], false, false);
            st8m(ob, rofs, (u32x2){nosent(pk2bf(__uint_as_float(b0[0]), __uint_as_float(b0[1]))), nosent(pk2bf(__uint_as_float(b2[0]), __uint_as_float(b2[1])))}, ~0ull);
            ob += 4 * 81920;
            asm volatile("" : "+s"(ob));
        });
        RAW_BARRIER();
        TT((hb >> 4) * 128 + 112 + (hb & 15), 7);
    }
}

__global__ void __launch_bounds__(256, 1) la_kernel(LaParams p) {
    extern __shared__ __attribute__((aligned(16))) char shm[];
    p.hop = align2m(p.ws + O_HOP);
    if (threadIdx.x == 0) {
        u32 xcc; asm volatile("s_getreg_b32 %0, hwreg(HW_REG_XCC_ID)" : "=s"(xcc));
        int role = 255;
        for (int a = 0; a < 8; ++a) {
            const u32 v = (xcc + a) & 7;
            const u32 k = __hip_atomic_fetch_add((gu32*)(((u32*)(p.ws + O_CTL)) + v * 32), 1u, __ATOMIC_RELAXED, __HIP_MEMORY_SCOPE_AGENT);
            if (k < 32) { role = v * 32 + k; break; }
        }
        *(int*)shm = role;
    }
    __syncthreads();
    const int role = __builtin_amdgcn_readfirstlane(*(int*)shm);
    __syncthreads();
    const int set = role >> 7, id = (role >> 5) & 3, k = role & 31;
    if (k < 8) qk_role(p, set, id * 8 + k, shm);
    else if (k < 28) comb_role(p, set, id * 20 + k - 8, shm);
    else bw_role(p, (role >> 5) * 4 + k - 28, shm);
}
__device__ __forceinline__ float row16_sum(float x) { x += fdpp<0x128>(x); x += fdpp<0x124>(x); x += fdpp<0x122>(x); return x + fdpp<0x121>(x); }

#define PFB 0
struct PrepParams { const u16* W[4]; const u16* x; const u16* mem0; const u16* wo; char* ws; };
constexpr int N_PF = PFB, N_LN = 128, N_WO = 200, N_FILL = 512;
__global__ void __launch_bounds__(256) prep_kernel(PrepParams q) {
    int b = blockIdx.x;
    const int t = threadIdx.x, lane = t & 63, w = t >> 6, fr = lane & 15, g = lane >> 4;
    char* hop = align2m(q.ws + O_HOP);
    if (b < N_LN) {
        const int tok = b * 16 + w * 4 + g;
        u32x4 d[3][2];
        float s = 0.f;
#pragma unroll
        for (int i = 0; i < 3; ++i) {
            const u32x4* a = (const u32x4*)(q.x + (size_t)tok * DM + min(fr + 16 * i, 39) * 16);
            d[i][0] = a[0]; d[i][1] = a[1];
        }
#pragma unroll
        for (int i = 0; i < 3; ++i)
#pragma unroll
            for (int e = 0; e < 8; ++e) if (fr + 16 * i < 40) s += bflo(d[i][e >> 2][e & 3]) + bfhi(d[i][e >> 2][e & 3]);
        const float mu = row16_sum(s) * (1.0f / DM);
        float v = 0.f;
#pragma unroll
        for (int i = 0; i < 3; ++i)
#pragma unroll
            for (int e = 0; e < 8; ++e) if (fr + 16 * i < 40) { const float a = bflo(d[i][e >> 2][e & 3]) - mu, c = bfhi(d[i][e >> 2][e & 3]) - mu; v = fmaf(a, a, fmaf(c, c, v)); }
        const float rs = __builtin_amdgcn_rsqf(row16_sum(v) * (1.0f / DM) + 1e-5f);
#pragma unroll
        for (int i = 0; i < 3; ++i) {
            const int blk = fr + 16 * i;
            if (blk >= 40) continue;
            const size_t o = (size_t)(tok >> 5) * 40960 + ((blk >> 1) * 2 + ((tok >> 4) & 1)) * 1024 + ((blk & 1) * 32 + (tok & 15)) * 16;
            u32 n[8];
#pragma unroll
            for (int e = 0; e < 8; ++e) n[e] = pk2bf((bflo(d[i][e >> 2][e & 3]) - mu) * rs, (bfhi(d[i][e >> 2][e & 3]) - mu) * rs);
            *(u32x4*)(q.ws + O_XR + o) = (u32x4){d[i][0][0], d[i][1][0], d[i][0][1], d[i][1][1]};
            *(u32x4*)(q.ws + O_XR + o + 256) = (u32x4){d[i][0][2], d[i][1][2], d[i][0][3], d[i][1][3]};
            *(u32x4*)(q.ws + O_XN + o) = (u32x4){n[0], n[4], n[1], n[5]};
            *(u32x4*)(q.ws + O_XN + o + 256) = (u32x4){n[2], n[6], n[3], n[7]};
        }
        return;
    }
    b -= N_LN;
    if (b < N_PF) {
        const u32x4* src = (const u32x4*)q.W[b & 3] + (size_t)(b >> 2) * 3840 + t;
        u32x4 v[15];
#pragma unroll
        for (int i = 0; i < 15; ++i) v[i] = src[i * 256];
#pragma unroll
        for (int i = 0; i < 15; ++i) asm volatile("" :: "v"(v[i]));
        return;
    }
    b -= N_PF;
    if (b < N_WO) {
        const int cb = b / 20, kk = b % 20;
        const u16* col = q.wo + cb * 64 + 16 * (fr >> 2) + 4 * w + (fr & 3);
        u32 v[8];
#pragma unroll
        for (int e = 0; e < 8; ++e) {
            const int row = kk < 16 ? (2 * kk + (g >> 1)) * 20 + 5 * (2 * (g & 1) + (e >> 2)) + (e & 3) : (8 * (kk - 16) + 2 * g + (e >> 2)) * 20 + 5 * (e & 3) + 4;
            v[e] = col[(size_t)row * DM];
        }
        *(u32x4*)(q.ws + O_WO + (size_t)cb * 81920 + (kk * 4 + w) * 1024 + lane * 16) = (u32x4){v[0] | v[1] << 16, v[2] | v[3] << 16, v[4] | v[5] << 16, v[6] | v[7] << 16};
        return;
    }
    b -= N_WO;
    const u32x4 SENT = {~0u, ~0u, ~0u, ~0u};
    const u32 i0 = b * 256 + t;
    if (b == 0) ((u32*)(q.ws + O_CTL))[t] = 0;
    if (i0 < 8 * 2 * 320) {
        const int sl = i0 / 640, e = i0 % 320;
        const u32x4 v = *(const u32x4*)(q.mem0 + (e & 7) * DM + ((sl & 1) * 40 + (e >> 3)) * 8);
        st16(hop + M0_OFF + (i0 / 320) * 8192 + e * 16, (u32x4){nosent(v[0]), nosent(v[1]), nosent(v[2]), nosent(v[3])});
    }
    for (u32 i = i0; i < HOP_BYTES / 16; i += N_FILL * 256) st16(hop + (size_t)i * 16, SENT);
    for (u32 i = i0; i < (O_XN - O_ST) / 16; i += N_FILL * 256) st16(q.ws + O_ST + (size_t)i * 16, SENT);
}

extern "C" void kernel_launch(void* const* d_in, const int* in_sizes, int n_in,
                              void* d_out, int out_size, void* d_ws, size_t ws_size,
                              hipStream_t stream) {
    PrepParams q;
    q.x = (const u16*)d_in[0]; q.mem0 = (const u16*)d_in[1];
    q.wo = (const u16*)d_in[10];
    for (int i = 0; i < 4; ++i) q.W[i] = (const u16*)d_in[2 + i];
    q.ws = (char*)d_ws;
    prep_kernel<<<N_PF + N_LN + N_WO + N_FILL, 256, 0, stream>>>(q);
    LaParams p;
    p.ws = (char*)d_ws; p.out = (u16*)d_out; p.hop = (char*)d_ws;
    p.Wq = (const u16*)d_in[2]; p.Wk = (const u16*)d_in[3]; p.Wv = (const u16*)d_in[4]; p.Wo = (const u16*)d_in[5];
    p.wqkv = (const u16*)d_in[8]; p.wphi = (const u16*)d_in[9];
    (void)hipFuncSetAttribute((const void*)la_kernel, hipFuncAttributeMaxDynamicSharedMemorySize, LA_LDS);
    la_kernel<<<256, 256, LA_LDS, stream>>>(p);
}
```

```cpp
#include <hip/hip_runtime.h>
#include <hip/hip_bf16.h>
#include <stdint.h>
#include <type_traits>

typedef unsigned short u16;
typedef unsigned int u32;
typedef unsigned long long u64;
using bf16x8 = __attribute__((ext_vector_type(8))) short;
using f32x4 = __attribute__((ext_vector_type(4))) float;

constexpr int S_TOK = 2048, DM = 640, NH = 32, DH = 192;
constexpr int MEM = 8, SEG = 32, NSEG = S_TOK / SEG;
constexpr int HD = NH * DH;

__device__ __forceinline__ float bf2f(u16 v) { return __uint_as_float(((u32)v) << 16); }
__device__ __forceinline__ u32 pk2bf(float lo, float hi) {
    typedef __attribute__((ext_vector_type(2))) float f32x2_t;
    typedef __attribute__((ext_vector_type(2))) __bf16 bf16x2_t;
    bf16x2_t r = __builtin_convertvector((f32x2_t){lo, hi}, bf16x2_t);
    return __builtin_bit_cast(u32, r);
}
__device__ __forceinline__ float rbf(float f) { return __uint_as_float(pk2bf(f, f) << 16); }
typedef __attribute__((ext_vector_type(8))) __bf16 bf8;
typedef __attribute__((ext_vector_type(16))) __bf16 bf16v;
typedef __attribute__((ext_vector_type(4))) __bf16 bf4;
typedef __attribute__((ext_vector_type(4))) short s16x4;
using u32x4 = __attribute__((ext_vector_type(4))) u32;
using u32x2 = __attribute__((ext_vector_type(2))) u32;
typedef __attribute__((address_space(1))) u32 gu32;
typedef __attribute__((address_space(1))) u64 gu64;
#define LDS3 __attribute__((address_space(3)))

constexpr int SLOT = 16384;
#define PCSV 3
constexpr int PCS = PCSV, P_SLOT0 = 0, M_SLOT0 = 16 * PCS, S_SLOT = M_SLOT0 + 32, T_SLOT = S_SLOT + 10, N_SLOT = T_SLOT + 2;
constexpr size_t HOP_BYTES = (size_t)N_SLOT * SLOT, M0_OFF = HOP_BYTES;
constexpr size_t PX_BUF = 4 * 32 * 640, PX_BYTES = 8 * PX_BUF;
constexpr int LA_LDS = 162816 + 256;
constexpr int SPIN_CAP = 1 << 16;
#define D1I 136
#define D2I 58
#define MG1 11
#define MG2 11
#define DUP 5
#define PACE 6
#define CLAG 42
#define T_PX 62
#define T_X1 70
#define T_OB 175
#define T_TOK 400
#define ST_LAG 6
#define OB_STEP 28
#define OB_COLD 20
#define NH_ 2
#define W0D 6
#define LATEP 2
#define SYNCB 1
#define PACE2 5
#define BPEEK 390
#define PLIN 1
#define SLK 6
#define RECP 122
#define FSL 8
#define GATE 0
#define PKK 18
#define B_LAG 65
#define ELATE 85

__device__ __forceinline__ void st16(void* p, u32x4 v) {
    asm volatile("global_store_dwordx4 %0, %1, off sc1\n\ts_nop 2" :: "v"(p), "v"(v) : "memory");
}
typedef __attribute__((address_space(1))) u32x4 gu32x4;
__device__ __forceinline__ u32x4 ld16(const char* base, u32 off, int imm) { return __builtin_amdgcn_global_load_b128((gu32x4*)(base + off + imm), "agent"); }
__device__ __forceinline__ u32 nosent(u32 v) { return min(v, 0xFFFFFFFEu); }
__device__ __forceinline__ u32 rt32() { return (u32)__builtin_amdgcn_s_memrealtime(); }
__device__ __forceinline__ void wait_rt(u32 t) {
    int d = (int)(t - rt32()) - 4;
    if (d > 4096 || d < 0) return;
    while (d > 7) { if (d > 18) __builtin_amdgcn_s_sleep(2); d = max((int)(t - rt32()) - 4, 0); }
    asm volatile("s_bitcmp1_b32 %0, 2\n\ts_cbranch_scc0 1f\n\ts_nop 15\n\ts_nop 7\n1:\n\ts_bitcmp1_b32 %0, 1\n\ts_cbranch_scc0 2f\n\ts_nop 12\n2:\n\t"
                 "s_bitcmp1_b32 %0, 0\n\ts_cbranch_scc0 3f\n\ts_nop 7\n3:" :: "s"(d) : "scc");
}
template <int N> __device__ __forceinline__ void dma_kb(const char* src, u32 lds_dst, u32 lane16) {
#pragma unroll
    for (int i = 0; i < N; i += 4) {
        u32 keep;
        if (N - i >= 4)
            asm volatile("s_mov_b32 %0, m0\n\ts_mov_b32 m0, %3\n\ts_nop 2\n\tglobal_load_lds_dwordx4 %1, %2 sc1\n\tglobal_load_lds_dwordx4 %1, %2 offset:1024 sc1\n\t"
                         "global_load_lds_dwordx4 %1, %2 offset:2048 sc1\n\tglobal_load_lds_dwordx4 %1, %2 offset:3072 sc1\n\ts_mov_b32 m0, %0"
                         : "=&s"(keep) : "v"(lane16), "s"(src + i * 1024), "s"(lds_dst + i * 1024) : "memory");
        else if (N - i == 2)
            asm volatile("s_mov_b32 %0, m0\n\ts_mov_b32 m0, %3\n\ts_nop 2\n\tglobal_load_lds_dwordx4 %1, %2 sc1\n\tglobal_load_lds_dwordx4 %1, %2 offset:1024 sc1\n\ts_mov_b32 m0, %0"
                         : "=&s"(keep) : "v"(lane16), "s"(src + i * 1024), "s"(lds_dst + i * 1024) : "memory");
        else
            asm volatile("s_mov_b32 %0, m0\n\ts_mov_b32 m0, %3\n\ts_nop 2\n\tglobal_load_lds_dwordx4 %1, %2 sc1\n\ts_mov_b32 m0, %0"
                         : "=&s"(keep) : "v"(lane16), "s"(src + i * 1024), "s"(lds_dst + i * 1024) : "memory");
    }
}
template <int OFF> __device__ __forceinline__ void dma_one(const char* src, u32 lds_dst, u32 lane16) {
    asm volatile("s_mov_b32 m0, %2\n\ts_nop 0\n\tglobal_load_lds_dwordx4 %0, %1 offset:%3 sc1" :: "v"(lane16), "s"(src), "s"(lds_dst), "n"(OFF) : "memory");
}
__device__ __forceinline__ u32 ringp(u32 a, u32 b) { return a + b; }
__device__ __forceinline__ void dma_gather(const char* src, u32 lds_dst, u32 off) {
    u32 keep;
    asm volatile("s_mov_b32 %0, m0\n\ts_mov_b32 m0, %3\n\ts_nop 2\n\tglobal_load_lds_dwordx4 %1, %2\n\ts_mov_b32 m0, %0"
                 : "=&s"(keep) : "v"(off), "s"(src), "s"(lds_dst) : "memory");
}
__device__ __forceinline__ void st16s(char* base, u32 off, u32x4 v) {
    asm volatile("s_nop 4\n\tglobal_store_dwordx4 %0, %1, %2 sc1\n\ts_nop 2" :: "v"(off), "v"(v), "s"(base) : "memory");
}
__device__ __forceinline__ void arm16(char* base, u32 off) {
    u32x4 v;
    asm volatile("v_mov_b64 %0, -1\n\tv_mov_b64 %1, -1" : "=v"(((u64*)&v)[0]), "=v"(((u64*)&v)[1]));
    st16s(base, off, v);
}
template <int B, int E, typename F> __device__ __forceinline__ void static_for(F&& f) {
    if constexpr (B < E) { f(std::integral_constant<int, B>()); static_for<B + 1, E>(f); }
}
#define CI(x) decltype(x)::value
#define RAW_BARRIER() do { asm volatile("s_waitcnt lgkmcnt(0)" ::: "memory"); __builtin_amdgcn_s_barrier(); asm volatile("" ::: "memory"); } while (0)
#define VM0() asm volatile("s_waitcnt vmcnt(0)" ::: "memory")
#define SCHEDB() __builtin_amdgcn_sched_barrier(0)
__device__ __forceinline__ bf8 lo8(const bf16v& v) { return __builtin_shufflevector(v, v, 0, 1, 2, 3, 4, 5, 6, 7); }
__device__ __forceinline__ bf8 hi8(const bf16v& v) { return __builtin_shufflevector(v, v, 8, 9, 10, 11, 12, 13, 14, 15); }
__device__ __forceinline__ bf16v cat8(bf8 a, bf8 b) { return __builtin_shufflevector(a, b, 0, 1, 2, 3, 4, 5, 6, 7, 8, 9, 10, 11, 12, 13, 14, 15); }
#define MMA_VV(c, a, b) asm volatile("v_mfma_f32_16x16x32_bf16 %0, %1, %2, %0" : "+v"(c) : "v"(a), "v"(b))
#define MMA_AV(c, a, b) asm volatile("v_mfma_f32_16x16x32_bf16 %0, %1, %2, %0" : "+v"(c) : "a"(a), "v"(b))
#define MMA_VA(c, a, b) asm volatile("v_mfma_f32_16x16x32_bf16 %0, %1, %2, %0" : "+v"(c) : "v"(a), "a"(b))
#define MMA0_VV(c, a, b) asm volatile("v_mfma_f32_16x16x32_bf16 %0, %1, %2, 0" : "=&v"(c) : "v"(a), "v"(b))
#define MMA0_AV(c, a, b) asm volatile("v_mfma_f32_16x16x32_bf16 %0, %1, %2, 0" : "=&v"(c) : "a"(a), "v"(b))
#define MMA0_VA(c, a, b) asm volatile("v_mfma_f32_16x16x32_bf16 %0, %1, %2, 0" : "=&v"(c) : "v"(a), "a"(b))
#define MMA16(c, a, b) asm volatile("v_mfma_f32_16x16x16_bf16 %0, %1, %2, %0" : "+v"(c) : "v"(a), "v"(b))
#define MMA16_0(c, a, b) asm volatile("v_mfma_f32_16x16x16_bf16 %0, %1, %2, 0" : "=&v"(c) : "v"(a), "v"(b))
#define SMM_V(c, a, b, i) asm volatile("v_smfmac_f32_16x16x64_bf16 %0, %1, %2, %3" : "+v"(c) : "v"(a), "v"(b), "v"(i))
#define SMM_A(c, a, b, i) asm volatile("v_smfmac_f32_16x16x64_bf16 %0, %1, %2, %3" : "+v"(c) : "v"(a), "a"(b), "v"(i))
#define NOPV() asm volatile("s_nop 1")
#define PIN(x) asm volatile("" : "+v"(x))
#define SETTLE() asm volatile("s_nop 7\n\ts_nop 3")
__device__ __forceinline__ bf8 pack8(f32x4 a, f32x4 b) {
    u32x4 r = {pk2bf(a[0], a[1]), pk2bf(a[2], a[3]), pk2bf(b[0], b[1]), pk2bf(b[2], b[3])};
    return __builtin_bit_cast(bf8, r);
}
__device__ __forceinline__ u32 max4(u32 m, u32x4 g) { m = max(max(m, g[0]), g[1]); return max(max(m, g[2]), g[3]); }
#define ZERO4(c) asm volatile("v_mov_b64 %0, 0\n\tv_mov_b64 %1, 0" : "=v"(((u64*)&(c))[0]), "=v"(((u64*)&(c))[1]))
__device__ __forceinline__ bf8 lds8(const char* shm, int off) { return *(const bf8*)(shm + off); }
__device__ __forceinline__ bf16v ldfrag(const char* p) { return cat8(*(const bf8*)p, *(const bf8*)(p + 1024)); }
template <int CTRL> __device__ __forceinline__ float fdpp(float x) {
    return __builtin_bit_cast(float, __builtin_amdgcn_mov_dpp(__builtin_bit_cast(int, x), CTRL, 0xf, 0xf, true));
}
__device__ __forceinline__ float halfsum(float a, float b) {
    auto r = __builtin_amdgcn_permlane32_swap(__float_as_uint(a), __float_as_uint(b), false, false);
    return __uint_as_float(r[0]) + __uint_as_float(r[1]);
}
__device__ __forceinline__ float grp_sum(float x) {
    auto t = __builtin_amdgcn_permlane16_swap(__float_as_uint(x), __float_as_uint(x), false, false);
    x = __uint_as_float(t[0]) + __uint_as_float(t[1]);
    return halfsum(x, x);
}
__device__ __forceinline__ float grp_max(float x) {
    auto t = __builtin_amdgcn_permlane16_swap(__float_as_uint(x), __float_as_uint(x), false, false);
    x = fmaxf(__uint_as_float(t[0]), __uint_as_float(t[1]));
    auto u = __builtin_amdgcn_permlane32_swap(__float_as_uint(x), __float_as_uint(x), false, false);
    return fmaxf(__uint_as_float(u[0]), __uint_as_float(u[1]));
}
__device__ __forceinline__ float bflo(u32 v) { return __uint_as_float(v << 16); }
__device__ __forceinline__ float bfhi(u32 v) { return __uint_as_float(v & 0xffff0000u); }
__device__ __forceinline__ float ln_scale(float var) { return __builtin_amdgcn_rcpf(rbf(__builtin_amdgcn_sqrtf(rbf(rbf(var) + 1e-5f)))); }
__device__ __forceinline__ u32 ln_pair(u32 v, float m, float rs) { const u32 d = pk2bf(bflo(v) - m, bfhi(v) - m); return pk2bf(bflo(d) * rs, bfhi(d) * rs); }
__device__ __forceinline__ void st4_lane0(char* base, u32 v) {
    u32 z, d;
    base = (char*)(((u64)(u32)__builtin_amdgcn_readfirstlane((u32)((u64)base >> 32)) << 32) | (u64)(u32)__builtin_amdgcn_readfirstlane((u32)(u64)base));
    asm volatile("s_mov_b64 exec, 1\n\tv_mov_b32 %0, 0\n\tv_mov_b32 %1, %3\n\ts_nop 1\n\tglobal_store_dword %0, %1, %2 sc1\n\ts_mov_b64 exec, -1" : "=&v"(z), "=&v"(d) : "s"(base), "s"(v) : "memory");
}
__device__ __forceinline__ void stamp2(char* hop, const int line) {
    const u32 t = nosent(rt32());
    st4_lane0(hop + (T_SLOT + 1) * SLOT + line * 128, t); st4_lane0(hop + (T_SLOT + 1) * SLOT + 8192 + line * 128, t);
}
__device__ __forceinline__ int smax(int a, int b) { int r; asm volatile("s_max_i32 %0, %1, %2" : "=s"(r) : "s"(a), "s"(b) : "scc"); return r; }
__device__ __forceinline__ int track(int d, int sample, int down) {
    int e, a, b;
    asm("s_sub_i32 %0, %3, %4\n\ts_cmp_gt_i32 %3, 0\n\ts_cselect_b32 %0, %0, 0\n\ts_min_i32 %1, %0, %6\n\ts_ashr_i32 %2, %0, 1\n\ts_max_i32 %2, %2, %5\n\t"
        "s_min_i32 %2, %2, 0\n\ts_max_i32 %1, %1, %2" : "=&s"(e), "=&s"(a), "=&s"(b) : "s"(sample), "s"(d), "s"(-down), "n"(DUP) : "scc");
    return d + a;
}
__device__ __forceinline__ u32 sched_peek(const char* p) { return __hip_atomic_load((const gu32*)p, __ATOMIC_RELAXED, __HIP_MEMORY_SCOPE_AGENT); }
__device__ __forceinline__ u32 sched_get(u32 v, const char* p, int& cap) {
    for (int spin = 0; v == ~0u; ++spin) {
        if (spin >= cap) { cap = 1; break; }
        __builtin_amdgcn_s_sleep(3);
        v = sched_peek(p);
    }
    return __builtin_amdgcn_readfirstlane(v);
}

#define TRDECL
#define TR(i)
#define TRV(i, v)
#define TRFLUSH(role)
#define TRC(i)
constexpr size_t ACT = (size_t)S_TOK * DM * 2;
constexpr size_t O_DBG = 0, O_CTL = 2u << 20, O_FL = O_CTL + 4096, O_PX = O_FL + 640 * 128, O_ST = O_PX + PX_BYTES, O_X1 = O_ST + 128 * 1280,
                 O_OB = O_X1 + 2 * ACT, O_XN = O_OB + 2 * ACT, O_XR = O_XN + 2 * ACT, O_WO = O_XR + 2 * ACT,
                 O_WVO = O_WO + 10 * 81920, O_HOP = O_WVO + (size_t)80 * 4 * 40 * 2048, WS_BYTES = O_HOP + (4u << 20);
#define TT(role, i)
#define CNT(i)
struct LaParams { char* ws; u16* out; char* hop; const u16* Wq; const u16* Wk; const u16* Wv; const u16* Wo; const u16* wqkv; const u16* wphi; };
__device__ __forceinline__ int kperm(int g, int e) { return 16 * (g >> 1) + 8 * ((e & 3) >> 1) + 4 * (g & 1) + 2 * (e >> 2) + (e & 1); }
__device__ __forceinline__ bf8 tr8(u32 a, u32 b) {
    const s16x4 lo = __builtin_amdgcn_ds_read_tr16_b64_v4i16((LDS3 s16x4*)(uintptr_t)a), hi = __builtin_amdgcn_ds_read_tr16_b64_v4i16((LDS3 s16x4*)(uintptr_t)b);
    return __builtin_bit_cast(bf8, __builtin_shufflevector(lo, hi, 0, 1, 2, 3, 4, 5, 6, 7));
}
__device__ __forceinline__ char* wsp(const LaParams& p) { char* b = p.ws; asm volatile("" : "+s"(b)); return b; }
template <int N, int SL = 4> __device__ __forceinline__ void wait_flags(const char* f, const u32 (&off)[N], int& cap) {
    for (int spin = 0;; ++spin) {
        u32 m = 0;
#pragma unroll
        for (int i = 0; i < N; ++i) m = max(m, sched_peek(f + off[i]));
        if (!__any(m == ~0u)) break;
        if (spin >= cap) { cap = 1; break; }
        __builtin_amdgcn_s_sleep(SL);
    }
    asm volatile("" ::: "memory");
}
__device__ __forceinline__ void wait_all_tokens(const LaParams& p, u32 lane, int& cap) {
    asm volatile("" : "+v"(lane));
    const u32 off[3] = {lane * 128, lane * 128 + 8192, min(lane + 128, 159u) * 128};
    wait_flags<3>(wsp(p) + O_FL + 192 * 128, off, cap);
}
__device__ __forceinline__ void wait_first_tokens(const LaParams& p, const int set, u32 lane, int& cap) {
    asm volatile("" : "+v"(lane));
    const u32 off[1] = {(set * 20 + min(lane, 39u) + (lane >= 20 ? 20 : 0)) * 128};
    wait_flags<1>(wsp(p) + O_FL, off, cap);
}
__device__ __forceinline__ float row16_sum(float x) { x += fdpp<0x128>(x); x += fdpp<0x124>(x); x += fdpp<0x122>(x); return x + fdpp<0x121>(x); }
__device__ __forceinline__ int get_side(const LaParams& p, int& cap) { return sched_get(sched_peek(wsp(p) + O_FL + 384 * 128), wsp(p) + O_FL + 384 * 128, cap) & 1; }
__device__ __forceinline__ char* align2m(char* p) { return p + ((size_t)(-(intptr_t)p) & ((2u << 20) - 1)); }

constexpr int Q_XA = 0, Q_PART = 20480, Q_TR = 48640, Q_MK = 54784, Q_WL = 55808, Q_ST = 162304, Q_PS = 44;

#define DPP1(op, ror) op "_dpp %0, %0, %0 row_ror:" ror " row_mask:0xf bank_mask:0xf\n\t"
#define DPP2(op, ror) DPP1(op, ror) op "_dpp %1, %1, %1 row_ror:" ror " row_mask:0xf bank_mask:0xf\n\ts_nop 0\n\t"
__device__ __forceinline__ void st16m(char* base, u32 off, u32x4 v, u64 mask) {
    asm volatile("s_mov_b64 exec, %3\n\ts_nop 3\n\tglobal_store_dwordx4 %0, %1, %2 sc1\n\ts_mov_b64 exec, -1\n\ts_nop 1" :: "v"(off), "v"(v), "s"(base), "s"(mask) : "memory");
}
__device__ __forceinline__ void st16f(char* base, u32 off, u32x4 v, u64 mask) {
    asm volatile("s_mov_b64 exec, %3\n\tglobal_store_dwordx4 %0, %1, %2 sc1\n\ts_mov_b64 exec, -1\n\ts_nop 1" :: "v"(off), "v"(v), "s"(base), "s"(mask) : "memory");
}
__device__ __forceinline__ void st16f64(char* base, u32 off, u32x4 v, u64 mask) {
    asm volatile("s_mov_b64 exec, %3\n\tglobal_store_dwordx4 %0, %1, %2 offset:64 sc1\n\ts_mov_b64 exec, -1\n\ts_nop 1" :: "v"(off), "v"(v), "s"(base), "s"(mask) : "memory");
}
__device__ __forceinline__ void st32(char* base, u32 off, u32x4 a, u32x4 b) {
    asm volatile("s_nop 4\n\tglobal_store_dwordx4 %0, %1, %3\n\tglobal_store_dwordx4 %0, %2, %3 offset:16\n\ts_nop 2" :: "v"(off), "v"(a), "v"(b), "s"(base) : "memory");
}
__device__ __forceinline__ void st8m(char* base, u32 off, u32x2 v, u64 mask) {
    asm volatile("s_mov_b64 exec, %3\n\ts_nop 3\n\tglobal_store_dwordx2 %0, %1, %2 sc1\n\ts_mov_b64 exec, -1" :: "v"(off), "v"(v), "s"(base), "s"(mask) : "memory");
}
__device__ __forceinline__ u32 pk2bf_abs(float lo, float hi) {
    u32 r;
    asm("v_cvt_pk_bf16_f32 %0, |%1|, |%2|" : "=v"(r) : "v"(lo), "v"(hi));
    return r;
}
__device__ __forceinline__ u32 p_line(const int h, const int piece, const u32 gap) {
    u32 a = (h >> 4) * gap + ((h >> 1) & 7) * 1024 + (h & 1) * 128, b = 2 * gap + h * 128;
    asm volatile("" : "+s"(a), "+s"(b));
    return piece < 4 ? a + piece * 256 : b;
}
constexpr float SM_C = 0.07216878364870322f * 1.4426950408889634f, NEG_BIG = -0x1p100f;
constexpr u64 SM_MASK = 0x0155015501550155ull;
template <typename MID> __device__ __forceinline__ void softmax_mem(const float* p, char* dst, char* dst2, const u32 off, MID&& mid) {
    f32x4 a[4];
#pragma unroll
    for (int i = 0; i < 4; ++i) a[i] = *(const f32x4*)(p + i * 40 * Q_PS);
    SCHEDB(); mid(); SCHEDB();
    f32x4 x = (a[0] + a[1]) + (a[2] + a[3]);
    float ml, mx = fmaxf(fmaxf(x[0], x[1]), fmaxf(x[2], x[3])), e0, e1, e2, e3;
    asm volatile("v_mul_f32 %5, %10, %0\n\ts_nop 0\n\t"
        DPP1("v_max_f32", "8") "v_fma_f32 %1, %6, %10, -%5\n\tv_fma_f32 %2, %7, %10, -%5\n\t"
        DPP1("v_max_f32", "4") "v_fma_f32 %3, %8, %10, -%5\n\tv_fma_f32 %4, %9, %10, -%5\n\t"
        DPP1("v_max_f32", "2") "v_exp_f32 %1, %1\n\tv_exp_f32 %2, %2\n\t"
        DPP1("v_max_f32", "1") "v_exp_f32 %3, %3\n\tv_fma_f32 %5, -%0, %10, %5\n\tv_exp_f32 %4, %4\n\tv_add_f32 %0, %1, %2\n\tv_exp_f32 %5, %5\n\tv_add_f32 %0, %0, %3\n\t"
        "v_add_f32 %0, %0, %4\n\tv_mul_f32 %0, %0, %5\n\tv_mul_f32 %1, %1, %5\n\tv_mul_f32 %2, %2, %5\n\t"
        DPP1("v_add_f32", "8") "v_mul_f32 %3, %3, %5\n\tv_mul_f32 %4, %4, %5\n\t" DPP1("v_add_f32", "4") "s_nop 1\n\t"
        DPP1("v_add_f32", "2") "s_nop 1\n\t" DPP1("v_add_f32", "1") "v_rcp_f32 %0, %0\n\ts_nop 0\n\t"
        "v_mul_f32 %1, %1, %0\n\tv_mul_f32 %2, %2, %0\n\tv_mul_f32 %3, %3, %0\n\tv_mul_f32 %4, %4, %0"
        : "+v"(mx), "=&v"(e0), "=&v"(e1), "=&v"(e2), "=&v"(e3), "=&v"(ml) : "v"(x[0]), "v"(x[1]), "v"(x[2]), "v"(x[3]), "s"(SM_C) : "memory");
    u32x4 o;
    asm volatile("v_cvt_pk_bf16_f32 %4, |%4|, |%6|\n\tv_cvt_pk_bf16_f32 %5, |%5|, |%7|\n\ts_nop 0\n\t"
                 "v_mov_b32_dpp %0, %4 quad_perm:[0,0,2,2] row_mask:0xf bank_mask:0xf\n\tv_mov_b32_dpp %1, %5 quad_perm:[0,0,2,2] row_mask:0xf bank_mask:0xf\n\t"
                 "v_mov_b32_dpp %2, %4 quad_perm:[1,1,3,3] row_mask:0xf bank_mask:0xf\n\tv_mov_b32_dpp %3, %5 quad_perm:[1,1,3,3] row_mask:0xf bank_mask:0xf"
                 : "=&v"(o[0]), "=&v"(o[1]), "=&v"(o[2]), "=&v"(o[3]), "+v"(e0), "+v"(e2) : "v"(e1), "v"(e3), "s"(dst), "s"(dst2));
    asm volatile("s_mov_b64 exec, %4\n\tglobal_store_dwordx4 %0, %1, %2 sc1\n\tglobal_store_dwordx4 %0, %1, %3 sc1\n\ts_mov_b64 exec, -1\n\ts_nop 1"
                 :: "v"(off), "v"(o), "s"(dst), "s"(dst2), "s"(0x03ff03ff03ff03ffull) : "memory");
}
struct SmTok {
    float x[2][4], b1[2][4], b2[2][4], b3[2][4], mx[2], sm[2], t0, t1;
    u32 d00, d01, d10, d11;
    static constexpr int N = 88;
    __device__ __forceinline__ void load(const float* p, const int j) {
        const int n = j & 1, k = j >> 1;
        const f32x4 a = *(const f32x4*)(p + (k * 40 + n * 4) * Q_PS);
#pragma unroll
        for (int i = 0; i < 4; ++i) (k == 0 ? x : (k == 1 ? b1 : (k == 2 ? b2 : b3)))[n][i] = a[i];
    }
    template <int I> __device__ __forceinline__ void piece(char* dst, const u32 off) {
        constexpr int n = I & 1, k = (I >> 1) - 12;
        float& t = n ? t1 : t0;
#define ROR(op, ror, v) asm volatile(op "_dpp %0, %0, %0 row_ror:" ror " row_mask:0xf bank_mask:0xf" : "+v"(v))
        if constexpr (k < -8) asm volatile("v_add_f32 %0, %0, %1" : "+v"(x[n][k + 12]) : "v"(b1[n][k + 12]));
        else if constexpr (k < -4) asm volatile("v_add_f32 %0, %0, %1" : "+v"(b2[n][k + 8]) : "v"(b3[n][k + 8]));
        else if constexpr (k < 0) asm volatile("v_add_f32 %0, %0, %1" : "+v"(x[n][k + 4]) : "v"(b2[n][k + 4]));
        else if constexpr (k == 0) asm volatile("v_max_f32 %0, %1, %2" : "=v"(mx[n]) : "v"(x[n][0]), "v"(x[n][1]));
        else if constexpr (k == 1) asm volatile("v_max3_f32 %0, %0, %1, %2" : "+v"(mx[n]) : "v"(x[n][2]), "v"(x[n][3]));
        else if constexpr (k == 2) ROR("v_max_f32", "8", mx[n]);
        else if constexpr (k == 3) ROR("v_max_f32", "4", mx[n]);
        else if constexpr (k == 4) ROR("v_max_f32", "2", mx[n]);
        else if constexpr (k == 5) ROR("v_max_f32", "1", mx[n]);
        else if constexpr (k == 6) { mx[n] *= SM_C; PIN(mx[n]); }
        else if constexpr (k < 11) { x[n][k - 7] = fmaf(x[n][k - 7], SM_C, -mx[n]); PIN(x[n][k - 7]); }
        else if constexpr (k < 15) asm volatile("v_exp_f32 %0, %0" : "+v"(x[n][k - 11]));
        else if constexpr (k == 15) asm volatile("v_add_f32 %0, %1, %2" : "=v"(sm[n]) : "v"(x[n][0]), "v"(x[n][1]));
        else if constexpr (k == 16) asm volatile("v_add_f32 %0, %1, %2" : "=v"(t) : "v"(x[n][2]), "v"(x[n][3]));
        else if constexpr (k == 17) asm volatile("v_add_f32 %0, %0, %1" : "+v"(sm[n]) : "v"(t));
        else if constexpr (k == 18) ROR("v_add_f32", "8", sm[n]);
        else if constexpr (k == 19) ROR("v_add_f32", "4", sm[n]);
        else if constexpr (k == 20) ROR("v_add_f32", "2", sm[n]);
        else if constexpr (k == 21) ROR("v_add_f32", "1", sm[n]);
        else if constexpr (k == 22) asm volatile("v_rcp_f32 %0, %0" : "+v"(sm[n]));
        else if constexpr (k < 27) asm volatile("v_mul_f32 %0, %0, %1" : "+v"(x[n][k - 23]) : "v"(sm[n]));
        else if constexpr (k == 27) asm volatile("v_cvt_pk_bf16_f32 %0, |%1|, |%2|" : "=v"(n ? d10 : d00) : "v"(x[n][0]), "v"(x[n][1]));
        else if constexpr (k == 28) asm volatile("v_cvt_pk_bf16_f32 %0, |%1|, |%2|" : "=v"(n ? d11 : d01) : "v"(x[n][2]), "v"(x[n][3]));
        else if constexpr (I == 84 || I == 87) {
            u32x4 o;
            asm volatile("v_mov_b32 %0, %4\n\tv_mov_b32 %1, %5\n\tv_mov_b32_dpp %2, %4 quad_perm:[1,0,3,2] row_mask:0xf bank_mask:0xf\n\tv_mov_b32_dpp %3, %5 quad_perm:[1,0,3,2] row_mask:0xf bank_mask:0xf"
                         : "=&v"(o[0]), "=&v"(o[1]), "=&v"(o[2]), "=&v"(o[3]) : "v"(n ? d10 : d00), "v"(n ? d11 : d01), "s"(dst));
            if constexpr (n == 0) st16f(dst, off, o, SM_MASK); else st16f64(dst, off, o, SM_MASK);
        }
    }
};

__host__ __device__ constexpr int qk_slot(int T, int ks) { return ks < 5 ? ks * 6 + T : (T < 4 ? 30 + (T >> 1) * 10 + (ks - 5) * 2 + (T & 1) : 25 + T * 5 + ks); }
constexpr int NLF = 13;
__host__ __device__ constexpr int lds_slot(int n) { constexpr int t[NLF] = {2, 6, 11, 15, 20, 24, 29, 33, 37, 41, 45, 49, 55}; return t[n]; }
__host__ __device__ constexpr int slot_lds(int sl) { for (int n = 0; n < NLF; ++n) if (lds_slot(n) == sl) return n; return -1; }
__host__ __device__ constexpr int slot_frag(int sl) { if (slot_lds(sl) >= 0) return 48 + slot_lds(sl); int n = 0; for (int i = 0; i < NLF; ++i) n += lds_slot(i) < sl; return sl - n; }
__host__ __device__ constexpr int qk_frag(int T, int ks) { return slot_frag(qk_slot(T, ks)); }
__host__ __device__ constexpr int slot_T(int sl) { return sl < 30 ? sl % 6 : (sl < 50 ? ((sl - 30) / 10) * 2 + (sl & 1) : (sl - 30) / 5); }
__host__ __device__ constexpr int slot_ks(int sl) { return sl < 30 ? sl / 6 : (sl < 50 ? 5 + ((sl - 30) % 10) / 2 : 5 + sl % 5); }
__host__ __device__ constexpr int lds_rank(int T, int ks) { int n = 0; for (int t = 0; t < T; ++t) n += qk_frag(t, ks) >= 48; return n; }
__host__ __device__ constexpr int lds_nth(int ks, int n) { for (int t = 0; t < 6; ++t) if (qk_frag(t, ks) >= 48 && lds_rank(t, ks) == n) return qk_frag(t, ks) - 48; return -1; }

__device__ __forceinline__ void qk_role(const LaParams& p, const int set, const int h, char* shm) {
    const int lane = threadIdx.x & 63, w = __builtin_amdgcn_readfirstlane(threadIdx.x >> 6);
    const int fr = lane & 15, g = lane >> 4;
    const u32 lds0 = (u32)(uintptr_t)(LDS3 char*)shm;
    const int sidx = (lane & 8) ? 0xEEEE : 0x4444;
    int cap = SPIN_CAP;
    char* mine = p.hop;
    int sch = (S_SLOT + w) * SLOT;
    int phys = 0;
    char* px = (wsp(p) + O_PX);
    const char* xn = (wsp(p) + O_XN);
    char* wl = shm + Q_WL + w * (NLF * 2048) + lane * 16;
    const char* xa = shm + Q_XA + lane * 16;

    bf16v WA[32], WV[28 - NLF];
    {
        bf16v WT[NLF];
        u32 vo[6], rd[3];
#pragma unroll
        for (int i = 0; i < 6; ++i) {
            const int id = i * 64 + lane, r = w * 16 + id / 24;
            vo[i] = (32 * (r >> 5) + kperm((r >> 2) & 3, ((r >> 4) & 1) * 4 + (r & 3))) * (HD * 2) + ((id % 24) ^ (r & 6)) * 16;
        }
#pragma unroll
        for (int i = 0; i < 3; ++i) rd[i] = lds0 + (g * 4 + (fr >> 2)) * 384 + ((w * 3 + i) ^ ((g & 1) * 2 + (fr >> 3))) * 32 + (fr & 3) * 8;
        auto issue = [&](const int j) {
            const char* src = (const char*)((j & 1) ? p.Wk : p.Wq) + (size_t)(j >> 1) * 64 * HD * 2 + h * 384;
#pragma unroll
            for (int i = 0; i < 6; ++i) dma_gather(src, lds0 + (j % 6) * 24576 + w * 6144 + i * 1024, vo[i]);
        };
#pragma unroll
        for (int j = 0; j < 5; ++j) issue(j);
        static_for<0, 20>([&](auto J) {
            constexpr int j = CI(J);
            asm volatile("s_waitcnt vmcnt(%0)" :: "n"((19 - j < 4 ? 19 - j : 4) * 6) : "memory");
            RAW_BARRIER();
            if constexpr (j + 5 < 20) issue(j + 5);
            static_for<0, 3>([&](auto PR) {
                constexpr int id = qk_frag(2 * CI(PR) + (j & 1), j >> 1);
                u32 a = rd[CI(PR)] + (j % 6) * 24576;
                asm volatile("" : "+v"(a));
                const bf16v f = cat8(tr8(a, a + 16 * 384), tr8(a + 32 * 384, a + 48 * 384));
                if constexpr (id < 32) WA[id] = f; else if constexpr (id < 48) WV[id - 32] = f; else WT[id - 48] = f;
            });
        });
        RAW_BARRIER();
        dma_kb<5>(xn + (size_t)set * 40960 + w * 5120, lds0 + Q_XA + w * 5120, lane * 16);
#pragma unroll
        for (int i = 0; i < NLF; ++i) { *(bf8*)(wl + i * 2048) = lo8(WT[i]); *(bf8*)(wl + i * 2048 + 1024) = hi8(WT[i]); }
    }
    TRDECL;
    bf8 QX[2], KX[2];
    u32x2 QY[2], KY[2];
    u32 peek = ~0u;
    int d1 = D1I, d2 = D2I, k1 = D1I + MG1;
    if (w == 3) *(u64*)(shm + Q_ST + lane * 8) = 0;
    SmTok smt;
    u64 tpub = 0, tnow = 0;
    u32 tref = 0, tpost = 0;
    u32 xoff = p_line(h, fr >> 1, 8192) + g * 16, ao1 = p_line(h, min(lane >> 3, 4), 8192) + (lane & 7) * 16, ao0;
    {
        u32 a = (h >> 4) * SLOT + (h & 15) * 512, b = 2 * SLOT + h * 256;
        asm volatile("" : "+s"(a), "+s"(b));
        ao0 = PLIN ? p_line(h, min(lane >> 3, 4), SLOT) + (lane & 7) * 16 : (lane < 32 ? a + lane * 16 : b + (lane & 15) * 16);
    }
    asm volatile("" : "+v"(xoff), "+v"(ao0), "+v"(ao1));
    auto rearm = [&](const int rot, const int i, char*& a, u32x4& v) {
        if (i == 0) asm volatile("v_mov_b64 %0, -1\n\tv_mov_b64 %1, -1" : "=v"(((u64*)&v)[0]), "=v"(((u64*)&v)[1]));
        if (i == 1) { a = mine + (P_SLOT0 + (((rot + 3) & 3) * 4 + w) * PCS) * SLOT; asm volatile("" : "+s"(a)); }
        if (i == 5) { a = px + (size_t)(set * 4 + ((rot + 2) & 3)) * PX_BUF + w * 20480; asm volatile("" : "+s"(a)); }
        if (i == 3 || i == 8) __builtin_amdgcn_global_store_b128((gu32x4*)(a + (i == 3 ? ao0 : ao1)), v, "agent");
    };
    auto fetch_stats = [&]() {
        if (__builtin_expect(h == 0 && w == 3, 0)) {
                u32 lo = lane;
                asm volatile("" : "+v"(lo));
                lo *= 128;
                u32 keep;
                asm volatile("s_mov_b32 %0, m0\n\ts_mov_b32 m0, %4\n\ts_nop 2\n\tglobal_load_lds_dword %1, %2 sc1\n\ts_add_u32 m0, m0, 256\n\ts_lshr_b64 exec, exec, 16\n\tglobal_load_lds_dword %1, %3 sc1\n\ts_mov_b64 exec, -1\n\ts_mov_b32 m0, %0"
                             : "=&s"(keep) : "v"(lo), "s"(mine + T_SLOT * SLOT), "s"(mine + (T_SLOT + 1) * SLOT), "s"(lds0 + Q_ST) : "memory", "scc");
        }
    };
    int down = 16;
    auto xproj = [&](const int snext, const int sprev, auto SOFT, char* xdst) {
        f32x4 acc[6][2];
        TR(8);
        const char* xs;
        u32 xl;
        bf8 x0 = *(const bf8*)(xa), x1 = *(const bf8*)(xa + 1024), lw[2];
        lw[0] = *(const bf8*)(wl);
        if constexpr (CI(SOFT)) RAW_BARRIER();
        auto pack = [&](const int T, const int tt) {
            PIN(acc[T][tt]);
            const u32 a = pk2bf(acc[T][tt][0], acc[T][tt][1]), b = pk2bf(acc[T][tt][2], acc[T][tt][3]);
            if (T < 4) { u32* d = (u32*)((T & 1) ? &KX[tt] : &QX[tt]) + (T & 2); d[0] = a; d[1] = b; }
            else if (T == 4) QY[tt] = (u32x2){a, b}; else KY[tt] = (u32x2){a, b};
            SCHEDB();
        };
        char* ao;
        u32x4 sent;
        static_for<0, 20>([&](auto K) {
            constexpr int k = CI(K), l0 = lds_nth(((k + 1) >> 1) % 10, 0), l1 = lds_nth(((k + 1) >> 1) % 10, 1);
            bf8 n0, n1, nw[2];
            if constexpr (k == 9) {
                TR(9);
                if constexpr (CI(SOFT)) asm volatile("s_waitcnt vmcnt(4)" ::: "memory"); else VM0();
                RAW_BARRIER();
                TR(10);
            }
            static_for<0, 6>([&](auto TT) {
                constexpr int T = CI(TT), id = qk_frag(T, k >> 1), rk = lds_rank(T, k >> 1);
                if constexpr (k < 19) {
                    if constexpr (T == 0) n0 = *(const bf8*)(xa + (k + 1) * 2048);
                    if constexpr (T == 1) n1 = *(const bf8*)(xa + (k + 1) * 2048 + 1024);
                    if constexpr (T == 2 && l0 >= 0) nw[0] = *(const bf8*)(wl + l0 * 2048 + ((k + 1) & 1) * 1024);
                    if constexpr (T == 3 && l1 >= 0) nw[1] = *(const bf8*)(wl + l1 * 2048 + ((k + 1) & 1) * 1024);
                }
                if constexpr (k == 0 && T == 1) { xs = xn + (size_t)(snext - 2) * 40960 + 20480 + w * 5120; xl = lds0 + Q_XA + 20480 + w * 5120; asm volatile("" : "+s"(xs), "+s"(xl)); }
                if constexpr (k == 9 && T == 1) { xs = xn + (size_t)min(snext, NSEG - 1) * 40960 + w * 5120; xl -= 20480 + 4096; asm volatile("" : "+s"(xs), "+s"(xl)); }
                if constexpr ((k == 2 || k == 13) && T == 1) { xs += 4096; xl += 4096; asm volatile("" : "+s"(xs), "+s"(xl)); }
                if constexpr (k < 3 && (T == 3 || T == 5) && 2 * k + (T == 5) < 5) dma_one<((2 * k + (T == 5)) & 3) * 1024>(xs, xl, lane * 16);
                if constexpr (k >= 9 && k < 14 && T == 4) dma_one<((k - 9) & 3) * 1024>(xs, xl, lane * 16);
                if constexpr (CI(SOFT) && k == 10 && T == 1) {
                    asm volatile("" : "+s"(tpub));
                    if (w == 1) st4_lane0(mine + (T_SLOT + 1) * SLOT + (16 + h) * 128, tref ? (u32)tpub - tref : 0);
                }
                if constexpr (CI(SOFT) && k == 16 && T == 4) fetch_stats();
                if constexpr (CI(SOFT) && !GATE && k == PKK && T == 4) peek = sched_peek(mine + sch + sprev * 128 + 4);
                if constexpr (CI(SOFT) && GATE && k == 12 && T == 1) peek = sched_peek(mine + sch + (sprev - 1) * 128 + 4);
                if constexpr (CI(SOFT) && GATE && k == PKK && T == 0) asm volatile("s_memrealtime %0" : "=s"(tnow));
                if constexpr (CI(SOFT) && GATE && k == PKK && T == 5) {
                    tpost = __builtin_amdgcn_readfirstlane(peek) + RECP;
                    asm volatile("s_waitcnt lgkmcnt(0)" : "+s"(tnow), "+s"(tpost) :: "memory");
                    peek = ~0u;
                    if ((int)((u32)tnow - tpost) >= 0) peek = sched_peek(mine + sch + sprev * 128 + 4);
                }
                SCHEDB();
                static_for<0, 2>([&](auto U) {
                    constexpr int u = CI(U);
                    const bf8 xx = u ? x1 : x0;
                    if constexpr (id < 32) {
                        const bf8 wf = (k & 1) ? hi8(WA[id]) : lo8(WA[id]);
                        if constexpr (k == 0) MMA0_AV(acc[T][u], wf, xx); else MMA_AV(acc[T][u], wf, xx);
                    } else {
                        bf8 wf;
                        if constexpr (id < 48) wf = (k & 1) ? hi8(WV[id - 32]) : lo8(WV[id - 32]); else wf = lw[rk];
                        if constexpr (k == 0) MMA0_VV(acc[T][u], wf, xx); else MMA_VV(acc[T][u], wf, xx);
                    }
                    SCHEDB();
                    if constexpr (CI(SOFT) && k * 12 + T * 2 + u < SmTok::N) smt.template piece<k * 12 + T * 2 + u>(xdst, xoff);
                    if constexpr (CI(SOFT) && k == 8) rearm((snext >> 1) + 2, T * 2 + u, ao, sent);
                    if constexpr (k == 19 && T >= 2) pack(T - 2, u);
                    SCHEDB();
                });
            });
            if constexpr (k < 19) { x0 = n0; x1 = n1; if constexpr (l0 >= 0) lw[0] = nw[0]; if constexpr (l1 >= 0) lw[1] = nw[1]; }
        });
        TR(11);
        pack(4, 0); pack(4, 1);
        asm volatile("s_nop 3");
        pack(5, 0); pack(5, 1);
    };
    {
        const f32x4 neg = {NEG_BIG, NEG_BIG, NEG_BIG, NEG_BIG};
        *(f32x4*)((float*)(shm + Q_PART) + (w * 40 + 8 + fr) * Q_PS + g * 8 + 4) = neg;
        if (lane < 40) *(f32x4*)((float*)(shm + Q_PART) + (w * 40 + lane) * Q_PS + 40) = neg;
        *(f32x4*)(shm + Q_MK + lane * 16) = (f32x4){4 * g > fr ? NEG_BIG : 0.f, 4 * g + 1 > fr ? NEG_BIG : 0.f, 4 * g + 2 > fr ? NEG_BIG : 0.f, 4 * g + 3 > fr ? NEG_BIG : 0.f};
    }
    float* pw0 = (float*)(shm + Q_PART) + (w * 40 + (fr & 7)) * Q_PS + g * 8 + (fr >> 3) * 4;
    float* pw2 = fr < 8 && g >= 2 ? (float*)(shm + Q_PART) + (w * 40 + fr) * Q_PS + 24 + g * 4 : (float*)(shm + Q_TR + w * 1536) + lane * 4;
    const float* prd = (const float*)(shm + Q_PART) + ((w & 1) * 4 + g) * Q_PS + min(fr, 10) * 4;
    const float* prt = (const float*)(shm + Q_PART) + (8 + 8 * w + g) * Q_PS + min(fr, 10) * 4;
    u32 trw = lds0 + Q_TR + w * 1536 + lane * 8, trr = lds0 + Q_TR + w * 1536 + (fr & 3) * 128 + (4 * g + (fr >> 2)) * 8;
    u32 trh = lds0 + Q_TR + w * 1536 + 1024 + (lane & 31) * 8 + (lane >> 5) * 4;
    u32 pw3 = lds0 + Q_PART + ((w * 40 + 8 + (lane >> 5) * 16 + fr) * Q_PS + 32 + (g & 1) * 4) * 4;
    u32 pp = lds0 + Q_PART + ((w * 40 + 8 + fr) * Q_PS + g * 8) * 4;
    asm volatile("" : "+v"(trw), "+v"(trr), "+v"(trh), "+v"(pw3), "+v"(pp));
    const int lead = __builtin_amdgcn_readfirstlane((u32)(h * 4 + (w ^ 3) - 1) >> 31);
    u32 pa = (h >> 4) * SLOT + (h & 15) * 512 + (w & 1) * 256, pbm = 2 * SLOT + h * 256 + (w & 1) * 128;
    asm volatile("" : "+s"(pa), "+s"(pbm));
    const u32 poff = (PLIN ? p_line(h, fr >> 1, SLOT) + (w & 1) * 64 : (fr < 8 ? pa + (fr >> 1) * 64 : pbm)) + g * 16 + (fr & 1) * (PCS * SLOT);

    for (int layer = 0; layer < 2; ++layer) {
    if (layer > 0) {
        if (w == 0) {
            __builtin_amdgcn_s_sleep(127);
            wait_rt(sched_get(~0u, mine + (T_SLOT + 1) * SLOT + 50 * 128, cap) + T_TOK);
            wait_first_tokens(p, set, lane, cap);
        }
        RAW_BARRIER();
        sch += 5 * SLOT; xn += ACT;
        TT(set * 128 + h, 1);
        dma_kb<5>(xn + (size_t)set * 40960 + w * 5120, lds0 + Q_XA + w * 5120, lane * 16);
    }
    VM0(); RAW_BARRIER();
    TT(set * 128 + h, 2);
    xproj(set + 2, set - 1, std::integral_constant<int, 0>(), nullptr);
    peek = ~0u;
    if (layer == 0) { phys = set ^ get_side(p, cap); mine += phys * 8192; }
    TT(set * 128 + h, 3);
    for (int s = set; s < NSEG; s += 2) {
        const int rot = (s >> 1) & 3;
        TR(0);
        u32 ytime = 0;
        const char* mb0 = (s > 0 ? mine + (M_SLOT0 + rot * 8) * SLOT : p.hop + M0_OFF + phys * 8192) + 2 * w * SLOT + 2048;
        const char* mb1 = mb0 + SLOT;
        char* pdst = mine + (P_SLOT0 + rot * 4 * PCS) * SLOT;
        char* pdst2 = pdst + 2 * PCS * SLOT;
        asm volatile("" : "+s"(mb0), "+s"(mb1), "+s"(pdst), "+s"(pdst2));
        if (s > 0) {
            if (GATE && __builtin_amdgcn_readfirstlane(peek) == ~0u) { wait_rt(tpost); peek = sched_peek(mine + sch + (s - 1) * 128 + 4); }
            ytime = sched_get(peek, mine + sch + (s - 1) * 128 + 4, cap);
            }
        f32x4 t[3];
        t[0] = *(const f32x4*)(xa + Q_MK);

        PIN(t[0]); PIN(QX[0]); PIN(QX[1]); PIN(KX[0]); PIN(KX[1]); PIN(QY[0]); PIN(QY[1]); PIN(KY[0]); PIN(KY[1]);
        u32 tight = 0;
        if (s > 0 && w == 0) wait_rt(ytime);
        if (w == 1) {
            const int sl = (int)(ytime - rt32());
            tight = min(max(SLK - sl, 0), SLK);
        }
        __builtin_amdgcn_s_barrier();
        TR(1); TRV(7, ytime);
        f32x4 acc[6];
        u32x4 A[10];
        bf8 Ll[2], Lh[2];
        s16x4 F[4];
        u32 r0[4], r1[4], d[2];
        float v[4];
        int tries = 0;
        auto fin = [&](const int pr, const int i) {
            if (i == 0) { PIN(acc[2 * pr]); PIN(acc[2 * pr + 1]); }
            if (i < 4) { auto r = __builtin_amdgcn_permlane32_swap(__float_as_uint(acc[2 * pr][i]), __float_as_uint(acc[2 * pr + 1][i]), false, false); r0[i] = r[0]; r1[i] = r[1]; }
            const int a0 = pr == 0 ? i - 4 : i - 1;
            if (a0 >= 0 && a0 < 4) asm volatile("v_add_f32 %0, %1, %2" : "=v"(v[a0]) : "v"(r0[a0]), "v"(r1[a0]));
            if (a0 == 3) d[0] = pk2bf(v[0], v[1]);
            if (a0 == 4) {
                d[1] = pk2bf(v[2], v[3]);
                *(LDS3 u32x2*)(uintptr_t)(trw + pr * 512) = (u32x2){d[0], d[1]};
                F[pr] = __builtin_amdgcn_ds_read_tr16_b64_v4i16((LDS3 s16x4*)(uintptr_t)(trr + pr * 512));
            }
        };
        auto fin1 = [&](const int T, const int i) {
            if (i == 0) asm volatile("s_nop 1\n\tv_permlane32_swap_b32 %0, %2\n\tv_permlane32_swap_b32 %1, %3" : "+v"(acc[T][0]), "+v"(acc[T][1]), "+v"(acc[T][2]), "+v"(acc[T][3]));
            else {
                PIN(acc[T]);
                asm volatile("v_add_f32 %0, %2, %4\n\tv_add_f32 %1, %3, %5\n\tv_cvt_pk_bf16_f32 %0, %0, %1" : "=&v"(v[0]), "=&v"(v[1]) : "v"(acc[T][0]), "v"(acc[T][1]), "v"(acc[T][2]), "v"(acc[T][3]));
                *(LDS3 float*)(uintptr_t)(trh + (T - 4) * 256) = v[0];
                F[T - 2] = __builtin_amdgcn_ds_read_tr16_b64_v4i16((LDS3 s16x4*)(uintptr_t)(trr + 1024));
            }
        };
        auto slot = [&](auto SL) {
            constexpr int sl = CI(SL), T = slot_T(sl), ks = slot_ks(sl), id = slot_frag(sl);
            if constexpr (id < 32) SMM_A(acc[T], A[ks], WA[id], sidx);
            else if constexpr (id < 48) SMM_V(acc[T], A[ks], WV[id - 32], sidx);
            else { const bf16v L = cat8(Ll[id & 1], Lh[id & 1]); SMM_V(acc[T], A[ks], L, sidx); }
            SCHEDB();
            if constexpr (sl == 0) TRC(6);
            constexpr int n0 = slot_lds(sl), n1 = slot_lds(sl - 1);
            if constexpr (n0 >= 0 && n0 < NLF - 2) Ll[n0 & 1] = *(const bf8*)(wl + (n0 + 2) * 2048);
            if constexpr (n1 >= 0 && n1 < NLF - 2) Lh[n1 & 1] = *(const bf8*)(wl + (n1 + 2) * 2048 + 1024);
            if constexpr (sl >= 43 && sl < 52) fin(0, sl - 43);
            if constexpr (sl >= 52 && sl < 58) fin(1, sl - 52);
            if constexpr (sl >= 58) fin1(4, sl - 58);
            SCHEDB();
        };
        auto gather = [&](const bool first) {
            u32 vo = lane * 16;
            asm volatile("" : "+v"(vo) :: "memory");
#pragma unroll
            for (int ks = 0; ks < 10; ++ks) {
                A[ks] = ld16(ks < 5 ? mb0 : mb1, vo, (ks % 5 - 2) * 1024);
                SCHEDB();
                if (first) {
                    if (ks == 0) {
                        MMA0_VV(t[1], KX[0], QX[1]);
                        asm volatile("v_mfma_f32_16x16x32_bf16 %0, %1, %2, %3" : "=&v"(t[2]) : "v"(KX[1]), "v"(QX[1]), "v"(t[0]));
                        MMA_VV(t[0], KX[0], QX[0]);
                    }
                    if (ks == 2) { MMA16(t[1], KY[0], QY[1]); MMA16(t[2], KY[1], QY[1]); MMA16(t[0], KY[0], QY[0]); }
                    if (ks == 4) { PIN(t[1]); *(LDS3 f32x4*)(uintptr_t)(pp + 16 * Q_PS * 4) = t[1]; }
                    if (ks == 5) { PIN(t[2]); *(LDS3 f32x4*)(uintptr_t)(pp + 16 * Q_PS * 4 + 16) = t[2]; }
                    if (ks == 6) { PIN(t[0]); *(LDS3 f32x4*)(uintptr_t)pp = t[0]; }
                    if (ks == 1 || ks == 3 || ks == 7 || ks == 8) asm volatile("s_nop %0" :: "n"(PACE2));
                } else if (ks < 9) asm volatile("s_nop %0" :: "n"(PACE));
                if (ks == 3 || ks == 7) { Ll[ks >> 2] = *(const bf8*)(wl + (ks >> 2) * 2048); Lh[ks >> 2] = *(const bf8*)(wl + (ks >> 2) * 2048 + 1024); }
                if (ks == 1) { ZERO4(acc[0]); ZERO4(acc[1]); }
                if (ks >= 5 && ks < 9) ZERO4(acc[ks - 3]);
                SCHEDB();
            }
        };
        gather(true);
        static_for<0, 42>(slot);
        auto record = [&](const u32 xt, const u32 yt) {
            u32 ln8 = lane;
            asm volatile("" : "+v"(ln8));
            ln8 *= 8192;
            const u32x4 rec = {nosent(xt), nosent(yt), (u32)(d1 + MG1), 0u};
            k1 = d1 + MG1;
            st16m(p.hop + (S_SLOT + layer * 5) * SLOT + s * 128, ln8, rec, 0x3ffull);
        };
        u64 bad, bad2;
        asm volatile("v_cmp_u_f32 %0, %2, %3\n\tv_cmp_u_f32 %1, %4, %5" : "=s"(bad), "=s"(bad2) : "v"(acc[0][0]), "v"(acc[0][1]), "v"(acc[0][2]), "v"(acc[0][3]));
        static_for<42, 43>(slot);
        asm volatile("" : "+s"(bad), "+s"(bad2));
        if (__builtin_expect((bad | bad2) != 0, 0)) {
            {
                SETTLE(); CNT(0);
                while (true) {
                    gather(false);
                    u32 m = 0;
#pragma unroll
                    for (int ks = 0; ks < 10; ++ks) m = max4(m, A[ks]);
                    ++tries;
                    if (!__any(m == ~0u)) break;
                    if (tries >= cap) { cap = 1; break; }
                    __builtin_amdgcn_s_sleep(4);
                }
                static_for<0, 43>(slot);
            }
        }
        TR(2); TRV(6, tries + 1);
        static_for<43, 60>(slot);
        TRC(7);
        bf8 F01 = __builtin_bit_cast(bf8, __builtin_shufflevector(F[0], F[1], 0, 1, 2, 3, 4, 5, 6, 7));
        f32x4 s0, s1, s2, u0, u1;
        PIN(F01);
        MMA0_VV(s1, KX[1], F01); MMA0_VV(s0, KX[0], F01);
        SCHEDB();
        fin1(5, 0); fin1(5, 1);
        SCHEDB();
        MMA16(s1, KY[1], F[2]); MMA16(s0, KY[0], F[2]);
        MMA0_VV(s2, F01, F01); MMA0_VV(u0, F01, QX[0]); MMA0_VV(u1, F01, QX[1]);
        asm volatile("v_mov_b32_dpp %0, %4 row_ror:8 row_mask:0xf bank_mask:0xc\n\tv_mov_b32_dpp %1, %5 row_ror:8 row_mask:0xf bank_mask:0xc\n\t"
                     "v_mov_b32_dpp %2, %6 row_ror:8 row_mask:0xf bank_mask:0xc\n\tv_mov_b32_dpp %3, %7 row_ror:8 row_mask:0xf bank_mask:0xc"
                     : "+v"(s0[0]), "+v"(s0[1]), "+v"(s0[2]), "+v"(s0[3]) : "v"(s1[0]), "v"(s1[1]), "v"(s1[2]), "v"(s1[3]));
        MMA16(s2, F[3], F[3]);
        SCHEDB();
        *(f32x4*)pw0 = s0; SCHEDB();
        MMA16(u0, F[3], QY[0]); MMA16(u1, F[3], QY[1]);
        asm volatile("s_nop 1");
        PIN(s2); *(f32x4*)pw2 = s2; SCHEDB();
        auto uwrite = [&]() {
            PIN(u0); PIN(u1);
            f32x4 u;
#pragma unroll
            for (int i = 0; i < 4; ++i) u[i] = __uint_as_float(__builtin_amdgcn_permlane32_swap(__float_as_uint(u0[i]), __float_as_uint(u1[i]), false, false)[1]);
            *(LDS3 f32x4*)(uintptr_t)pw3 = u;
        };
        if (__builtin_expect(w < 2, 1)) {
            RAW_BARRIER();
            TR(3);
            softmax_mem(prd, pdst, pdst2, poff, uwrite);
            asm volatile("s_memrealtime %0" : "=s"(tpub));
            TR(4);
        } else {
            RAW_BARRIER();
            TR(3);
            uwrite();
            if (lead) {
                const u32 xt = tries == 0 && s > 0 ? ytime + k1 : rt32() + ELATE;
                u32 lo = lane * 4;
                asm volatile("" : "+v"(lo));
                int sta = *(const int*)(shm + Q_ST + lo), stb = *(const int*)(shm + Q_ST + 256 + lo);
                record(xt, xt + d2 + MG2);
                asm volatile("s_nop 1\n\t" DPP2("v_max_i32", "8") DPP2("v_max_i32", "4") DPP2("v_max_i32", "2") DPP2("v_max_i32", "1") : "+v"(sta), "+v"(stb));
                const int a0 = __builtin_amdgcn_readlane(sta, 0), a1 = __builtin_amdgcn_readlane(sta, 16), a2 = __builtin_amdgcn_readlane(sta, 32), a3 = __builtin_amdgcn_readlane(sta, 48);
                const int b0 = __builtin_amdgcn_readlane(stb, 0), b1 = __builtin_amdgcn_readlane(stb, 16), b2 = __builtin_amdgcn_readlane(stb, 32);
                SCHEDB();
                d1 = track(d1, smax(b1, b2), down); d2 = track(d2, smax(smax(smax(a0, a1), smax(a2, a3)), b0), down);
                down = s > 10 ? 1 : down;
            }
            if (w == 2 && s < 2 && layer > 0) wait_all_tokens(p, lane, cap);
        }
        RAW_BARRIER();
#pragma unroll
        for (int j = 0; j < 8; ++j) smt.load(prt, j);

        tref = tries == 0 && s > 0 ? ytime - tight : 0;
        char* xdst = px + (size_t)(set * 4 + rot) * PX_BUF + w * 20480;
        TR(5); TRFLUSH(set * 128 + h);
        if (s + 2 >= NSEG) {
            static_for<0, SmTok::N>([&](auto I) { smt.template piece<CI(I)>(xdst, xoff); SCHEDB(); if constexpr (CI(I) & 1) asm volatile("s_nop 0"); SCHEDB(); });
            TT(set * 128 + h, 0);
            __builtin_amdgcn_s_sleep(64);
            char* ao;
            u32x4 sent;
#pragma unroll
            for (int i = 0; i < 10; ++i) rearm(rot, i, ao, sent);
            break;
        }
        xproj(s + 4, s + 1, std::integral_constant<int, 1>(), xdst);
    }
    }
}

constexpr int C_YL = 0, C_RED = 40960, C_EX = 49152, C_XN = 53248;
__device__ __forceinline__ void comb_role(const LaParams& p, const int set, const int c, char* shm) {
    const int lane = threadIdx.x & 63, w = __builtin_amdgcn_readfirstlane(threadIdx.x >> 6);
    const int fr = lane & 15, g = lane >> 4;
    const u32 lds0 = (u32)(uintptr_t)(LDS3 char*)shm;
    const int sidx = (lane & 8) ? 0xEEEE : 0x4444;
    int cap = SPIN_CAP;
    char* mine = p.hop;
    char* other = p.hop;
    int phys = 0, sch = (S_SLOT + w) * SLOT;
    u32 k1 = D1I + MG1;
    char* px = (wsp(p) + O_PX);
    const char* xn = (wsp(p) + O_XN);
    char* const yl0 = shm + C_YL + lane * 16;
    const u32 pvo = PLIN ? lane * 16 : (fr >> 2) * 256 + g * 64 + (fr & 3) * 16, pvm = PLIN ? lane * 16 : g * 512 + (fr >> 2) * 128 + (fr & 3) * 16;
    int cb = (set * 80 + c) % 10, rb = (set * 80 + c) / 10;

    {
        const int t = threadIdx.x, h = (set * 80 + c) / 5, cu = c % 5;
        u32x4 st[12];
        const u16* src = p.Wo + ((size_t)h * DH + (t >> 4)) * DM + cu * 128 + (t & 15) * 8;
#pragma unroll
        for (int i = 0; i < 12; ++i) asm volatile("global_load_dwordx4 %0, %1, off" : "=v"(st[i]) : "v"(src + (size_t)i * 16 * DM) : "memory");
        u32 vo[12];
#pragma unroll
        for (int i = 0; i < 12; ++i) {
            const int id = i * 64 + lane, r = id / 24;
            vo[i] = kperm((r >> 2) & 3, (r >> 4) * 4 + (r & 3)) * (HD * 2) + ((id % 24) ^ (r & 7)) * 16;
        }
        const u32 ring = lds0 + 57344 + w * 24576;
        auto issue = [&](const int j) {
            const char* a = (const char*)p.Wv + (size_t)(((j + cu) % 5) * 4 + w) * 32 * HD * 2 + h * 384;
#pragma unroll
            for (int i = 0; i < 12; ++i) dma_gather(a, ring + (j & 1) * 12288 + i * 1024, vo[i]);
        };
        SCHEDB();
        issue(0);
        const u32 ra = ring + fr * 384 + ((g ^ (fr & 3)) + (fr & 4)) * 16;
        const int e = t >> 4, pr = ((e >> 2) & 1) * 8 + (e >> 3) * 4 + (e & 3);
        asm volatile("s_waitcnt vmcnt(12)" : "+v"(st[0]), "+v"(st[1]), "+v"(st[2]), "+v"(st[3]), "+v"(st[4]), "+v"(st[5]),
                     "+v"(st[6]), "+v"(st[7]), "+v"(st[8]), "+v"(st[9]), "+v"(st[10]), "+v"(st[11]) :: "memory");
#pragma unroll
        for (int i = 0; i < 12; ++i) *(u32x4*)(shm + (i * 16 + pr) * 288 + (t & 15) * 16) = st[i];
        issue(1);
        RAW_BARRIER();
        { const int layer = 0; TT(set * 128 + 32 + c, 12); }
        bf8 B[8][6], A[2][2][6];
        const u32 rd = lds0 + ((g >> 1) * 16 + (g & 1) * 4 + (fr >> 2)) * 288 + (fr & 3) * 8;
#pragma unroll
        for (int n = 0; n < 8; ++n)
#pragma unroll
            for (int k = 0; k < 6; ++k) B[n][k] = tr8(rd + k * 32 * 288 + n * 32, rd + (k * 32 + 8) * 288 + n * 32);
        auto lda = [&](const int j, const int k) {
#pragma unroll
            for (int u = 0; u < 2; ++u) A[j & 1][u][k] = *(LDS3 const bf8*)(uintptr_t)((ra ^ ((k & 1) * 64)) + (j & 1) * 12288 + u * 6144 + (k >> 1) * 128);
        };
        asm volatile("s_waitcnt vmcnt(12)" ::: "memory");
#pragma unroll
        for (int k = 0; k < 6; ++k) lda(0, k);
        f32x4 cc[2][2];
        char* dst;
        const char* nsrc;
        static_for<0, 41>([&](auto N) {
            constexpr int n = CI(N) % 8, j = CI(N) / 8, q = CI(N) - 1;
            if constexpr (n == 7 && j < 4) asm volatile("s_waitcnt vmcnt(%0)" :: "n"(j == 0 ? 18 : (j < 3 ? 21 : 9)) : "memory");
            if constexpr (n == 1) { dst = wsp(p) + O_WVO + ((size_t)(h * 20 + ((j + cu) % 5) * 4 + w) * 40 + cu * 8 + 4) * 1024; asm volatile("" : "+s"(dst)); }
            if constexpr (n == 0 && j < 3) { nsrc = (const char*)p.Wv + (size_t)(((j + 2 + cu) % 5) * 4 + w) * 32 * HD * 2 + h * 384; asm volatile("" : "+s"(nsrc)); }
            u32x4 o;
#pragma unroll
            for (int k = 0; k < 6; ++k) {
                if constexpr (j < 5) {
#pragma unroll
                    for (int u = 0; u < 2; ++u) { if (k == 0) MMA0_VA(cc[n & 1][u], A[j & 1][u][k], B[n][k]); else MMA_VA(cc[n & 1][u], A[j & 1][u][k], B[n][k]); }
                } else if (k == 0) asm volatile("s_nop 7");
                SCHEDB();
                if constexpr (j < 3 && n >= 1 && n < 7) {
                    if (k == 0 || k == 2) asm volatile("s_mov_b32 m0, %2\n\ts_nop 0\n\tglobal_load_lds_dwordx4 %0, %1" :: "v"(vo[2 * (n - 1) + k / 2]), "s"(nsrc), "s"(ringp(ring, (j & 1) * 12288 + (2 * (n - 1) + k / 2) * 1024)) : "memory");
                }
                if constexpr (n == 7 && j < 4) lda(j + 1, k);
                if constexpr (q >= 0) {
                    if (k == 3) { PIN(cc[q & 1][0]); o[0] = pk2bf(cc[q & 1][0][0], cc[q & 1][0][1]); o[1] = pk2bf(cc[q & 1][0][2], cc[q & 1][0][3]); }
                    if (k == 4) { PIN(cc[q & 1][1]); o[2] = pk2bf(cc[q & 1][1][0], cc[q & 1][1][1]); o[3] = pk2bf(cc[q & 1][1][2], cc[q & 1][1][3]); }
                    if (k == 5) __builtin_amdgcn_global_store_b128((gu32x4*)(dst + (u32)(lane * 16) + (q % 8 - 4) * 1024), o, "agent");
                }
                SCHEDB();
            }
        });
        { const int layer = 0; TT(set * 128 + 32 + c, 13); }
        VM0(); RAW_BARRIER();
        { const int layer = 0; TT(set * 128 + 32 + c, 14); }
        if (w == 0) st4_lane0(wsp(p) + O_FL + (400 + set * 80 + c) * 128, 1u);
        dma_kb<10>(xn + (size_t)set * 40960 + w * 10240, lds0 + C_XN + w * 10240, lane * 16);
        if (w == 0) {
            __builtin_amdgcn_s_sleep(8);
            const u32 off[1] = {(u32)(400 + (lane & 31) * 5 + (c >> 4)) * 128};
            wait_flags<1, FSL>(wsp(p) + O_FL, off, cap);
        }
        RAW_BARRIER();
        { const int layer = 0; TT(set * 128 + 32 + c, 15); }
    }
    bf16v WA[32], WV[8];
    {
        u32 vo = (w * 8 + (fr >> 3)) * 819200 + g * 256 + (fr & 7) * 16;
        asm volatile("" : "+v"(vo));
        auto ldw = [&](const int ks, const int t) {
            const char* a = wsp(p) + O_WVO + (size_t)(t * 1600 + ks * 80 + (c >> 1)) * 1024 + (c & 1) * 128;
            const char* b = a + 40960;
            asm volatile("" : "+s"(b));
            return cat8(__builtin_bit_cast(bf8, ld16(a, vo, 0)), __builtin_bit_cast(bf8, ld16(b, vo, 0)));
        };
#pragma unroll
        for (int i = 0; i < 32; ++i) WA[i] = ldw(i >> 2, i & 3);
#pragma unroll
        for (int i = 0; i < 8; ++i) WV[i] = ldw(8 + (i >> 2), i & 3);
    }
    auto yx = [&](f32x4 (&acc)[4][2], const int sy, auto&& extra) {
        const char* xa = shm + C_XN + ((sy >> 1) & 1) * 40960 + lane * 16;
        const char* xs = (sy + 2 < NSEG ? xn + (size_t)(sy + 2) * 40960 : (wsp(p) + O_WO) + cb * 81920) + w * 10240;
        u32 xl = lds0 + C_XN + (~(sy >> 1) & 1) * 40960 + w * 10240;
        bf8 x0 = *(const bf8*)(xa), x1 = *(const bf8*)(xa + 1024);
        const char* xs3[3] = {xs, xs + 4096, xs + 8192};
        u32 xl3[3] = {xl, xl + 4096, xl + 8192};
        asm volatile("" : "+s"(xs3[0]), "+s"(xs3[1]), "+s"(xs3[2]), "+s"(xl3[0]), "+s"(xl3[1]), "+s"(xl3[2]));
        static_for<0, 20>([&](auto K) {
            constexpr int k = CI(K);
            bf8 n0, n1;
            static_for<0, 4>([&](auto TT_) {
                constexpr int t = CI(TT_), id = (k >> 1) * 4 + t, dn = 2 * (k - 3) + t - 2;
                if constexpr (k < 19 && t == 0) n0 = *(const bf8*)(xa + (k + 1) * 2048);
                if constexpr (k < 19 && t == 1) n1 = *(const bf8*)(xa + (k + 1) * 2048 + 1024);
                if constexpr (t >= 2 && k >= 3 && k < 8) dma_one<(dn & 3) * 1024>(xs3[dn >> 2], xl3[dn >> 2], lane * 16);
                if constexpr (t >= 2) extra(2 * k + t - 2);
                SCHEDB();
                if constexpr (id < 32) {
                    const bf8 wf = (k & 1) ? hi8(WA[id]) : lo8(WA[id]);
                    if constexpr (k == 0) { MMA0_VA(acc[t][0], x0, wf); MMA0_VA(acc[t][1], x1, wf); } else { MMA_VA(acc[t][0], x0, wf); MMA_VA(acc[t][1], x1, wf); }
                } else {
                    const bf8 wf = (k & 1) ? hi8(WV[id - 32]) : lo8(WV[id - 32]);
                    MMA_VV(acc[t][0], x0, wf); MMA_VV(acc[t][1], x1, wf);
                }
                SCHEDB();
            });
            if constexpr (k < 19) { x0 = n0; x1 = n1; }
        });
    };

    for (int layer = 0; layer < 2; ++layer) {
    u32 ynext = ~0u;
    if (layer > 0) k1 = sched_get(~0u, mine + sch - 5 * SLOT + (62 + set) * 128 + 8, cap);
    VM0(); RAW_BARRIER();
    TT(set * 128 + 32 + c, 11);
    if (layer > 0 && w == 0) st4_lane0(wsp(p) + O_FL + (192 + rb * 10 + cb) * 128, 1u);
    {
        f32x4 ya[4][2];
        yx(ya, set, [](int) {});
        asm volatile("s_nop 7");
#pragma unroll
        for (int t = 0; t < 4; ++t) { PIN(ya[t][0]); PIN(ya[t][1]); *(bf8*)(yl0 + (w * 5 + t) * 1024) = pack8(ya[t][0], ya[t][1]); }
        RAW_BARRIER();
    }
    if (layer == 0) { phys = set ^ get_side(p, cap); mine += phys * 8192; other += (phys ^ 1) * 8192; }
    else if (w == 3) wait_all_tokens(p, lane, cap);
    TT(set * 128 + 32 + c, 10);
    for (int s = set; s < NSEG; s += 2) {
        const int rot = (s >> 1) & 3;
        char* yl = yl0 + (rot & 1) * 20480;
        TRDECL; TR(0);
        const char* mb0 = (s > 0 ? mine + (M_SLOT0 + rot * 8) * SLOT : p.hop + M0_OFF + phys * 8192) + 2 * w * SLOT + 2048;
        const char* mb1 = mb0 + SLOT;
        asm volatile("" : "+s"(mb0), "+s"(mb1));
        u32 yt = 0;
        bool late = s == 0;
        if (s > 0) { yt = sched_get(ynext, mine + sch + (s - 1) * 128 + 4, cap); wait_rt(yt + CLAG); }
        auto ldy = [&](const int f) { return *(const bf8*)(yl + (f < 16 ? (f >> 2) * 5 + (f & 3) : (f - 16) * 5 + 4) * 1024); };
        bf8 Y0[20 - 3 * NH_];
        TR(1);
        {
            f32x4 acc[4];
            u32x4 A[10];
            auto gather = [&]() {
                u32 vo = lane * 16;
                asm volatile("" : "+v"(vo) :: "memory");
#pragma unroll
                for (int ks = 0; ks < 10; ++ks) {
                    A[ks] = ld16(ks < 5 ? mb0 : mb1, vo, (ks % 5 - 2) * 1024);
                    SCHEDB();
                    if (ks < 4) ZERO4(acc[ks]);
                    if (ks < 9) asm volatile("s_nop %0" :: "n"(PACE));
                    SCHEDB();
                }
            };
            auto mproj = [&]() {
#pragma unroll
                for (int ks = 0; ks < 10; ++ks) {
#pragma unroll
                    for (int t = 0; t < 4; ++t) { if (ks < 8) SMM_A(acc[t], A[ks], WA[(ks * 4 + t) & 31], sidx); else SMM_V(acc[t], A[ks], WV[(ks * 4 + t) & 7], sidx); }
                }
                asm volatile("s_nop 7");
#pragma unroll
                for (int t = 0; t < 4; ++t) PIN(acc[t]);
            };
            gather();
#pragma unroll
            for (int i = 0; i < 20 - 3 * NH_; ++i) Y0[i] = ldy(i);
            SCHEDB();
            mproj();
            if (__builtin_expect(__any(__builtin_isunordered(acc[0][0], acc[0][1]) || __builtin_isunordered(acc[0][2], acc[0][3])), 0)) {
                CNT(1);
                late = true;
                for (int tries = 1;; ++tries) {
                    gather();
                    u32 m = 0;
#pragma unroll
                    for (int ks = 0; ks < 10; ++ks) m = max4(m, A[ks]);
                    if (!__any(m == ~0u)) break;
                    if (tries >= cap) { cap = 1; break; }
                    __builtin_amdgcn_s_sleep(8);
                }
                mproj();
            }
#pragma unroll
            for (int pr = 0; pr < 2; ++pr) {
                float v[4];
#pragma unroll
                for (int i = 0; i < 4; ++i) v[i] = halfsum(acc[2 * pr][i], acc[2 * pr + 1][i]);
                *(u32x2*)(shm + C_YL + (rot & 1) * 20480 + (w * 5 + 4) * 1024 + ((2 * pr + (g >> 1)) * 16 + fr) * 16 + (g & 1) * 8) = (u32x2){pk2bf(v[0], v[1]), pk2bf(v[2], v[3])};
            }
        }
        RAW_BARRIER();
        TR(2);
        u32 took = 0;
        if (s < NSEG - 1) {
            const char* pb[3];
#pragma unroll
            for (int i = 0; i < 3; ++i) { pb[i] = mine + (P_SLOT0 + (rot * 4 + c / 20) * PCS + i) * SLOT + 4096; asm volatile("" : "+s"(pb[i])); }
            auto ldp = [&](const int f, const u32 vo, const u32 vm) { return f < 16 ? ld16(pb[f >> 3], vo, ((f & 7) - 4) * 1024) : ld16(pb[2], vm, PLIN ? (f - 20) * 1024 : (f - 18) * 2048); };
            u32 xt;
            auto partial = [&](auto NF, auto F0, f32x4& acc, auto&& lastp) {
                constexpr int nf = CI(NF), f0 = CI(F0);
                bf8 (&Y)[nf] = Y0;
#pragma unroll
                for (int i = 0; i < nf; ++i) PIN(Y[i]);
                u32 vo = pvo;
                asm volatile("" : "+v"(vo));
                xt = late ? sched_get(~0u, mine + sch + s * 128, cap) : yt + k1;
                TR(3);
                if (!SYNCB || f0 == 0) wait_rt(xt);
                if (SYNCB) __builtin_amdgcn_s_barrier();
                TRV(7, xt); TRC(6);
                if (W0D > 0 && f0 == 0) asm volatile("s_nop %0" :: "n"(W0D));
                asm volatile("" ::: "memory");
                u32x4 P[nf];
#pragma unroll
                for (int i = 0; i < nf; ++i) { P[i] = ldp(f0 + i, vo, vo); SCHEDB(); }
#pragma unroll
                for (int i = 0; i < nf; ++i) {
                    if (i == nf - LATEP) lastp();
                    if (i == 0) MMA0_VV(acc, Y[i], P[i]); else MMA_VV(acc, Y[i], P[i]);
                    if (i == 0) TRC(7);
                    SCHEDB();
                }
                TRC(8);
            };
            f32x4 acc;
            if (w > 0) {
                bf8 Y[NH_];
                const char* q[NH_];
                u32 vo[NH_], ex = lds0 + C_EX + (w - 1) * 1024 + lane * 16;
#pragma unroll
                for (int i = 0; i < NH_; ++i) {
                    const int f = 20 - (4 - w) * NH_ + i;
                    Y[i] = ldy(f);
                    q[i] = pb[0] + (f >> 3) * SLOT + (f & 7) * (PLIN || f < 16 ? 1024 : 2048) - 4096;
                    vo[i] = f < 16 ? pvo : pvm;
                    asm volatile("" : "+s"(q[i]));
                    PIN(vo[i]);
                }
#pragma unroll
                for (int i = 0; i < NH_; ++i) PIN(Y[i]);
                PIN(ex);
                __builtin_amdgcn_s_barrier();
                TRC(6);
                asm volatile("" ::: "memory");
                u32x4 P[NH_];
#pragma unroll
                for (int i = 0; i < NH_; ++i) { P[i] = ld16(q[i], vo[i], 0); SCHEDB(); }
#pragma unroll
                for (int i = 0; i < NH_; ++i) { if (i == 0) MMA0_VV(acc, Y[i], P[i]); else MMA_VV(acc, Y[i], P[i]); if (i == 0) TRC(7); }
                TRC(8);
                asm volatile("s_nop 7" : "+v"(acc));
                *(LDS3 f32x4*)(uintptr_t)ex = acc;
                RAW_BARRIER();
            } else {
                const int rn = ((s + 1) >> 1) & 3;
                u32 dst = g * 2 * SLOT + (lane & 7) * 16;
                char* dstb = other + (M_SLOT0 + rn * 8 + c / 40) * SLOT + (c % 40) * 128;
                u32 exa = lds0 + C_EX + lane * 16;
                asm volatile("" : "+s"(dstb), "+v"(dst), "+v"(exa));
                f32x4 h1, h2, h3;
                partial(std::integral_constant<int, 20 - 3 * NH_>(), std::integral_constant<int, 0>(), acc, [&] {
                    __builtin_amdgcn_s_barrier();
                    asm volatile("" ::: "memory");
                    h1 = *(LDS3 const f32x4*)(uintptr_t)exa; h2 = *(LDS3 const f32x4*)(uintptr_t)(exa + 1024); h3 = *(LDS3 const f32x4*)(uintptr_t)(exa + 2048);
                    SCHEDB();
                });
                h1 = (h1 + h2) + h3;
                asm volatile("s_nop 2" : "+v"(acc), "+v"(h1));
                acc += h1;
                float v0, v1;
                auto merge = [&]() {
                    auto a = __builtin_amdgcn_permlane32_swap(__float_as_uint(acc[0]), __float_as_uint(acc[2]), false, false);
                    auto b = __builtin_amdgcn_permlane32_swap(__float_as_uint(acc[1]), __float_as_uint(acc[3]), false, false);
                    v0 = __uint_as_float(a[0]) + fdpp<0x128>(__uint_as_float(a[1]));
                    v1 = __uint_as_float(b[0]) + fdpp<0x128>(__uint_as_float(b[1]));
                };
                merge();
                int tries = 0;
                if (__builtin_expect(__any(__builtin_isunordered(v0, v1)), 0)) {
                    CNT(2);
                    wait_rt(sched_get(~0u, mine + sch + s * 128, cap));
                    while (true) {
                        u32 vo = pvo, vm = pvm, m = 0;
                        asm volatile("" : "+v"(vo), "+v"(vm) :: "memory");
                        u32x4 P[20];
#pragma unroll
                        for (int i = 0; i < 20; ++i) P[i] = ldp(i, vo, vm);
                        f32x4 hh[4];
#pragma unroll
                        for (int i = 0; i < 20; ++i) {
                            const bf8 y = ldy(i);
                            const int k = i < 20 - 3 * NH_ ? 0 : (i - 20 + 4 * NH_) / NH_;
                            if (i == 0 || i == 20 - 3 * NH_ || i == 20 - 2 * NH_ || i == 20 - NH_) MMA0_VV(hh[k], y, P[i]); else MMA_VV(hh[k], y, P[i]);
                            m = max4(m, P[i]);
                            SCHEDB();
                        }
                        asm volatile("s_nop 7" : "+v"(hh[0]), "+v"(hh[1]), "+v"(hh[2]), "+v"(hh[3]));
                        acc = hh[0] + ((hh[1] + hh[2]) + hh[3]);
                        ++tries;
                        if (!__any(m == ~0u)) break;
                        if (tries >= cap) { cap = 1; break; }
                        __builtin_amdgcn_s_sleep(4);
                    }
                    merge();
                }
                TRV(6, tries + 1);
                asm volatile("" :: "s"(dstb));
                const u32 dw = nosent(pk2bf(v0, v1));
                auto e = __builtin_amdgcn_permlane32_swap(dw, dw, false, false);
                auto e0 = __builtin_amdgcn_permlane16_swap(e[0], e[0], false, false);
                auto e1 = __builtin_amdgcn_permlane16_swap(e[1], e[1], false, false);
                st16f(dstb, dst, (u32x4){e0[0], e1[0], e0[1], e1[1]}, 0x00ff00ff00ff00ffull);
                TR(4);
                took = tries == 0 && s > 0 ? rt32() - xt : 0;
            }
            if (s == NSEG - 2) RAW_BARRIER();
        } else wait_rt(sched_get(~0u, mine + sch + s * 128, cap) + T_PX);
        auto tokens = [&](auto MORE) {
            f32x4 oa[4], ya[4][2];
            u32x4 P[20];
            u32 vo = lane * 16 + w * 4096, vm = lane * 16 + 16384 + w * 1024;
            asm volatile("" : "+v"(vo), "+v"(vm));
            const char* pb[4];
#pragma unroll
            for (int i = 0; i < 4; ++i) { pb[i] = px + (size_t)(set * 4 + rot) * PX_BUF + i * 20480; asm volatile("" : "+s"(pb[i])); }
            auto ldp = [&](const int i) { P[i] = i % 5 == 4 ? ld16(pb[i / 5], vm, 0) : ld16(pb[i / 5], vo, (i % 5) * 1024); };
            int ln = lane;
            asm volatile("" : "+v"(ln));
            u32 rg, peek, peek2;
            bf8 y[5];
            auto side = [&](const int i) {
                if (i == 4) rg = sched_peek((wsp(p) + O_XR) + layer * ACT + (size_t)s * 40960 + ((c >> 2) * 2 + (w >> 1)) * 1024 + (c & 2) * 256 + (w & 1) * 128 + (c & 1) * 4 + (u32)((ln & 2) * 128 + (ln >> 3) * 16 + (ln & 1) * 8));
                if (i >= 16 && i < 36) ldp(i - 16);
                if (i == 2 && CI(MORE) && w == 0) st4_lane0(mine + (T_SLOT + (c >> 6)) * SLOT + (c & 63) * 128, took);
                if (i == 5 && CI(MORE)) peek2 = sched_peek(mine + sch + s * 128 + 8);
                if (i >= 35) y[i - 35] = *(const bf8*)(yl + (w * 5 + i - 35) * 1024);
            };
            TRC(9);
            if constexpr (CI(MORE)) yx(ya, s + 2, side);
            else {
#pragma unroll
                for (int i = 0; i < 40; ++i) side(i);
            }
            TRC(10);
            bf8 yn[4];
            auto outx = [&](const bool first) {
                if (!first) {
#pragma unroll
                    for (int i = 0; i < 20; ++i) PIN(P[i]);
                    NOPV();
                }
#pragma unroll
                for (int q = 0; q < 4; ++q)
#pragma unroll
                    for (int j = 0; j < 5; ++j) {
                        if (j == 0) MMA0_VV(oa[q], y[j], P[q * 5 + j]); else MMA_VV(oa[q], y[j], P[q * 5 + j]);
                        if (CI(MORE) && first && q * 5 + j >= 3 && q * 5 + j < 19) {
                            const int e = q * 5 + j - 3;
                            SCHEDB();
                            if ((e & 1) == 0) PIN(ya[e >> 2][(e >> 1) & 1]);
                            ((u32*)&yn[e >> 2])[e & 3] = pk2bf(ya[e >> 2][(e >> 1) & 1][(e & 1) * 2], ya[e >> 2][(e >> 1) & 1][(e & 1) * 2 + 1]);
                            if ((e & 3) == 3) *(bf8*)(yl0 + (~rot & 1) * 20480 + (w * 5 + (e >> 2)) * 1024) = yn[e >> 2];
                            SCHEDB();
                        }
                    }
                if (CI(MORE) && first) { SCHEDB(); peek = sched_peek(mine + sch + (w == 0 ? s + 1 : s) * 128 + 4); SCHEDB(); }
                asm volatile("s_nop 7");
#pragma unroll
                for (int q = 0; q < 4; ++q) PIN(oa[q]);
            };
            outx(true);
            if (__builtin_expect(__any(__builtin_isunordered(oa[0][0], oa[1][0]) || __builtin_isunordered(oa[2][0], oa[3][0])), 0)) {
                CNT(3);
                for (int tries = 1;; ++tries) {
                    asm volatile("" : "+v"(vo), "+v"(vm) :: "memory");
                    u32 m = 0;
#pragma unroll
                    for (int i = 0; i < 20; ++i) ldp(i);
#pragma unroll
                    for (int i = 0; i < 20; ++i) m = max4(m, P[i]);
                    if (!__any(m == ~0u)) break;
                    if (tries >= cap) { cap = 1; break; }
                    __builtin_amdgcn_s_sleep(8);
                }
                outx(false);
            }
            if constexpr (!CI(MORE)) { if (c == 0 && w == 0) stamp2(p.hop, 48 + layer * 4 + set); }
            const bool own = (fr < 8) == (g < 2);
#pragma unroll
            for (int q = 0; q < 4; q += 2) {
                f32x4 o;
#pragma unroll
                for (int i = 0; i < 4; ++i) { const float t = fdpp<0x128>(oa[q + 1][i]); o[i] = own ? oa[q][i] : t; }
                *(f32x4*)(shm + C_RED + ((w * 2 + (g >> 1)) * 32 + (q + !own) * 8 + (fr & 7)) * 32 + (g & 1) * 16) = o;
            }
            if constexpr (CI(MORE)) { if (w == 0) *(u32*)(shm + C_EX + 3072) = peek; else asm volatile("" :: "v"(peek)); }
            RAW_BARRIER();
            TR(5);
            typedef __attribute__((ext_vector_type(2))) float f32x2;
            const char* rp = shm + C_RED + (ln & 4) * 1024 + w * 256 + (ln >> 3) * 32 + (ln & 3) * 8;
            f32x2 a = (*(const f32x2*)rp + *(const f32x2*)(rp + 1024)) + (*(const f32x2*)(rp + 2048) + *(const f32x2*)(rp + 3072));
            if constexpr (CI(MORE)) { k1 = sched_get(peek2, mine + sch + s * 128 + 8, cap); ynext = __builtin_amdgcn_readfirstlane(*(const u32*)(shm + C_EX + 3072)); asm volatile("" : "+s"(ynext), "+s"(k1)); }
            a[0] += fdpp<0x104>(a[0]); a[1] += fdpp<0x104>(a[1]);
            const u32 y2 = pk2bf(a[0], a[1]), d = nosent(pk2bf(bflo(rg) + bflo(y2), bfhi(rg) + bfhi(y2)));
            const u32x4 o4 = {d, (u32)__builtin_amdgcn_mov_dpp(d, 0x55, 0xf, 0xf, true), (u32)__builtin_amdgcn_mov_dpp(d, 0xaa, 0xf, 0xf, true), (u32)__builtin_amdgcn_mov_dpp(d, 0xff, 0xf, 0xf, true)};
            st16m((wsp(p) + O_X1) + layer * ACT + (size_t)s * 40960 + c * 512 + w * 128, ln * 2, o4, 0x0101010101010101ull);
            if (w == 1) {
                u32x4 v;
                asm volatile("v_mov_b64 %0, -1\n\tv_mov_b64 %1, -1" : "=v"(((u64*)&v)[0]), "=v"(((u64*)&v)[1]));
                st16m(other + (M_SLOT0 + (((s + 7) >> 1) & 3) * 8 + c / 40) * SLOT + (c % 40) * 128, (ln >> 4) * 2 * SLOT + (ln & 7) * 16, v, 0x00ff00ff00ff00ffull);
            }
            TRC(11); TRFLUSH(set * 128 + 32 + c);
        };
        if (s + 2 >= NSEG) { tokens(std::integral_constant<int, 0>()); break; }
        tokens(std::integral_constant<int, 1>());
    }
    TT(set * 128 + 32 + c, 0);
    asm volatile("" : "+s"(cb), "+s"(rb));
    dma_kb<10>((wsp(p) + O_WO) + cb * 81920 + 40960 + w * 10240, lds0 + C_XN + 40960 + w * 10240, lane * 16);
    if (w == 0) { const u32 t = sched_get(~0u, mine + (T_SLOT + 1) * SLOT + (50 + layer * 4) * 128, cap); *(u32*)(shm + C_EX) = t; }
    VM0(); RAW_BARRIER();
    wait_rt(__builtin_amdgcn_readfirstlane(*(const u32*)(shm + C_EX)) + T_OB + OB_STEP * w + (layer == 0 ? OB_COLD + 8 * w : 0));
    {
        int ln = lane;
        asm volatile("" : "+v"(ln));
        u32x4 O[20], R[2];
        u32 dw[8];
        auto ready = [&](const int T) {
            const char* ob = (wsp(p) + O_OB) + layer * ACT + (size_t)T * 20480 + 16384;
            const u32 off[1] = {(u32)(ln & 31) * 128};
            wait_flags<1>(ob, off, cap);
        };
        auto load = [&](const int T) {
            const char* ob0 = (wsp(p) + O_OB) + layer * ACT + (size_t)T * 20480 + 4096;
            const char* ob1 = ob0 + 8192;
            const char* ob2 = ob0 + 12288;
            const char* xb = (wsp(p) + O_X1) + layer * ACT + (size_t)(T >> 1) * 40960 + cb * 4096 + (T & 1) * 256;
            u32 vo = ln * 16, vr = (ln >> 4) * 256 + (ln & 15) * 8, vx = (ln >> 4) * 1024 + (ln & 15) * 16;
            asm volatile("" : "+v"(vo), "+v"(vr), "+v"(vx), "+s"(ob0), "+s"(ob1), "+s"(ob2), "+s"(xb) :: "memory");
#pragma unroll
            for (int k = 0; k < 16; ++k) { O[k] = ld16(k < 8 ? ob0 : ob1, vo, ((k & 7) - 4) * 1024); SCHEDB(); }
#pragma unroll
            for (int k = 0; k < 4; ++k) {
                const u64 lo = __hip_atomic_load((const gu64*)(ob2 + vr + k * 1024), __ATOMIC_RELAXED, __HIP_MEMORY_SCOPE_AGENT);
                const u64 hi = __hip_atomic_load((const gu64*)(ob2 + vr + k * 1024 + 128), __ATOMIC_RELAXED, __HIP_MEMORY_SCOPE_AGENT);
                O[16 + k] = (u32x4){(u32)lo, (u32)(lo >> 32), (u32)hi, (u32)(hi >> 32)};
            }
            R[0] = ld16(xb, vx, 0); R[1] = ld16(xb, vx, 512);
            SCHEDB();
        };
        auto gemm = [&]() {
            u32 wa0 = lds0 + C_XN + ln * 16, wa1 = wa0 + 40960;
            asm volatile("" : "+v"(wa0), "+v"(wa1));
            f32x4 acc[4];
            bf8 wf[8];
#pragma unroll
            for (int t = 0; t < 8; ++t) wf[t] = *(LDS3 const bf8*)(uintptr_t)(wa0 + t * 1024);
#pragma unroll
            for (int k = 0; k < 20; ++k) {
                if (k >= 16) { PIN(O[k]); NOPV(); }
#pragma unroll
                for (int t = 0; t < 4; ++t) {
                    if (k == 0) MMA0_VV(acc[t], wf[t], O[k]); else MMA_VV(acc[t], wf[(k & 1) * 4 + t], O[k]);
                    SCHEDB();
                    if (k < 18) wf[(k & 1) * 4 + t] = *(LDS3 const bf8*)(uintptr_t)((k < 8 ? wa0 : wa1) + (((k + 2) % 10) * 4 + t) * 1024);
                    SCHEDB();
                }
            }
            asm volatile("s_nop 7" : "+v"(acc[0]), "+v"(acc[1]), "+v"(acc[2]), "+v"(acc[3]));
            float chk = 0.f;
#pragma unroll
            for (int i = 0; i < 8; ++i) {
                const u32 r = R[i >> 2][i & 3], y = pk2bf(acc[i >> 1][(i & 1) * 2], acc[i >> 1][(i & 1) * 2 + 1]);
                const float a = bflo(r) + bflo(y), b = bfhi(r) + bfhi(y);
                dw[i] = pk2bf(a, b);
                chk += a;
            }
            return __any(chk != chk);
        };
        const int T0 = rb + 16 * w;
        load(T0);
        static_for<0, 2>([&](auto PASS) {
            constexpr int pass = CI(PASS);
            const int T = T0 + 64 * pass;
            TT(set * 128 + 32 + c, 1 + pass * 4);
            if (__builtin_expect(gemm(), 0)) {
                for (int tries = 0;; ++tries) {
                    ready(T); load(T);
                    u32 m = max4(max4(0, R[0]), R[1]);
#pragma unroll
                    for (int k = 0; k < 20; ++k) m = max4(m, O[k]);
                    if (!__any(m == ~0u)) break;
                    if (tries >= cap) { cap = 1; break; }
                    __builtin_amdgcn_s_sleep(6);
                }
                gemm();
            }
            TT(set * 128 + 32 + c, 2 + pass * 4);
            if (layer == 1) {
                st32((char*)p.out + (size_t)T * 16 * DM * 2 + cb * 128, (ln & 15) * (DM * 2) + (ln >> 4) * 32, (u32x4){dw[0], dw[1], dw[2], dw[3]}, (u32x4){dw[4], dw[5], dw[6], dw[7]});
            } else {
            float s1 = 0.f, s2 = 0.f;
#pragma unroll
            for (int i = 0; i < 8; ++i) { const float a = bflo(dw[i]), b = bfhi(dw[i]); s1 += a + b; s2 = fmaf(a, a, fmaf(b, b, s2)); }
            s1 = grp_sum(s1); s2 = grp_sum(s2);
            char* stl = (wsp(p) + O_ST) + (size_t)T * 1280;
            st8m(stl + cb * 128, ln * 8, (u32x2){nosent(__float_as_uint(s1)), nosent(__float_as_uint(s2))}, 0xffffull);
            const size_t fo = ACT + (size_t)(T >> 1) * 40960 + (cb * 4 + (T & 1)) * 1024;
            const u32 lo = (ln >> 5) * 2048 + ((ln >> 4) & 1) * 512 + (ln & 15) * 16;
            __builtin_amdgcn_s_sleep(ST_LAG);
            for (int spin = 0;; ++spin) {
                u32 m = 0;
                s1 = 0.f; s2 = 0.f;
#pragma unroll
                for (int i = 0; i < 3; ++i) {
                    const int k = (ln >> 4) + 4 * i;
                    const u64 v = __hip_atomic_load((const gu64*)(stl + min(k, 9) * 128 + (ln & 15) * 8), __ATOMIC_RELAXED, __HIP_MEMORY_SCOPE_AGENT);
                    m = max(m, max((u32)v, (u32)(v >> 32)));
                    if (k < 10) { s1 += __uint_as_float((u32)v); s2 += __uint_as_float((u32)(v >> 32)); }
                }
                if (!__any(m == ~0u)) break;
                if (spin >= cap) { cap = 1; break; }
                __builtin_amdgcn_s_sleep(2);
            }
            TT(set * 128 + 32 + c, 3 + pass * 4);
            s1 = grp_sum(s1); s2 = grp_sum(s2);
            const float mu = s1 * (1.0f / DM), mr = rbf(mu), rs = ln_scale(fmaxf(fmaf(-mu, mu, s2 * (1.0f / DM)), 0.f));
            u32 dn[8];
#pragma unroll
            for (int i = 0; i < 8; ++i) dn[i] = ln_pair(dw[i], mr, rs);
            st16s((wsp(p) + O_XN) + fo, lo, (u32x4){dn[0], dn[4], dn[1], dn[5]}); st16s((wsp(p) + O_XN) + fo + 256, lo, (u32x4){dn[2], dn[6], dn[3], dn[7]});
            st16s((wsp(p) + O_XR) + fo, lo, (u32x4){dw[0], dw[4], dw[1], dw[5]}); st16s((wsp(p) + O_XR) + fo + 256, lo, (u32x4){dw[2], dw[6], dw[3], dw[7]});
            TT(set * 128 + 32 + c, 4 + pass * 4);
            if (pass == 0 && w == 0) { VM0(); st4_lane0(wsp(p) + O_FL + (rb * 10 + cb) * 128, 1u); }
            }
            if constexpr (pass == 0) load(T + 64);
        });
    }
    if (layer == 1) break;
    sch += 5 * SLOT; xn += ACT;
    if (w == 0) wait_first_tokens(p, set, lane, cap);
    RAW_BARRIER();
    TT(set * 128 + 32 + c, 9);
    dma_kb<10>(xn + (size_t)set * 40960 + w * 10240, lds0 + C_XN + w * 10240, lane * 16);
    }
}

constexpr int B_PQ = 0, B_IM = 131072, B_CS = 155648, B_IS = 176;
__device__ __forceinline__ void bw_role(const LaParams& p, const int hb, char* shm) {
    const int lane = threadIdx.x & 63, w = __builtin_amdgcn_readfirstlane(threadIdx.x >> 6);
    const int fr = lane & 15, g = lane >> 4;
    int cap = SPIN_CAP;
    if (hb == 0 && w == 0) {
        u32 best[2] = {~0u, ~0u};
        const u32 tc = rt32();
        for (int i = 0; i < 256; ++i) {
#pragma unroll
            for (int sd = 0; sd < 2; ++sd) {
                u64 t0, t1;
                u32 z = 0, one = 1;
                asm volatile("s_memtime %0\n\ts_mov_b64 exec, 1\n\ts_waitcnt lgkmcnt(0)\n\tglobal_atomic_add %2, %3, %2, %4 sc0 sc1\n\ts_waitcnt vmcnt(0)\n\t"
                             "s_memtime %1\n\ts_mov_b64 exec, -1\n\ts_waitcnt lgkmcnt(0)"
                             : "=&s"(t0), "=&s"(t1), "+v"(one) : "v"(z), "s"(p.hop + M0_OFF + sd * 8192 + 6144 + (i & 3) * 256) : "memory");
                best[sd] = min(best[sd], (u32)(t1 - t0));
            }
            const u32 el = rt32() - tc, lo = min(best[0], best[1]), hi = max(best[0], best[1]);
            if (el > 1900u || (el > 600u && lo < 700u && hi - lo > 130u && hi - lo < 420u)) break;
        }
        u32 xcc; asm volatile("s_getreg_b32 %0, hwreg(HW_REG_XCC_ID)" : "=s"(xcc));
        st4_lane0(wsp(p) + O_FL + 384 * 128, ((xcc >> 2) & 1) ^ (best[1] < best[0] ? 1u : 0u));
    }
    bf8 WA[64], WV[16];
    {
        const int t = threadIdx.x;
        float* xs = (float*)(shm + 81920);
        float* ph = xs + 128 * 61;
        for (int i = t; i < 400; i += 256) ph[i] = bf2f(p.wphi[hb * 400 + i]);
        const int j = 4 * w + (fr & 3), G = fr >> 2;
        for (int d0 = 0; d0 < DM; d0 += 128) {
            u32 v15[15];
#pragma unroll
            for (int k = 0; k < 15; ++k) { const int i = t + k * 256; v15[k] = *(const u32*)(p.wqkv + (size_t)(d0 + i / 30) * 1920 + ((i % 30) / 10) * 640 + hb * 20 + (i % 10) * 2); }
            __syncthreads();
#pragma unroll
            for (int k = 0; k < 15; ++k) { const int i = t + k * 256; float* o = xs + (i / 30) * 61 + (i % 30) * 2; o[0] = bflo(v15[k]); o[1] = bfhi(v15[k]); }
            __syncthreads();
            for (int kk = 0; kk < 4; ++kk) {
                float v[8];
                const float* xr = xs + (kk * 32 + g * 8) * 61;
                if (j < 10) {
                    const int m = j / 5, f = 5 * G + j % 5;
#pragma unroll
                    for (int e = 0; e < 8; ++e) v[e] = 0.f;
#pragma unroll 4
                    for (int i = 0; i < 20; ++i) {
                        const float pw = ph[i * 20 + f];
#pragma unroll
                        for (int e = 0; e < 8; ++e) v[e] = fmaf(xr[e * 61 + m * 20 + i], pw, v[e]);
                    }
                } else {
#pragma unroll
                    for (int e = 0; e < 8; ++e) v[e] = j < 15 ? xr[e * 61 + 40 + 5 * G + min(j, 14) - 10] : 1.0f;
                }
                *(u32x4*)(shm + ((d0 / 32 + kk) * 4 + w) * 1024 + lane * 16) = (u32x4){pk2bf(v[0], v[1]), pk2bf(v[2], v[3]), pk2bf(v[4], v[5]), pk2bf(v[6], v[7])};
            }
        }
        __syncthreads();
#pragma unroll
        for (int i = 0; i < 64; ++i) WA[i] = *(const bf8*)(shm + i * 1024 + lane * 16);
#pragma unroll
        for (int i = 0; i < 16; ++i) WV[i] = *(const bf8*)(shm + (64 + i) * 1024 + lane * 16);
        __syncthreads();
    }
    auto proj = [&](f32x4 (&acc)[4], f32x4& sq, auto&& fetch) {
        u32x4 X[20];
#pragma unroll
        for (int k = 0; k < 12; ++k) { X[k] = fetch(k); SCHEDB(); }
#pragma unroll
        for (int k = 0; k < 20; ++k) {
#pragma unroll
            for (int t = 0; t < 4; ++t) {
                if (k == 0) MMA0_AV(acc[t], WA[t], X[k]); else if (k < 16) MMA_AV(acc[t], WA[(k * 4 + t) & 63], X[k]); else MMA_VV(acc[t], WV[(k * 4 + t) & 15], X[k]);
            }
            if (k == 0) MMA0_VV(sq, X[k], X[k]); else MMA_VV(sq, X[k], X[k]);
            SCHEDB();
            if (k < 8) X[k + 12] = fetch(k + 12);
            SCHEDB();
        }
        asm volatile("s_nop 7" : "+v"(acc[0]), "+v"(acc[1]), "+v"(acc[2]), "+v"(acc[3]), "+v"(sq));
    };
    {
        f32x4 t, cs[4];
        u32x4 o1 = {0x3f803f80u, 0x3f803f80u, 0x3f803f80u, 0x3f803f80u};
        asm volatile("s_nop 1" : "+v"(o1));
        proj(cs, t, [&](int) { return o1; });
#pragma unroll
        for (int i = 0; i < 4; ++i) *(f32x4*)(shm + B_CS + i * 1024 + lane * 16) = cs[i];
    }
    char* im = shm + B_IM + w * 6144;
    const u32 imr = (u32)(uintptr_t)(LDS3 char*)im + (4 * g + (fr >> 2)) * B_IS + (fr & 3) * 8;
    typedef __attribute__((ext_vector_type(32))) u32 u32x32;
    for (int layer = 0; layer < 2; ++layer) {
        const char* sch = p.hop + (hb & 1) * 8192 + (S_SLOT + layer * 5 + 4) * SLOT;
        f32x4 kv[3][2];
#pragma unroll
        for (int i = 0; i < 6; ++i) ZERO4(kv[i >> 1][i & 1]);
        u32x32 q5;
        u32 tn = 0;
        for (int it = 0; it < 32; ++it) {
            const int s = 2 * it + (w >> 1);
            if (s + 2 < NSEG) { wait_rt(tn); tn = sched_get(~0u, sch + (s + 1) * 128 + 4, cap); wait_rt(tn + B_LAG); tn += BPEEK; }
            else wait_rt(sched_get(~0u, p.hop + (hb & 1) * 8192 + (T_SLOT + 1) * SLOT + (48 + layer * 4 + (s & 1)) * 128, cap) + T_X1);
            const char* xb = (wsp(p) + O_X1) + layer * ACT + (size_t)s * 40960 + (w & 1) * 256;
            if (it >= 30) TT((hb >> 4) * 128 + 112 + (hb & 15), (it - 30) * 3);
            f32x4 acc[4], sq;
            const char* xk[5];
#pragma unroll
            for (int i = 0; i < 5; ++i) { xk[i] = xb + i * 8192 + 4096; asm volatile("" : "+s"(xk[i])); }
            for (int pass = 0;; ++pass) {
                u32 vo = g * 512 + fr * 16;
                asm volatile("" : "+v"(vo) :: "memory");
                auto fetch = [&](const int k) { return ld16(xk[k >> 2], vo, ((k & 3) - 2) * 2048); };
                proj(acc, sq, fetch);
                if (pass > 0 || !__any(acc[3][3] != acc[3][3])) break;
                CNT(4);
                for (int tries = 0;; ++tries) {
                    asm volatile("" : "+v"(vo) :: "memory");
                    u32 m = 0;
#pragma unroll
                    for (int k = 0; k < 20; k += 10) {
#pragma unroll
                        for (int i = 0; i < 10; ++i) m = max4(m, fetch(k + i));
                        SCHEDB();
                    }
                    if (!__any(m == ~0u)) break;
                    if (tries >= cap) { cap = 1; break; }
                    __builtin_amdgcn_s_sleep(6);
                }
            }
            if (it >= 30) TT((hb >> 4) * 128 + 112 + (hb & 15), (it - 30) * 3 + 1);
            if (s == NSEG - 1 && hb == 5 && w == 3) stamp2(p.hop, 50 + layer * 4);
            const float dsel = (fr & 2) ? ((fr & 1) ? sq[3] : sq[2]) : ((fr & 1) ? sq[1] : sq[0]);
            const float s2 = grp_sum(g == (fr >> 2) ? dsel : 0.f);
            const float mu = acc[3][3] * (1.0f / DM), rs = __builtin_amdgcn_rsqf(fmaxf(fmaf(-mu, mu, s2 * (1.0f / DM)), 0.f) + 1e-5f);
            float v[15];
            f32x4 cs[4];
#pragma unroll
            for (int i = 0; i < 4; ++i) cs[i] = *(const f32x4*)(shm + B_CS + i * 1024 + lane * 16);
#pragma unroll
            for (int j = 0; j < 15; ++j) v[j] = fmaf(-mu, cs[j >> 2][j & 3], acc[j >> 2][j & 3]) * rs;
            u32 ph[2][5];
#pragma unroll
            for (int m = 0; m < 2; ++m) {
                const float* x = v + 5 * m;
                const float mx = grp_max(fmaxf(fmaxf(fmaxf(x[0], x[1]), fmaxf(x[2], x[3])), x[4]));
                const float mn = -grp_max(-fminf(fminf(fminf(x[0], x[1]), fminf(x[2], x[3])), x[4]));
                float e[10], a = 0.f, b = 0.f;
#pragma unroll
                for (int j = 0; j < 5; ++j) {
                    e[j] = __builtin_amdgcn_exp2f((x[j] - mx) * 1.4426950408889634f); a += e[j];
                    e[5 + j] = __builtin_amdgcn_exp2f((mn - x[j]) * 1.4426950408889634f); b += e[5 + j];
                }
                a = __builtin_amdgcn_rcpf(grp_sum(a)); b = __builtin_amdgcn_rcpf(grp_sum(b));
#pragma unroll
                for (int j = 0; j < 5; ++j) { e[j] *= a; e[5 + j] *= b; }
#pragma unroll
                for (int j = 0; j < 5; ++j) ph[m][j] = pk2bf(e[2 * j], e[2 * j + 1]);
            }
            *(u32x4*)(shm + B_PQ + (w * 32 + it) * 1024 + lane * 16) = (u32x4){ph[0][0], ph[0][1], ph[0][2], ph[0][3]};
            q5[it] = ph[0][4];
            char* row = im + fr * B_IS;
            *(u32x2*)(row + g * 8) = (u32x2){ph[1][0], ph[1][1]};
            *(u32x2*)(row + 32 + g * 8) = (u32x2){ph[1][2], ph[1][3]};
            *(u32x2*)(row + 64 + g * 8) = (u32x2){ph[1][4], 0u};
            *(u32x2*)(row + 96 + g * 8) = (u32x2){pk2bf(v[10], v[11]), pk2bf(v[12], v[13])};
            *(u32x2*)(row + 128 + g * 8) = (u32x2){pk2bf(v[14], 1.0f), 0u};
            s16x4 tf[5];
#pragma unroll
            for (int i = 0; i < 5; ++i) tf[i] = __builtin_amdgcn_ds_read_tr16_b64_v4i16((LDS3 s16x4*)(uintptr_t)(imr + i * 32));
#pragma unroll
            for (int i = 0; i < 6; ++i) MMA16(kv[i >> 1][i & 1], tf[i >> 1], tf[3 + (i & 1)]);
            if (it >= 30) TT((hb >> 4) * 128 + 112 + (hb & 15), (it - 30) * 3 + 2);
        }
        asm volatile("s_nop 7" : "+v"(kv[0][0]), "+v"(kv[0][1]), "+v"(kv[1][0]), "+v"(kv[1][1]), "+v"(kv[2][0]), "+v"(kv[2][1]));
#pragma unroll
        for (int i = 0; i < 6; ++i) *(f32x4*)(im + i * 1024 + lane * 16) = kv[i >> 1][i & 1];
        RAW_BARRIER();
        bf8 ka[2];
        u32x2 kb[2];
#pragma unroll
        for (int dt = 0; dt < 2; ++dt) {
            f32x4 t[3];
#pragma unroll
            for (int ft = 0; ft < 3; ++ft) {
                const char* a = shm + B_IM + (ft * 2 + dt) * 1024 + lane * 16;
                t[ft] = (*(const f32x4*)a + *(const f32x4*)(a + 6144)) + (*(const f32x4*)(a + 12288) + *(const f32x4*)(a + 18432));
            }
            ka[dt] = pack8(t[0], t[1]);
            kb[dt] = (u32x2){pk2bf(t[2][0], t[2][1]), pk2bf(t[2][2], t[2][3])};
            PIN(ka[dt]); PIN(kb[dt]);
        }
        char* ob = (wsp(p) + O_OB) + layer * ACT + (size_t)w * 20480 + hb * 512;
        TT((hb >> 4) * 128 + 112 + (hb & 15), 6);
        const u32 rofs = g * 81920 + 16384 - hb * 384 + fr * 8, mofs = (g & 1) * 81920 + (g >> 1) * 256 + fr * 16;
        f32x4 o[2][4][2];
        bf8 qa[2][4];
        u32x2 qb[4];
        auto ldq = [&](const int gr) {
#pragma unroll
            for (int u = 0; u < 4; ++u) qa[gr & 1][u] = *(const bf8*)(shm + B_PQ + (w * 32 + gr * 4 + u) * 1024 + lane * 16);
        };
        auto mm = [&](const int gr, const int k) {
            SCHEDB();
            if (k < 4) { qb[k] = (u32x2){q5[gr * 4 + k], 0u}; PIN(qb[k]); }
            if (k < 8) MMA0_VV(o[gr & 1][k >> 1][k & 1], ka[k & 1], qa[gr & 1][k >> 1]);
            else MMA16(o[gr & 1][(k - 8) >> 1][k & 1], kb[k & 1], qb[(k - 8) >> 1]);
            SCHEDB();
        };
        ldq(0); ldq(1);
        NOPV();
#pragma unroll
        for (int k = 0; k < 16; ++k) mm(0, k);
        static_for<0, 8>([&](auto IT) {
            constexpr int gr = CI(IT);
            auto nx = [&](const int k) { if constexpr (gr < 7) mm(gr + 1, k); else { SCHEDB(); asm volatile("s_nop 0"); SCHEDB(); } };
            if constexpr (gr == 7) asm volatile("s_nop 7");
            u32 d[4][2];
            float x[4];
            nx(0); nx(1);
#pragma unroll
            for (int u = 0; u < 4; ++u) {
                f32x4& a = o[gr & 1][u][0];
                f32x4& b = o[gr & 1][u][1];
                PIN(a); PIN(b);
                float inv = __builtin_amdgcn_rcpf(b[1] + 1e-6f), y0, y1, y2, y3;
                nx(2 + 3 * u);
                asm("v_mul_f32 %0, %4, %8\n\tv_mul_f32 %1, %5, %8\n\tv_mul_f32 %2, %6, %8" : "=&v"(y0), "=&v"(y1), "=&v"(y2), "=&v"(y3) : "v"(a[0]), "v"(a[1]), "v"(a[2]), "v"(a[3]), "v"(inv));
                nx(3 + 3 * u);
                y3 = a[3] * inv; x[u] = b[0] * inv;
                d[u][0] = pk2bf(y0, y1);
                nx(4 + 3 * u);
                d[u][1] = pk2bf(y2, y3);
                if (u & 1) {
                    auto t0 = __builtin_amdgcn_permlane16_swap(d[u - 1][0], d[u][0], false, false);
                    auto t1 = __builtin_amdgcn_permlane16_swap(d[u - 1][1], d[u][1], false, false);
                    st16s(ob + (u - 1) * 81920, mofs, (u32x4){nosent(t0[0]), nosent(t1[0]), nosent(t0[1]), nosent(t1[1])});
                }
            }
            if constexpr (gr < 6) ldq(gr + 2);
            nx(14);
            auto a0 = __builtin_amdgcn_permlane32_swap(__float_as_uint(x[0]), __float_as_uint(x[2]), false, false);
            auto a1 = __builtin_amdgcn_permlane32_swap(__float_as_uint(x[1]), __float_as_uint(x[3]), false, false);
            nx(15);
            auto b0 = __builtin_amdgcn_permlane16_swap(a0[0], a1[0], false, false);
            auto b2 = __builtin_amdgcn_permlane16_swap(a0[1], a1[1], false, false);
            st8m(ob, rofs, (u32x2){nosent(pk2bf(__uint_as_float(b0[0]), __uint_as_float(b0[1]))), nosent(pk2bf(__uint_as_float(b2[0]), __uint_as_float(b2[1])))}, ~0ull);
            ob += 4 * 81920;
            asm volatile("" : "+s"(ob));
        });
        RAW_BARRIER();
        TT((hb >> 4) * 128 + 112 + (hb & 15), 7);
    }
}

__global__ void __launch_bounds__(256, 1) la_kernel(LaParams p) {
    extern __shared__ __attribute__((aligned(16))) char shm[];
    p.hop = align2m(p.ws + O_HOP);
    if (threadIdx.x == 0) {
        u32 xcc; asm volatile("s_getreg_b32 %0, hwreg(HW_REG_XCC_ID)" : "=s"(xcc));
        int role = 255;
        for (int a = 0; a < 8; ++a) {
            const u32 v = (xcc + a) & 7;
            const u32 k = __hip_atomic_fetch_add((gu32*)(((u32*)(p.ws + O_CTL)) + v * 32), 1u, __ATOMIC_RELAXED, __HIP_MEMORY_SCOPE_AGENT);
            if (k < 32) { role = v * 32 + k; break; }
        }
        *(int*)shm = role;
    }
    __syncthreads();
    const int role = __builtin_amdgcn_readfirstlane(*(int*)shm);
    __syncthreads();
    const int set = role >> 7, id = (role >> 5) & 3, k = role & 31;
    if (k < 8) qk_role(p, set, id * 8 + k, shm);
    else if (k < 28) comb_role(p, set, id * 20 + k - 8, shm);
    else bw_role(p, (role >> 5) * 4 + k - 28, shm);
}

struct PrepParams { const u16* x; const u16* mem0; const u16* wo; char* ws; };
constexpr int N_LN = 128, N_WO = 200, N_FILL = 512;
__global__ void __launch_bounds__(256) prep_kernel(PrepParams q) {
    int b = blockIdx.x;
    const int t = threadIdx.x, lane = t & 63, w = t >> 6, fr = lane & 15, g = lane >> 4;
    char* hop = align2m(q.ws + O_HOP);
    if (b < N_LN) {
        const int tok = b * 16 + w * 4 + g;
        u32x4 d[3][2];
        float s = 0.f;
#pragma unroll
        for (int i = 0; i < 3; ++i) {
            const u32x4* a = (const u32x4*)(q.x + (size_t)tok * DM + min(fr + 16 * i, 39) * 16);
            d[i][0] = a[0]; d[i][1] = a[1];
        }
#pragma unroll
        for (int i = 0; i < 3; ++i)
#pragma unroll
            for (int e = 0; e < 8; ++e) if (fr + 16 * i < 40) s += bflo(d[i][e >> 2][e & 3]) + bfhi(d[i][e >> 2][e & 3]);
        const float mu = row16_sum(s) * (1.0f / DM);
        float v = 0.f;
#pragma unroll
        for (int i = 0; i < 3; ++i)
#pragma unroll
            for (int e = 0; e < 8; ++e) if (fr + 16 * i < 40) { const float a = bflo(d[i][e >> 2][e & 3]) - mu, c = bfhi(d[i][e >> 2][e & 3]) - mu; v = fmaf(a, a, fmaf(c, c, v)); }
        const float rs = ln_scale(row16_sum(v) * (1.0f / DM)), mr = rbf(mu);
#pragma unroll
        for (int i = 0; i < 3; ++i) {
            const int blk = fr + 16 * i;
            if (blk >= 40) continue;
            const size_t o = (size_t)(tok >> 5) * 40960 + ((blk >> 1) * 2 + ((tok >> 4) & 1)) * 1024 + ((blk & 1) * 32 + (tok & 15)) * 16;
            u32 n[8];
#pragma unroll
            for (int e = 0; e < 8; ++e) n[e] = ln_pair(d[i][e >> 2][e & 3], mr, rs);
            *(u32x4*)(q.ws + O_XR + o) = (u32x4){d[i][0][0], d[i][1][0], d[i][0][1], d[i][1][1]};
            *(u32x4*)(q.ws + O_XR + o + 256) = (u32x4){d[i][0][2], d[i][1][2], d[i][0][3], d[i][1][3]};
            *(u32x4*)(q.ws + O_XN + o) = (u32x4){n[0], n[4], n[1], n[5]};
            *(u32x4*)(q.ws + O_XN + o + 256) = (u32x4){n[2], n[6], n[3], n[7]};
        }
        return;
    }
    b -= N_LN;
    if (b < N_WO) {
        const int cb = b / 20, kk = b % 20;
        const u16* col = q.wo + cb * 64 + 16 * (fr >> 2) + 4 * w + (fr & 3);
        u32 v[8];
#pragma unroll
        for (int e = 0; e < 8; ++e) {
            const int row = kk < 16 ? (2 * kk + (g >> 1)) * 20 + 5 * (2 * (g & 1) + (e >> 2)) + (e & 3) : (8 * (kk - 16) + 2 * g + (e >> 2)) * 20 + 5 * (e & 3) + 4;
            v[e] = col[(size_t)row * DM];
        }
        *(u32x4*)(q.ws + O_WO + (size_t)cb * 81920 + (kk * 4 + w) * 1024 + lane * 16) = (u32x4){v[0] | v[1] << 16, v[2] | v[3] << 16, v[4] | v[5] << 16, v[6] | v[7] << 16};
        return;
    }
    b -= N_WO;
    const u32x4 SENT = {~0u, ~0u, ~0u, ~0u};
    const u32 i0 = b * 256 + t;
    if (b == 0) ((u32*)(q.ws + O_CTL))[t] = 0;
    if (i0 < 8 * 2 * 320) {
        const int sl = i0 / 640, e = i0 % 320;
        const u32x4 v = *(const u32x4*)(q.mem0 + (e & 7) * DM + ((sl & 1) * 40 + (e >> 3)) * 8);
        st16(hop + M0_OFF + (i0 / 320) * 8192 + e * 16, (u32x4){nosent(v[0]), nosent(v[1]), nosent(v[2]), nosent(v[3])});
    }
    for (u32 i = i0; i < HOP_BYTES / 16; i += N_FILL * 256) st16(hop + (size_t)i * 16, SENT);
    for (u32 i = i0; i < (O_XN - O_FL) / 16; i += N_FILL * 256) st16(q.ws + O_FL + (size_t)i * 16, SENT);
}

extern "C" void kernel_launch(void* const* d_in, const int* in_sizes, int n_in,
                              void* d_out, int out_size, void* d_ws, size_t ws_size,
                              hipStream_t stream) {
    PrepParams q;
    q.x = (const u16*)d_in[0]; q.mem0 = (const u16*)d_in[1];
    q.wo = (const u16*)d_in[10];
    q.ws = (char*)d_ws;
    prep_kernel<<<N_LN + N_WO + N_FILL, 256, 0, stream>>>(q);
    LaParams p;
    p.ws = (char*)d_ws; p.out = (u16*)d_out; p.hop = (char*)d_ws;
    p.Wq = (const u16*)d_in[2]; p.Wk = (const u16*)d_in[3]; p.Wv = (const u16*)d_in[4]; p.Wo = (const u16*)d_in[5];
    p.wqkv = (const u16*)d_in[8]; p.wphi = (const u16*)d_in[9];
    (void)hipFuncSetAttribute((const void*)la_kernel, hipFuncAttributeMaxDynamicSharedMemorySize, LA_LDS);
    la_kernel<<<256, 256, LA_LDS, stream>>>(p);
}
```

```cpp
#include <hip/hip_runtime.h>
#include <hip/hip_bf16.h>
#include <stdint.h>
#include <type_traits>

typedef unsigned short u16;
typedef unsigned int u32;
typedef unsigned long long u64;
using bf16x8 = __attribute__((ext_vector_type(8))) short;
using f32x4 = __attribute__((ext_vector_type(4))) float;

constexpr int S_TOK = 2048, DM = 640, NH = 32, DH = 192;
constexpr int MEM = 8, SEG = 32, NSEG = S_TOK / SEG;
constexpr int HD = NH * DH;

__device__ __forceinline__ float bf2f(u16 v) { return __uint_as_float(((u32)v) << 16); }
__device__ __forceinline__ u32 pk2bf(float lo, float hi) {
    typedef __attribute__((ext_vector_type(2))) float f32x2_t;
    typedef __attribute__((ext_vector_type(2))) __bf16 bf16x2_t;
    bf16x2_t r = __builtin_convertvector((f32x2_t){lo, hi}, bf16x2_t);
    return __builtin_bit_cast(u32, r);
}
__device__ __forceinline__ float rbf(float f) { return __uint_as_float(pk2bf(f, f) << 16); }
typedef __attribute__((ext_vector_type(8))) __bf16 bf8;
typedef __attribute__((ext_vector_type(16))) __bf16 bf16v;
typedef __attribute__((ext_vector_type(4))) __bf16 bf4;
typedef __attribute__((ext_vector_type(4))) short s16x4;
using u32x4 = __attribute__((ext_vector_type(4))) u32;
using u32x2 = __attribute__((ext_vector_type(2))) u32;
typedef __attribute__((address_space(1))) u32 gu32;
typedef __attribute__((address_space(1))) u64 gu64;
#define LDS3 __attribute__((address_space(3)))

constexpr int SLOT = 16384;
#define PCSV 3
constexpr int PCS = PCSV, P_SLOT0 = 0, M_SLOT0 = 16 * PCS, S_SLOT = M_SLOT0 + 32, T_SLOT = S_SLOT + 10, N_SLOT = T_SLOT + 2;
constexpr size_t HOP_BYTES = (size_t)N_SLOT * SLOT, M0_OFF = HOP_BYTES;
constexpr size_t PX_BUF = 4 * 32 * 640, PX_BYTES = 8 * PX_BUF;
constexpr int LA_LDS = 162816 + 256;
constexpr int SPIN_CAP = 1 << 16;
#define D1I 131
#define D2I 53
#define MG1 11
#define MG2 11
#define DUP 5
#define PACE 6
#define CLAG 48
#define T_PX 26
#define T_X1 75
#define T_OB 150
#define T_TOK 350
#define ST_LAG 6
#define OB_STEP 25
#define OB_COLD 20
#define NH_ 2
#define W0D 6
#define LATEP 2
#define SYNCB 1
#define PACE2 5
#define BPEEK 390
#define PLIN 1
#define SLK 6
#define FSL 8
#define E0 110
#define HOLD 4
#define DOWN 1
#define EY 35
#define BOOST 4
#define DSH "1"
#define PAUSE 520u
#define BSL 40
#define PKK 18
#define PKT 2
#define B_LAG 65
#define ELATE 85

__device__ __forceinline__ void st16(void* p, u32x4 v) {
    asm volatile("global_store_dwordx4 %0, %1, off sc1\n\ts_nop 2" :: "v"(p), "v"(v) : "memory");
}
typedef __attribute__((address_space(1))) u32x4 gu32x4;
__device__ __forceinline__ u32x4 ld16(const char* base, u32 off, int imm) { return __builtin_amdgcn_global_load_b128((gu32x4*)(base + off + imm), "agent"); }
__device__ __forceinline__ u32 nosent(u32 v) { return min(v, 0xFFFFFFFEu); }
__device__ __forceinline__ u32 rt32() { return (u32)__builtin_amdgcn_s_memrealtime(); }
__device__ __forceinline__ void wait_rt(u32 t) {
    int d = (int)(t - rt32()) - 4;
    if (d > 4096 || d < 0) return;
    while (d > 7) { if (d > 18) __builtin_amdgcn_s_sleep(2); d = max((int)(t - rt32()) - 4, 0); }
    asm volatile("s_bitcmp1_b32 %0, 2\n\ts_cbranch_scc0 1f\n\ts_nop 15\n\ts_nop 7\n1:\n\ts_bitcmp1_b32 %0, 1\n\ts_cbranch_scc0 2f\n\ts_nop 12\n2:\n\t"
                 "s_bitcmp1_b32 %0, 0\n\ts_cbranch_scc0 3f\n\ts_nop 7\n3:" :: "s"(d) : "scc");
}
template <int N> __device__ __forceinline__ void dma_kb(const char* src, u32 lds_dst, u32 lane16) {
#pragma unroll
    for (int i = 0; i < N; i += 4) {
        u32 keep;
        if (N - i >= 4)
            asm volatile("s_mov_b32 %0, m0\n\ts_mov_b32 m0, %3\n\ts_nop 2\n\tglobal_load_lds_dwordx4 %1, %2 sc1\n\tglobal_load_lds_dwordx4 %1, %2 offset:1024 sc1\n\t"
                         "global_load_lds_dwordx4 %1, %2 offset:2048 sc1\n\tglobal_load_lds_dwordx4 %1, %2 offset:3072 sc1\n\ts_mov_b32 m0, %0"
                         : "=&s"(keep) : "v"(lane16), "s"(src + i * 1024), "s"(lds_dst + i * 1024) : "memory");
        else if (N - i == 2)
            asm volatile("s_mov_b32 %0, m0\n\ts_mov_b32 m0, %3\n\ts_nop 2\n\tglobal_load_lds_dwordx4 %1, %2 sc1\n\tglobal_load_lds_dwordx4 %1, %2 offset:1024 sc1\n\ts_mov_b32 m0, %0"
                         : "=&s"(keep) : "v"(lane16), "s"(src + i * 1024), "s"(lds_dst + i * 1024) : "memory");
        else
            asm volatile("s_mov_b32 %0, m0\n\ts_mov_b32 m0, %3\n\ts_nop 2\n\tglobal_load_lds_dwordx4 %1, %2 sc1\n\ts_mov_b32 m0, %0"
                         : "=&s"(keep) : "v"(lane16), "s"(src + i * 1024), "s"(lds_dst + i * 1024) : "memory");
    }
}
template <int OFF> __device__ __forceinline__ void dma_one(const char* src, u32 lds_dst, u32 lane16) {
    asm volatile("s_mov_b32 m0, %2\n\ts_nop 0\n\tglobal_load_lds_dwordx4 %0, %1 offset:%3 sc1" :: "v"(lane16), "s"(src), "s"(lds_dst), "n"(OFF) : "memory");
}
__device__ __forceinline__ u32 ringp(u32 a, u32 b) { return a + b; }
__device__ __forceinline__ void dma_gather(const char* src, u32 lds_dst, u32 off) {
    u32 keep;
    asm volatile("s_mov_b32 %0, m0\n\ts_mov_b32 m0, %3\n\ts_nop 2\n\tglobal_load_lds_dwordx4 %1, %2\n\ts_mov_b32 m0, %0"
                 : "=&s"(keep) : "v"(off), "s"(src), "s"(lds_dst) : "memory");
}
__device__ __forceinline__ void st16s(char* base, u32 off, u32x4 v) {
    asm volatile("s_nop 4\n\tglobal_store_dwordx4 %0, %1, %2 sc1\n\ts_nop 2" :: "v"(off), "v"(v), "s"(base) : "memory");
}
__device__ __forceinline__ void arm16(char* base, u32 off) {
    u32x4 v;
    asm volatile("v_mov_b64 %0, -1\n\tv_mov_b64 %1, -1" : "=v"(((u64*)&v)[0]), "=v"(((u64*)&v)[1]));
    st16s(base, off, v);
}
template <int B, int E, typename F> __device__ __forceinline__ void static_for(F&& f) {
    if constexpr (B < E) { f(std::integral_constant<int, B>()); static_for<B + 1, E>(f); }
}
#define CI(x) decltype(x)::value
#define RAW_BARRIER() do { asm volatile("s_waitcnt lgkmcnt(0)" ::: "memory"); __builtin_amdgcn_s_barrier(); asm volatile("" ::: "memory"); } while (0)
#define VM0() asm volatile("s_waitcnt vmcnt(0)" ::: "memory")
#define SCHEDB() __builtin_amdgcn_sched_barrier(0)
__device__ __forceinline__ bf8 lo8(const bf16v& v) { return __builtin_shufflevector(v, v, 0, 1, 2, 3, 4, 5, 6, 7); }
__device__ __forceinline__ bf8 hi8(const bf16v& v) { return __builtin_shufflevector(v, v, 8, 9, 10, 11, 12, 13, 14, 15); }
__device__ __forceinline__ bf16v cat8(bf8 a, bf8 b) { return __builtin_shufflevector(a, b, 0, 1, 2, 3, 4, 5, 6, 7, 8, 9, 10, 11, 12, 13, 14, 15); }
#define MMA_VV(c, a, b) asm volatile("v_mfma_f32_16x16x32_bf16 %0, %1, %2, %0" : "+v"(c) : "v"(a), "v"(b))
#define MMA_AV(c, a, b) asm volatile("v_mfma_f32_16x16x32_bf16 %0, %1, %2, %0" : "+v"(c) : "a"(a), "v"(b))
#define MMA_VA(c, a, b) asm volatile("v_mfma_f32_16x16x32_bf16 %0, %1, %2, %0" : "+v"(c) : "v"(a), "a"(b))
#define MMA0_VV(c, a, b) asm volatile("v_mfma_f32_16x16x32_bf16 %0, %1, %2, 0" : "=&v"(c) : "v"(a), "v"(b))
#define MMA0_AV(c, a, b) asm volatile("v_mfma_f32_16x16x32_bf16 %0, %1, %2, 0" : "=&v"(c) : "a"(a), "v"(b))
#define MMA0_VA(c, a, b) asm volatile("v_mfma_f32_16x16x32_bf16 %0, %1, %2, 0" : "=&v"(c) : "v"(a), "a"(b))
#define MMA16(c, a, b) asm volatile("v_mfma_f32_16x16x16_bf16 %0, %1, %2, %0" : "+v"(c) : "v"(a), "v"(b))
#define MMA16_0(c, a, b) asm volatile("v_mfma_f32_16x16x16_bf16 %0, %1, %2, 0" : "=&v"(c) : "v"(a), "v"(b))
#define SMM_V(c, a, b, i) asm volatile("v_smfmac_f32_16x16x64_bf16 %0, %1, %2, %3" : "+v"(c) : "v"(a), "v"(b), "v"(i))
#define SMM_A(c, a, b, i) asm volatile("v_smfmac_f32_16x16x64_bf16 %0, %1, %2, %3" : "+v"(c) : "v"(a), "a"(b), "v"(i))
#define NOPV() asm volatile("s_nop 1")
#define PIN(x) asm volatile("" : "+v"(x))
#define SETTLE() asm volatile("s_nop 7\n\ts_nop 3")
__device__ __forceinline__ bf8 pack8(f32x4 a, f32x4 b) {
    u32x4 r = {pk2bf(a[0], a[1]), pk2bf(a[2], a[3]), pk2bf(b[0], b[1]), pk2bf(b[2], b[3])};
    return __builtin_bit_cast(bf8, r);
}
__device__ __forceinline__ u32 max4(u32 m, u32x4 g) { m = max(max(m, g[0]), g[1]); return max(max(m, g[2]), g[3]); }
#define ZERO4(c) asm volatile("v_mov_b64 %0, 0\n\tv_mov_b64 %1, 0" : "=v"(((u64*)&(c))[0]), "=v"(((u64*)&(c))[1]))
__device__ __forceinline__ bf8 lds8(const char* shm, int off) { return *(const bf8*)(shm + off); }
__device__ __forceinline__ bf16v ldfrag(const char* p) { return cat8(*(const bf8*)p, *(const bf8*)(p + 1024)); }
template <int CTRL> __device__ __forceinline__ float fdpp(float x) {
    return __builtin_bit_cast(float, __builtin_amdgcn_mov_dpp(__builtin_bit_cast(int, x), CTRL, 0xf, 0xf, true));
}
__device__ __forceinline__ float halfsum(float a, float b) {
    auto r = __builtin_amdgcn_permlane32_swap(__float_as_uint(a), __float_as_uint(b), false, false);
    return __uint_as_float(r[0]) + __uint_as_float(r[1]);
}
__device__ __forceinline__ float grp_sum(float x) {
    auto t = __builtin_amdgcn_permlane16_swap(__float_as_uint(x), __float_as_uint(x), false, false);
    x = __uint_as_float(t[0]) + __uint_as_float(t[1]);
    return halfsum(x, x);
}
__device__ __forceinline__ float grp_max(float x) {
    auto t = __builtin_amdgcn_permlane16_swap(__float_as_uint(x), __float_as_uint(x), false, false);
    x = fmaxf(__uint_as_float(t[0]), __uint_as_float(t[1]));
    auto u = __builtin_amdgcn_permlane32_swap(__float_as_uint(x), __float_as_uint(x), false, false);
    return fmaxf(__uint_as_float(u[0]), __uint_as_float(u[1]));
}
__device__ __forceinline__ float bflo(u32 v) { return __uint_as_float(v << 16); }
__device__ __forceinline__ float bfhi(u32 v) { return __uint_as_float(v & 0xffff0000u); }
__device__ __forceinline__ float ln_scale(float var) { return __builtin_amdgcn_rcpf(rbf(__builtin_amdgcn_sqrtf(rbf(rbf(var) + 1e-5f)))); }
__device__ __forceinline__ u32 ln_pair(u32 v, float m, float rs) { const u32 d = pk2bf(bflo(v) - m, bfhi(v) - m); return pk2bf(bflo(d) * rs, bfhi(d) * rs); }
__device__ __forceinline__ void st4_lane0(char* base, u32 v) {
    u32 z, d;
    base = (char*)(((u64)(u32)__builtin_amdgcn_readfirstlane((u32)((u64)base >> 32)) << 32) | (u64)(u32)__builtin_amdgcn_readfirstlane((u32)(u64)base));
    asm volatile("s_mov_b64 exec, 1\n\tv_mov_b32 %0, 0\n\tv_mov_b32 %1, %3\n\ts_nop 1\n\tglobal_store_dword %0, %1, %2 sc1\n\ts_mov_b64 exec, -1" : "=&v"(z), "=&v"(d) : "s"(base), "s"(v) : "memory");
}
__device__ __forceinline__ void stamp2(char* hop, const int line) {
    const u32 t = nosent(rt32());
    st4_lane0(hop + (T_SLOT + 1) * SLOT + line * 128, t); st4_lane0(hop + (T_SLOT + 1) * SLOT + 8192 + line * 128, t);
}
__device__ __forceinline__ int smax(int a, int b) { int r; asm volatile("s_max_i32 %0, %1, %2" : "=s"(r) : "s"(a), "s"(b) : "scc"); return r; }
__device__ __forceinline__ int track(int d, int sample, int down) {
    int e, a, b;
    asm("s_sub_i32 %0, %3, %4\n\ts_cmp_gt_i32 %3, 0\n\ts_cselect_b32 %0, %0, 0\n\ts_min_i32 %1, %0, %6\n\ts_ashr_i32 %2, %0, " DSH "\n\ts_max_i32 %2, %2, %5\n\t"
        "s_min_i32 %2, %2, 0\n\ts_max_i32 %1, %1, %2" : "=&s"(e), "=&s"(a), "=&s"(b) : "s"(sample), "s"(d), "s"(-down), "n"(DUP) : "scc");
    return d + a;
}
__device__ __forceinline__ u32 sched_peek(const char* p) { return __hip_atomic_load((const gu32*)p, __ATOMIC_RELAXED, __HIP_MEMORY_SCOPE_AGENT); }
template <int SL = 3> __device__ __forceinline__ u32 sched_get(u32 v, const char* p, int& cap) {
    for (int spin = 0; v == ~0u; ++spin) {
        if (spin >= cap) { cap = 1; break; }
        __builtin_amdgcn_s_sleep(SL);
        v = sched_peek(p);
    }
    return __builtin_amdgcn_readfirstlane(v);
}

#define TRDECL
#define TR(i)
#define TRV(i, v)
#define TRFLUSH(role)
#define TRC(i)
constexpr size_t ACT = (size_t)S_TOK * DM * 2;
constexpr size_t O_DBG = 0, O_CTL = 2u << 20, O_FL = O_CTL + 4096, O_PX = O_FL + 640 * 128, O_ST = O_PX + PX_BYTES, O_X1 = O_ST + 128 * 1280,
                 O_OB = O_X1 + 2 * ACT, O_XN = O_OB + 2 * ACT, O_XR = O_XN + 2 * ACT, O_WO = O_XR + 2 * ACT,
                 O_WVO = O_WO + 10 * 81920, O_HOP = O_WVO + (size_t)80 * 4 * 40 * 2048, WS_BYTES = O_HOP + (4u << 20);
#define TT(role, i)
#define CNT(i)
struct LaParams { char* ws; u16* out; char* hop; const u16* Wq; const u16* Wk; const u16* Wv; const u16* Wo; const u16* wqkv; const u16* wphi; };
__device__ __forceinline__ int kperm(int g, int e) { return 16 * (g >> 1) + 8 * ((e & 3) >> 1) + 4 * (g & 1) + 2 * (e >> 2) + (e & 1); }
__device__ __forceinline__ bf8 tr8(u32 a, u32 b) {
    const s16x4 lo = __builtin_amdgcn_ds_read_tr16_b64_v4i16((LDS3 s16x4*)(uintptr_t)a), hi = __builtin_amdgcn_ds_read_tr16_b64_v4i16((LDS3 s16x4*)(uintptr_t)b);
    return __builtin_bit_cast(bf8, __builtin_shufflevector(lo, hi, 0, 1, 2, 3, 4, 5, 6, 7));
}
__device__ __forceinline__ char* wsp(const LaParams& p) { char* b = p.ws; asm volatile("" : "+s"(b)); return b; }
template <int N, int SL = 4> __device__ __forceinline__ void wait_flags(const char* f, const u32 (&off)[N], int& cap) {
    for (int spin = 0;; ++spin) {
        u32 m = 0;
#pragma unroll
        for (int i = 0; i < N; ++i) m = max(m, sched_peek(f + off[i]));
        if (!__any(m == ~0u)) break;
        if (spin >= cap) { cap = 1; break; }
        __builtin_amdgcn_s_sleep(SL);
    }
    asm volatile("" ::: "memory");
}
__device__ __forceinline__ void wait_all_tokens(const LaParams& p, u32 lane, int& cap) {
    asm volatile("" : "+v"(lane));
    const u32 off[3] = {lane * 128, lane * 128 + 8192, min(lane + 128, 159u) * 128};
    wait_flags<3>(wsp(p) + O_FL + 192 * 128, off, cap);
}
__device__ __forceinline__ void wait_first_tokens(const LaParams& p, const int set, u32 lane, int& cap) {
    asm volatile("" : "+v"(lane));
    const u32 off[1] = {(set * 20 + min(lane, 39u) + (lane >= 20 ? 20 : 0)) * 128};
    wait_flags<1>(wsp(p) + O_FL, off, cap);
}
__device__ __forceinline__ float row16_sum(float x) { x += fdpp<0x128>(x); x += fdpp<0x124>(x); x += fdpp<0x122>(x); return x + fdpp<0x121>(x); }
__device__ __forceinline__ int get_side(const LaParams& p, int& cap) { return sched_get(sched_peek(wsp(p) + O_FL + 384 * 128), wsp(p) + O_FL + 384 * 128, cap) & 1; }
__device__ __forceinline__ char* align2m(char* p) { return p + ((size_t)(-(intptr_t)p) & ((2u << 20) - 1)); }

constexpr int Q_XA = 0, Q_PART = 20480, Q_TR = 48640, Q_MK = 54784, Q_WL = 55808, Q_ST = 162304, Q_PS = 44;

#define DPP1(op, ror) op "_dpp %0, %0, %0 row_ror:" ror " row_mask:0xf bank_mask:0xf\n\t"
#define DPP2(op, ror) DPP1(op, ror) op "_dpp %1, %1, %1 row_ror:" ror " row_mask:0xf bank_mask:0xf\n\ts_nop 0\n\t"
__device__ __forceinline__ void st16m(char* base, u32 off, u32x4 v, u64 mask) {
    asm volatile("s_mov_b64 exec, %3\n\ts_nop 3\n\tglobal_store_dwordx4 %0, %1, %2 sc1\n\ts_mov_b64 exec, -1\n\ts_nop 1" :: "v"(off), "v"(v), "s"(base), "s"(mask) : "memory");
}
__device__ __forceinline__ void st16f(char* base, u32 off, u32x4 v, u64 mask) {
    asm volatile("s_mov_b64 exec, %3\n\tglobal_store_dwordx4 %0, %1, %2 sc1\n\ts_mov_b64 exec, -1\n\ts_nop 1" :: "v"(off), "v"(v), "s"(base), "s"(mask) : "memory");
}
__device__ __forceinline__ void st16f64(char* base, u32 off, u32x4 v, u64 mask) {
    asm volatile("s_mov_b64 exec, %3\n\tglobal_store_dwordx4 %0, %1, %2 offset:64 sc1\n\ts_mov_b64 exec, -1\n\ts_nop 1" :: "v"(off), "v"(v), "s"(base), "s"(mask) : "memory");
}
__device__ __forceinline__ void st32(char* base, u32 off, u32x4 a, u32x4 b) {
    asm volatile("s_nop 4\n\tglobal_store_dwordx4 %0, %1, %3\n\tglobal_store_dwordx4 %0, %2, %3 offset:16\n\ts_nop 2" :: "v"(off), "v"(a), "v"(b), "s"(base) : "memory");
}
__device__ __forceinline__ void st8m(char* base, u32 off, u32x2 v, u64 mask) {
    asm volatile("s_mov_b64 exec, %3\n\ts_nop 3\n\tglobal_store_dwordx2 %0, %1, %2 sc1\n\ts_mov_b64 exec, -1" :: "v"(off), "v"(v), "s"(base), "s"(mask) : "memory");
}
__device__ __forceinline__ u32 pk2bf_abs(float lo, float hi) {
    u32 r;
    asm("v_cvt_pk_bf16_f32 %0, |%1|, |%2|" : "=v"(r) : "v"(lo), "v"(hi));
    return r;
}
__device__ __forceinline__ u32 p_line(const int h, const int piece, const u32 gap) {
    u32 a = (h >> 4) * gap + ((h >> 1) & 7) * 1024 + (h & 1) * 128, b = 2 * gap + h * 128;
    asm volatile("" : "+s"(a), "+s"(b));
    return piece < 4 ? a + piece * 256 : b;
}
constexpr float SM_C = 0.07216878364870322f * 1.4426950408889634f, NEG_BIG = -0x1p100f;
constexpr u64 SM_MASK = 0x0155015501550155ull;
template <typename MID> __device__ __forceinline__ void softmax_mem(const float* p, char* dst, char* dst2, const u32 off, MID&& mid) {
    f32x4 a[4];
#pragma unroll
    for (int i = 0; i < 4; ++i) a[i] = *(const f32x4*)(p + i * 40 * Q_PS);
    SCHEDB(); mid(); SCHEDB();
    f32x4 x = (a[0] + a[1]) + (a[2] + a[3]);
    float ml, mx = fmaxf(fmaxf(x[0], x[1]), fmaxf(x[2], x[3])), e0, e1, e2, e3;
    asm volatile("v_mul_f32 %5, %10, %0\n\ts_nop 0\n\t"
        DPP1("v_max_f32", "8") "v_fma_f32 %1, %6, %10, -%5\n\tv_fma_f32 %2, %7, %10, -%5\n\t"
        DPP1("v_max_f32", "4") "v_fma_f32 %3, %8, %10, -%5\n\tv_fma_f32 %4, %9, %10, -%5\n\t"
        DPP1("v_max_f32", "2") "v_exp_f32 %1, %1\n\tv_exp_f32 %2, %2\n\t"
        DPP1("v_max_f32", "1") "v_exp_f32 %3, %3\n\tv_fma_f32 %5, -%0, %10, %5\n\tv_exp_f32 %4, %4\n\tv_add_f32 %0, %1, %2\n\tv_exp_f32 %5, %5\n\tv_add_f32 %0, %0, %3\n\t"
        "v_add_f32 %0, %0, %4\n\tv_mul_f32 %0, %0, %5\n\tv_mul_f32 %1, %1, %5\n\tv_mul_f32 %2, %2, %5\n\t"
        DPP1("v_add_f32", "8") "v_mul_f32 %3, %3, %5\n\tv_mul_f32 %4, %4, %5\n\t" DPP1("v_add_f32", "4") "s_nop 1\n\t"
        DPP1("v_add_f32", "2") "s_nop 1\n\t" DPP1("v_add_f32", "1") "v_rcp_f32 %0, %0\n\ts_nop 0\n\t"
        "v_mul_f32 %1, %1, %0\n\tv_mul_f32 %2, %2, %0\n\tv_mul_f32 %3, %3, %0\n\tv_mul_f32 %4, %4, %0"
        : "+v"(mx), "=&v"(e0), "=&v"(e1), "=&v"(e2), "=&v"(e3), "=&v"(ml) : "v"(x[0]), "v"(x[1]), "v"(x[2]), "v"(x[3]), "s"(SM_C) : "memory");
    u32x4 o;
    asm volatile("v_cvt_pk_bf16_f32 %4, |%4|, |%6|\n\tv_cvt_pk_bf16_f32 %5, |%5|, |%7|\n\ts_nop 0\n\t"
                 "v_mov_b32_dpp %0, %4 quad_perm:[0,0,2,2] row_mask:0xf bank_mask:0xf\n\tv_mov_b32_dpp %1, %5 quad_perm:[0,0,2,2] row_mask:0xf bank_mask:0xf\n\t"
                 "v_mov_b32_dpp %2, %4 quad_perm:[1,1,3,3] row_mask:0xf bank_mask:0xf\n\tv_mov_b32_dpp %3, %5 quad_perm:[1,1,3,3] row_mask:0xf bank_mask:0xf"
                 : "=&v"(o[0]), "=&v"(o[1]), "=&v"(o[2]), "=&v"(o[3]), "+v"(e0), "+v"(e2) : "v"(e1), "v"(e3), "s"(dst), "s"(dst2));
    asm volatile("s_mov_b64 exec, %4\n\tglobal_store_dwordx4 %0, %1, %2 sc1\n\tglobal_store_dwordx4 %0, %1, %3 sc1\n\ts_mov_b64 exec, -1\n\ts_nop 1"
                 :: "v"(off), "v"(o), "s"(dst), "s"(dst2), "s"(0x03ff03ff03ff03ffull) : "memory");
}
struct SmTok {
    float x[2][4], b1[2][4], b2[2][4], b3[2][4], mx[2], sm[2], t0, t1;
    u32 d00, d01, d10, d11;
    static constexpr int N = 88;
    __device__ __forceinline__ void load(const float* p, const int j) {
        const int n = j & 1, k = j >> 1;
        const f32x4 a = *(const f32x4*)(p + (k * 40 + n * 4) * Q_PS);
#pragma unroll
        for (int i = 0; i < 4; ++i) (k == 0 ? x : (k == 1 ? b1 : (k == 2 ? b2 : b3)))[n][i] = a[i];
    }
    template <int I> __device__ __forceinline__ void piece(char* dst, const u32 off) {
        constexpr int n = I & 1, k = (I >> 1) - 12;
        float& t = n ? t1 : t0;
#define ROR(op, ror, v) asm volatile(op "_dpp %0, %0, %0 row_ror:" ror " row_mask:0xf bank_mask:0xf" : "+v"(v))
        if constexpr (k < -8) asm volatile("v_add_f32 %0, %0, %1" : "+v"(x[n][k + 12]) : "v"(b1[n][k + 12]));
        else if constexpr (k < -4) asm volatile("v_add_f32 %0, %0, %1" : "+v"(b2[n][k + 8]) : "v"(b3[n][k + 8]));
        else if constexpr (k < 0) asm volatile("v_add_f32 %0, %0, %1" : "+v"(x[n][k + 4]) : "v"(b2[n][k + 4]));
        else if constexpr (k == 0) asm volatile("v_max_f32 %0, %1, %2" : "=v"(mx[n]) : "v"(x[n][0]), "v"(x[n][1]));
        else if constexpr (k == 1) asm volatile("v_max3_f32 %0, %0, %1, %2" : "+v"(mx[n]) : "v"(x[n][2]), "v"(x[n][3]));
        else if constexpr (k == 2) ROR("v_max_f32", "8", mx[n]);
        else if constexpr (k == 3) ROR("v_max_f32", "4", mx[n]);
        else if constexpr (k == 4) ROR("v_max_f32", "2", mx[n]);
        else if constexpr (k == 5) ROR("v_max_f32", "1", mx[n]);
        else if constexpr (k == 6) { mx[n] *= SM_C; PIN(mx[n]); }
        else if constexpr (k < 11) { x[n][k - 7] = fmaf(x[n][k - 7], SM_C, -mx[n]); PIN(x[n][k - 7]); }
        else if constexpr (k < 15) asm volatile("v_exp_f32 %0, %0" : "+v"(x[n][k - 11]));
        else if constexpr (k == 15) asm volatile("v_add_f32 %0, %1, %2" : "=v"(sm[n]) : "v"(x[n][0]), "v"(x[n][1]));
        else if constexpr (k == 16) asm volatile("v_add_f32 %0, %1, %2" : "=v"(t) : "v"(x[n][2]), "v"(x[n][3]));
        else if constexpr (k == 17) asm volatile("v_add_f32 %0, %0, %1" : "+v"(sm[n]) : "v"(t));
        else if constexpr (k == 18) ROR("v_add_f32", "8", sm[n]);
        else if constexpr (k == 19) ROR("v_add_f32", "4", sm[n]);
        else if constexpr (k == 20) ROR("v_add_f32", "2", sm[n]);
        else if constexpr (k == 21) ROR("v_add_f32", "1", sm[n]);
        else if constexpr (k == 22) asm volatile("v_rcp_f32 %0, %0" : "+v"(sm[n]));
        else if constexpr (k < 27) asm volatile("v_mul_f32 %0, %0, %1" : "+v"(x[n][k - 23]) : "v"(sm[n]));
        else if constexpr (k == 27) asm volatile("v_cvt_pk_bf16_f32 %0, |%1|, |%2|" : "=v"(n ? d10 : d00) : "v"(x[n][0]), "v"(x[n][1]));
        else if constexpr (k == 28) asm volatile("v_cvt_pk_bf16_f32 %0, |%1|, |%2|" : "=v"(n ? d11 : d01) : "v"(x[n][2]), "v"(x[n][3]));
        else if constexpr (I == 84 || I == 87) {
            u32x4 o;
            asm volatile("v_mov_b32 %0, %4\n\tv_mov_b32 %1, %5\n\tv_mov_b32_dpp %2, %4 quad_perm:[1,0,3,2] row_mask:0xf bank_mask:0xf\n\tv_mov_b32_dpp %3, %5 quad_perm:[1,0,3,2] row_mask:0xf bank_mask:0xf"
                         : "=&v"(o[0]), "=&v"(o[1]), "=&v"(o[2]), "=&v"(o[3]) : "v"(n ? d10 : d00), "v"(n ? d11 : d01), "s"(dst));
            if constexpr (n == 0) st16f(dst, off, o, SM_MASK); else st16f64(dst, off, o, SM_MASK);
        }
    }
};

__host__ __device__ constexpr int qk_slot(int T, int ks) { return ks < 5 ? ks * 6 + T : (T < 4 ? 30 + (T >> 1) * 10 + (ks - 5) * 2 + (T & 1) : 25 + T * 5 + ks); }
constexpr int NLF = 13;
__host__ __device__ constexpr int lds_slot(int n) { constexpr int t[NLF] = {2, 6, 11, 15, 20, 24, 29, 33, 37, 41, 45, 49, 55}; return t[n]; }
__host__ __device__ constexpr int slot_lds(int sl) { for (int n = 0; n < NLF; ++n) if (lds_slot(n) == sl) return n; return -1; }
__host__ __device__ constexpr int slot_frag(int sl) { if (slot_lds(sl) >= 0) return 48 + slot_lds(sl); int n = 0; for (int i = 0; i < NLF; ++i) n += lds_slot(i) < sl; return sl - n; }
__host__ __device__ constexpr int qk_frag(int T, int ks) { return slot_frag(qk_slot(T, ks)); }
__host__ __device__ constexpr int slot_T(int sl) { return sl < 30 ? sl % 6 : (sl < 50 ? ((sl - 30) / 10) * 2 + (sl & 1) : (sl - 30) / 5); }
__host__ __device__ constexpr int slot_ks(int sl) { return sl < 30 ? sl / 6 : (sl < 50 ? 5 + ((sl - 30) % 10) / 2 : 5 + sl % 5); }
__host__ __device__ constexpr int lds_rank(int T, int ks) { int n = 0; for (int t = 0; t < T; ++t) n += qk_frag(t, ks) >= 48; return n; }
__host__ __device__ constexpr int lds_nth(int ks, int n) { for (int t = 0; t < 6; ++t) if (qk_frag(t, ks) >= 48 && lds_rank(t, ks) == n) return qk_frag(t, ks) - 48; return -1; }

__device__ __forceinline__ void qk_role(const LaParams& p, const int set, const int h, char* shm) {
    const int lane = threadIdx.x & 63, w = __builtin_amdgcn_readfirstlane(threadIdx.x >> 6);
    const int fr = lane & 15, g = lane >> 4;
    const u32 lds0 = (u32)(uintptr_t)(LDS3 char*)shm;
    const int sidx = (lane & 8) ? 0xEEEE : 0x4444;
    int cap = SPIN_CAP;
    char* mine = p.hop;
    int sch = (S_SLOT + w) * SLOT;
    int phys = 0;
    char* px = (wsp(p) + O_PX);
    const char* xn = (wsp(p) + O_XN);
    char* wl = shm + Q_WL + w * (NLF * 2048) + lane * 16;
    const char* xa = shm + Q_XA + lane * 16;

    bf16v WA[32], WV[28 - NLF];
    {
        bf16v WT[NLF];
        u32 vo[6], rd[3];
#pragma unroll
        for (int i = 0; i < 6; ++i) {
            const int id = i * 64 + lane, r = w * 16 + id / 24;
            vo[i] = (32 * (r >> 5) + kperm((r >> 2) & 3, ((r >> 4) & 1) * 4 + (r & 3))) * (HD * 2) + ((id % 24) ^ (r & 6)) * 16;
        }
#pragma unroll
        for (int i = 0; i < 3; ++i) rd[i] = lds0 + (g * 4 + (fr >> 2)) * 384 + ((w * 3 + i) ^ ((g & 1) * 2 + (fr >> 3))) * 32 + (fr & 3) * 8;
        auto issue = [&](const int j) {
            const char* src = (const char*)((j & 1) ? p.Wk : p.Wq) + (size_t)(j >> 1) * 64 * HD * 2 + h * 384;
#pragma unroll
            for (int i = 0; i < 6; ++i) dma_gather(src, lds0 + (j % 6) * 24576 + w * 6144 + i * 1024, vo[i]);
        };
#pragma unroll
        for (int j = 0; j < 5; ++j) issue(j);
        static_for<0, 20>([&](auto J) {
            constexpr int j = CI(J);
            asm volatile("s_waitcnt vmcnt(%0)" :: "n"((19 - j < 4 ? 19 - j : 4) * 6) : "memory");
            RAW_BARRIER();
            if constexpr (j + 5 < 20) issue(j + 5);
            static_for<0, 3>([&](auto PR) {
                constexpr int id = qk_frag(2 * CI(PR) + (j & 1), j >> 1);
                u32 a = rd[CI(PR)] + (j % 6) * 24576;
                asm volatile("" : "+v"(a));
                const bf16v f = cat8(tr8(a, a + 16 * 384), tr8(a + 32 * 384, a + 48 * 384));
                if constexpr (id < 32) WA[id] = f; else if constexpr (id < 48) WV[id - 32] = f; else WT[id - 48] = f;
            });
        });
        RAW_BARRIER();
        dma_kb<5>(xn + (size_t)set * 40960 + w * 5120, lds0 + Q_XA + w * 5120, lane * 16);
#pragma unroll
        for (int i = 0; i < NLF; ++i) { *(bf8*)(wl + i * 2048) = lo8(WT[i]); *(bf8*)(wl + i * 2048 + 1024) = hi8(WT[i]); }
    }
    TRDECL;
    bf8 QX[2], KX[2];
    u32x2 QY[2], KY[2];
    u32 peek = ~0u;
    int d1 = D1I, d2 = D2I, k1 = D1I + MG1;
    if (w == 3) *(u64*)(shm + Q_ST + lane * 8) = lane < 63 ? 0 : (u64)D1I | ((u64)D2I << 32);
    const int lead = __builtin_amdgcn_readfirstlane((u32)(h * 4 + (w ^ 2) - 1) >> 31);
    SmTok smt;
    u32 took = 0;
    u32 xoff = p_line(h, fr >> 1, 8192) + g * 16, ao1 = p_line(h, min(lane >> 3, 4), 8192) + (lane & 7) * 16, ao0;
    {
        u32 a = (h >> 4) * SLOT + (h & 15) * 512, b = 2 * SLOT + h * 256;
        asm volatile("" : "+s"(a), "+s"(b));
        ao0 = PLIN ? p_line(h, min(lane >> 3, 4), SLOT) + (lane & 7) * 16 : (lane < 32 ? a + lane * 16 : b + (lane & 15) * 16);
    }
    asm volatile("" : "+v"(xoff), "+v"(ao0), "+v"(ao1));
    auto rearm = [&](const int rot, const int i, char*& a, u32x4& v) {
        if (i == 0) asm volatile("v_mov_b64 %0, -1\n\tv_mov_b64 %1, -1" : "=v"(((u64*)&v)[0]), "=v"(((u64*)&v)[1]));
        if (i == 1) { a = mine + (P_SLOT0 + (((rot + 3) & 3) * 4 + w) * PCS) * SLOT; asm volatile("" : "+s"(a)); }
        if (i == 5) { a = px + (size_t)(set * 4 + ((rot + 2) & 3)) * PX_BUF + w * 20480; asm volatile("" : "+s"(a)); }
        if (i == 3 || i == 8) __builtin_amdgcn_global_store_b128((gu32x4*)(a + (i == 3 ? ao0 : ao1)), v, "agent");
    };
    const char* mb0;
    const char* mb1;
    char* pdst;
    char* pdst2;
    int soft;
    u32 ytime;
    f32x4 tmask;
    auto next = [&](const int s, const int i) {
        const int rot = (s >> 1) & 3;
        if (i == 0) { mb0 = mine + (M_SLOT0 + rot * 8 + 2 * w) * SLOT + 2048; asm volatile("" : "+s"(mb0)); }
        if (i == 1) { mb1 = mb0 + SLOT; soft = (u32)((w - 2) & (s - (NSEG - 1))) >> 31; asm volatile("" : "+s"(mb1), "+s"(soft)); }
        if (i == 2) { pdst = mine + (P_SLOT0 + rot * 4 * PCS) * SLOT; pdst2 = pdst + 2 * PCS * SLOT; asm volatile("" : "+s"(pdst), "+s"(pdst2)); }
        if (i == 3) tmask = *(const f32x4*)(xa + Q_MK);
    };
    auto fetch_stats = [&]() {
        if (__builtin_expect(lead != 0, 0)) {
                u32 lo = lane;
                asm volatile("" : "+v"(lo));
                lo *= 128;
                u32 keep;
                asm volatile("s_mov_b32 %0, m0\n\ts_mov_b32 m0, %4\n\ts_nop 2\n\tglobal_load_lds_dword %1, %2 sc1\n\ts_add_u32 m0, m0, 256\n\ts_lshr_b64 exec, exec, 16\n\tglobal_load_lds_dword %1, %3 sc1\n\ts_mov_b64 exec, -1\n\ts_mov_b32 m0, %0"
                             : "=&s"(keep) : "v"(lo), "s"(mine + T_SLOT * SLOT), "s"(mine + (T_SLOT + 1) * SLOT), "s"(lds0 + Q_ST) : "memory", "scc");
        }
    };
    int hold = 0;
    u32 ylast = 0;
    auto xproj = [&](const int snext, const int sprev, auto SOFT, char* xdst) {
        f32x4 acc[6][2];
        TR(8);
        const char* xs;
        u32 xl;
        bf8 x0 = *(const bf8*)(xa), x1 = *(const bf8*)(xa + 1024), lw[2];
        lw[0] = *(const bf8*)(wl);
        if constexpr (CI(SOFT)) RAW_BARRIER();
        auto pack = [&](const int T, const int tt) {
            PIN(acc[T][tt]);
            const u32 a = pk2bf(acc[T][tt][0], acc[T][tt][1]), b = pk2bf(acc[T][tt][2], acc[T][tt][3]);
            if (T < 4) { u32* d = (u32*)((T & 1) ? &KX[tt] : &QX[tt]) + (T & 2); d[0] = a; d[1] = b; }
            else if (T == 4) QY[tt] = (u32x2){a, b}; else KY[tt] = (u32x2){a, b};
            SCHEDB();
        };
        char* ao;
        u32x4 sent;
        static_for<0, 20>([&](auto K) {
            constexpr int k = CI(K), l0 = lds_nth(((k + 1) >> 1) % 10, 0), l1 = lds_nth(((k + 1) >> 1) % 10, 1);
            bf8 n0, n1, nw[2];
            if constexpr (k == 9) {
                TR(9);
                if constexpr (CI(SOFT)) asm volatile("s_waitcnt vmcnt(4)" ::: "memory"); else VM0();
                RAW_BARRIER();
                TR(10);
            }
            static_for<0, 6>([&](auto TT) {
                constexpr int T = CI(TT), id = qk_frag(T, k >> 1), rk = lds_rank(T, k >> 1);
                if constexpr (k < 19) {
                    if constexpr (T == 0) n0 = *(const bf8*)(xa + (k + 1) * 2048);
                    if constexpr (T == 1) n1 = *(const bf8*)(xa + (k + 1) * 2048 + 1024);
                    if constexpr (T == 2 && l0 >= 0) nw[0] = *(const bf8*)(wl + l0 * 2048 + ((k + 1) & 1) * 1024);
                    if constexpr (T == 3 && l1 >= 0) nw[1] = *(const bf8*)(wl + l1 * 2048 + ((k + 1) & 1) * 1024);
                }
                if constexpr (k == 0 && T == 1) { xs = xn + (size_t)(snext - 2) * 40960 + 20480 + w * 5120; xl = lds0 + Q_XA + 20480 + w * 5120; asm volatile("" : "+s"(xs), "+s"(xl)); }
                if constexpr (k == 9 && T == 1) { xs = xn + (size_t)min(snext, NSEG - 1) * 40960 + w * 5120; xl -= 20480 + 4096; asm volatile("" : "+s"(xs), "+s"(xl)); }
                if constexpr ((k == 2 || k == 13) && T == 1) { xs += 4096; xl += 4096; asm volatile("" : "+s"(xs), "+s"(xl)); }
                if constexpr (k < 3 && (T == 3 || T == 5) && 2 * k + (T == 5) < 5) dma_one<((2 * k + (T == 5)) & 3) * 1024>(xs, xl, lane * 16);
                if constexpr (k >= 9 && k < 14 && T == 4) dma_one<((k - 9) & 3) * 1024>(xs, xl, lane * 16);
                if constexpr (CI(SOFT) && k == 10 && T == 1) {
                    if (w == 1) st4_lane0(mine + (T_SLOT + 1) * SLOT + (16 + h) * 128, took);
                }
                if constexpr (CI(SOFT) && k == 16 && T == 4) fetch_stats();
                if constexpr (CI(SOFT) && k == PKK && T == PKT) peek = sched_peek(mine + sch + sprev * 128 + 4);
                if constexpr (CI(SOFT) && k == 15 && T >= 3) next(snext - 2, T - 3);
                if constexpr (CI(SOFT) && k == 19 && T == 3) next(snext - 2, 3);
                SCHEDB();
                static_for<0, 2>([&](auto U) {
                    constexpr int u = CI(U);
                    const bf8 xx = u ? x1 : x0;
                    if constexpr (id < 32) {
                        const bf8 wf = (k & 1) ? hi8(WA[id]) : lo8(WA[id]);
                        if constexpr (k == 0) MMA0_AV(acc[T][u], wf, xx); else MMA_AV(acc[T][u], wf, xx);
                    } else {
                        bf8 wf;
                        if constexpr (id < 48) wf = (k & 1) ? hi8(WV[id - 32]) : lo8(WV[id - 32]); else wf = lw[rk];
                        if constexpr (k == 0) MMA0_VV(acc[T][u], wf, xx); else MMA_VV(acc[T][u], wf, xx);
                    }
                    SCHEDB();
                    if constexpr (CI(SOFT) && k * 12 + T * 2 + u < SmTok::N) smt.template piece<k * 12 + T * 2 + u>(xdst, xoff);
                    if constexpr (CI(SOFT) && k == 8) rearm((snext >> 1) + 2, T * 2 + u, ao, sent);
                    if constexpr (k == 19 && T >= 2) pack(T - 2, u);
                    SCHEDB();
                });
            });
            if constexpr (k < 19) { x0 = n0; x1 = n1; if constexpr (l0 >= 0) lw[0] = nw[0]; if constexpr (l1 >= 0) lw[1] = nw[1]; }
        });
        TR(11);
        pack(4, 0); pack(4, 1);
        asm volatile("s_nop 3");
        pack(5, 0); pack(5, 1);
        if constexpr (CI(SOFT)) ytime = __builtin_amdgcn_readfirstlane(peek);
    };
    {
        const f32x4 neg = {NEG_BIG, NEG_BIG, NEG_BIG, NEG_BIG};
        *(f32x4*)((float*)(shm + Q_PART) + (w * 40 + 8 + fr) * Q_PS + g * 8 + 4) = neg;
        if (lane < 40) *(f32x4*)((float*)(shm + Q_PART) + (w * 40 + lane) * Q_PS + 40) = neg;
        *(f32x4*)(shm + Q_MK + lane * 16) = (f32x4){4 * g > fr ? NEG_BIG : 0.f, 4 * g + 1 > fr ? NEG_BIG : 0.f, 4 * g + 2 > fr ? NEG_BIG : 0.f, 4 * g + 3 > fr ? NEG_BIG : 0.f};
    }
    float* pw0 = (float*)(shm + Q_PART) + (w * 40 + (fr & 7)) * Q_PS + g * 8 + (fr >> 3) * 4;
    float* pw2 = fr < 8 && g >= 2 ? (float*)(shm + Q_PART) + (w * 40 + fr) * Q_PS + 24 + g * 4 : (float*)(shm + Q_TR + w * 1536) + lane * 4;
    const float* prd = (const float*)(shm + Q_PART) + ((w & 1) * 4 + g) * Q_PS + min(fr, 10) * 4;
    const float* prt = (const float*)(shm + Q_PART) + (8 + 8 * w + g) * Q_PS + min(fr, 10) * 4;
    u32 trw = lds0 + Q_TR + w * 1536 + lane * 8, trr = lds0 + Q_TR + w * 1536 + (fr & 3) * 128 + (4 * g + (fr >> 2)) * 8;
    u32 trh = lds0 + Q_TR + w * 1536 + 1024 + (lane & 31) * 8 + (lane >> 5) * 4;
    u32 pw3 = lds0 + Q_PART + ((w * 40 + 8 + (lane >> 5) * 16 + fr) * Q_PS + 32 + (g & 1) * 4) * 4;
    u32 pp = lds0 + Q_PART + ((w * 40 + 8 + fr) * Q_PS + g * 8) * 4;
    asm volatile("" : "+v"(trw), "+v"(trr), "+v"(trh), "+v"(pw3), "+v"(pp));
    u32 pa = (h >> 4) * SLOT + (h & 15) * 512 + (w & 1) * 256, pbm = 2 * SLOT + h * 256 + (w & 1) * 128;
    asm volatile("" : "+s"(pa), "+s"(pbm));
    const u32 poff = (PLIN ? p_line(h, fr >> 1, SLOT) + (w & 1) * 64 : (fr < 8 ? pa + (fr >> 1) * 64 : pbm)) + g * 16 + (fr & 1) * (PCS * SLOT);

    for (int layer = 0; layer < 2; ++layer) {
    if (layer > 0) {
        if (w == 0) {
            __builtin_amdgcn_s_sleep(127);
            wait_rt(sched_get(~0u, mine + (T_SLOT + 1) * SLOT + 50 * 128, cap) + T_TOK);
            wait_first_tokens(p, set, lane, cap);
        }
        RAW_BARRIER();
        sch += 5 * SLOT; xn += ACT;
        TT(set * 128 + h, 1);
        dma_kb<5>(xn + (size_t)set * 40960 + w * 5120, lds0 + Q_XA + w * 5120, lane * 16);
    }
    VM0(); RAW_BARRIER();
    TT(set * 128 + h, 2);
    xproj(set + 2, set - 1, std::integral_constant<int, 0>(), nullptr);
    if (layer == 0) { phys = set ^ get_side(p, cap); mine += phys * 8192; }
    next(set, 0);
    if (set == 0) mb0 = p.hop + M0_OFF + phys * 8192 + 2 * w * SLOT + 2048;
    next(set, 1); next(set, 2); next(set, 3);
    ytime = set ? ~0u : rt32();
    TT(set * 128 + h, 3);
    for (int s = set; s < NSEG; s += 2) {
        const int rot = (s >> 1) & 3;
        TR(0);
        if (__builtin_expect(ytime == ~0u, 0)) ytime = sched_get(~0u, mine + sch + (s - 1) * 128 + 4, cap);
        f32x4 t[3];
        t[0] = tmask;
        PIN(t[0]); PIN(QX[0]); PIN(QX[1]); PIN(KX[0]); PIN(KX[1]); PIN(QY[0]); PIN(QY[1]); PIN(KY[0]); PIN(KY[1]);
        int tight = 0;
        if (w == 0) wait_rt(ytime);
        else {
            const int sl = (int)(ytime - rt32());
            tight = SLK - smax(-smax(-sl, -SLK), 0);
        }
        __builtin_amdgcn_s_barrier();
        TR(1); TRV(7, ytime);
        f32x4 acc[6];
        u32x4 A[10];
        bf8 Ll[2], Lh[2];
        s16x4 F[4];
        u32 r0[4], r1[4], d[2];
        float v[4];
        int tries = 0;
        auto fin = [&](const int pr, const int i) {
            if (i == 0) { PIN(acc[2 * pr]); PIN(acc[2 * pr + 1]); }
            if (i < 4) { auto r = __builtin_amdgcn_permlane32_swap(__float_as_uint(acc[2 * pr][i]), __float_as_uint(acc[2 * pr + 1][i]), false, false); r0[i] = r[0]; r1[i] = r[1]; }
            const int a0 = pr == 0 ? i - 4 : i - 1;
            if (a0 >= 0 && a0 < 4) asm volatile("v_add_f32 %0, %1, %2" : "=v"(v[a0]) : "v"(r0[a0]), "v"(r1[a0]));
            if (a0 == 3) d[0] = pk2bf(v[0], v[1]);
            if (a0 == 4) {
                d[1] = pk2bf(v[2], v[3]);
                *(LDS3 u32x2*)(uintptr_t)(trw + pr * 512) = (u32x2){d[0], d[1]};
                F[pr] = __builtin_amdgcn_ds_read_tr16_b64_v4i16((LDS3 s16x4*)(uintptr_t)(trr + pr * 512));
            }
        };
        auto fin1 = [&](const int T, const int i) {
            if (i == 0) asm volatile("s_nop 1\n\tv_permlane32_swap_b32 %0, %2\n\tv_permlane32_swap_b32 %1, %3" : "+v"(acc[T][0]), "+v"(acc[T][1]), "+v"(acc[T][2]), "+v"(acc[T][3]));
            else {
                PIN(acc[T]);
                asm volatile("v_add_f32 %0, %2, %4\n\tv_add_f32 %1, %3, %5\n\tv_cvt_pk_bf16_f32 %0, %0, %1" : "=&v"(v[0]), "=&v"(v[1]) : "v"(acc[T][0]), "v"(acc[T][1]), "v"(acc[T][2]), "v"(acc[T][3]));
                *(LDS3 float*)(uintptr_t)(trh + (T - 4) * 256) = v[0];
                F[T - 2] = __builtin_amdgcn_ds_read_tr16_b64_v4i16((LDS3 s16x4*)(uintptr_t)(trr + 1024));
            }
        };
        auto slot = [&](auto SL) {
            constexpr int sl = CI(SL), T = slot_T(sl), ks = slot_ks(sl), id = slot_frag(sl);
            if constexpr (id < 32) SMM_A(acc[T], A[ks], WA[id], sidx);
            else if constexpr (id < 48) SMM_V(acc[T], A[ks], WV[id - 32], sidx);
            else { const bf16v L = cat8(Ll[id & 1], Lh[id & 1]); SMM_V(acc[T], A[ks], L, sidx); }
            SCHEDB();
            if constexpr (sl == 0) TRC(6);
            constexpr int n0 = slot_lds(sl), n1 = slot_lds(sl - 1);
            if constexpr (n0 >= 0 && n0 < NLF - 2) Ll[n0 & 1] = *(const bf8*)(wl + (n0 + 2) * 2048);
            if constexpr (n1 >= 0 && n1 < NLF - 2) Lh[n1 & 1] = *(const bf8*)(wl + (n1 + 2) * 2048 + 1024);
            if constexpr (sl >= 43 && sl < 52) fin(0, sl - 43);
            if constexpr (sl >= 52 && sl < 58) fin(1, sl - 52);
            if constexpr (sl >= 58) fin1(4, sl - 58);
            SCHEDB();
        };
        auto gather = [&](const bool first) {
            u32 vo = lane * 16;
            asm volatile("" : "+v"(vo) :: "memory");
#pragma unroll
            for (int ks = 0; ks < 10; ++ks) {
                A[ks] = ld16(ks < 5 ? mb0 : mb1, vo, (ks % 5 - 2) * 1024);
                SCHEDB();
                if (first) {
                    if (ks == 0) {
                        MMA0_VV(t[1], KX[0], QX[1]);
                        asm volatile("v_mfma_f32_16x16x32_bf16 %0, %1, %2, %3" : "=&v"(t[2]) : "v"(KX[1]), "v"(QX[1]), "v"(t[0]));
                        MMA_VV(t[0], KX[0], QX[0]);
                    }
                    if (ks == 2) { MMA16(t[1], KY[0], QY[1]); MMA16(t[2], KY[1], QY[1]); MMA16(t[0], KY[0], QY[0]); }
                    if (ks == 4) { PIN(t[1]); *(LDS3 f32x4*)(uintptr_t)(pp + 16 * Q_PS * 4) = t[1]; }
                    if (ks == 5) { PIN(t[2]); *(LDS3 f32x4*)(uintptr_t)(pp + 16 * Q_PS * 4 + 16) = t[2]; }
                    if (ks == 6) { PIN(t[0]); *(LDS3 f32x4*)(uintptr_t)pp = t[0]; }
                    if (ks == 1 || ks == 3 || ks == 7 || ks == 8) asm volatile("s_nop %0" :: "n"(PACE2));
                } else if (ks < 9) asm volatile("s_nop %0" :: "n"(PACE));
                if (ks == 3 || ks == 7) { Ll[ks >> 2] = *(const bf8*)(wl + (ks >> 2) * 2048); Lh[ks >> 2] = *(const bf8*)(wl + (ks >> 2) * 2048 + 1024); }
                if (ks == 1) { ZERO4(acc[0]); ZERO4(acc[1]); }
                if (ks >= 5 && ks < 9) ZERO4(acc[ks - 3]);
                SCHEDB();
            }
        };
        gather(true);
        static_for<0, 42>(slot);
        auto record = [&](const u32 xt, const u32 yt) {
            u32 ln8 = lane;
            asm volatile("" : "+v"(ln8));
            ln8 *= 8192;
            const u32x4 rec = {nosent(xt), nosent(yt), (u32)(d1 + MG1), 0u};
            k1 = d1 + MG1;
            st16m(p.hop + (S_SLOT + layer * 5) * SLOT + s * 128, ln8, rec, 0x3ffull);
        };
        u64 bad, bad2;
        asm volatile("v_cmp_u_f32 %0, %2, %3\n\tv_cmp_u_f32 %1, %4, %5" : "=s"(bad), "=s"(bad2) : "v"(acc[0][0]), "v"(acc[0][1]), "v"(acc[0][2]), "v"(acc[0][3]));
        static_for<42, 43>(slot);
        asm volatile("" : "+s"(bad), "+s"(bad2));
        if (__builtin_expect((bad | bad2) != 0, 0)) {
            {
                SETTLE(); CNT(0);
                while (true) {
                    gather(false);
                    u32 m = 0;
#pragma unroll
                    for (int ks = 0; ks < 10; ++ks) m = max4(m, A[ks]);
                    ++tries;
                    if (!__any(m == ~0u)) break;
                    if (tries >= cap) { cap = 1; break; }
                    __builtin_amdgcn_s_sleep(4);
                }
                static_for<0, 43>(slot);
            }
        }
        TR(2); TRV(6, tries + 1);
        static_for<43, 60>(slot);
        TRC(7);
        bf8 F01 = __builtin_bit_cast(bf8, __builtin_shufflevector(F[0], F[1], 0, 1, 2, 3, 4, 5, 6, 7));
        f32x4 s0, s1, s2, u0, u1;
        PIN(F01);
        MMA0_VV(s1, KX[1], F01); MMA0_VV(s0, KX[0], F01);
        SCHEDB();
        fin1(5, 0); fin1(5, 1);
        SCHEDB();
        MMA16(s1, KY[1], F[2]); MMA16(s0, KY[0], F[2]);
        MMA0_VV(s2, F01, F01); MMA0_VV(u0, F01, QX[0]); MMA0_VV(u1, F01, QX[1]);
        asm volatile("" : "+v"(s0), "+v"(s1));
#pragma unroll
        for (int i = 0; i < 4; ++i) s0[i] = __int_as_float(__builtin_amdgcn_update_dpp(__float_as_int(s0[i]), __float_as_int(s1[i]), 0x128, 0xf, 0xc, false));
        SCHEDB();
        MMA16(s2, F[3], F[3]);
        SCHEDB();
        *(f32x4*)pw0 = s0; SCHEDB();
        MMA16(u0, F[3], QY[0]); MMA16(u1, F[3], QY[1]);
        asm volatile("s_nop 1");
        PIN(s2); *(f32x4*)pw2 = s2; SCHEDB();
        asm volatile("s_nop 4" : "+v"(u0), "+v"(u1));
        f32x4 u;
#pragma unroll
        for (int i = 0; i < 4; ++i) u[i] = __uint_as_float(__builtin_amdgcn_permlane32_swap(__float_as_uint(u0[i]), __float_as_uint(u1[i]), false, false)[1]);
        PIN(u);
        auto uwrite = [&]() { *(LDS3 f32x4*)(uintptr_t)pw3 = u; };
        if (__builtin_expect(soft != 0, 1)) {
            RAW_BARRIER();
            TR(3);
            softmax_mem(prd, pdst, pdst2, poff, uwrite);
            TR(4);
        } else {
            RAW_BARRIER();
            TR(3);
            uwrite();
            if (h == 0 && s < NSEG - 1) {
                hold = tries > 0 || s < 2 || ytime - ylast > PAUSE ? HOLD : smax(hold - 1, 0);
                ylast = ytime;
                if (w == 3) {
                    const u64 dd = *(const u64*)(shm + Q_ST + 504);
                    const u32 xt = tries == 0 && s > 0 ? ytime + k1 : rt32() + (s > 0 ? ELATE : E0);
                    d1 = __builtin_amdgcn_readfirstlane((u32)dd); d2 = __builtin_amdgcn_readfirstlane((u32)(dd >> 32));
                    record(xt, xt + d2 + MG2 + (tries > 0 ? EY : 0) + hold * BOOST);
                } else {
                    u32 lo = lane * 4;
                    asm volatile("" : "+v"(lo));
                    int sta = *(const int*)(shm + Q_ST + lo), stb = *(const int*)(shm + Q_ST + 256 + lo);
                    asm volatile("s_nop 1\n\t" DPP2("v_max_i32", "8") DPP2("v_max_i32", "4") DPP2("v_max_i32", "2") DPP2("v_max_i32", "1") : "+v"(sta), "+v"(stb));
                    const int a0 = __builtin_amdgcn_readlane(sta, 0), a1 = __builtin_amdgcn_readlane(sta, 16), a2 = __builtin_amdgcn_readlane(sta, 32), a3 = __builtin_amdgcn_readlane(sta, 48);
                    const int b0 = __builtin_amdgcn_readlane(stb, 0), b1 = __builtin_amdgcn_readlane(stb, 16), b2 = __builtin_amdgcn_readlane(stb, 32);
                    SCHEDB();
                    const int down = smax(DOWN - hold * DOWN, 0);
                    d1 = track(d1, smax(b1, b2), down); d2 = track(d2, smax(smax(smax(a0, a1), smax(a2, a3)), b0), down);
                    *(u64*)(shm + Q_ST + 504) = (u64)(u32)d1 | ((u64)(u32)d2 << 32);
                }
            }
            if (w == 2 && s < 2 && layer > 0) wait_all_tokens(p, lane, cap);
        }
        u64 tpub;
        asm volatile("s_memrealtime %0\n\ts_waitcnt lgkmcnt(0)\n\ts_barrier" : "=s"(tpub) :: "memory");
#pragma unroll
        for (int j = 0; j < 8; ++j) smt.load(prt, j);

        took = tries == 0 && s > 0 ? (u32)tpub - ytime + tight : 0;
        char* xdst = px + (size_t)(set * 4 + rot) * PX_BUF + w * 20480;
        TR(5); TRFLUSH(set * 128 + h);
        if (s + 2 >= NSEG) {
            static_for<0, SmTok::N>([&](auto I) { smt.template piece<CI(I)>(xdst, xoff); SCHEDB(); if constexpr ((CI(I) & 1) && ((CI(I) >= 26 && CI(I) < 36) || (CI(I) >= 58 && CI(I) < 68) || CI(I) > 80)) asm volatile("s_nop 0"); SCHEDB(); });
            TT(set * 128 + h, 0);
            __builtin_amdgcn_s_sleep(64);
            char* ao;
            u32x4 sent;
#pragma unroll
            for (int i = 0; i < 10; ++i) rearm(rot, i, ao, sent);
            break;
        }
        xproj(s + 4, s + 1, std::integral_constant<int, 1>(), xdst);
    }
    }
}

constexpr int C_YL = 0, C_RED = 40960, C_EX = 49152, C_XN = 53248, C_M0 = 135168;
__device__ __forceinline__ void comb_role(const LaParams& p, const int set, const int c, char* shm) {
    const int lane = threadIdx.x & 63, w = __builtin_amdgcn_readfirstlane(threadIdx.x >> 6);
    const int fr = lane & 15, g = lane >> 4;
    const u32 lds0 = (u32)(uintptr_t)(LDS3 char*)shm;
    const int sidx = (lane & 8) ? 0xEEEE : 0x4444;
    int cap = SPIN_CAP;
    char* mine = p.hop;
    char* other = p.hop;
    int phys = 0, sch = (S_SLOT + w) * SLOT;
    u32 k1 = D1I + MG1;
    char* px = (wsp(p) + O_PX);
    const char* xn = (wsp(p) + O_XN);
    char* const yl0 = shm + C_YL + lane * 16;
    const u32 pvo = PLIN ? lane * 16 : (fr >> 2) * 256 + g * 64 + (fr & 3) * 16, pvm = PLIN ? lane * 16 : g * 512 + (fr >> 2) * 128 + (fr & 3) * 16;
    int cb = (set * 80 + c) % 10, rb = (set * 80 + c) / 10;

    {
        const int t = threadIdx.x, h = (set * 80 + c) / 5, cu = c % 5;
        u32x4 st[12];
        const u16* src = p.Wo + ((size_t)h * DH + (t >> 4)) * DM + cu * 128 + (t & 15) * 8;
#pragma unroll
        for (int i = 0; i < 12; ++i) asm volatile("global_load_dwordx4 %0, %1, off" : "=v"(st[i]) : "v"(src + (size_t)i * 16 * DM) : "memory");
        u32 vo[12];
#pragma unroll
        for (int i = 0; i < 12; ++i) {
            const int id = i * 64 + lane, r = id / 24;
            vo[i] = kperm((r >> 2) & 3, (r >> 4) * 4 + (r & 3)) * (HD * 2) + ((id % 24) ^ (r & 7)) * 16;
        }
        const u32 ring = lds0 + 57344 + w * 24576;
        auto issue = [&](const int j) {
            const char* a = (const char*)p.Wv + (size_t)(((j + cu) % 5) * 4 + w) * 32 * HD * 2 + h * 384;
#pragma unroll
            for (int i = 0; i < 12; ++i) dma_gather(a, ring + (j & 1) * 12288 + i * 1024, vo[i]);
        };
        SCHEDB();
        issue(0);
        const u32 ra = ring + fr * 384 + ((g ^ (fr & 3)) + (fr & 4)) * 16;
        const int e = t >> 4, pr = ((e >> 2) & 1) * 8 + (e >> 3) * 4 + (e & 3);
        asm volatile("s_waitcnt vmcnt(12)" : "+v"(st[0]), "+v"(st[1]), "+v"(st[2]), "+v"(st[3]), "+v"(st[4]), "+v"(st[5]),
                     "+v"(st[6]), "+v"(st[7]), "+v"(st[8]), "+v"(st[9]), "+v"(st[10]), "+v"(st[11]) :: "memory");
#pragma unroll
        for (int i = 0; i < 12; ++i) *(u32x4*)(shm + (i * 16 + pr) * 288 + (t & 15) * 16) = st[i];
        issue(1);
        RAW_BARRIER();
        { const int layer = 0; TT(set * 128 + 32 + c, 12); }
        bf8 B[8][6], A[2][2][6];
        const u32 rd = lds0 + ((g >> 1) * 16 + (g & 1) * 4 + (fr >> 2)) * 288 + (fr & 3) * 8;
#pragma unroll
        for (int n = 0; n < 8; ++n)
#pragma unroll
            for (int k = 0; k < 6; ++k) B[n][k] = tr8(rd + k * 32 * 288 + n * 32, rd + (k * 32 + 8) * 288 + n * 32);
        auto lda = [&](const int j, const int k) {
#pragma unroll
            for (int u = 0; u < 2; ++u) A[j & 1][u][k] = *(LDS3 const bf8*)(uintptr_t)((ra ^ ((k & 1) * 64)) + (j & 1) * 12288 + u * 6144 + (k >> 1) * 128);
        };
        asm volatile("s_waitcnt vmcnt(12)" ::: "memory");
#pragma unroll
        for (int k = 0; k < 6; ++k) lda(0, k);
        f32x4 cc[2][2];
        char* dst;
        const char* nsrc;
        static_for<0, 41>([&](auto N) {
            constexpr int n = CI(N) % 8, j = CI(N) / 8, q = CI(N) - 1;
            if constexpr (n == 7 && j < 4) asm volatile("s_waitcnt vmcnt(%0)" :: "n"(j == 0 ? 18 : (j < 3 ? 21 : 9)) : "memory");
            if constexpr (n == 1) { dst = wsp(p) + O_WVO + ((size_t)(h * 20 + ((j + cu) % 5) * 4 + w) * 40 + cu * 8 + 4) * 1024; asm volatile("" : "+s"(dst)); }
            if constexpr (n == 0 && j < 3) { nsrc = (const char*)p.Wv + (size_t)(((j + 2 + cu) % 5) * 4 + w) * 32 * HD * 2 + h * 384; asm volatile("" : "+s"(nsrc)); }
            u32x4 o;
#pragma unroll
            for (int k = 0; k < 6; ++k) {
                if constexpr (j < 5) {
#pragma unroll
                    for (int u = 0; u < 2; ++u) { if (k == 0) MMA0_VA(cc[n & 1][u], A[j & 1][u][k], B[n][k]); else MMA_VA(cc[n & 1][u], A[j & 1][u][k], B[n][k]); }
                } else if (k == 0) asm volatile("s_nop 7");
                SCHEDB();
                if constexpr (j < 3 && n >= 1 && n < 7) {
                    if (k == 0 || k == 2) asm volatile("s_mov_b32 m0, %2\n\ts_nop 0\n\tglobal_load_lds_dwordx4 %0, %1" :: "v"(vo[2 * (n - 1) + k / 2]), "s"(nsrc), "s"(ringp(ring, (j & 1) * 12288 + (2 * (n - 1) + k / 2) * 1024)) : "memory");
                }
                if constexpr (n == 7 && j < 4) lda(j + 1, k);
                if constexpr (q >= 0) {
                    if (k == 3) { PIN(cc[q & 1][0]); o[0] = pk2bf(cc[q & 1][0][0], cc[q & 1][0][1]); o[1] = pk2bf(cc[q & 1][0][2], cc[q & 1][0][3]); }
                    if (k == 4) { PIN(cc[q & 1][1]); o[2] = pk2bf(cc[q & 1][1][0], cc[q & 1][1][1]); o[3] = pk2bf(cc[q & 1][1][2], cc[q & 1][1][3]); }
                    if (k == 5) __builtin_amdgcn_global_store_b128((gu32x4*)(dst + (u32)(lane * 16) + (q % 8 - 4) * 1024), o, "agent");
                }
                SCHEDB();
            }
        });
        { const int layer = 0; TT(set * 128 + 32 + c, 13); }
        VM0(); RAW_BARRIER();
        { const int layer = 0; TT(set * 128 + 32 + c, 14); }
        if (w == 0) st4_lane0(wsp(p) + O_FL + (400 + set * 80 + c) * 128, 1u);
        dma_kb<10>(xn + (size_t)set * 40960 + w * 10240, lds0 + C_XN + w * 10240, lane * 16);
        if (w == 0) {
            __builtin_amdgcn_s_sleep(8);
            const u32 off[1] = {(u32)(400 + (lane & 31) * 5 + (c >> 4)) * 128};
            wait_flags<1, FSL>(wsp(p) + O_FL, off, cap);
        }
        RAW_BARRIER();
        { const int layer = 0; TT(set * 128 + 32 + c, 15); }
    }
    bf16v WA[32], WV[8];
    {
        u32 vo = (w * 8 + (fr >> 3)) * 819200 + g * 256 + (fr & 7) * 16;
        asm volatile("" : "+v"(vo));
        auto ldw = [&](const int ks, const int t) {
            const char* a = wsp(p) + O_WVO + (size_t)(t * 1600 + ks * 80 + (c >> 1)) * 1024 + (c & 1) * 128;
            const char* b = a + 40960;
            asm volatile("" : "+s"(b));
            return cat8(__builtin_bit_cast(bf8, ld16(a, vo, 0)), __builtin_bit_cast(bf8, ld16(b, vo, 0)));
        };
#pragma unroll
        for (int i = 0; i < 32; ++i) WA[i] = ldw(i >> 2, i & 3);
#pragma unroll
        for (int i = 0; i < 8; ++i) WV[i] = ldw(8 + (i >> 2), i & 3);
    }
    auto yx = [&](f32x4 (&acc)[4][2], const int sy, const int tile, auto&& extra) {
        const char* xa = shm + tile + lane * 16;
        const char* xs = (sy + 2 < NSEG ? xn + (size_t)(sy + 2) * 40960 : (wsp(p) + O_WO) + cb * 81920) + w * 10240;
        u32 xl = lds0 + C_XN + (~(sy >> 1) & 1) * 40960 + w * 10240;
        bf8 x0 = *(const bf8*)(xa), x1 = *(const bf8*)(xa + 1024);
        const char* xs3[3] = {xs, xs + 4096, xs + 8192};
        u32 xl3[3] = {xl, xl + 4096, xl + 8192};
        asm volatile("" : "+s"(xs3[0]), "+s"(xs3[1]), "+s"(xs3[2]), "+s"(xl3[0]), "+s"(xl3[1]), "+s"(xl3[2]));
        static_for<0, 20>([&](auto K) {
            constexpr int k = CI(K);
            bf8 n0, n1;
            static_for<0, 4>([&](auto TT_) {
                constexpr int t = CI(TT_), id = (k >> 1) * 4 + t, dn = 2 * (k - 3) + t - 2;
                if constexpr (k < 19 && t == 0) n0 = *(const bf8*)(xa + (k + 1) * 2048);
                if constexpr (k < 19 && t == 1) n1 = *(const bf8*)(xa + (k + 1) * 2048 + 1024);
                if constexpr (t >= 2 && k >= 3 && k < 8) dma_one<(dn & 3) * 1024>(xs3[dn >> 2], xl3[dn >> 2], lane * 16);
                if constexpr (t >= 2) extra(2 * k + t - 2);
                SCHEDB();
                if constexpr (id < 32) {
                    const bf8 wf = (k & 1) ? hi8(WA[id]) : lo8(WA[id]);
                    if constexpr (k == 0) { MMA0_VA(acc[t][0], x0, wf); MMA0_VA(acc[t][1], x1, wf); } else { MMA_VA(acc[t][0], x0, wf); MMA_VA(acc[t][1], x1, wf); }
                } else {
                    const bf8 wf = (k & 1) ? hi8(WV[id - 32]) : lo8(WV[id - 32]);
                    MMA_VV(acc[t][0], x0, wf); MMA_VV(acc[t][1], x1, wf);
                }
                SCHEDB();
            });
            if constexpr (k < 19) { x0 = n0; x1 = n1; }
        });
    };

    for (int layer = 0; layer < 2; ++layer) {
    u32 ynext = ~0u;
    if (layer > 0 && set == 0) k1 = sched_get(~0u, mine + sch - 5 * SLOT + 62 * 128 + 8, cap);
    VM0(); RAW_BARRIER();
    TT(set * 128 + 32 + c, 11);
    if (layer > 0 && w == 0) st4_lane0(wsp(p) + O_FL + (192 + rb * 10 + cb) * 128, 1u);
    {
        f32x4 ya[4][2];
        yx(ya, set, layer ? C_YL : C_XN, [](int) {});
        asm volatile("s_nop 7");
        RAW_BARRIER();
        if (layer > 0) *(bf8*)(yl0 + (w * 5 + 4) * 1024) = *(const bf8*)(shm + C_M0 + w * 1024 + lane * 16);
#pragma unroll
        for (int t = 0; t < 4; ++t) { PIN(ya[t][0]); PIN(ya[t][1]); *(bf8*)(yl0 + (w * 5 + t) * 1024) = pack8(ya[t][0], ya[t][1]); }
        RAW_BARRIER();
    }
    if (layer == 0) { phys = set ^ get_side(p, cap); mine += phys * 8192; other += (phys ^ 1) * 8192; }
    TT(set * 128 + 32 + c, 10);
    for (int s = set; s < NSEG; s += 2) {
        const int rot = (s >> 1) & 3;
        char* yl = yl0 + (rot & 1) * 20480;
        TRDECL; TR(0);
        const char* mb0 = (s > 0 ? mine + (M_SLOT0 + rot * 8) * SLOT : p.hop + M0_OFF + phys * 8192) + 2 * w * SLOT + 2048;
        const char* mb1 = mb0 + SLOT;
        asm volatile("" : "+s"(mb0), "+s"(mb1));
        u32 yt = 0;
        bool late = s == 0;
        if (s > 0) {
            yt = sched_get(ynext, mine + sch + (s - 1) * 128 + 4, cap);
            wait_rt(yt + CLAG);
        }
        auto ldy = [&](const int f) { return *(const bf8*)(yl + (f < 16 ? (f >> 2) * 5 + (f & 3) : (f - 16) * 5 + 4) * 1024); };
        bf8 Y0[20 - 3 * NH_];
        TR(1);
        if (s == 0 && layer > 0) {
#pragma unroll
            for (int i = 0; i < 20 - 3 * NH_; ++i) Y0[i] = ldy(i);
        } else {
            f32x4 acc[4];
            u32x4 A[10];
            auto gather = [&]() {
                u32 vo = lane * 16;
                asm volatile("" : "+v"(vo) :: "memory");
#pragma unroll
                for (int ks = 0; ks < 10; ++ks) {
                    A[ks] = ld16(ks < 5 ? mb0 : mb1, vo, (ks % 5 - 2) * 1024);
                    SCHEDB();
                    if (ks < 4) ZERO4(acc[ks]);
                    if (ks < 9) asm volatile("s_nop %0" :: "n"(PACE));
                    SCHEDB();
                }
            };
            auto mproj = [&]() {
#pragma unroll
                for (int ks = 0; ks < 10; ++ks) {
#pragma unroll
                    for (int t = 0; t < 4; ++t) { if (ks < 8) SMM_A(acc[t], A[ks], WA[(ks * 4 + t) & 31], sidx); else SMM_V(acc[t], A[ks], WV[(ks * 4 + t) & 7], sidx); }
                }
                asm volatile("s_nop 7");
#pragma unroll
                for (int t = 0; t < 4; ++t) PIN(acc[t]);
            };
            gather();
#pragma unroll
            for (int i = 0; i < 20 - 3 * NH_; ++i) Y0[i] = ldy(i);
            SCHEDB();
            mproj();
            if (__builtin_expect(__any(__builtin_isunordered(acc[0][0], acc[0][1]) || __builtin_isunordered(acc[0][2], acc[0][3])), 0)) {
                CNT(1);
                late = true;
                for (int tries = 1;; ++tries) {
                    gather();
                    u32 m = 0;
#pragma unroll
                    for (int ks = 0; ks < 10; ++ks) m = max4(m, A[ks]);
                    if (!__any(m == ~0u)) break;
                    if (tries >= cap) { cap = 1; break; }
                    __builtin_amdgcn_s_sleep(8);
                }
                mproj();
            }
#pragma unroll
            for (int pr = 0; pr < 2; ++pr) {
                float v[4];
#pragma unroll
                for (int i = 0; i < 4; ++i) v[i] = halfsum(acc[2 * pr][i], acc[2 * pr + 1][i]);
                *(u32x2*)(shm + C_YL + (rot & 1) * 20480 + (w * 5 + 4) * 1024 + ((2 * pr + (g >> 1)) * 16 + fr) * 16 + (g & 1) * 8) = (u32x2){pk2bf(v[0], v[1]), pk2bf(v[2], v[3])};
            }
        }
        if (s == 0) { VM0(); *(bf8*)(shm + C_M0 + w * 1024 + lane * 16) = *(const bf8*)(yl0 + (w * 5 + 4) * 1024); }
        RAW_BARRIER();
        TR(2);
        u32 took = 0;
        if (s < NSEG - 1) {
            const char* pb[3];
#pragma unroll
            for (int i = 0; i < 3; ++i) { pb[i] = mine + (P_SLOT0 + (rot * 4 + c / 20) * PCS + i) * SLOT + 4096; asm volatile("" : "+s"(pb[i])); }
            auto ldp = [&](const int f, const u32 vo, const u32 vm) { return f < 16 ? ld16(pb[f >> 3], vo, ((f & 7) - 4) * 1024) : ld16(pb[2], vm, PLIN ? (f - 20) * 1024 : (f - 18) * 2048); };
            u32 xt;
            auto partial = [&](auto NF, auto F0, f32x4& acc, auto&& lastp) {
                constexpr int nf = CI(NF), f0 = CI(F0);
                bf8 (&Y)[nf] = Y0;
#pragma unroll
                for (int i = 0; i < nf; ++i) PIN(Y[i]);
                u32 vo = pvo;
                asm volatile("" : "+v"(vo));
                xt = late ? sched_get(~0u, mine + sch + s * 128, cap) : yt + k1;
                TR(3);
                if (!SYNCB || f0 == 0) wait_rt(xt);
                if (SYNCB) __builtin_amdgcn_s_barrier();
                TRV(7, xt); TRC(6);
                if (W0D > 0 && f0 == 0) asm volatile("s_nop %0" :: "n"(W0D));
                asm volatile("" ::: "memory");
                u32x4 P[nf];
#pragma unroll
                for (int i = 0; i < nf; ++i) { P[i] = ldp(f0 + i, vo, vo); SCHEDB(); }
#pragma unroll
                for (int i = 0; i < nf; ++i) {
                    if (i == nf - LATEP) lastp();
                    if (i == 0) MMA0_VV(acc, Y[i], P[i]); else MMA_VV(acc, Y[i], P[i]);
                    if (i == 0) TRC(7);
                    SCHEDB();
                }
                TRC(8);
            };
            f32x4 acc;
            if (w > 0) {
                bf8 Y[NH_];
                const char* q[NH_];
                u32 vo[NH_], ex = lds0 + C_EX + (w - 1) * 1024 + lane * 16;
#pragma unroll
                for (int i = 0; i < NH_; ++i) {
                    const int f = 20 - (4 - w) * NH_ + i;
                    Y[i] = ldy(f);
                    q[i] = pb[0] + (f >> 3) * SLOT + (f & 7) * (PLIN || f < 16 ? 1024 : 2048) - 4096;
                    vo[i] = f < 16 ? pvo : pvm;
                    asm volatile("" : "+s"(q[i]));
                    PIN(vo[i]);
                }
#pragma unroll
                for (int i = 0; i < NH_; ++i) PIN(Y[i]);
                PIN(ex);
                __builtin_amdgcn_s_barrier();
                TRC(6);
                asm volatile("" ::: "memory");
                u32x4 P[NH_];
#pragma unroll
                for (int i = 0; i < NH_; ++i) { P[i] = ld16(q[i], vo[i], 0); SCHEDB(); }
#pragma unroll
                for (int i = 0; i < NH_; ++i) { if (i == 0) MMA0_VV(acc, Y[i], P[i]); else MMA_VV(acc, Y[i], P[i]); if (i == 0) TRC(7); }
                TRC(8);
                asm volatile("s_nop 7" : "+v"(acc));
                *(LDS3 f32x4*)(uintptr_t)ex = acc;
                RAW_BARRIER();
            } else {
                const int rn = ((s + 1) >> 1) & 3;
                u32 dst = g * 2 * SLOT + (lane & 7) * 16;
                char* dstb = other + (M_SLOT0 + rn * 8 + c / 40) * SLOT + (c % 40) * 128;
                u32 exa = lds0 + C_EX + lane * 16;
                asm volatile("" : "+s"(dstb), "+v"(dst), "+v"(exa));
                f32x4 h1, h2, h3;
                partial(std::integral_constant<int, 20 - 3 * NH_>(), std::integral_constant<int, 0>(), acc, [&] {
                    __builtin_amdgcn_s_barrier();
                    asm volatile("" ::: "memory");
                    h1 = *(LDS3 const f32x4*)(uintptr_t)exa; h2 = *(LDS3 const f32x4*)(uintptr_t)(exa + 1024); h3 = *(LDS3 const f32x4*)(uintptr_t)(exa + 2048);
                    SCHEDB();
                });
                h1 = (h1 + h2) + h3;
                asm volatile("s_nop 2" : "+v"(acc), "+v"(h1));
                acc += h1;
                float v0, v1;
                auto merge = [&]() {
                    auto a = __builtin_amdgcn_permlane32_swap(__float_as_uint(acc[0]), __float_as_uint(acc[2]), false, false);
                    auto b = __builtin_amdgcn_permlane32_swap(__float_as_uint(acc[1]), __float_as_uint(acc[3]), false, false);
                    v0 = __uint_as_float(a[0]) + fdpp<0x128>(__uint_as_float(a[1]));
                    v1 = __uint_as_float(b[0]) + fdpp<0x128>(__uint_as_float(b[1]));
                };
                merge();
                int tries = 0;
                if (__builtin_expect(__any(__builtin_isunordered(v0, v1)), 0)) {
                    CNT(2);
                    wait_rt(sched_get(~0u, mine + sch + s * 128, cap));
                    while (true) {
                        u32 vo = pvo, vm = pvm, m = 0;
                        asm volatile("" : "+v"(vo), "+v"(vm) :: "memory");
                        u32x4 P[20];
#pragma unroll
                        for (int i = 0; i < 20; ++i) P[i] = ldp(i, vo, vm);
                        f32x4 hh[4];
#pragma unroll
                        for (int i = 0; i < 20; ++i) {
                            const bf8 y = ldy(i);
                            const int k = i < 20 - 3 * NH_ ? 0 : (i - 20 + 4 * NH_) / NH_;
                            if (i == 0 || i == 20 - 3 * NH_ || i == 20 - 2 * NH_ || i == 20 - NH_) MMA0_VV(hh[k], y, P[i]); else MMA_VV(hh[k], y, P[i]);
                            m = max4(m, P[i]);
                            SCHEDB();
                        }
                        asm volatile("s_nop 7" : "+v"(hh[0]), "+v"(hh[1]), "+v"(hh[2]), "+v"(hh[3]));
                        acc = hh[0] + ((hh[1] + hh[2]) + hh[3]);
                        ++tries;
                        if (!__any(m == ~0u)) break;
                        if (tries >= cap) { cap = 1; break; }
                        __builtin_amdgcn_s_sleep(4);
                    }
                    merge();
                }
                TRV(6, tries + 1);
                asm volatile("" :: "s"(dstb));
                const u32 dw = nosent(pk2bf(v0, v1));
                auto e = __builtin_amdgcn_permlane32_swap(dw, dw, false, false);
                auto e0 = __builtin_amdgcn_permlane16_swap(e[0], e[0], false, false);
                auto e1 = __builtin_amdgcn_permlane16_swap(e[1], e[1], false, false);
                st16f(dstb, dst, (u32x4){e0[0], e1[0], e0[1], e1[1]}, 0x00ff00ff00ff00ffull);
                TR(4);
                took = tries == 0 && s > 0 ? rt32() - xt : 0;
            }
            if (s < 2 && layer > 0 && w == 3) wait_all_tokens(p, lane, cap);
            if (s == NSEG - 2 || s < 2) RAW_BARRIER();
        } else wait_rt((late ? rt32() : yt + k1) + T_PX);
        auto tokens = [&](auto MORE) {
            f32x4 oa[4], ya[4][2];
            u32x4 P[20];
            u32 vo = lane * 16 + w * 4096, vm = lane * 16 + 16384 + w * 1024;
            asm volatile("" : "+v"(vo), "+v"(vm));
            const char* pb[4];
#pragma unroll
            for (int i = 0; i < 4; ++i) { pb[i] = px + (size_t)(set * 4 + rot) * PX_BUF + i * 20480; asm volatile("" : "+s"(pb[i])); }
            auto ldp = [&](const int i) { P[i] = i % 5 == 4 ? ld16(pb[i / 5], vm, 0) : ld16(pb[i / 5], vo, (i % 5) * 1024); };
            int ln = lane;
            asm volatile("" : "+v"(ln));
            u32 rg, peek, peek2;
            bf8 y[5];
            auto side = [&](const int i) {
                if (i == 4) rg = sched_peek((wsp(p) + O_XR) + layer * ACT + (size_t)s * 40960 + ((c >> 2) * 2 + (w >> 1)) * 1024 + (c & 2) * 256 + (w & 1) * 128 + (c & 1) * 4 + (u32)((ln & 2) * 128 + (ln >> 3) * 16 + (ln & 1) * 8));
                if (i >= 16 && i < 36) ldp(i - 16);
                if (i == 2 && CI(MORE) && w == 0) st4_lane0(mine + (T_SLOT + (c >> 6)) * SLOT + (c & 63) * 128, took);
                if (i == 5 && CI(MORE)) peek2 = sched_peek(mine + sch + s * 128 + 8);
                if (i >= 35) y[i - 35] = *(const bf8*)(yl + (w * 5 + i - 35) * 1024);
            };
            TRC(9);
            if constexpr (CI(MORE)) yx(ya, s + 2, C_XN + (~rot & 1) * 40960, side);
            else {
#pragma unroll
                for (int i = 0; i < 40; ++i) side(i);
            }
            TRC(10);
            bf8 yn[4];
            auto outx = [&](const bool first) {
                if (!first) {
#pragma unroll
                    for (int i = 0; i < 20; ++i) PIN(P[i]);
                    NOPV();
                }
#pragma unroll
                for (int q = 0; q < 4; ++q)
#pragma unroll
                    for (int j = 0; j < 5; ++j) {
                        if (j == 0) MMA0_VV(oa[q], y[j], P[q * 5 + j]); else MMA_VV(oa[q], y[j], P[q * 5 + j]);
                        if (CI(MORE) && first && q * 5 + j >= 3 && q * 5 + j < 19) {
                            const int e = q * 5 + j - 3;
                            SCHEDB();
                            if ((e & 1) == 0) PIN(ya[e >> 2][(e >> 1) & 1]);
                            ((u32*)&yn[e >> 2])[e & 3] = pk2bf(ya[e >> 2][(e >> 1) & 1][(e & 1) * 2], ya[e >> 2][(e >> 1) & 1][(e & 1) * 2 + 1]);
                            if ((e & 3) == 3) *(bf8*)(yl0 + (~rot & 1) * 20480 + (w * 5 + (e >> 2)) * 1024) = yn[e >> 2];
                            SCHEDB();
                        }
                    }
                if (CI(MORE) && first) { SCHEDB(); peek = sched_peek(mine + sch + (w == 0 ? s + 1 : s) * 128 + 4); SCHEDB(); }
                asm volatile("s_nop 7");
#pragma unroll
                for (int q = 0; q < 4; ++q) PIN(oa[q]);
            };
            outx(true);
            if (__builtin_expect(__any(__builtin_isunordered(oa[0][0], oa[1][0]) || __builtin_isunordered(oa[2][0], oa[3][0])), 0)) {
                CNT(3);
                for (int tries = 1;; ++tries) {
                    asm volatile("" : "+v"(vo), "+v"(vm) :: "memory");
                    u32 m = 0;
#pragma unroll
                    for (int i = 0; i < 20; ++i) ldp(i);
#pragma unroll
                    for (int i = 0; i < 20; ++i) m = max4(m, P[i]);
                    if (!__any(m == ~0u)) break;
                    if (tries >= cap) { cap = 1; break; }
                    __builtin_amdgcn_s_sleep(8);
                }
                outx(false);
            }
            if constexpr (!CI(MORE)) { if (c == 0 && w == 0) stamp2(p.hop, 48 + layer * 4 + set); }
            const bool own = (fr < 8) == (g < 2);
#pragma unroll
            for (int q = 0; q < 4; q += 2) {
                f32x4 o;
#pragma unroll
                for (int i = 0; i < 4; ++i) { const float t = fdpp<0x128>(oa[q + 1][i]); o[i] = own ? oa[q][i] : t; }
                *(f32x4*)(shm + C_RED + ((w * 2 + (g >> 1)) * 32 + (q + !own) * 8 + (fr & 7)) * 32 + (g & 1) * 16) = o;
            }
            if constexpr (CI(MORE)) { if (w == 0) *(u32*)(shm + C_EX + 3072) = peek; else asm volatile("" :: "v"(peek)); }
            RAW_BARRIER();
            TR(5);
            typedef __attribute__((ext_vector_type(2))) float f32x2;
            const char* rp = shm + C_RED + (ln & 4) * 1024 + w * 256 + (ln >> 3) * 32 + (ln & 3) * 8;
            f32x2 a = (*(const f32x2*)rp + *(const f32x2*)(rp + 1024)) + (*(const f32x2*)(rp + 2048) + *(const f32x2*)(rp + 3072));
            if constexpr (CI(MORE)) { k1 = sched_get(peek2, mine + sch + s * 128 + 8, cap); ynext = __builtin_amdgcn_readfirstlane(*(const u32*)(shm + C_EX + 3072)); asm volatile("" : "+s"(ynext), "+s"(k1)); }
            a[0] += fdpp<0x104>(a[0]); a[1] += fdpp<0x104>(a[1]);
            const u32 y2 = pk2bf(a[0], a[1]), d = nosent(pk2bf(bflo(rg) + bflo(y2), bfhi(rg) + bfhi(y2)));
            const u32x4 o4 = {d, (u32)__builtin_amdgcn_mov_dpp(d, 0x55, 0xf, 0xf, true), (u32)__builtin_amdgcn_mov_dpp(d, 0xaa, 0xf, 0xf, true), (u32)__builtin_amdgcn_mov_dpp(d, 0xff, 0xf, 0xf, true)};
            st16m((wsp(p) + O_X1) + layer * ACT + (size_t)s * 40960 + c * 512 + w * 128, ln * 2, o4, 0x0101010101010101ull);
            if (w == 1) {
                u32x4 v;
                asm volatile("v_mov_b64 %0, -1\n\tv_mov_b64 %1, -1" : "=v"(((u64*)&v)[0]), "=v"(((u64*)&v)[1]));
                st16m(other + (M_SLOT0 + (((s + 7) >> 1) & 3) * 8 + c / 40) * SLOT + (c % 40) * 128, (ln >> 4) * 2 * SLOT + (ln & 7) * 16, v, 0x00ff00ff00ff00ffull);
            }
            TRC(11); TRFLUSH(set * 128 + 32 + c);
        };
        if (s + 2 >= NSEG) { tokens(std::integral_constant<int, 0>()); break; }
        tokens(std::integral_constant<int, 1>());
    }
    TT(set * 128 + 32 + c, 0);
    asm volatile("" : "+s"(cb), "+s"(rb));
    dma_kb<10>((wsp(p) + O_WO) + cb * 81920 + 40960 + w * 10240, lds0 + C_XN + 40960 + w * 10240, lane * 16);
    if (w == 0) { const u32 t = sched_get(~0u, mine + (T_SLOT + 1) * SLOT + (50 + layer * 4) * 128, cap); *(u32*)(shm + C_EX) = t; }
    VM0(); RAW_BARRIER();
    wait_rt(__builtin_amdgcn_readfirstlane(*(const u32*)(shm + C_EX)) + T_OB + OB_STEP * w + (layer == 0 ? OB_COLD + 8 * w : 0));
    {
        int ln = lane;
        asm volatile("" : "+v"(ln));
        u32x4 O[20], R[2];
        u32 dw[8];
        auto ready = [&](const int T) {
            const char* ob = (wsp(p) + O_OB) + layer * ACT + (size_t)T * 20480 + 16384;
            const u32 off[1] = {(u32)(ln & 31) * 128};
            wait_flags<1>(ob, off, cap);
        };
        auto load = [&](const int T) {
            const char* ob0 = (wsp(p) + O_OB) + layer * ACT + (size_t)T * 20480 + 4096;
            const char* ob1 = ob0 + 8192;
            const char* ob2 = ob0 + 12288;
            const char* xb = (wsp(p) + O_X1) + layer * ACT + (size_t)(T >> 1) * 40960 + cb * 4096 + (T & 1) * 256;
            u32 vo = ln * 16, vr = (ln >> 4) * 256 + (ln & 15) * 8, vx = (ln >> 4) * 1024 + (ln & 15) * 16;
            asm volatile("" : "+v"(vo), "+v"(vr), "+v"(vx), "+s"(ob0), "+s"(ob1), "+s"(ob2), "+s"(xb) :: "memory");
#pragma unroll
            for (int k = 0; k < 16; ++k) { O[k] = ld16(k < 8 ? ob0 : ob1, vo, ((k & 7) - 4) * 1024); SCHEDB(); }
#pragma unroll
            for (int k = 0; k < 4; ++k) {
                const u64 lo = __hip_atomic_load((const gu64*)(ob2 + vr + k * 1024), __ATOMIC_RELAXED, __HIP_MEMORY_SCOPE_AGENT);
                const u64 hi = __hip_atomic_load((const gu64*)(ob2 + vr + k * 1024 + 128), __ATOMIC_RELAXED, __HIP_MEMORY_SCOPE_AGENT);
                O[16 + k] = (u32x4){(u32)lo, (u32)(lo >> 32), (u32)hi, (u32)(hi >> 32)};
            }
            R[0] = ld16(xb, vx, 0); R[1] = ld16(xb, vx, 512);
            SCHEDB();
        };
        auto gemm = [&]() {
            u32 wa0 = lds0 + C_XN + ln * 16, wa1 = wa0 + 40960;
            asm volatile("" : "+v"(wa0), "+v"(wa1));
            f32x4 acc[4];
            bf8 wf[8];
#pragma unroll
            for (int t = 0; t < 8; ++t) wf[t] = *(LDS3 const bf8*)(uintptr_t)(wa0 + t * 1024);
#pragma unroll
            for (int k = 0; k < 20; ++k) {
                if (k >= 16) { PIN(O[k]); NOPV(); }
#pragma unroll
                for (int t = 0; t < 4; ++t) {
                    if (k == 0) MMA0_VV(acc[t], wf[t], O[k]); else MMA_VV(acc[t], wf[(k & 1) * 4 + t], O[k]);
                    SCHEDB();
                    if (k < 18) wf[(k & 1) * 4 + t] = *(LDS3 const bf8*)(uintptr_t)((k < 8 ? wa0 : wa1) + (((k + 2) % 10) * 4 + t) * 1024);
                    SCHEDB();
                }
            }
            asm volatile("s_nop 7" : "+v"(acc[0]), "+v"(acc[1]), "+v"(acc[2]), "+v"(acc[3]));
            float chk = 0.f;
#pragma unroll
            for (int i = 0; i < 8; ++i) {
                const u32 r = R[i >> 2][i & 3], y = pk2bf(acc[i >> 1][(i & 1) * 2], acc[i >> 1][(i & 1) * 2 + 1]);
                const float a = bflo(r) + bflo(y), b = bfhi(r) + bfhi(y);
                dw[i] = pk2bf(a, b);
                chk += a;
            }
            return __any(chk != chk);
        };
        const int T0 = rb + 16 * w;
        load(T0);
        static_for<0, 2>([&](auto PASS) {
            constexpr int pass = CI(PASS);
            const int T = T0 + 64 * pass;
            TT(set * 128 + 32 + c, 1 + pass * 4);
            if (__builtin_expect(gemm(), 0)) {
                for (int tries = 0;; ++tries) {
                    ready(T); load(T);
                    u32 m = max4(max4(0, R[0]), R[1]);
#pragma unroll
                    for (int k = 0; k < 20; ++k) m = max4(m, O[k]);
                    if (!__any(m == ~0u)) break;
                    if (tries >= cap) { cap = 1; break; }
                    __builtin_amdgcn_s_sleep(6);
                }
                gemm();
            }
            TT(set * 128 + 32 + c, 2 + pass * 4);
            if (layer == 1) {
                st32((char*)p.out + (size_t)T * 16 * DM * 2 + cb * 128, (ln & 15) * (DM * 2) + (ln >> 4) * 32, (u32x4){dw[0], dw[1], dw[2], dw[3]}, (u32x4){dw[4], dw[5], dw[6], dw[7]});
                if constexpr (pass == 0) load(T + 64);
            } else {
            float s1 = 0.f, s2 = 0.f;
#pragma unroll
            for (int i = 0; i < 8; ++i) { const float a = bflo(dw[i]), b = bfhi(dw[i]); s1 += a + b; s2 = fmaf(a, a, fmaf(b, b, s2)); }
            s1 = grp_sum(s1); s2 = grp_sum(s2);
            char* stl = (wsp(p) + O_ST) + (size_t)T * 1280;
            st8m(stl + cb * 128, ln * 8, (u32x2){nosent(__float_as_uint(s1)), nosent(__float_as_uint(s2))}, 0xffffull);
            const size_t fo = ACT + (size_t)(T >> 1) * 40960 + (cb * 4 + (T & 1)) * 1024;
            const u32 lo = (ln >> 5) * 2048 + ((ln >> 4) & 1) * 512 + (ln & 15) * 16;
            if constexpr (pass == 0) __builtin_amdgcn_s_sleep(ST_LAG);
            else {
                wait_first_tokens(p, set, lane, cap);
                dma_kb<10>(xn + ACT + (size_t)set * 40960 + w * 10240, lds0 + C_YL + w * 10240, lane * 16);
            }
            for (int spin = 0;; ++spin) {
                u32 m = 0;
                s1 = 0.f; s2 = 0.f;
#pragma unroll
                for (int i = 0; i < 3; ++i) {
                    const int k = (ln >> 4) + 4 * i;
                    const u64 v = __hip_atomic_load((const gu64*)(stl + min(k, 9) * 128 + (ln & 15) * 8), __ATOMIC_RELAXED, __HIP_MEMORY_SCOPE_AGENT);
                    m = max(m, max((u32)v, (u32)(v >> 32)));
                    if (k < 10) { s1 += __uint_as_float((u32)v); s2 += __uint_as_float((u32)(v >> 32)); }
                }
                if (!__any(m == ~0u)) break;
                if (spin >= cap) { cap = 1; break; }
                __builtin_amdgcn_s_sleep(2);
            }
            TT(set * 128 + 32 + c, 3 + pass * 4);
            if constexpr (pass == 0) { if (w > 0) load(T + 64); }
            s1 = grp_sum(s1); s2 = grp_sum(s2);
            const float mu = s1 * (1.0f / DM), mr = rbf(mu), rs = ln_scale(fmaxf(fmaf(-mu, mu, s2 * (1.0f / DM)), 0.f));
            u32 dn[8];
#pragma unroll
            for (int i = 0; i < 8; ++i) dn[i] = ln_pair(dw[i], mr, rs);
            st16s((wsp(p) + O_XN) + fo, lo, (u32x4){dn[0], dn[4], dn[1], dn[5]}); st16s((wsp(p) + O_XN) + fo + 256, lo, (u32x4){dn[2], dn[6], dn[3], dn[7]});
            st16s((wsp(p) + O_XR) + fo, lo, (u32x4){dw[0], dw[4], dw[1], dw[5]}); st16s((wsp(p) + O_XR) + fo + 256, lo, (u32x4){dw[2], dw[6], dw[3], dw[7]});
            TT(set * 128 + 32 + c, 4 + pass * 4);
            if constexpr (pass == 0) { if (w == 0) { load(T + 64); asm volatile("s_waitcnt vmcnt(18)" ::: "memory"); st4_lane0(wsp(p) + O_FL + (rb * 10 + cb) * 128, 1u); } }
            }
        });
    }
    if (layer == 1) break;
    sch += 5 * SLOT; xn += ACT;
    TT(set * 128 + 32 + c, 9);
    }
}

constexpr int B_PQ = 0, B_IM = 131072, B_CS = 155648, B_IS = 176;
__device__ __forceinline__ void bw_role(const LaParams& p, const int hb, char* shm) {
    const int lane = threadIdx.x & 63, w = __builtin_amdgcn_readfirstlane(threadIdx.x >> 6);
    const int fr = lane & 15, g = lane >> 4;
    int cap = SPIN_CAP;
    if (hb == 0 && w == 0) {
        u32 best[2] = {~0u, ~0u};
        const u32 tc = rt32();
        for (int i = 0; i < 256; ++i) {
#pragma unroll
            for (int sd = 0; sd < 2; ++sd) {
                u64 t0, t1;
                u32 z = 0, one = 1;
                asm volatile("s_memtime %0\n\ts_mov_b64 exec, 1\n\ts_waitcnt lgkmcnt(0)\n\tglobal_atomic_add %2, %3, %2, %4 sc0 sc1\n\ts_waitcnt vmcnt(0)\n\t"
                             "s_memtime %1\n\ts_mov_b64 exec, -1\n\ts_waitcnt lgkmcnt(0)"
                             : "=&s"(t0), "=&s"(t1), "+v"(one) : "v"(z), "s"(p.hop + M0_OFF + sd * 8192 + 6144 + (i & 3) * 256) : "memory");
                best[sd] = min(best[sd], (u32)(t1 - t0));
            }
            const u32 el = rt32() - tc, lo = min(best[0], best[1]), hi = max(best[0], best[1]);
            if (el > 1900u || (el > 600u && lo < 700u && hi - lo > 130u && hi - lo < 420u)) break;
        }
        u32 xcc; asm volatile("s_getreg_b32 %0, hwreg(HW_REG_XCC_ID)" : "=s"(xcc));
        st4_lane0(wsp(p) + O_FL + 384 * 128, ((xcc >> 2) & 1) ^ (best[1] < best[0] ? 1u : 0u));
    }
    bf8 WA[64], WV[16];
    {
        const int t = threadIdx.x;
        float* xs = (float*)(shm + 81920);
        float* ph = xs + 128 * 61;
        for (int i = t; i < 400; i += 256) ph[i] = bf2f(p.wphi[hb * 400 + i]);
        const int j = 4 * w + (fr & 3), G = fr >> 2;
        for (int d0 = 0; d0 < DM; d0 += 128) {
            u32 v15[15];
#pragma unroll
            for (int k = 0; k < 15; ++k) { const int i = t + k * 256; v15[k] = *(const u32*)(p.wqkv + (size_t)(d0 + i / 30) * 1920 + ((i % 30) / 10) * 640 + hb * 20 + (i % 10) * 2); }
            __syncthreads();
#pragma unroll
            for (int k = 0; k < 15; ++k) { const int i = t + k * 256; float* o = xs + (i / 30) * 61 + (i % 30) * 2; o[0] = bflo(v15[k]); o[1] = bfhi(v15[k]); }
            __syncthreads();
            for (int kk = 0; kk < 4; ++kk) {
                float v[8];
                const float* xr = xs + (kk * 32 + g * 8) * 61;
                if (j < 10) {
                    const int m = j / 5, f = 5 * G + j % 5;
#pragma unroll
                    for (int e = 0; e < 8; ++e) v[e] = 0.f;
#pragma unroll 4
                    for (int i = 0; i < 20; ++i) {
                        const float pw = ph[i * 20 + f];
#pragma unroll
                        for (int e = 0; e < 8; ++e) v[e] = fmaf(xr[e * 61 + m * 20 + i], pw, v[e]);
                    }
                } else {
#pragma unroll
                    for (int e = 0; e < 8; ++e) v[e] = j < 15 ? xr[e * 61 + 40 + 5 * G + min(j, 14) - 10] : 1.0f;
                }
                *(u32x4*)(shm + ((d0 / 32 + kk) * 4 + w) * 1024 + lane * 16) = (u32x4){pk2bf(v[0], v[1]), pk2bf(v[2], v[3]), pk2bf(v[4], v[5]), pk2bf(v[6], v[7])};
            }
        }
        __syncthreads();
#pragma unroll
        for (int i = 0; i < 64; ++i) WA[i] = *(const bf8*)(shm + i * 1024 + lane * 16);
#pragma unroll
        for (int i = 0; i < 16; ++i) WV[i] = *(const bf8*)(shm + (64 + i) * 1024 + lane * 16);
        __syncthreads();
    }
    auto proj = [&](f32x4 (&acc)[4], f32x4& sq, auto&& fetch) {
        u32x4 X[20];
#pragma unroll
        for (int k = 0; k < 12; ++k) { X[k] = fetch(k); SCHEDB(); }
#pragma unroll
        for (int k = 0; k < 20; ++k) {
#pragma unroll
            for (int t = 0; t < 4; ++t) {
                if (k == 0) MMA0_AV(acc[t], WA[t], X[k]); else if (k < 16) MMA_AV(acc[t], WA[(k * 4 + t) & 63], X[k]); else MMA_VV(acc[t], WV[(k * 4 + t) & 15], X[k]);
            }
            if (k == 0) MMA0_VV(sq, X[k], X[k]); else MMA_VV(sq, X[k], X[k]);
            SCHEDB();
            if (k < 8) X[k + 12] = fetch(k + 12);
            SCHEDB();
        }
        asm volatile("s_nop 7" : "+v"(acc[0]), "+v"(acc[1]), "+v"(acc[2]), "+v"(acc[3]), "+v"(sq));
    };
    {
        f32x4 t, cs[4];
        u32x4 o1 = {0x3f803f80u, 0x3f803f80u, 0x3f803f80u, 0x3f803f80u};
        asm volatile("s_nop 1" : "+v"(o1));
        proj(cs, t, [&](int) { return o1; });
#pragma unroll
        for (int i = 0; i < 4; ++i) *(f32x4*)(shm + B_CS + i * 1024 + lane * 16) = cs[i];
    }
    char* im = shm + B_IM + w * 6144;
    const u32 imr = (u32)(uintptr_t)(LDS3 char*)im + (4 * g + (fr >> 2)) * B_IS + (fr & 3) * 8;
    typedef __attribute__((ext_vector_type(32))) u32 u32x32;
    for (int layer = 0; layer < 2; ++layer) {
        const char* sch = p.hop + (hb & 1) * 8192 + (S_SLOT + layer * 5 + 4) * SLOT;
        f32x4 kv[3][2];
#pragma unroll
        for (int i = 0; i < 6; ++i) ZERO4(kv[i >> 1][i & 1]);
        u32x32 q5;
        u32 tn = 0;
        float vq[5];
        auto phi = [&](const float* x, u32 (&ph)[5]) {
            const float mx = grp_max(fmaxf(fmaxf(fmaxf(x[0], x[1]), fmaxf(x[2], x[3])), x[4]));
            const float mn = -grp_max(-fminf(fminf(fminf(x[0], x[1]), fminf(x[2], x[3])), x[4]));
            float e[10], a = 0.f, b = 0.f;
#pragma unroll
            for (int j = 0; j < 5; ++j) {
                e[j] = __builtin_amdgcn_exp2f((x[j] - mx) * 1.4426950408889634f); a += e[j];
                e[5 + j] = __builtin_amdgcn_exp2f((mn - x[j]) * 1.4426950408889634f); b += e[5 + j];
            }
            a = __builtin_amdgcn_rcpf(grp_sum(a)); b = __builtin_amdgcn_rcpf(grp_sum(b));
#pragma unroll
            for (int j = 0; j < 5; ++j) { e[j] *= a; e[5 + j] *= b; }
#pragma unroll
            for (int j = 0; j < 5; ++j) ph[j] = pk2bf(e[2 * j], e[2 * j + 1]);
        };
        auto phiq = [&](const int it) {
            u32 ph[5];
            phi(vq, ph);
            *(u32x4*)(shm + B_PQ + (w * 32 + it) * 1024 + lane * 16) = (u32x4){ph[0], ph[1], ph[2], ph[3]};
            q5[it] = ph[4];
        };
        for (int it = 0; it < 32; ++it) {
            const int s = 2 * it + (w >> 1);
            if (s + 2 < NSEG) { wait_rt(tn); tn = sched_get<BSL>(sched_peek(sch + (s + 1) * 128 + 4), sch + (s + 1) * 128 + 4, cap); wait_rt(tn + B_LAG); tn += BPEEK; }
            else wait_rt(sched_get(~0u, p.hop + (hb & 1) * 8192 + (T_SLOT + 1) * SLOT + (48 + layer * 4 + (s & 1)) * 128, cap) + T_X1);
            const char* xb = (wsp(p) + O_X1) + layer * ACT + (size_t)s * 40960 + (w & 1) * 256;
            if (it >= 30) TT((hb >> 4) * 128 + 112 + (hb & 15), (it - 30) * 3);
            f32x4 acc[4], sq;
            const char* xk[5];
#pragma unroll
            for (int i = 0; i < 5; ++i) { xk[i] = xb + i * 8192 + 4096; asm volatile("" : "+s"(xk[i])); }
            for (int pass = 0;; ++pass) {
                u32 vo = g * 512 + fr * 16;
                asm volatile("" : "+v"(vo) :: "memory");
                auto fetch = [&](const int k) { return ld16(xk[k >> 2], vo, ((k & 3) - 2) * 2048); };
                proj(acc, sq, fetch);
                if (pass > 0 || !__any(acc[3][3] != acc[3][3])) break;
                CNT(4);
                for (int tries = 0;; ++tries) {
                    asm volatile("" : "+v"(vo) :: "memory");
                    u32 m = 0;
#pragma unroll
                    for (int k = 0; k < 20; k += 10) {
#pragma unroll
                        for (int i = 0; i < 10; ++i) m = max4(m, fetch(k + i));
                        SCHEDB();
                    }
                    if (!__any(m == ~0u)) break;
                    if (tries >= cap) { cap = 1; break; }
                    __builtin_amdgcn_s_sleep(6);
                }
            }
            if (it >= 30) TT((hb >> 4) * 128 + 112 + (hb & 15), (it - 30) * 3 + 1);
            if (s == NSEG - 1 && hb == 5 && w == 3) stamp2(p.hop, 50 + layer * 4);
            const float dsel = (fr & 2) ? ((fr & 1) ? sq[3] : sq[2]) : ((fr & 1) ? sq[1] : sq[0]);
            const float s2 = grp_sum(g == (fr >> 2) ? dsel : 0.f);
            const float mu = acc[3][3] * (1.0f / DM), rs = __builtin_amdgcn_rsqf(fmaxf(fmaf(-mu, mu, s2 * (1.0f / DM)), 0.f) + 1e-5f);
            float v[15];
            f32x4 cs[4];
#pragma unroll
            for (int i = 0; i < 4; ++i) cs[i] = *(const f32x4*)(shm + B_CS + i * 1024 + lane * 16);
#pragma unroll
            for (int j = 0; j < 15; ++j) v[j] = fmaf(-mu, cs[j >> 2][j & 3], acc[j >> 2][j & 3]) * rs;
            u32 ph[5];
            phi(v + 5, ph);
            char* row = im + fr * B_IS;
            *(u32x2*)(row + g * 8) = (u32x2){ph[0], ph[1]};
            *(u32x2*)(row + 32 + g * 8) = (u32x2){ph[2], ph[3]};
            *(u32x2*)(row + 64 + g * 8) = (u32x2){ph[4], 0u};
            *(u32x2*)(row + 96 + g * 8) = (u32x2){pk2bf(v[10], v[11]), pk2bf(v[12], v[13])};
            *(u32x2*)(row + 128 + g * 8) = (u32x2){pk2bf(v[14], 1.0f), 0u};
            s16x4 tf[5];
#pragma unroll
            for (int i = 0; i < 5; ++i) tf[i] = __builtin_amdgcn_ds_read_tr16_b64_v4i16((LDS3 s16x4*)(uintptr_t)(imr + i * 32));
#pragma unroll
            for (int i = 0; i < 6; ++i) MMA16(kv[i >> 1][i & 1], tf[i >> 1], tf[3 + (i & 1)]);
#pragma unroll
            for (int j = 0; j < 5; ++j) vq[j] = v[j];
            if (it < 31) phiq(it);
            if (it >= 30) TT((hb >> 4) * 128 + 112 + (hb & 15), (it - 30) * 3 + 2);
        }
        asm volatile("s_nop 7" : "+v"(kv[0][0]), "+v"(kv[0][1]), "+v"(kv[1][0]), "+v"(kv[1][1]), "+v"(kv[2][0]), "+v"(kv[2][1]));
#pragma unroll
        for (int i = 0; i < 6; ++i) *(f32x4*)(im + i * 1024 + lane * 16) = kv[i >> 1][i & 1];
        RAW_BARRIER();
        bf8 ka[2];
        u32x2 kb[2];
#pragma unroll
        for (int dt = 0; dt < 2; ++dt) {
            f32x4 t[3];
#pragma unroll
            for (int ft = 0; ft < 3; ++ft) {
                const char* a = shm + B_IM + (ft * 2 + dt) * 1024 + lane * 16;
                t[ft] = (*(const f32x4*)a + *(const f32x4*)(a + 6144)) + (*(const f32x4*)(a + 12288) + *(const f32x4*)(a + 18432));
            }
            ka[dt] = pack8(t[0], t[1]);
            kb[dt] = (u32x2){pk2bf(t[2][0], t[2][1]), pk2bf(t[2][2], t[2][3])};
            PIN(ka[dt]); PIN(kb[dt]);
        }
        char* ob = (wsp(p) + O_OB) + layer * ACT + (size_t)w * 20480 + hb * 512;
        TT((hb >> 4) * 128 + 112 + (hb & 15), 6);
        u32 rofs = g * 81920 + 16384 - hb * 384 + fr * 8, mofs = (g & 1) * 81920 + (g >> 1) * 256 + fr * 16, mofs2 = mofs + 2 * 81920;
        asm volatile("" : "+v"(rofs), "+v"(mofs), "+v"(mofs2));
        f32x4 o[2][4][2];
        bf8 qa[2][4];
        u32x2 qb[4];
        auto ldq = [&](const int gr) {
#pragma unroll
            for (int u = 0; u < 4; ++u) qa[gr & 1][u] = *(const bf8*)(shm + B_PQ + (w * 32 + gr * 4 + u) * 1024 + lane * 16);
        };
        auto mm = [&](const int gr, const int k) {
            SCHEDB();
            if (k < 4) { qb[k] = (u32x2){q5[gr * 4 + k], 0u}; PIN(qb[k]); }
            if (k < 8) MMA0_VV(o[gr & 1][k >> 1][k & 1], ka[k & 1], qa[gr & 1][k >> 1]);
            else MMA16(o[gr & 1][(k - 8) >> 1][k & 1], kb[k & 1], qb[(k - 8) >> 1]);
            SCHEDB();
        };
        ldq(0); ldq(1);
        NOPV();
#pragma unroll
        for (int k = 0; k < 16; ++k) mm(0, k);
        static_for<0, 8>([&](auto IT) {
            constexpr int gr = CI(IT);
            auto nx = [&](const int k) { if constexpr (gr < 7) mm(gr + 1, k); else { SCHEDB(); asm volatile("s_nop 0"); SCHEDB(); } };
            if constexpr (gr == 7) asm volatile("s_nop 7");
            u32 d[4][2];
            float x[4];
            nx(0); nx(1);
#pragma unroll
            for (int u = 0; u < 4; ++u) {
                f32x4& a = o[gr & 1][u][0];
                f32x4& b = o[gr & 1][u][1];
                PIN(a); PIN(b);
                float inv = __builtin_amdgcn_rcpf(b[1] + 1e-6f), y0, y1, y2, y3;
                nx(2 + 3 * u);
                asm("v_mul_f32 %0, %4, %8\n\tv_mul_f32 %1, %5, %8\n\tv_mul_f32 %2, %6, %8" : "=&v"(y0), "=&v"(y1), "=&v"(y2), "=&v"(y3) : "v"(a[0]), "v"(a[1]), "v"(a[2]), "v"(a[3]), "v"(inv));
                nx(3 + 3 * u);
                y3 = a[3] * inv; x[u] = b[0] * inv;
                d[u][0] = pk2bf(y0, y1);
                nx(4 + 3 * u);
                d[u][1] = pk2bf(y2, y3);
                if (u & 1) {
                    auto t0 = __builtin_amdgcn_permlane16_swap(d[u - 1][0], d[u][0], false, false);
                    auto t1 = __builtin_amdgcn_permlane16_swap(d[u - 1][1], d[u][1], false, false);
                    __builtin_amdgcn_global_store_b128((gu32x4*)(ob + (u == 1 ? mofs : mofs2)), (u32x4){nosent(t0[0]), nosent(t1[0]), nosent(t0[1]), nosent(t1[1])}, "agent");
                }
            }
            if constexpr (gr == 5) phiq(31);
            if constexpr (gr < 6) ldq(gr + 2);
            nx(14);
            auto a0 = __builtin_amdgcn_permlane32_swap(__float_as_uint(x[0]), __float_as_uint(x[2]), false, false);
            auto a1 = __builtin_amdgcn_permlane32_swap(__float_as_uint(x[1]), __float_as_uint(x[3]), false, false);
            nx(15);
            auto b0 = __builtin_amdgcn_permlane16_swap(a0[0], a1[0], false, false);
            auto b2 = __builtin_amdgcn_permlane16_swap(a0[1], a1[1], false, false);
            __hip_atomic_store((gu64*)(ob + rofs), (u64)nosent(pk2bf(__uint_as_float(b0[0]), __uint_as_float(b0[1]))) | ((u64)nosent(pk2bf(__uint_as_float(b2[0]), __uint_as_float(b2[1]))) << 32), __ATOMIC_RELAXED, __HIP_MEMORY_SCOPE_AGENT);
            ob += 4 * 81920;
            asm volatile("" : "+s"(ob));
        });
        RAW_BARRIER();
        TT((hb >> 4) * 128 + 112 + (hb & 15), 7);
    }
}

__global__ void __launch_bounds__(256, 1) la_kernel(LaParams p) {
    extern __shared__ __attribute__((aligned(16))) char shm[];
    p.hop = align2m(p.ws + O_HOP);
    if (threadIdx.x == 0) {
        u32 xcc; asm volatile("s_getreg_b32 %0, hwreg(HW_REG_XCC_ID)" : "=s"(xcc));
        int role = 255;
        for (int a = 0; a < 8; ++a) {
            const u32 v = (xcc + a) & 7;
            const u32 k = __hip_atomic_fetch_add((gu32*)(((u32*)(p.ws + O_CTL)) + v * 32), 1u, __ATOMIC_RELAXED, __HIP_MEMORY_SCOPE_AGENT);
            if (k < 32) { role = v * 32 + k; break; }
        }
        *(int*)shm = role;
    }
    __syncthreads();
    const int role = __builtin_amdgcn_readfirstlane(*(int*)shm);
    __syncthreads();
    const int set = role >> 7, id = (role >> 5) & 3, k = role & 31;
    if (k < 8) qk_role(p, set, id * 8 + k, shm);
    else if (k < 28) comb_role(p, set, id * 20 + k - 8, shm);
    else bw_role(p, (role >> 5) * 4 + k - 28, shm);
}

struct PrepParams { const u16* x; const u16* mem0; const u16* wo; char* ws; };
constexpr int N_LN = 128, N_WO = 200, N_FILL = 512;
__global__ void __launch_bounds__(256) prep_kernel(PrepParams q) {
    int b = blockIdx.x;
    const int t = threadIdx.x, lane = t & 63, w = t >> 6, fr = lane & 15, g = lane >> 4;
    char* hop = align2m(q.ws + O_HOP);
    if (b < N_LN) {
        const int tok = b * 16 + w * 4 + g;
        u32x4 d[3][2];
        float s = 0.f;
#pragma unroll
        for (int i = 0; i < 3; ++i) {
            const u32x4* a = (const u32x4*)(q.x + (size_t)tok * DM + min(fr + 16 * i, 39) * 16);
            d[i][0] = a[0]; d[i][1] = a[1];
        }
#pragma unroll
        for (int i = 0; i < 3; ++i)
#pragma unroll
            for (int e = 0; e < 8; ++e) if (fr + 16 * i < 40) s += bflo(d[i][e >> 2][e & 3]) + bfhi(d[i][e >> 2][e & 3]);
        const float mu = row16_sum(s) * (1.0f / DM);
        float v = 0.f;
#pragma unroll
        for (int i = 0; i < 3; ++i)
#pragma unroll
            for (int e = 0; e < 8; ++e) if (fr + 16 * i < 40) { const float a = bflo(d[i][e >> 2][e & 3]) - mu, c = bfhi(d[i][e >> 2][e & 3]) - mu; v = fmaf(a, a, fmaf(c, c, v)); }
        const float rs = ln_scale(row16_sum(v) * (1.0f / DM)), mr = rbf(mu);
#pragma unroll
        for (int i = 0; i < 3; ++i) {
            const int blk = fr + 16 * i;
            if (blk >= 40) continue;
            const size_t o = (size_t)(tok >> 5) * 40960 + ((blk >> 1) * 2 + ((tok >> 4) & 1)) * 1024 + ((blk & 1) * 32 + (tok & 15)) * 16;
            u32 n[8];
#pragma unroll
            for (int e = 0; e < 8; ++e) n[e] = ln_pair(d[i][e >> 2][e & 3], mr, rs);
            *(u32x4*)(q.ws + O_XR + o) = (u32x4){d[i][0][0], d[i][1][0], d[i][0][1], d[i][1][1]};
            *(u32x4*)(q.ws + O_XR + o + 256) = (u32x4){d[i][0][2], d[i][1][2], d[i][0][3], d[i][1][3]};
            *(u32x4*)(q.ws + O_XN + o) = (u32x4){n[0], n[4], n[1], n[5]};
            *(u32x4*)(q.ws + O_XN + o + 256) = (u32x4){n[2], n[6], n[3], n[7]};
        }
        return;
    }
    b -= N_LN;
    if (b < N_WO) {
        const int cb = b / 20, kk = b % 20;
        const u16* col = q.wo + cb * 64 + 16 * (fr >> 2) + 4 * w + (fr & 3);
        u32 v[8];
#pragma unroll
        for (int e = 0; e < 8; ++e) {
            const int row = kk < 16 ? (2 * kk + (g >> 1)) * 20 + 5 * (2 * (g & 1) + (e >> 2)) + (e & 3) : (8 * (kk - 16) + 2 * g + (e >> 2)) * 20 + 5 * (e & 3) + 4;
            v[e] = col[(size_t)row * DM];
        }
        *(u32x4*)(q.ws + O_WO + (size_t)cb * 81920 + (kk * 4 + w) * 1024 + lane * 16) = (u32x4){v[0] | v[1] << 16, v[2] | v[3] << 16, v[4] | v[5] << 16, v[6] | v[7] << 16};
        return;
    }
    b -= N_WO;
    const u32x4 SENT = {~0u, ~0u, ~0u, ~0u};
    const u32 i0 = b * 256 + t;
    if (b == 0) ((u32*)(q.ws + O_CTL))[t] = 0;
    if (i0 < 8 * 2 * 320) {
        const int sl = i0 / 640, e = i0 % 320;
        const u32x4 v = *(const u32x4*)(q.mem0 + (e & 7) * DM + ((sl & 1) * 40 + (e >> 3)) * 8);
        st16(hop + M0_OFF + (i0 / 320) * 8192 + e * 16, (u32x4){nosent(v[0]), nosent(v[1]), nosent(v[2]), nosent(v[3])});
    }
    for (u32 i = i0; i < HOP_BYTES / 16; i += N_FILL * 256) st16(hop + (size_t)i * 16, SENT);
    for (u32 i = i0; i < (O_XN - O_FL) / 16; i += N_FILL * 256) st16(q.ws + O_FL + (size_t)i * 16, SENT);
}

extern "C" void kernel_launch(void* const* d_in, const int* in_sizes, int n_in,
                              void* d_out, int out_size, void* d_ws, size_t ws_size,
                              hipStream_t stream) {
    PrepParams q;
    q.x = (const u16*)d_in[0]; q.mem0 = (const u16*)d_in[1];
    q.wo = (const u16*)d_in[10];
    q.ws = (char*)d_ws;
    prep_kernel<<<N_LN + N_WO + N_FILL, 256, 0, stream>>>(q);
    LaParams p;
    p.ws = (char*)d_ws; p.out = (u16*)d_out; p.hop = (char*)d_ws;
    p.Wq = (const u16*)d_in[2]; p.Wk = (const u16*)d_in[3]; p.Wv = (const u16*)d_in[4]; p.Wo = (const u16*)d_in[5];
    p.wqkv = (const u16*)d_in[8]; p.wphi = (const u16*)d_in[9];
    (void)hipFuncSetAttribute((const void*)la_kernel, hipFuncAttributeMaxDynamicSharedMemorySize, LA_LDS);
    la_kernel<<<256, 256, LA_LDS, stream>>>(p);
}
```

```cpp
#include <hip/hip_runtime.h>
#include <hip/hip_bf16.h>
#include <stdint.h>
#include <type_traits>

typedef unsigned short u16;
typedef unsigned int u32;
typedef unsigned long long u64;
using bf16x8 = __attribute__((ext_vector_type(8))) short;
using f32x4 = __attribute__((ext_vector_type(4))) float;

constexpr int S_TOK = 2048, DM = 640, NH = 32, DH = 192;
constexpr int MEM = 8, SEG = 32, NSEG = S_TOK / SEG;
constexpr int HD = NH * DH;

__device__ __forceinline__ float bf2f(u16 v) { return __uint_as_float(((u32)v) << 16); }
__device__ __forceinline__ u32 pk2bf(float lo, float hi) {
    typedef __attribute__((ext_vector_type(2))) float f32x2_t;
    typedef __attribute__((ext_vector_type(2))) __bf16 bf16x2_t;
    bf16x2_t r = __builtin_convertvector((f32x2_t){lo, hi}, bf16x2_t);
    return __builtin_bit_cast(u32, r);
}
__device__ __forceinline__ float rbf(float f) { return __uint_as_float(pk2bf(f, f) << 16); }
typedef __attribute__((ext_vector_type(8))) __bf16 bf8;
typedef __attribute__((ext_vector_type(16))) __bf16 bf16v;
typedef __attribute__((ext_vector_type(4))) __bf16 bf4;
typedef __attribute__((ext_vector_type(4))) short s16x4;
using u32x4 = __attribute__((ext_vector_type(4))) u32;
using u32x2 = __attribute__((ext_vector_type(2))) u32;
typedef __attribute__((address_space(1))) u32 gu32;
typedef __attribute__((address_space(1))) u64 gu64;
#define LDS3 __attribute__((address_space(3)))

constexpr int SLOT = 16384;
#define PCSV 3
constexpr int PCS = PCSV, P_SLOT0 = 0, M_SLOT0 = 16 * PCS, S_SLOT = M_SLOT0 + 32, T_SLOT = S_SLOT + 10, N_SLOT = T_SLOT + 2;
constexpr size_t HOP_BYTES = (size_t)N_SLOT * SLOT, M0_OFF = HOP_BYTES;
constexpr size_t PX_BUF = 4 * 32 * 640, PX_BYTES = 8 * PX_BUF;
constexpr int LA_LDS = 162816 + 256;
constexpr int SPIN_CAP = 1 << 16;
#define D1I 131
#define D2I 53
#define MG1 10
#define MG2 11
#define DUP 5
#define PACE 6
#define CLAG 48
#define T_PX 26
#define T_X1 75
#define T_OB 150
#define T_TOK 350
#define ST_LAG 6
#define OB_STEP 25
#define OB_COLD 20
#define NH_ 2
#define W0D 6
#define LATEP 2
#define SYNCB 1
#define PACE2 5
#define BPEEK 400
#define PLIN 1
#define SLK 6
#define FSL 8
#define E0 110
#define HOLD 4
#define DOWN 1
#define EY 35
#define BOOST 3
#define DSH "1"
#define PAUSE 160u
#define BSL 40
#define PKK 18
#define PKT 2
#define BPF 20
#define BUMP 2
#define WRONG 0u
#define RECV 4
#define B_LAG 65
#define ELATE 85

__device__ __forceinline__ void st16(void* p, u32x4 v) {
    asm volatile("global_store_dwordx4 %0, %1, off sc1\n\ts_nop 2" :: "v"(p), "v"(v) : "memory");
}
typedef __attribute__((address_space(1))) u32x4 gu32x4;
__device__ __forceinline__ u32x4 ld16(const char* base, u32 off, int imm) { return __builtin_amdgcn_global_load_b128((gu32x4*)(base + off + imm), "agent"); }
__device__ __forceinline__ u32 nosent(u32 v) { return min(v, 0xFFFFFFFEu); }
__device__ __forceinline__ u32 rt32() { return (u32)__builtin_amdgcn_s_memrealtime(); }
__device__ __forceinline__ void wait_rt(u32 t) {
    int d = (int)(t - rt32()) - 4;
    if (d > 4096 || d < 0) return;
    while (d > 7) { if (d > 18) __builtin_amdgcn_s_sleep(2); d = max((int)(t - rt32()) - 4, 0); }
    asm volatile("s_bitcmp1_b32 %0, 2\n\ts_cbranch_scc0 1f\n\ts_nop 15\n\ts_nop 7\n1:\n\ts_bitcmp1_b32 %0, 1\n\ts_cbranch_scc0 2f\n\ts_nop 12\n2:\n\t"
                 "s_bitcmp1_b32 %0, 0\n\ts_cbranch_scc0 3f\n\ts_nop 7\n3:" :: "s"(d) : "scc");
}
template <int N> __device__ __forceinline__ void dma_kb(const char* src, u32 lds_dst, u32 lane16) {
#pragma unroll
    for (int i = 0; i < N; i += 4) {
        u32 keep;
        if (N - i >= 4)
            asm volatile("s_mov_b32 %0, m0\n\ts_mov_b32 m0, %3\n\ts_nop 2\n\tglobal_load_lds_dwordx4 %1, %2 sc1\n\tglobal_load_lds_dwordx4 %1, %2 offset:1024 sc1\n\t"
                         "global_load_lds_dwordx4 %1, %2 offset:2048 sc1\n\tglobal_load_lds_dwordx4 %1, %2 offset:3072 sc1\n\ts_mov_b32 m0, %0"
                         : "=&s"(keep) : "v"(lane16), "s"(src + i * 1024), "s"(lds_dst + i * 1024) : "memory");
        else if (N - i == 2)
            asm volatile("s_mov_b32 %0, m0\n\ts_mov_b32 m0, %3\n\ts_nop 2\n\tglobal_load_lds_dwordx4 %1, %2 sc1\n\tglobal_load_lds_dwordx4 %1, %2 offset:1024 sc1\n\ts_mov_b32 m0, %0"
                         : "=&s"(keep) : "v"(lane16), "s"(src + i * 1024), "s"(lds_dst + i * 1024) : "memory");
        else
            asm volatile("s_mov_b32 %0, m0\n\ts_mov_b32 m0, %3\n\ts_nop 2\n\tglobal_load_lds_dwordx4 %1, %2 sc1\n\ts_mov_b32 m0, %0"
                         : "=&s"(keep) : "v"(lane16), "s"(src + i * 1024), "s"(lds_dst + i * 1024) : "memory");
    }
}
template <int OFF> __device__ __forceinline__ void dma_one(const char* src, u32 lds_dst, u32 lane16) {
    asm volatile("s_mov_b32 m0, %2\n\ts_nop 0\n\tglobal_load_lds_dwordx4 %0, %1 offset:%3 sc1" :: "v"(lane16), "s"(src), "s"(lds_dst), "n"(OFF) : "memory");
}
__device__ __forceinline__ u32 ringp(u32 a, u32 b) { return a + b; }
__device__ __forceinline__ void dma_gather(const char* src, u32 lds_dst, u32 off) {
    u32 keep;
    asm volatile("s_mov_b32 %0, m0\n\ts_mov_b32 m0, %3\n\ts_nop 2\n\tglobal_load_lds_dwordx4 %1, %2\n\ts_mov_b32 m0, %0"
                 : "=&s"(keep) : "v"(off), "s"(src), "s"(lds_dst) : "memory");
}
__device__ __forceinline__ void st16s(char* base, u32 off, u32x4 v) {
    asm volatile("s_nop 4\n\tglobal_store_dwordx4 %0, %1, %2 sc1\n\ts_nop 2" :: "v"(off), "v"(v), "s"(base) : "memory");
}
__device__ __forceinline__ void arm16(char* base, u32 off) {
    u32x4 v;
    asm volatile("v_mov_b64 %0, -1\n\tv_mov_b64 %1, -1" : "=v"(((u64*)&v)[0]), "=v"(((u64*)&v)[1]));
    st16s(base, off, v);
}
template <int B, int E, typename F> __device__ __forceinline__ void static_for(F&& f) {
    if constexpr (B < E) { f(std::integral_constant<int, B>()); static_for<B + 1, E>(f); }
}
#define CI(x) decltype(x)::value
#define RAW_BARRIER() do { asm volatile("s_waitcnt lgkmcnt(0)" ::: "memory"); __builtin_amdgcn_s_barrier(); asm volatile("" ::: "memory"); } while (0)
#define VM0() asm volatile("s_waitcnt vmcnt(0)" ::: "memory")
#define SCHEDB() __builtin_amdgcn_sched_barrier(0)
__device__ __forceinline__ bf8 lo8(const bf16v& v) { return __builtin_shufflevector(v, v, 0, 1, 2, 3, 4, 5, 6, 7); }
__device__ __forceinline__ bf8 hi8(const bf16v& v) { return __builtin_shufflevector(v, v, 8, 9, 10, 11, 12, 13, 14, 15); }
__device__ __forceinline__ bf16v cat8(bf8 a, bf8 b) { return __builtin_shufflevector(a, b, 0, 1, 2, 3, 4, 5, 6, 7, 8, 9, 10, 11, 12, 13, 14, 15); }
#define MMA_VV(c, a, b) asm volatile("v_mfma_f32_16x16x32_bf16 %0, %1, %2, %0" : "+v"(c) : "v"(a), "v"(b))
#define MMA_AV(c, a, b) asm volatile("v_mfma_f32_16x16x32_bf16 %0, %1, %2, %0" : "+v"(c) : "a"(a), "v"(b))
#define MMA_VA(c, a, b) asm volatile("v_mfma_f32_16x16x32_bf16 %0, %1, %2, %0" : "+v"(c) : "v"(a), "a"(b))
#define MMA0_VV(c, a, b) asm volatile("v_mfma_f32_16x16x32_bf16 %0, %1, %2, 0" : "=&v"(c) : "v"(a), "v"(b))
#define MMA0_AV(c, a, b) asm volatile("v_mfma_f32_16x16x32_bf16 %0, %1, %2, 0" : "=&v"(c) : "a"(a), "v"(b))
#define MMA0_VA(c, a, b) asm volatile("v_mfma_f32_16x16x32_bf16 %0, %1, %2, 0" : "=&v"(c) : "v"(a), "a"(b))
#define MMA16(c, a, b) asm volatile("v_mfma_f32_16x16x16_bf16 %0, %1, %2, %0" : "+v"(c) : "v"(a), "v"(b))
#define MMA16_0(c, a, b) asm volatile("v_mfma_f32_16x16x16_bf16 %0, %1, %2, 0" : "=&v"(c) : "v"(a), "v"(b))
#define SMM_V(c, a, b, i) asm volatile("v_smfmac_f32_16x16x64_bf16 %0, %1, %2, %3" : "+v"(c) : "v"(a), "v"(b), "v"(i))
#define SMM_A(c, a, b, i) asm volatile("v_smfmac_f32_16x16x64_bf16 %0, %1, %2, %3" : "+v"(c) : "v"(a), "a"(b), "v"(i))
#define NOPV() asm volatile("s_nop 1")
#define PIN(x) asm volatile("" : "+v"(x))
#define SETTLE() asm volatile("s_nop 7\n\ts_nop 3")
__device__ __forceinline__ bf8 pack8(f32x4 a, f32x4 b) {
    u32x4 r = {pk2bf(a[0], a[1]), pk2bf(a[2], a[3]), pk2bf(b[0], b[1]), pk2bf(b[2], b[3])};
    return __builtin_bit_cast(bf8, r);
}
__device__ __forceinline__ u32 max4(u32 m, u32x4 g) { m = max(max(m, g[0]), g[1]); return max(max(m, g[2]), g[3]); }
#define ZERO4(c) asm volatile("v_mov_b64 %0, 0\n\tv_mov_b64 %1, 0" : "=v"(((u64*)&(c))[0]), "=v"(((u64*)&(c))[1]))
__device__ __forceinline__ bf8 lds8(const char* shm, int off) { return *(const bf8*)(shm + off); }
__device__ __forceinline__ bf16v ldfrag(const char* p) { return cat8(*(const bf8*)p, *(const bf8*)(p + 1024)); }
template <int CTRL> __device__ __forceinline__ float fdpp(float x) {
    return __builtin_bit_cast(float, __builtin_amdgcn_mov_dpp(__builtin_bit_cast(int, x), CTRL, 0xf, 0xf, true));
}
__device__ __forceinline__ float halfsum(float a, float b) {
    auto r = __builtin_amdgcn_permlane32_swap(__float_as_uint(a), __float_as_uint(b), false, false);
    return __uint_as_float(r[0]) + __uint_as_float(r[1]);
}
__device__ __forceinline__ float grp_sum(float x) {
    auto t = __builtin_amdgcn_permlane16_swap(__float_as_uint(x), __float_as_uint(x), false, false);
    x = __uint_as_float(t[0]) + __uint_as_float(t[1]);
    return halfsum(x, x);
}
__device__ __forceinline__ float grp_max(float x) {
    auto t = __builtin_amdgcn_permlane16_swap(__float_as_uint(x), __float_as_uint(x), false, false);
    x = fmaxf(__uint_as_float(t[0]), __uint_as_float(t[1]));
    auto u = __builtin_amdgcn_permlane32_swap(__float_as_uint(x), __float_as_uint(x), false, false);
    return fmaxf(__uint_as_float(u[0]), __uint_as_float(u[1]));
}
__device__ __forceinline__ float bflo(u32 v) { return __uint_as_float(v << 16); }
__device__ __forceinline__ float bfhi(u32 v) { return __uint_as_float(v & 0xffff0000u); }
__device__ __forceinline__ float ln_scale(float var) { return __builtin_amdgcn_rcpf(rbf(__builtin_amdgcn_sqrtf(rbf(rbf(var) + 1e-5f)))); }
__device__ __forceinline__ u32 ln_pair(u32 v, float m, float rs) { const u32 d = pk2bf(bflo(v) - m, bfhi(v) - m); return pk2bf(bflo(d) * rs, bfhi(d) * rs); }
__device__ __forceinline__ void st4_lane0(char* base, u32 v) {
    u32 z, d;
    base = (char*)(((u64)(u32)__builtin_amdgcn_readfirstlane((u32)((u64)base >> 32)) << 32) | (u64)(u32)__builtin_amdgcn_readfirstlane((u32)(u64)base));
    asm volatile("s_mov_b64 exec, 1\n\tv_mov_b32 %0, 0\n\tv_mov_b32 %1, %3\n\ts_nop 1\n\tglobal_store_dword %0, %1, %2 sc1\n\ts_mov_b64 exec, -1" : "=&v"(z), "=&v"(d) : "s"(base), "s"(v) : "memory");
}
__device__ __forceinline__ void stamp2(char* hop, const int line) {
    const u32 t = nosent(rt32());
    st4_lane0(hop + (T_SLOT + 1) * SLOT + line * 128, t); st4_lane0(hop + (T_SLOT + 1) * SLOT + 8192 + line * 128, t);
}
__device__ __forceinline__ int smax(int a, int b) { int r; asm volatile("s_max_i32 %0, %1, %2" : "=s"(r) : "s"(a), "s"(b) : "scc"); return r; }
__device__ __forceinline__ int track(int d, int sample, int down) {
    int e, a, b;
    asm("s_sub_i32 %0, %3, %4\n\ts_cmp_gt_i32 %3, 0\n\ts_cselect_b32 %0, %0, 0\n\ts_min_i32 %1, %0, %6\n\ts_ashr_i32 %2, %0, " DSH "\n\ts_max_i32 %2, %2, %5\n\t"
        "s_min_i32 %2, %2, 0\n\ts_max_i32 %1, %1, %2" : "=&s"(e), "=&s"(a), "=&s"(b) : "s"(sample), "s"(d), "s"(-down), "n"(DUP) : "scc");
    return d + a;
}
__device__ __forceinline__ u32 sched_peek(const char* p) { return __hip_atomic_load((const gu32*)p, __ATOMIC_RELAXED, __HIP_MEMORY_SCOPE_AGENT); }
template <int SL = 3> __device__ __forceinline__ u32 sched_get(u32 v, const char* p, int& cap) {
    for (int spin = 0; v == ~0u; ++spin) {
        if (spin >= cap) { cap = 1; break; }
        __builtin_amdgcn_s_sleep(SL);
        v = sched_peek(p);
    }
    return __builtin_amdgcn_readfirstlane(v);
}

#define TRDECL
#define TR(i)
#define TRV(i, v)
#define TRFLUSH(role)
#define TRC(i)
constexpr size_t ACT = (size_t)S_TOK * DM * 2;
constexpr size_t O_DBG = 0, O_CTL = 2u << 20, O_FL = O_CTL + 4096, O_PX = O_FL + 640 * 128, O_ST = O_PX + PX_BYTES, O_X1 = O_ST + 128 * 1280,
                 O_OB = O_X1 + 2 * ACT, O_XN = O_OB + 2 * ACT, O_XR = O_XN + 2 * ACT, O_WO = O_XR + 2 * ACT,
                 O_WVO = O_WO + 10 * 81920, O_HOP = O_WVO + (size_t)80 * 4 * 40 * 2048, WS_BYTES = O_HOP + (4u << 20);
#define TT(role, i)
#define CNT(i)
struct LaParams { char* ws; u16* out; char* hop; const u16* Wq; const u16* Wk; const u16* Wv; const u16* Wo; const u16* wqkv; const u16* wphi; };
__device__ __forceinline__ int kperm(int g, int e) { return 16 * (g >> 1) + 8 * ((e & 3) >> 1) + 4 * (g & 1) + 2 * (e >> 2) + (e & 1); }
__device__ __forceinline__ bf8 tr8(u32 a, u32 b) {
    const s16x4 lo = __builtin_amdgcn_ds_read_tr16_b64_v4i16((LDS3 s16x4*)(uintptr_t)a), hi = __builtin_amdgcn_ds_read_tr16_b64_v4i16((LDS3 s16x4*)(uintptr_t)b);
    return __builtin_bit_cast(bf8, __builtin_shufflevector(lo, hi, 0, 1, 2, 3, 4, 5, 6, 7));
}
__device__ __forceinline__ char* wsp(const LaParams& p) { char* b = p.ws; asm volatile("" : "+s"(b)); return b; }
template <int N, int SL = 4> __device__ __forceinline__ void wait_flags(const char* f, const u32 (&off)[N], int& cap) {
    for (int spin = 0;; ++spin) {
        u32 m = 0;
#pragma unroll
        for (int i = 0; i < N; ++i) m = max(m, sched_peek(f + off[i]));
        if (!__any(m == ~0u)) break;
        if (spin >= cap) { cap = 1; break; }
        __builtin_amdgcn_s_sleep(SL);
    }
    asm volatile("" ::: "memory");
}
__device__ __forceinline__ void wait_all_tokens(const LaParams& p, u32 lane, int& cap) {
    asm volatile("" : "+v"(lane));
    const u32 off[3] = {lane * 128, lane * 128 + 8192, min(lane + 128, 159u) * 128};
    wait_flags<3>(wsp(p) + O_FL + 192 * 128, off, cap);
}
__device__ __forceinline__ void wait_first_tokens(const LaParams& p, const int set, u32 lane, int& cap) {
    asm volatile("" : "+v"(lane));
    const u32 off[1] = {(set * 20 + min(lane, 39u) + (lane >= 20 ? 20 : 0)) * 128};
    wait_flags<1>(wsp(p) + O_FL, off, cap);
}
__device__ __forceinline__ float row16_sum(float x) { x += fdpp<0x128>(x); x += fdpp<0x124>(x); x += fdpp<0x122>(x); return x + fdpp<0x121>(x); }
__device__ __forceinline__ int get_side(const LaParams& p, int& cap) { return sched_get(sched_peek(wsp(p) + O_FL + 384 * 128), wsp(p) + O_FL + 384 * 128, cap) & 1; }
__device__ __forceinline__ char* align2m(char* p) { return p + ((size_t)(-(intptr_t)p) & ((2u << 20) - 1)); }

constexpr int Q_XA = 0, Q_PART = 20480, Q_TR = 48640, Q_MK = 54784, Q_WL = 55808, Q_ST = 162304, Q_PS = 44;

#define DPP1(op, ror) op "_dpp %0, %0, %0 row_ror:" ror " row_mask:0xf bank_mask:0xf\n\t"
#define DPP2(op, ror) DPP1(op, ror) op "_dpp %1, %1, %1 row_ror:" ror " row_mask:0xf bank_mask:0xf\n\ts_nop 0\n\t"
__device__ __forceinline__ void st16m(char* base, u32 off, u32x4 v, u64 mask) {
    asm volatile("s_mov_b64 exec, %3\n\ts_nop 3\n\tglobal_store_dwordx4 %0, %1, %2 sc1\n\ts_mov_b64 exec, -1\n\ts_nop 1" :: "v"(off), "v"(v), "s"(base), "s"(mask) : "memory");
}
__device__ __forceinline__ void st16f(char* base, u32 off, u32x4 v, u64 mask) {
    asm volatile("s_mov_b64 exec, %3\n\tglobal_store_dwordx4 %0, %1, %2 sc1\n\ts_mov_b64 exec, -1\n\ts_nop 1" :: "v"(off), "v"(v), "s"(base), "s"(mask) : "memory");
}
__device__ __forceinline__ void st16f64(char* base, u32 off, u32x4 v, u64 mask) {
    asm volatile("s_mov_b64 exec, %3\n\tglobal_store_dwordx4 %0, %1, %2 offset:64 sc1\n\ts_mov_b64 exec, -1\n\ts_nop 1" :: "v"(off), "v"(v), "s"(base), "s"(mask) : "memory");
}
__device__ __forceinline__ void st32(char* base, u32 off, u32x4 a, u32x4 b) {
    asm volatile("s_nop 4\n\tglobal_store_dwordx4 %0, %1, %3\n\tglobal_store_dwordx4 %0, %2, %3 offset:16\n\ts_nop 2" :: "v"(off), "v"(a), "v"(b), "s"(base) : "memory");
}
__device__ __forceinline__ void st8m(char* base, u32 off, u32x2 v, u64 mask) {
    asm volatile("s_mov_b64 exec, %3\n\ts_nop 3\n\tglobal_store_dwordx2 %0, %1, %2 sc1\n\ts_mov_b64 exec, -1" :: "v"(off), "v"(v), "s"(base), "s"(mask) : "memory");
}
__device__ __forceinline__ u32 pk2bf_abs(float lo, float hi) {
    u32 r;
    asm("v_cvt_pk_bf16_f32 %0, |%1|, |%2|" : "=v"(r) : "v"(lo), "v"(hi));
    return r;
}
__device__ __forceinline__ u32 p_line(const int h, const int piece, const u32 gap) {
    u32 a = (h >> 4) * gap + ((h >> 1) & 7) * 1024 + (h & 1) * 128, b = 2 * gap + h * 128;
    asm volatile("" : "+s"(a), "+s"(b));
    return piece < 4 ? a + piece * 256 : b;
}
constexpr float SM_C = 0.07216878364870322f * 1.4426950408889634f, NEG_BIG = -0x1p100f;
constexpr u64 SM_MASK = 0x0155015501550155ull;
template <typename MID> __device__ __forceinline__ void softmax_mem(const float* p, char* dst, char* dst2, const u32 off, MID&& mid) {
    f32x4 a[4];
#pragma unroll
    for (int i = 0; i < 4; ++i) a[i] = *(const f32x4*)(p + i * 40 * Q_PS);
    SCHEDB(); mid(); SCHEDB();
    f32x4 x = (a[0] + a[1]) + (a[2] + a[3]);
    float ml, mx = fmaxf(fmaxf(x[0], x[1]), fmaxf(x[2], x[3])), e0, e1, e2, e3;
    asm volatile("v_mul_f32 %5, %10, %0\n\ts_nop 0\n\t"
        DPP1("v_max_f32", "8") "v_fma_f32 %1, %6, %10, -%5\n\tv_fma_f32 %2, %7, %10, -%5\n\t"
        DPP1("v_max_f32", "4") "v_fma_f32 %3, %8, %10, -%5\n\tv_fma_f32 %4, %9, %10, -%5\n\t"
        DPP1("v_max_f32", "2") "v_exp_f32 %1, %1\n\tv_exp_f32 %2, %2\n\t"
        DPP1("v_max_f32", "1") "v_exp_f32 %3, %3\n\tv_fma_f32 %5, -%0, %10, %5\n\tv_exp_f32 %4, %4\n\tv_add_f32 %0, %1, %2\n\tv_exp_f32 %5, %5\n\tv_add_f32 %0, %0, %3\n\t"
        "v_add_f32 %0, %0, %4\n\tv_mul_f32 %0, %0, %5\n\tv_mul_f32 %1, %1, %5\n\tv_mul_f32 %2, %2, %5\n\t"
        DPP1("v_add_f32", "8") "v_mul_f32 %3, %3, %5\n\tv_mul_f32 %4, %4, %5\n\t" DPP1("v_add_f32", "4") "s_nop 1\n\t"
        DPP1("v_add_f32", "2") "s_nop 1\n\t" DPP1("v_add_f32", "1") "v_rcp_f32 %0, %0\n\ts_nop 0\n\t"
        "v_mul_f32 %1, %1, %0\n\tv_mul_f32 %2, %2, %0\n\tv_mul_f32 %3, %3, %0\n\tv_mul_f32 %4, %4, %0"
        : "+v"(mx), "=&v"(e0), "=&v"(e1), "=&v"(e2), "=&v"(e3), "=&v"(ml) : "v"(x[0]), "v"(x[1]), "v"(x[2]), "v"(x[3]), "s"(SM_C) : "memory");
    u32x4 o;
    asm volatile("v_cvt_pk_bf16_f32 %4, |%4|, |%6|\n\tv_cvt_pk_bf16_f32 %5, |%5|, |%7|\n\ts_nop 0\n\t"
                 "v_mov_b32_dpp %0, %4 quad_perm:[0,0,2,2] row_mask:0xf bank_mask:0xf\n\tv_mov_b32_dpp %1, %5 quad_perm:[0,0,2,2] row_mask:0xf bank_mask:0xf\n\t"
                 "v_mov_b32_dpp %2, %4 quad_perm:[1,1,3,3] row_mask:0xf bank_mask:0xf\n\tv_mov_b32_dpp %3, %5 quad_perm:[1,1,3,3] row_mask:0xf bank_mask:0xf"
                 : "=&v"(o[0]), "=&v"(o[1]), "=&v"(o[2]), "=&v"(o[3]), "+v"(e0), "+v"(e2) : "v"(e1), "v"(e3), "s"(dst), "s"(dst2));
    asm volatile("s_mov_b64 exec, %4\n\tglobal_store_dwordx4 %0, %1, %2 sc1\n\tglobal_store_dwordx4 %0, %1, %3 sc1\n\ts_mov_b64 exec, -1\n\ts_nop 1"
                 :: "v"(off), "v"(o), "s"(dst), "s"(dst2), "s"(0x03ff03ff03ff03ffull) : "memory");
}
struct SmTok {
    float x[2][4], b1[2][4], b2[2][4], b3[2][4], mx[2], sm[2], t0, t1;
    u32 d00, d01, d10, d11;
    static constexpr int N = 88;
    __device__ __forceinline__ void load(const float* p, const int j) {
        const int n = j & 1, k = j >> 1;
        const f32x4 a = *(const f32x4*)(p + (k * 40 + n * 4) * Q_PS);
#pragma unroll
        for (int i = 0; i < 4; ++i) (k == 0 ? x : (k == 1 ? b1 : (k == 2 ? b2 : b3)))[n][i] = a[i];
    }
    template <int I> __device__ __forceinline__ void piece(char* dst, const u32 off) {
        constexpr int n = I & 1, k = (I >> 1) - 12;
        float& t = n ? t1 : t0;
#define ROR(op, ror, v) asm volatile(op "_dpp %0, %0, %0 row_ror:" ror " row_mask:0xf bank_mask:0xf" : "+v"(v))
        if constexpr (k < -8) asm volatile("v_add_f32 %0, %0, %1" : "+v"(x[n][k + 12]) : "v"(b1[n][k + 12]));
        else if constexpr (k < -4) asm volatile("v_add_f32 %0, %0, %1" : "+v"(b2[n][k + 8]) : "v"(b3[n][k + 8]));
        else if constexpr (k < 0) asm volatile("v_add_f32 %0, %0, %1" : "+v"(x[n][k + 4]) : "v"(b2[n][k + 4]));
        else if constexpr (k == 0) asm volatile("v_max_f32 %0, %1, %2" : "=v"(mx[n]) : "v"(x[n][0]), "v"(x[n][1]));
        else if constexpr (k == 1) asm volatile("v_max3_f32 %0, %0, %1, %2" : "+v"(mx[n]) : "v"(x[n][2]), "v"(x[n][3]));
        else if constexpr (k == 2) ROR("v_max_f32", "8", mx[n]);
        else if constexpr (k == 3) ROR("v_max_f32", "4", mx[n]);
        else if constexpr (k == 4) ROR("v_max_f32", "2", mx[n]);
        else if constexpr (k == 5) ROR("v_max_f32", "1", mx[n]);
        else if constexpr (k == 6) { mx[n] *= SM_C; PIN(mx[n]); }
        else if constexpr (k < 11) { x[n][k - 7] = fmaf(x[n][k - 7], SM_C, -mx[n]); PIN(x[n][k - 7]); }
        else if constexpr (k < 15) asm volatile("v_exp_f32 %0, %0" : "+v"(x[n][k - 11]));
        else if constexpr (k == 15) asm volatile("v_add_f32 %0, %1, %2" : "=v"(sm[n]) : "v"(x[n][0]), "v"(x[n][1]));
        else if constexpr (k == 16) asm volatile("v_add_f32 %0, %1, %2" : "=v"(t) : "v"(x[n][2]), "v"(x[n][3]));
        else if constexpr (k == 17) asm volatile("v_add_f32 %0, %0, %1" : "+v"(sm[n]) : "v"(t));
        else if constexpr (k == 18) ROR("v_add_f32", "8", sm[n]);
        else if constexpr (k == 19) ROR("v_add_f32", "4", sm[n]);
        else if constexpr (k == 20) ROR("v_add_f32", "2", sm[n]);
        else if constexpr (k == 21) ROR("v_add_f32", "1", sm[n]);
        else if constexpr (k == 22) asm volatile("v_rcp_f32 %0, %0" : "+v"(sm[n]));
        else if constexpr (k < 27) asm volatile("v_mul_f32 %0, %0, %1" : "+v"(x[n][k - 23]) : "v"(sm[n]));
        else if constexpr (k == 27) asm volatile("v_cvt_pk_bf16_f32 %0, |%1|, |%2|" : "=v"(n ? d10 : d00) : "v"(x[n][0]), "v"(x[n][1]));
        else if constexpr (k == 28) asm volatile("v_cvt_pk_bf16_f32 %0, |%1|, |%2|" : "=v"(n ? d11 : d01) : "v"(x[n][2]), "v"(x[n][3]));
        else if constexpr (I == 84 || I == 87) {
            u32x4 o;
            asm volatile("v_mov_b32 %0, %4\n\tv_mov_b32 %1, %5\n\tv_mov_b32_dpp %2, %4 quad_perm:[1,0,3,2] row_mask:0xf bank_mask:0xf\n\tv_mov_b32_dpp %3, %5 quad_perm:[1,0,3,2] row_mask:0xf bank_mask:0xf"
                         : "=&v"(o[0]), "=&v"(o[1]), "=&v"(o[2]), "=&v"(o[3]) : "v"(n ? d10 : d00), "v"(n ? d11 : d01), "s"(dst));
            if constexpr (n == 0) st16f(dst, off, o, SM_MASK); else st16f64(dst, off, o, SM_MASK);
        }
    }
};

__host__ __device__ constexpr int qk_slot(int T, int ks) { return ks < 5 ? ks * 6 + T : (T < 4 ? 30 + (T >> 1) * 10 + (ks - 5) * 2 + (T & 1) : 25 + T * 5 + ks); }
constexpr int NLF = 13;
__host__ __device__ constexpr int lds_slot(int n) { constexpr int t[NLF] = {2, 6, 11, 15, 20, 24, 29, 33, 37, 41, 45, 49, 55}; return t[n]; }
__host__ __device__ constexpr int slot_lds(int sl) { for (int n = 0; n < NLF; ++n) if (lds_slot(n) == sl) return n; return -1; }
__host__ __device__ constexpr int slot_frag(int sl) { if (slot_lds(sl) >= 0) return 48 + slot_lds(sl); int n = 0; for (int i = 0; i < NLF; ++i) n += lds_slot(i) < sl; return sl - n; }
__host__ __device__ constexpr int qk_frag(int T, int ks) { return slot_frag(qk_slot(T, ks)); }
__host__ __device__ constexpr int slot_T(int sl) { return sl < 30 ? sl % 6 : (sl < 50 ? ((sl - 30) / 10) * 2 + (sl & 1) : (sl - 30) / 5); }
__host__ __device__ constexpr int slot_ks(int sl) { return sl < 30 ? sl / 6 : (sl < 50 ? 5 + ((sl - 30) % 10) / 2 : 5 + sl % 5); }
__host__ __device__ constexpr int lds_rank(int T, int ks) { int n = 0; for (int t = 0; t < T; ++t) n += qk_frag(t, ks) >= 48; return n; }
__host__ __device__ constexpr int lds_nth(int ks, int n) { for (int t = 0; t < 6; ++t) if (qk_frag(t, ks) >= 48 && lds_rank(t, ks) == n) return qk_frag(t, ks) - 48; return -1; }

__device__ __forceinline__ void qk_role(const LaParams& p, const int set, const int h, char* shm) {
    const int lane = threadIdx.x & 63, w = __builtin_amdgcn_readfirstlane(threadIdx.x >> 6);
    const int fr = lane & 15, g = lane >> 4;
    const u32 lds0 = (u32)(uintptr_t)(LDS3 char*)shm;
    const int sidx = (lane & 8) ? 0xEEEE : 0x4444;
    int cap = SPIN_CAP;
    char* mine = p.hop;
    int sch = (S_SLOT + w) * SLOT;
    int phys = 0;
    char* px = (wsp(p) + O_PX);
    const char* xn = (wsp(p) + O_XN);
    char* wl = shm + Q_WL + w * (NLF * 2048) + lane * 16;
    const char* xa = shm + Q_XA + lane * 16;

    bf16v WA[32], WV[28 - NLF];
    {
        bf16v WT[NLF];
        u32 vo[6], rd[3];
#pragma unroll
        for (int i = 0; i < 6; ++i) {
            const int id = i * 64 + lane, r = w * 16 + id / 24;
            vo[i] = (32 * (r >> 5) + kperm((r >> 2) & 3, ((r >> 4) & 1) * 4 + (r & 3))) * (HD * 2) + ((id % 24) ^ (r & 6)) * 16;
        }
#pragma unroll
        for (int i = 0; i < 3; ++i) rd[i] = lds0 + (g * 4 + (fr >> 2)) * 384 + ((w * 3 + i) ^ ((g & 1) * 2 + (fr >> 3))) * 32 + (fr & 3) * 8;
        auto issue = [&](const int j) {
            const char* src = (const char*)((j & 1) ? p.Wk : p.Wq) + (size_t)(j >> 1) * 64 * HD * 2 + h * 384;
#pragma unroll
            for (int i = 0; i < 6; ++i) dma_gather(src, lds0 + (j % 6) * 24576 + w * 6144 + i * 1024, vo[i]);
        };
#pragma unroll
        for (int j = 0; j < 5; ++j) issue(j);
        static_for<0, 20>([&](auto J) {
            constexpr int j = CI(J);
            asm volatile("s_waitcnt vmcnt(%0)" :: "n"((19 - j < 4 ? 19 - j : 4) * 6) : "memory");
            RAW_BARRIER();
            if constexpr (j + 5 < 20) issue(j + 5);
            static_for<0, 3>([&](auto PR) {
                constexpr int id = qk_frag(2 * CI(PR) + (j & 1), j >> 1);
                u32 a = rd[CI(PR)] + (j % 6) * 24576;
                asm volatile("" : "+v"(a));
                const bf16v f = cat8(tr8(a, a + 16 * 384), tr8(a + 32 * 384, a + 48 * 384));
                if constexpr (id < 32) WA[id] = f; else if constexpr (id < 48) WV[id - 32] = f; else WT[id - 48] = f;
            });
        });
        RAW_BARRIER();
        dma_kb<5>(xn + (size_t)set * 40960 + w * 5120, lds0 + Q_XA + w * 5120, lane * 16);
#pragma unroll
        for (int i = 0; i < NLF; ++i) { *(bf8*)(wl + i * 2048) = lo8(WT[i]); *(bf8*)(wl + i * 2048 + 1024) = hi8(WT[i]); }
    }
    TRDECL;
    bf8 QX[2], KX[2];
    u32x2 QY[2], KY[2];
    u32 peek = ~0u;
    int d1 = D1I, d2 = D2I, k1 = D1I + MG1;
    if (w == 3) *(u64*)(shm + Q_ST + lane * 8) = lane < 63 ? 0 : (u64)D1I | ((u64)D2I << 32);
    const int lead = __builtin_amdgcn_readfirstlane((u32)(h * 4 + (w ^ 2) - 1) >> 31);
    SmTok smt;
    u32 took = 0, tvis = 0;
    u64 own;
    u32 xoff = p_line(h, fr >> 1, 8192) + g * 16, ao1 = p_line(h, min(lane >> 3, 4), 8192) + (lane & 7) * 16, ao0;
    {
        u32 a = (h >> 4) * SLOT + (h & 15) * 512, b = 2 * SLOT + h * 256;
        asm volatile("" : "+s"(a), "+s"(b));
        ao0 = PLIN ? p_line(h, min(lane >> 3, 4), SLOT) + (lane & 7) * 16 : (lane < 32 ? a + lane * 16 : b + (lane & 15) * 16);
    }
    asm volatile("" : "+v"(xoff), "+v"(ao0), "+v"(ao1));
    auto rearm = [&](const int rot, const int i, char*& a, u32x4& v) {
        if (i == 0) asm volatile("v_mov_b64 %0, -1\n\tv_mov_b64 %1, -1" : "=v"(((u64*)&v)[0]), "=v"(((u64*)&v)[1]));
        if (i == 1) { a = mine + (P_SLOT0 + (((rot + 3) & 3) * 4 + w) * PCS) * SLOT; asm volatile("" : "+s"(a)); }
        if (i == 5) { a = px + (size_t)(set * 4 + ((rot + 2) & 3)) * PX_BUF + w * 20480; asm volatile("" : "+s"(a)); }
        if (i == 3 || i == 8) __builtin_amdgcn_global_store_b128((gu32x4*)(a + (i == 3 ? ao0 : ao1)), v, "agent");
    };
    const char* mb0;
    const char* mb1;
    char* pdst;
    char* pdst2;
    int soft;
    u32 ytime;
    f32x4 tmask;
    auto next = [&](const int s, const int i) {
        const int rot = (s >> 1) & 3;
        if (i == 0) { mb0 = mine + (M_SLOT0 + rot * 8 + 2 * w) * SLOT + 2048; asm volatile("" : "+s"(mb0)); }
        if (i == 1) { mb1 = mb0 + SLOT; soft = (u32)((w - 2) & (s - (NSEG - 1))) >> 31; asm volatile("" : "+s"(mb1), "+s"(soft)); }
        if (i == 2) { pdst = mine + (P_SLOT0 + rot * 4 * PCS) * SLOT; pdst2 = pdst + 2 * PCS * SLOT; asm volatile("" : "+s"(pdst), "+s"(pdst2)); }
        if (i == 3) tmask = *(const f32x4*)(xa + Q_MK);
    };
    auto fetch_stats = [&]() {
        if (__builtin_expect(lead != 0, 0)) {
                u32 lo = lane;
                asm volatile("" : "+v"(lo));
                lo *= 128;
                u32 keep;
                asm volatile("s_mov_b32 %0, m0\n\ts_mov_b32 m0, %4\n\ts_nop 2\n\tglobal_load_lds_dword %1, %2 sc1\n\ts_add_u32 m0, m0, 256\n\ts_lshr_b64 exec, exec, 16\n\tglobal_load_lds_dword %1, %3 sc1\n\ts_mov_b64 exec, -1\n\ts_mov_b32 m0, %0"
                             : "=&s"(keep) : "v"(lo), "s"(mine + T_SLOT * SLOT), "s"(mine + (T_SLOT + 1) * SLOT), "s"(lds0 + Q_ST) : "memory", "scc");
        }
    };
    int hold = 0;
    u32 ylast = 0;
    auto xproj = [&](const int snext, const int sprev, auto SOFT, char* xdst) {
        f32x4 acc[6][2];
        TR(8);
        const char* xs;
        u32 xl;
        bf8 x0 = *(const bf8*)(xa), x1 = *(const bf8*)(xa + 1024), lw[2];
        lw[0] = *(const bf8*)(wl);
        if constexpr (CI(SOFT)) RAW_BARRIER();
        auto pack = [&](const int T, const int tt) {
            PIN(acc[T][tt]);
            const u32 a = pk2bf(acc[T][tt][0], acc[T][tt][1]), b = pk2bf(acc[T][tt][2], acc[T][tt][3]);
            if (T < 4) { u32* d = (u32*)((T & 1) ? &KX[tt] : &QX[tt]) + (T & 2); d[0] = a; d[1] = b; }
            else if (T == 4) QY[tt] = (u32x2){a, b}; else KY[tt] = (u32x2){a, b};
            SCHEDB();
        };
        char* ao;
        u32x4 sent;
        static_for<0, 20>([&](auto K) {
            constexpr int k = CI(K), l0 = lds_nth(((k + 1) >> 1) % 10, 0), l1 = lds_nth(((k + 1) >> 1) % 10, 1);
            bf8 n0, n1, nw[2];
            if constexpr (k == 9) {
                TR(9);
                if constexpr (CI(SOFT)) asm volatile("s_waitcnt vmcnt(4)" ::: "memory"); else VM0();
                RAW_BARRIER();
                TR(10);
            }
            static_for<0, 6>([&](auto TT) {
                constexpr int T = CI(TT), id = qk_frag(T, k >> 1), rk = lds_rank(T, k >> 1);
                if constexpr (k < 19) {
                    if constexpr (T == 0) n0 = *(const bf8*)(xa + (k + 1) * 2048);
                    if constexpr (T == 1) n1 = *(const bf8*)(xa + (k + 1) * 2048 + 1024);
                    if constexpr (T == 2 && l0 >= 0) nw[0] = *(const bf8*)(wl + l0 * 2048 + ((k + 1) & 1) * 1024);
                    if constexpr (T == 3 && l1 >= 0) nw[1] = *(const bf8*)(wl + l1 * 2048 + ((k + 1) & 1) * 1024);
                }
                if constexpr (k == 0 && T == 1) { xs = xn + (size_t)(snext - 2) * 40960 + 20480 + w * 5120; xl = lds0 + Q_XA + 20480 + w * 5120; asm volatile("" : "+s"(xs), "+s"(xl)); }
                if constexpr (k == 9 && T == 1) { xs = xn + (size_t)min(snext, NSEG - 1) * 40960 + w * 5120; xl -= 20480 + 4096; asm volatile("" : "+s"(xs), "+s"(xl)); }
                if constexpr ((k == 2 || k == 13) && T == 1) { xs += 4096; xl += 4096; asm volatile("" : "+s"(xs), "+s"(xl)); }
                if constexpr (k < 3 && (T == 3 || T == 5) && 2 * k + (T == 5) < 5) dma_one<((2 * k + (T == 5)) & 3) * 1024>(xs, xl, lane * 16);
                if constexpr (k >= 9 && k < 14 && T == 4) dma_one<((k - 9) & 3) * 1024>(xs, xl, lane * 16);
                if constexpr (CI(SOFT) && k == 10 && T == 1) {
                    if (w == 1) st4_lane0(mine + (T_SLOT + 1) * SLOT + (16 + h) * 128, took);
                }
                if constexpr (CI(SOFT) && k == 16 && T == 4) fetch_stats();
                if constexpr (CI(SOFT) && k == 11 && T == 1) own = __hip_atomic_load((const gu64*)(mine + sch + (sprev - 1) * 128 + 8), __ATOMIC_RELAXED, __HIP_MEMORY_SCOPE_AGENT);
                if constexpr (CI(SOFT) && k == 16 && T == 1) { tvis = __builtin_amdgcn_readfirstlane((u32)own + (u32)(own >> 32)) + RECV; asm volatile("" : "+s"(tvis)); }
                if constexpr (CI(SOFT) && k == PKK && T == PKT) peek = sched_peek(mine + sch + ((int)(rt32() - tvis) >= 0 ? sprev * 128 + 4 : (sprev - 1) * 128 + 16));
                if constexpr (CI(SOFT) && k == 15 && T >= 3) next(snext - 2, T - 3);
                if constexpr (CI(SOFT) && k == 19 && T == 3) next(snext - 2, 3);
                SCHEDB();
                static_for<0, 2>([&](auto U) {
                    constexpr int u = CI(U);
                    const bf8 xx = u ? x1 : x0;
                    if constexpr (id < 32) {
                        const bf8 wf = (k & 1) ? hi8(WA[id]) : lo8(WA[id]);
                        if constexpr (k == 0) MMA0_AV(acc[T][u], wf, xx); else MMA_AV(acc[T][u], wf, xx);
                    } else {
                        bf8 wf;
                        if constexpr (id < 48) wf = (k & 1) ? hi8(WV[id - 32]) : lo8(WV[id - 32]); else wf = lw[rk];
                        if constexpr (k == 0) MMA0_VV(acc[T][u], wf, xx); else MMA_VV(acc[T][u], wf, xx);
                    }
                    SCHEDB();
                    if constexpr (CI(SOFT) && k * 12 + T * 2 + u < SmTok::N) smt.template piece<k * 12 + T * 2 + u>(xdst, xoff);
                    if constexpr (CI(SOFT) && k == 8) rearm((snext >> 1) + 2, T * 2 + u, ao, sent);
                    if constexpr (k == 19 && T >= 2) pack(T - 2, u);
                    SCHEDB();
                });
            });
            if constexpr (k < 19) { x0 = n0; x1 = n1; if constexpr (l0 >= 0) lw[0] = nw[0]; if constexpr (l1 >= 0) lw[1] = nw[1]; }
        });
        TR(11);
        pack(4, 0); pack(4, 1);
        asm volatile("s_nop 3");
        pack(5, 0); pack(5, 1);
        if constexpr (CI(SOFT)) ytime = __builtin_amdgcn_readfirstlane(peek);
    };
    {
        const f32x4 neg = {NEG_BIG, NEG_BIG, NEG_BIG, NEG_BIG};
        *(f32x4*)((float*)(shm + Q_PART) + (w * 40 + 8 + fr) * Q_PS + g * 8 + 4) = neg;
        if (lane < 40) *(f32x4*)((float*)(shm + Q_PART) + (w * 40 + lane) * Q_PS + 40) = neg;
        *(f32x4*)(shm + Q_MK + lane * 16) = (f32x4){4 * g > fr ? NEG_BIG : 0.f, 4 * g + 1 > fr ? NEG_BIG : 0.f, 4 * g + 2 > fr ? NEG_BIG : 0.f, 4 * g + 3 > fr ? NEG_BIG : 0.f};
    }
    float* pw0 = (float*)(shm + Q_PART) + (w * 40 + (fr & 7)) * Q_PS + g * 8 + (fr >> 3) * 4;
    float* pw2 = fr < 8 && g >= 2 ? (float*)(shm + Q_PART) + (w * 40 + fr) * Q_PS + 24 + g * 4 : (float*)(shm + Q_TR + w * 1536) + lane * 4;
    const float* prd = (const float*)(shm + Q_PART) + ((w & 1) * 4 + g) * Q_PS + min(fr, 10) * 4;
    const float* prt = (const float*)(shm + Q_PART) + (8 + 8 * w + g) * Q_PS + min(fr, 10) * 4;
    u32 trw = lds0 + Q_TR + w * 1536 + lane * 8, trr = lds0 + Q_TR + w * 1536 + (fr & 3) * 128 + (4 * g + (fr >> 2)) * 8;
    u32 trh = lds0 + Q_TR + w * 1536 + 1024 + (lane & 31) * 8 + (lane >> 5) * 4;
    u32 pw3 = lds0 + Q_PART + ((w * 40 + 8 + (lane >> 5) * 16 + fr) * Q_PS + 32 + (g & 1) * 4) * 4;
    u32 pp = lds0 + Q_PART + ((w * 40 + 8 + fr) * Q_PS + g * 8) * 4;
    asm volatile("" : "+v"(trw), "+v"(trr), "+v"(trh), "+v"(pw3), "+v"(pp));
    u32 pa = (h >> 4) * SLOT + (h & 15) * 512 + (w & 1) * 256, pbm = 2 * SLOT + h * 256 + (w & 1) * 128;
    asm volatile("" : "+s"(pa), "+s"(pbm));
    const u32 poff = (PLIN ? p_line(h, fr >> 1, SLOT) + (w & 1) * 64 : (fr < 8 ? pa + (fr >> 1) * 64 : pbm)) + g * 16 + (fr & 1) * (PCS * SLOT);

    for (int layer = 0; layer < 2; ++layer) {
    if (layer > 0) {
        if (w == 0) {
            __builtin_amdgcn_s_sleep(127);
            wait_rt(sched_get(~0u, mine + (T_SLOT + 1) * SLOT + 50 * 128, cap) + T_TOK);
            wait_first_tokens(p, set, lane, cap);
        }
        RAW_BARRIER();
        sch += 5 * SLOT; xn += ACT;
        TT(set * 128 + h, 1);
        dma_kb<5>(xn + (size_t)set * 40960 + w * 5120, lds0 + Q_XA + w * 5120, lane * 16);
    }
    VM0(); RAW_BARRIER();
    TT(set * 128 + h, 2);
    xproj(set + 2, set - 1, std::integral_constant<int, 0>(), nullptr);
    if (layer == 0) { phys = set ^ get_side(p, cap); mine += phys * 8192; }
    next(set, 0);
    if (set == 0) mb0 = p.hop + M0_OFF + phys * 8192 + 2 * w * SLOT + 2048;
    next(set, 1); next(set, 2); next(set, 3);
    ytime = set ? ~0u : rt32();
    TT(set * 128 + h, 3);
    for (int s = set; s < NSEG; s += 2) {
        const int rot = (s >> 1) & 3;
        TR(0);
        int early = 0;
        if (ytime == ~0u) { early = SLK; wait_rt(tvis); ytime = sched_get(sched_peek(mine + sch + (s - 1) * 128 + 4), mine + sch + (s - 1) * 128 + 4, cap); }
        f32x4 t[3];
        t[0] = tmask;
        PIN(t[0]); PIN(QX[0]); PIN(QX[1]); PIN(KX[0]); PIN(KX[1]); PIN(QY[0]); PIN(QY[1]); PIN(KY[0]); PIN(KY[1]);
        int tight = 0;
        if (w == 0) wait_rt(ytime);
        else {
            const int sl = (int)(ytime - rt32()) + early;
            tight = SLK - smax(-smax(-sl, -SLK), 0);
        }
        __builtin_amdgcn_s_barrier();
        TR(1); TRV(7, ytime);
        f32x4 acc[6];
        u32x4 A[10];
        bf8 Ll[2], Lh[2];
        s16x4 F[4];
        u32 r0[4], r1[4], d[2];
        float v[4];
        int tries = 0;
        auto fin = [&](const int pr, const int i) {
            if (i == 0) { PIN(acc[2 * pr]); PIN(acc[2 * pr + 1]); }
            if (i < 4) { auto r = __builtin_amdgcn_permlane32_swap(__float_as_uint(acc[2 * pr][i]), __float_as_uint(acc[2 * pr + 1][i]), false, false); r0[i] = r[0]; r1[i] = r[1]; }
            const int a0 = pr == 0 ? i - 4 : i - 1;
            if (a0 >= 0 && a0 < 4) asm volatile("v_add_f32 %0, %1, %2" : "=v"(v[a0]) : "v"(r0[a0]), "v"(r1[a0]));
            if (a0 == 3) d[0] = pk2bf(v[0], v[1]);
            if (a0 == 4) {
                d[1] = pk2bf(v[2], v[3]);
                *(LDS3 u32x2*)(uintptr_t)(trw + pr * 512) = (u32x2){d[0], d[1]};
                F[pr] = __builtin_amdgcn_ds_read_tr16_b64_v4i16((LDS3 s16x4*)(uintptr_t)(trr + pr * 512));
            }
        };
        auto fin1 = [&](const int T, const int i) {
            if (i == 0) asm volatile("s_nop 1\n\tv_permlane32_swap_b32 %0, %2\n\tv_permlane32_swap_b32 %1, %3" : "+v"(acc[T][0]), "+v"(acc[T][1]), "+v"(acc[T][2]), "+v"(acc[T][3]));
            else {
                PIN(acc[T]);
                asm volatile("v_add_f32 %0, %2, %4\n\tv_add_f32 %1, %3, %5\n\tv_cvt_pk_bf16_f32 %0, %0, %1" : "=&v"(v[0]), "=&v"(v[1]) : "v"(acc[T][0]), "v"(acc[T][1]), "v"(acc[T][2]), "v"(acc[T][3]));
                *(LDS3 float*)(uintptr_t)(trh + (T - 4) * 256) = v[0];
                F[T - 2] = __builtin_amdgcn_ds_read_tr16_b64_v4i16((LDS3 s16x4*)(uintptr_t)(trr + 1024));
            }
        };
        auto slot = [&](auto SL) {
            constexpr int sl = CI(SL), T = slot_T(sl), ks = slot_ks(sl), id = slot_frag(sl);
            if constexpr (id < 32) SMM_A(acc[T], A[ks], WA[id], sidx);
            else if constexpr (id < 48) SMM_V(acc[T], A[ks], WV[id - 32], sidx);
            else { const bf16v L = cat8(Ll[id & 1], Lh[id & 1]); SMM_V(acc[T], A[ks], L, sidx); }
            SCHEDB();
            if constexpr (sl == 0) TRC(6);
            constexpr int n0 = slot_lds(sl), n1 = slot_lds(sl - 1);
            if constexpr (n0 >= 0 && n0 < NLF - 2) Ll[n0 & 1] = *(const bf8*)(wl + (n0 + 2) * 2048);
            if constexpr (n1 >= 0 && n1 < NLF - 2) Lh[n1 & 1] = *(const bf8*)(wl + (n1 + 2) * 2048 + 1024);
            if constexpr (sl >= 43 && sl < 52) fin(0, sl - 43);
            if constexpr (sl >= 52 && sl < 58) fin(1, sl - 52);
            if constexpr (sl >= 58) fin1(4, sl - 58);
            SCHEDB();
        };
        auto gather = [&](const bool first) {
            u32 vo = lane * 16;
            asm volatile("" : "+v"(vo) :: "memory");
#pragma unroll
            for (int ks = 0; ks < 10; ++ks) {
                A[ks] = ld16(ks < 5 ? mb0 : mb1, vo, (ks % 5 - 2) * 1024);
                SCHEDB();
                if (first) {
                    if (ks == 0) {
                        MMA0_VV(t[1], KX[0], QX[1]);
                        asm volatile("v_mfma_f32_16x16x32_bf16 %0, %1, %2, %3" : "=&v"(t[2]) : "v"(KX[1]), "v"(QX[1]), "v"(t[0]));
                        MMA_VV(t[0], KX[0], QX[0]);
                    }
                    if (ks == 2) { MMA16(t[1], KY[0], QY[1]); MMA16(t[2], KY[1], QY[1]); MMA16(t[0], KY[0], QY[0]); }
                    if (ks == 4) { PIN(t[1]); *(LDS3 f32x4*)(uintptr_t)(pp + 16 * Q_PS * 4) = t[1]; }
                    if (ks == 5) { PIN(t[2]); *(LDS3 f32x4*)(uintptr_t)(pp + 16 * Q_PS * 4 + 16) = t[2]; }
                    if (ks == 6) { PIN(t[0]); *(LDS3 f32x4*)(uintptr_t)pp = t[0]; }
                    if (ks == 1 || ks == 3 || ks == 7 || ks == 8) asm volatile("s_nop %0" :: "n"(PACE2));
                } else if (ks < 9) asm volatile("s_nop %0" :: "n"(PACE));
                if (ks == 3 || ks == 7) { Ll[ks >> 2] = *(const bf8*)(wl + (ks >> 2) * 2048); Lh[ks >> 2] = *(const bf8*)(wl + (ks >> 2) * 2048 + 1024); }
                if (ks == 1) { ZERO4(acc[0]); ZERO4(acc[1]); }
                if (ks >= 5 && ks < 9) ZERO4(acc[ks - 3]);
                SCHEDB();
            }
        };
        gather(true);
        static_for<0, 42>(slot);
        int passed = 0;
        auto record = [&](const u32 xt, const u32 yt) {
            u32 ln8 = lane;
            asm volatile("" : "+v"(ln8));
            ln8 *= 8192;
            const u32x4 rec = {nosent(xt), nosent(yt), (u32)(d1 + MG1), yt};
            k1 = d1 + MG1;
            char* at = p.hop + (S_SLOT + layer * 5) * SLOT + s * 128;
            asm volatile("s_waitcnt lgkmcnt(0)\n\ts_barrier\n\ts_mov_b64 exec, 0x3ff\n\ts_nop 1\n\tglobal_store_dwordx4 %0, %1, %2 sc1\n\ts_mov_b64 exec, -1\n\ts_nop 0"
                         :: "v"(ln8), "v"(rec), "s"(at) : "memory");
            passed = 1;
        };
        u64 bad, bad2;
        asm volatile("v_cmp_u_f32 %0, %2, %3\n\tv_cmp_u_f32 %1, %4, %5" : "=s"(bad), "=s"(bad2) : "v"(acc[0][0]), "v"(acc[0][1]), "v"(acc[0][2]), "v"(acc[0][3]));
        static_for<42, 43>(slot);
        asm volatile("" : "+s"(bad), "+s"(bad2));
        if (__builtin_expect((bad | bad2) != 0, 0)) {
            {
                SETTLE(); CNT(0);
                while (true) {
                    gather(false);
                    u32 m = 0;
#pragma unroll
                    for (int ks = 0; ks < 10; ++ks) m = max4(m, A[ks]);
                    ++tries;
                    if (!__any(m == ~0u)) break;
                    if (tries >= cap) { cap = 1; break; }
                    __builtin_amdgcn_s_sleep(4);
                }
                static_for<0, 43>(slot);
            }
        }
        TR(2); TRV(6, tries + 1);
        static_for<43, 60>(slot);
        TRC(7);
        bf8 F01 = __builtin_bit_cast(bf8, __builtin_shufflevector(F[0], F[1], 0, 1, 2, 3, 4, 5, 6, 7));
        f32x4 s0, s1, s2, u0, u1;
        PIN(F01);
        MMA0_VV(s1, KX[1], F01); MMA0_VV(s0, KX[0], F01);
        SCHEDB();
        fin1(5, 0); fin1(5, 1);
        SCHEDB();
        MMA16(s1, KY[1], F[2]); MMA16(s0, KY[0], F[2]);
        MMA0_VV(s2, F01, F01); MMA0_VV(u0, F01, QX[0]); MMA0_VV(u1, F01, QX[1]);
        asm volatile("" : "+v"(s0), "+v"(s1));
#pragma unroll
        for (int i = 0; i < 4; ++i) s0[i] = __int_as_float(__builtin_amdgcn_update_dpp(__float_as_int(s0[i]), __float_as_int(s1[i]), 0x128, 0xf, 0xc, false));
        SCHEDB();
        MMA16(s2, F[3], F[3]);
        SCHEDB();
        *(f32x4*)pw0 = s0; SCHEDB();
        MMA16(u0, F[3], QY[0]); MMA16(u1, F[3], QY[1]);
        asm volatile("s_nop 1");
        PIN(s2); *(f32x4*)pw2 = s2; SCHEDB();
        asm volatile("s_nop 4" : "+v"(u0), "+v"(u1));
        f32x4 u;
#pragma unroll
        for (int i = 0; i < 4; ++i) u[i] = __uint_as_float(__builtin_amdgcn_permlane32_swap(__float_as_uint(u0[i]), __float_as_uint(u1[i]), false, false)[1]);
        PIN(u);
        auto uwrite = [&]() { *(LDS3 f32x4*)(uintptr_t)pw3 = u; };
        if (__builtin_expect(soft != 0, 1)) {
            RAW_BARRIER();
            TR(3);
            softmax_mem(prd, pdst, pdst2, poff, uwrite);
            TR(4);
        } else {
            RAW_BARRIER();
            TR(3);
            uwrite();
            if (h == 0 && s < NSEG - 1) {
                hold = tries > 0 || s < 2 || ytime - ylast > 2u * (d1 + d2) + PAUSE ? HOLD : smax(hold - 1, 0);
                ylast = ytime;
                if (w == 3) {
                    const u64 dd = *(const u64*)(shm + Q_ST + 504);
                    const u32 xt = tries == 0 && s > 0 ? ytime + k1 : rt32() + (s > 0 ? ELATE : E0);
                    d1 = __builtin_amdgcn_readfirstlane((u32)dd); d2 = __builtin_amdgcn_readfirstlane((u32)(dd >> 32));
                    record(xt, xt + d2 + MG2 + (tries > 0 ? EY : 0) + hold * BOOST);
                } else {
                    u32 lo = lane * 4;
                    asm volatile("" : "+v"(lo));
                    int sta = *(const int*)(shm + Q_ST + lo), stb = *(const int*)(shm + Q_ST + 256 + lo);
                    asm volatile("s_nop 1\n\t" DPP2("v_max_i32", "8") DPP2("v_max_i32", "4") DPP2("v_max_i32", "2") DPP2("v_max_i32", "1") : "+v"(sta), "+v"(stb));
                    const int a0 = __builtin_amdgcn_readlane(sta, 0), a1 = __builtin_amdgcn_readlane(sta, 16), a2 = __builtin_amdgcn_readlane(sta, 32), a3 = __builtin_amdgcn_readlane(sta, 48);
                    const int b0 = __builtin_amdgcn_readlane(stb, 0), b1 = __builtin_amdgcn_readlane(stb, 16), b2 = __builtin_amdgcn_readlane(stb, 32);
                    SCHEDB();
                    const int down = smax(DOWN - hold * DOWN, 0);
                    d1 = track(d1, smax(b1, b2), down); d2 = track(d2, smax(smax(smax(a0, a1), smax(a2, a3)), b0), down);
                    if (hold == HOLD && s > 2) { d1 += BUMP; d2 += BUMP; }
                    *(u64*)(shm + Q_ST + 504) = (u64)(u32)d1 | ((u64)(u32)d2 << 32);
                }
            }
            if (w == 2 && s < 2 && layer > 0) wait_all_tokens(p, lane, cap);
        }
        u64 tpub = 0;
        if (__builtin_expect(passed == 0, 1)) asm volatile("s_memrealtime %0\n\ts_waitcnt lgkmcnt(0)\n\ts_barrier" : "=s"(tpub) :: "memory");
#pragma unroll
        for (int j = 0; j < 8; ++j) smt.load(prt, j);

        took = tries == 0 && s > 0 ? (u32)tpub - ytime + tight : 0;
        char* xdst = px + (size_t)(set * 4 + rot) * PX_BUF + w * 20480;
        TR(5); TRFLUSH(set * 128 + h);
        if (s + 2 >= NSEG) {
            static_for<0, SmTok::N>([&](auto I) { smt.template piece<CI(I)>(xdst, xoff); SCHEDB(); if constexpr ((CI(I) & 1) && ((CI(I) >= 26 && CI(I) < 36) || (CI(I) >= 58 && CI(I) < 68) || CI(I) > 80)) asm volatile("s_nop 0"); SCHEDB(); });
            TT(set * 128 + h, 0);
            __builtin_amdgcn_s_sleep(64);
            char* ao;
            u32x4 sent;
#pragma unroll
            for (int i = 0; i < 10; ++i) rearm(rot, i, ao, sent);
            break;
        }
        xproj(s + 4, s + 1, std::integral_constant<int, 1>(), xdst);
    }
    }
}

constexpr int C_YL = 0, C_RED = 40960, C_EX = 49152, C_XN = 53248, C_M0 = 135168;
__device__ __forceinline__ void comb_role(const LaParams& p, const int set, const int c, char* shm) {
    const int lane = threadIdx.x & 63, w = __builtin_amdgcn_readfirstlane(threadIdx.x >> 6);
    const int fr = lane & 15, g = lane >> 4;
    const u32 lds0 = (u32)(uintptr_t)(LDS3 char*)shm;
    const int sidx = (lane & 8) ? 0xEEEE : 0x4444;
    int cap = SPIN_CAP;
    char* mine = p.hop;
    char* other = p.hop;
    int phys = 0, sch = (S_SLOT + w) * SLOT;
    u32 k1 = D1I + MG1;
    char* px = (wsp(p) + O_PX);
    const char* xn = (wsp(p) + O_XN);
    char* const yl0 = shm + C_YL + lane * 16;
    const u32 pvo = PLIN ? lane * 16 : (fr >> 2) * 256 + g * 64 + (fr & 3) * 16, pvm = PLIN ? lane * 16 : g * 512 + (fr >> 2) * 128 + (fr & 3) * 16;
    int cb = (set * 80 + c) % 10, rb = (set * 80 + c) / 10;

    {
        const int t = threadIdx.x, h = (set * 80 + c) / 5, cu = c % 5;
        u32x4 st[12];
        const u16* src = p.Wo + ((size_t)h * DH + (t >> 4)) * DM + cu * 128 + (t & 15) * 8;
#pragma unroll
        for (int i = 0; i < 12; ++i) asm volatile("global_load_dwordx4 %0, %1, off" : "=v"(st[i]) : "v"(src + (size_t)i * 16 * DM) : "memory");
        u32 vo[12];
#pragma unroll
        for (int i = 0; i < 12; ++i) {
            const int id = i * 64 + lane, r = id / 24;
            vo[i] = kperm((r >> 2) & 3, (r >> 4) * 4 + (r & 3)) * (HD * 2) + ((id % 24) ^ (r & 7)) * 16;
        }
        const u32 ring = lds0 + 57344 + w * 24576;
        auto issue = [&](const int j) {
            const char* a = (const char*)p.Wv + (size_t)(((j + cu) % 5) * 4 + w) * 32 * HD * 2 + h * 384;
#pragma unroll
            for (int i = 0; i < 12; ++i) dma_gather(a, ring + (j & 1) * 12288 + i * 1024, vo[i]);
        };
        SCHEDB();
        issue(0);
        const u32 ra = ring + fr * 384 + ((g ^ (fr & 3)) + (fr & 4)) * 16;
        const int e = t >> 4, pr = ((e >> 2) & 1) * 8 + (e >> 3) * 4 + (e & 3);
        asm volatile("s_waitcnt vmcnt(12)" : "+v"(st[0]), "+v"(st[1]), "+v"(st[2]), "+v"(st[3]), "+v"(st[4]), "+v"(st[5]),
                     "+v"(st[6]), "+v"(st[7]), "+v"(st[8]), "+v"(st[9]), "+v"(st[10]), "+v"(st[11]) :: "memory");
#pragma unroll
        for (int i = 0; i < 12; ++i) *(u32x4*)(shm + (i * 16 + pr) * 288 + (t & 15) * 16) = st[i];
        issue(1);
        RAW_BARRIER();
        { const int layer = 0; TT(set * 128 + 32 + c, 12); }
        bf8 B[8][6], A[2][2][6];
        const u32 rd = lds0 + ((g >> 1) * 16 + (g & 1) * 4 + (fr >> 2)) * 288 + (fr & 3) * 8;
#pragma unroll
        for (int n = 0; n < 8; ++n)
#pragma unroll
            for (int k = 0; k < 6; ++k) B[n][k] = tr8(rd + k * 32 * 288 + n * 32, rd + (k * 32 + 8) * 288 + n * 32);
        auto lda = [&](const int j, const int k) {
#pragma unroll
            for (int u = 0; u < 2; ++u) A[j & 1][u][k] = *(LDS3 const bf8*)(uintptr_t)((ra ^ ((k & 1) * 64)) + (j & 1) * 12288 + u * 6144 + (k >> 1) * 128);
        };
        asm volatile("s_waitcnt vmcnt(12)" ::: "memory");
#pragma unroll
        for (int k = 0; k < 6; ++k) lda(0, k);
        f32x4 cc[2][2];
        char* dst;
        const char* nsrc;
        static_for<0, 41>([&](auto N) {
            constexpr int n = CI(N) % 8, j = CI(N) / 8, q = CI(N) - 1;
            if constexpr (n == 7 && j < 4) asm volatile("s_waitcnt vmcnt(%0)" :: "n"(j == 0 ? 18 : (j < 3 ? 21 : 9)) : "memory");
            if constexpr (n == 1) { dst = wsp(p) + O_WVO + ((size_t)(h * 20 + ((j + cu) % 5) * 4 + w) * 40 + cu * 8 + 4) * 1024; asm volatile("" : "+s"(dst)); }
            if constexpr (n == 0 && j < 3) { nsrc = (const char*)p.Wv + (size_t)(((j + 2 + cu) % 5) * 4 + w) * 32 * HD * 2 + h * 384; asm volatile("" : "+s"(nsrc)); }
            u32x4 o;
#pragma unroll
            for (int k = 0; k < 6; ++k) {
                if constexpr (j < 5) {
#pragma unroll
                    for (int u = 0; u < 2; ++u) { if (k == 0) MMA0_VA(cc[n & 1][u], A[j & 1][u][k], B[n][k]); else MMA_VA(cc[n & 1][u], A[j & 1][u][k], B[n][k]); }
                } else if (k == 0) asm volatile("s_nop 7");
                SCHEDB();
                if constexpr (j < 3 && n >= 1 && n < 7) {
                    if (k == 0 || k == 2) asm volatile("s_mov_b32 m0, %2\n\ts_nop 0\n\tglobal_load_lds_dwordx4 %0, %1" :: "v"(vo[2 * (n - 1) + k / 2]), "s"(nsrc), "s"(ringp(ring, (j & 1) * 12288 + (2 * (n - 1) + k / 2) * 1024)) : "memory");
                }
                if constexpr (n == 7 && j < 4) lda(j + 1, k);
                if constexpr (q >= 0) {
                    if (k == 3) { PIN(cc[q & 1][0]); o[0] = pk2bf(cc[q & 1][0][0], cc[q & 1][0][1]); o[1] = pk2bf(cc[q & 1][0][2], cc[q & 1][0][3]); }
                    if (k == 4) { PIN(cc[q & 1][1]); o[2] = pk2bf(cc[q & 1][1][0], cc[q & 1][1][1]); o[3] = pk2bf(cc[q & 1][1][2], cc[q & 1][1][3]); }
                    if (k == 5) __builtin_amdgcn_global_store_b128((gu32x4*)(dst + (u32)(lane * 16) + (q % 8 - 4) * 1024), o, "agent");
                }
                SCHEDB();
            }
        });
        { const int layer = 0; TT(set * 128 + 32 + c, 13); }
        VM0(); RAW_BARRIER();
        { const int layer = 0; TT(set * 128 + 32 + c, 14); }
        if (w == 0) st4_lane0(wsp(p) + O_FL + (400 + set * 80 + c) * 128, 1u);
        dma_kb<10>(xn + (size_t)set * 40960 + w * 10240, lds0 + C_XN + w * 10240, lane * 16);
        if (w == 0) {
            __builtin_amdgcn_s_sleep(8);
            const u32 off[1] = {(u32)(400 + (lane & 31) * 5 + (c >> 4)) * 128};
            wait_flags<1, FSL>(wsp(p) + O_FL, off, cap);
        }
        RAW_BARRIER();
        { const int layer = 0; TT(set * 128 + 32 + c, 15); }
    }
    bf16v WA[32], WV[8];
    {
        u32 vo = (w * 8 + (fr >> 3)) * 819200 + g * 256 + (fr & 7) * 16;
        asm volatile("" : "+v"(vo));
        auto ldw = [&](const int ks, const int t) {
            const char* a = wsp(p) + O_WVO + (size_t)(t * 1600 + ks * 80 + (c >> 1)) * 1024 + (c & 1) * 128;
            const char* b = a + 40960;
            asm volatile("" : "+s"(b));
            return cat8(__builtin_bit_cast(bf8, ld16(a, vo, 0)), __builtin_bit_cast(bf8, ld16(b, vo, 0)));
        };
#pragma unroll
        for (int i = 0; i < 32; ++i) WA[i] = ldw(i >> 2, i & 3);
#pragma unroll
        for (int i = 0; i < 8; ++i) WV[i] = ldw(8 + (i >> 2), i & 3);
    }
    auto yx = [&](f32x4 (&acc)[4][2], const int sy, const int tile, auto&& extra) {
        const char* xa = shm + tile + lane * 16;
        const char* xs = (sy + 2 < NSEG ? xn + (size_t)(sy + 2) * 40960 : (wsp(p) + O_WO) + cb * 81920) + w * 10240;
        u32 xl = lds0 + C_XN + (~(sy >> 1) & 1) * 40960 + w * 10240;
        bf8 x0 = *(const bf8*)(xa), x1 = *(const bf8*)(xa + 1024);
        const char* xs3[3] = {xs, xs + 4096, xs + 8192};
        u32 xl3[3] = {xl, xl + 4096, xl + 8192};
        asm volatile("" : "+s"(xs3[0]), "+s"(xs3[1]), "+s"(xs3[2]), "+s"(xl3[0]), "+s"(xl3[1]), "+s"(xl3[2]));
        static_for<0, 20>([&](auto K) {
            constexpr int k = CI(K);
            bf8 n0, n1;
            static_for<0, 4>([&](auto TT_) {
                constexpr int t = CI(TT_), id = (k >> 1) * 4 + t, dn = 2 * (k - 3) + t - 2;
                if constexpr (k < 19 && t == 0) n0 = *(const bf8*)(xa + (k + 1) * 2048);
                if constexpr (k < 19 && t == 1) n1 = *(const bf8*)(xa + (k + 1) * 2048 + 1024);
                if constexpr (t >= 2 && k >= 3 && k < 8) dma_one<(dn & 3) * 1024>(xs3[dn >> 2], xl3[dn >> 2], lane * 16);
                if constexpr (t >= 2) extra(2 * k + t - 2);
                SCHEDB();
                if constexpr (id < 32) {
                    const bf8 wf = (k & 1) ? hi8(WA[id]) : lo8(WA[id]);
                    if constexpr (k == 0) { MMA0_VA(acc[t][0], x0, wf); MMA0_VA(acc[t][1], x1, wf); } else { MMA_VA(acc[t][0], x0, wf); MMA_VA(acc[t][1], x1, wf); }
                } else {
                    const bf8 wf = (k & 1) ? hi8(WV[id - 32]) : lo8(WV[id - 32]);
                    MMA_VV(acc[t][0], x0, wf); MMA_VV(acc[t][1], x1, wf);
                }
                SCHEDB();
            });
            if constexpr (k < 19) { x0 = n0; x1 = n1; }
        });
    };

    for (int layer = 0; layer < 2; ++layer) {
    u32 ynext = ~0u;
    if (layer > 0 && set == 0) k1 = sched_get(~0u, mine + sch - 5 * SLOT + 62 * 128 + 8, cap);
    VM0(); RAW_BARRIER();
    TT(set * 128 + 32 + c, 11);
    if (layer > 0 && w == 0) st4_lane0(wsp(p) + O_FL + (192 + rb * 10 + cb) * 128, 1u);
    {
        f32x4 ya[4][2];
        yx(ya, set, layer ? C_YL : C_XN, [](int) {});
        asm volatile("s_nop 7");
        RAW_BARRIER();
        if (layer > 0) *(bf8*)(yl0 + (w * 5 + 4) * 1024) = *(const bf8*)(shm + C_M0 + w * 1024 + lane * 16);
#pragma unroll
        for (int t = 0; t < 4; ++t) { PIN(ya[t][0]); PIN(ya[t][1]); *(bf8*)(yl0 + (w * 5 + t) * 1024) = pack8(ya[t][0], ya[t][1]); }
        RAW_BARRIER();
    }
    if (layer == 0) { phys = set ^ get_side(p, cap); mine += phys * 8192; other += (phys ^ 1) * 8192; }
    TT(set * 128 + 32 + c, 10);
    for (int s = set; s < NSEG; s += 2) {
        const int rot = (s >> 1) & 3;
        char* yl = yl0 + (rot & 1) * 20480;
        TRDECL; TR(0);
        const char* mb0 = (s > 0 ? mine + (M_SLOT0 + rot * 8) * SLOT : p.hop + M0_OFF + phys * 8192) + 2 * w * SLOT + 2048;
        const char* mb1 = mb0 + SLOT;
        asm volatile("" : "+s"(mb0), "+s"(mb1));
        u32 yt = 0;
        bool late = s == 0;
        if (s > 0) {
            yt = sched_get(ynext, mine + sch + (s - 1) * 128 + 4, cap);
            wait_rt(yt + CLAG);
        }
        auto ldy = [&](const int f) { return *(const bf8*)(yl + (f < 16 ? (f >> 2) * 5 + (f & 3) : (f - 16) * 5 + 4) * 1024); };
        bf8 Y0[20 - 3 * NH_];
        TR(1);
        if (s == 0 && layer > 0) {
#pragma unroll
            for (int i = 0; i < 20 - 3 * NH_; ++i) Y0[i] = ldy(i);
        } else {
            f32x4 acc[4];
            u32x4 A[10];
            auto gather = [&]() {
                u32 vo = lane * 16;
                asm volatile("" : "+v"(vo) :: "memory");
#pragma unroll
                for (int ks = 0; ks < 10; ++ks) {
                    A[ks] = ld16(ks < 5 ? mb0 : mb1, vo, (ks % 5 - 2) * 1024);
                    SCHEDB();
                    if (ks < 4) ZERO4(acc[ks]);
                    if (ks < 9) asm volatile("s_nop %0" :: "n"(PACE));
                    SCHEDB();
                }
            };
            auto mproj = [&]() {
#pragma unroll
                for (int ks = 0; ks < 10; ++ks) {
#pragma unroll
                    for (int t = 0; t < 4; ++t) { if (ks < 8) SMM_A(acc[t], A[ks], WA[(ks * 4 + t) & 31], sidx); else SMM_V(acc[t], A[ks], WV[(ks * 4 + t) & 7], sidx); }
                }
                asm volatile("s_nop 7");
#pragma unroll
                for (int t = 0; t < 4; ++t) PIN(acc[t]);
            };
            gather();
#pragma unroll
            for (int i = 0; i < 20 - 3 * NH_; ++i) Y0[i] = ldy(i);
            SCHEDB();
            mproj();
            if (__builtin_expect(__any(__builtin_isunordered(acc[0][0], acc[0][1]) || __builtin_isunordered(acc[0][2], acc[0][3])), 0)) {
                CNT(1);
                late = true;
                for (int tries = 1;; ++tries) {
                    gather();
                    u32 m = 0;
#pragma unroll
                    for (int ks = 0; ks < 10; ++ks) m = max4(m, A[ks]);
                    if (!__any(m == ~0u)) break;
                    if (tries >= cap) { cap = 1; break; }
                    __builtin_amdgcn_s_sleep(8);
                }
                mproj();
            }
#pragma unroll
            for (int pr = 0; pr < 2; ++pr) {
                float v[4];
#pragma unroll
                for (int i = 0; i < 4; ++i) v[i] = halfsum(acc[2 * pr][i], acc[2 * pr + 1][i]);
                *(u32x2*)(shm + C_YL + (rot & 1) * 20480 + (w * 5 + 4) * 1024 + ((2 * pr + (g >> 1)) * 16 + fr) * 16 + (g & 1) * 8) = (u32x2){pk2bf(v[0], v[1]), pk2bf(v[2], v[3])};
            }
        }
        if (s == 0) { VM0(); *(bf8*)(shm + C_M0 + w * 1024 + lane * 16) = *(const bf8*)(yl0 + (w * 5 + 4) * 1024); }
        RAW_BARRIER();
        TR(2);
        u32 took = 0;
        if (s < NSEG - 1) {
            const char* pb[3];
#pragma unroll
            for (int i = 0; i < 3; ++i) { pb[i] = mine + (P_SLOT0 + (rot * 4 + c / 20) * PCS + i) * SLOT + 4096; asm volatile("" : "+s"(pb[i])); }
            auto ldp = [&](const int f, const u32 vo, const u32 vm) { return f < 16 ? ld16(pb[f >> 3], vo, ((f & 7) - 4) * 1024) : ld16(pb[2], vm, PLIN ? (f - 20) * 1024 : (f - 18) * 2048); };
            u32 xt;
            auto partial = [&](auto NF, auto F0, f32x4& acc, auto&& lastp) {
                constexpr int nf = CI(NF), f0 = CI(F0);
                bf8 (&Y)[nf] = Y0;
#pragma unroll
                for (int i = 0; i < nf; ++i) PIN(Y[i]);
                u32 vo = pvo;
                asm volatile("" : "+v"(vo));
                xt = late ? sched_get(~0u, mine + sch + s * 128, cap) : yt + k1;
                TR(3);
                if (!SYNCB || f0 == 0) wait_rt(xt);
                if (SYNCB) __builtin_amdgcn_s_barrier();
                TRV(7, xt); TRC(6);
                if (W0D > 0 && f0 == 0) asm volatile("s_nop %0" :: "n"(W0D));
                asm volatile("" ::: "memory");
                u32x4 P[nf];
#pragma unroll
                for (int i = 0; i < nf; ++i) { P[i] = ldp(f0 + i, vo, vo); SCHEDB(); }
#pragma unroll
                for (int i = 0; i < nf; ++i) {
                    if (i == nf - LATEP) lastp();
                    if (i == 0) MMA0_VV(acc, Y[i], P[i]); else MMA_VV(acc, Y[i], P[i]);
                    if (i == 0) TRC(7);
                    SCHEDB();
                }
                TRC(8);
            };
            f32x4 acc;
            if (w > 0) {
                bf8 Y[NH_];
                const char* q[NH_];
                u32 vo[NH_], ex = lds0 + C_EX + (w - 1) * 1024 + lane * 16;
#pragma unroll
                for (int i = 0; i < NH_; ++i) {
                    const int f = 20 - (4 - w) * NH_ + i;
                    Y[i] = ldy(f);
                    q[i] = pb[0] + (f >> 3) * SLOT + (f & 7) * (PLIN || f < 16 ? 1024 : 2048) - 4096;
                    vo[i] = f < 16 ? pvo : pvm;
                    asm volatile("" : "+s"(q[i]));
                    PIN(vo[i]);
                }
#pragma unroll
                for (int i = 0; i < NH_; ++i) PIN(Y[i]);
                PIN(ex);
                __builtin_amdgcn_s_barrier();
                TRC(6);
                asm volatile("" ::: "memory");
                u32x4 P[NH_];
#pragma unroll
                for (int i = 0; i < NH_; ++i) { P[i] = ld16(q[i], vo[i], 0); SCHEDB(); }
#pragma unroll
                for (int i = 0; i < NH_; ++i) { if (i == 0) MMA0_VV(acc, Y[i], P[i]); else MMA_VV(acc, Y[i], P[i]); if (i == 0) TRC(7); }
                TRC(8);
                asm volatile("s_nop 7" : "+v"(acc));
                *(LDS3 f32x4*)(uintptr_t)ex = acc;
                RAW_BARRIER();
            } else {
                const int rn = ((s + 1) >> 1) & 3;
                u32 dst = g * 2 * SLOT + (lane & 7) * 16;
                char* dstb = other + (M_SLOT0 + rn * 8 + c / 40) * SLOT + (c % 40) * 128;
                u32 exa = lds0 + C_EX + lane * 16;
                asm volatile("" : "+s"(dstb), "+v"(dst), "+v"(exa));
                f32x4 h1, h2, h3;
                partial(std::integral_constant<int, 20 - 3 * NH_>(), std::integral_constant<int, 0>(), acc, [&] {
                    __builtin_amdgcn_s_barrier();
                    asm volatile("" ::: "memory");
                    h1 = *(LDS3 const f32x4*)(uintptr_t)exa; h2 = *(LDS3 const f32x4*)(uintptr_t)(exa + 1024); h3 = *(LDS3 const f32x4*)(uintptr_t)(exa + 2048);
                    SCHEDB();
                });
                h1 = (h1 + h2) + h3;
                asm volatile("s_nop 2" : "+v"(acc), "+v"(h1));
                acc += h1;
                float v0, v1;
                auto merge = [&]() {
                    auto a = __builtin_amdgcn_permlane32_swap(__float_as_uint(acc[0]), __float_as_uint(acc[2]), false, false);
                    auto b = __builtin_amdgcn_permlane32_swap(__float_as_uint(acc[1]), __float_as_uint(acc[3]), false, false);
                    v0 = __uint_as_float(a[0]) + fdpp<0x128>(__uint_as_float(a[1]));
                    v1 = __uint_as_float(b[0]) + fdpp<0x128>(__uint_as_float(b[1]));
                };
                merge();
                int tries = 0;
                if (__builtin_expect(__any(__builtin_isunordered(v0, v1)), 0)) {
                    CNT(2);
                    wait_rt(sched_get(~0u, mine + sch + s * 128, cap));
                    while (true) {
                        u32 vo = pvo, vm = pvm, m = 0;
                        asm volatile("" : "+v"(vo), "+v"(vm) :: "memory");
                        u32x4 P[20];
#pragma unroll
                        for (int i = 0; i < 20; ++i) P[i] = ldp(i, vo, vm);
                        f32x4 hh[4];
#pragma unroll
                        for (int i = 0; i < 20; ++i) {
                            const bf8 y = ldy(i);
                            const int k = i < 20 - 3 * NH_ ? 0 : (i - 20 + 4 * NH_) / NH_;
                            if (i == 0 || i == 20 - 3 * NH_ || i == 20 - 2 * NH_ || i == 20 - NH_) MMA0_VV(hh[k], y, P[i]); else MMA_VV(hh[k], y, P[i]);
                            m = max4(m, P[i]);
                            SCHEDB();
                        }
                        asm volatile("s_nop 7" : "+v"(hh[0]), "+v"(hh[1]), "+v"(hh[2]), "+v"(hh[3]));
                        acc = hh[0] + ((hh[1] + hh[2]) + hh[3]);
                        ++tries;
                        if (!__any(m == ~0u)) break;
                        if (tries >= cap) { cap = 1; break; }
                        __builtin_amdgcn_s_sleep(4);
                    }
                    merge();
                }
                TRV(6, tries + 1);
                asm volatile("" :: "s"(dstb));
                const u32 dw = nosent(pk2bf(v0, v1));
                auto e = __builtin_amdgcn_permlane32_swap(dw, dw, false, false);
                auto e0 = __builtin_amdgcn_permlane16_swap(e[0], e[0], false, false);
                auto e1 = __builtin_amdgcn_permlane16_swap(e[1], e[1], false, false);
                st16f(dstb, dst, (u32x4){e0[0], e1[0], e0[1], e1[1]}, 0x00ff00ff00ff00ffull);
                TR(4);
                took = tries == 0 && s > 0 ? rt32() - xt : 0;
            }
            if (s < 2 && layer > 0 && w == 3) wait_all_tokens(p, lane, cap);
            if (s == NSEG - 2 || s < 2) RAW_BARRIER();
        } else wait_rt((late ? rt32() : yt + k1) + T_PX);
        auto tokens = [&](auto MORE) {
            f32x4 oa[4], ya[4][2];
            u32x4 P[20];
            u32 vo = lane * 16 + w * 4096, vm = lane * 16 + 16384 + w * 1024;
            asm volatile("" : "+v"(vo), "+v"(vm));
            const char* pb[4];
#pragma unroll
            for (int i = 0; i < 4; ++i) { pb[i] = px + (size_t)(set * 4 + rot) * PX_BUF + i * 20480; asm volatile("" : "+s"(pb[i])); }
            auto ldp = [&](const int i) { P[i] = i % 5 == 4 ? ld16(pb[i / 5], vm, 0) : ld16(pb[i / 5], vo, (i % 5) * 1024); };
            int ln = lane;
            asm volatile("" : "+v"(ln));
            u32 rg, peek;
            u64 peek2;
            bf8 y[5];
            auto side = [&](const int i) {
                if (i == 4) rg = sched_peek((wsp(p) + O_XR) + layer * ACT + (size_t)s * 40960 + ((c >> 2) * 2 + (w >> 1)) * 1024 + (c & 2) * 256 + (w & 1) * 128 + (c & 1) * 4 + (u32)((ln & 2) * 128 + (ln >> 3) * 16 + (ln & 1) * 8));
                if (i >= 16 && i < 36) ldp(i - 16);
                if (i == 2 && CI(MORE) && w == 0) st4_lane0(mine + (T_SLOT + (c >> 6)) * SLOT + (c & 63) * 128, took);
                if (i == 15 && CI(MORE)) peek2 = __hip_atomic_load((const gu64*)(mine + sch + s * 128 + 8), __ATOMIC_RELAXED, __HIP_MEMORY_SCOPE_AGENT);
                if (i >= 35) y[i - 35] = *(const bf8*)(yl + (w * 5 + i - 35) * 1024);
            };
            TRC(9);
            if constexpr (CI(MORE)) yx(ya, s + 2, C_XN + (~rot & 1) * 40960, side);
            else {
#pragma unroll
                for (int i = 0; i < 40; ++i) side(i);
            }
            TRC(10);
            bf8 yn[4];
            auto outx = [&](const bool first) {
                if (!first) {
#pragma unroll
                    for (int i = 0; i < 20; ++i) PIN(P[i]);
                    NOPV();
                }
#pragma unroll
                for (int q = 0; q < 4; ++q)
#pragma unroll
                    for (int j = 0; j < 5; ++j) {
                        if (j == 0) MMA0_VV(oa[q], y[j], P[q * 5 + j]); else MMA_VV(oa[q], y[j], P[q * 5 + j]);
                        if (CI(MORE) && first && q * 5 + j >= 3 && q * 5 + j < 19) {
                            const int e = q * 5 + j - 3;
                            SCHEDB();
                            if ((e & 1) == 0) PIN(ya[e >> 2][(e >> 1) & 1]);
                            ((u32*)&yn[e >> 2])[e & 3] = pk2bf(ya[e >> 2][(e >> 1) & 1][(e & 1) * 2], ya[e >> 2][(e >> 1) & 1][(e & 1) * 2 + 1]);
                            if ((e & 3) == 3) *(bf8*)(yl0 + (~rot & 1) * 20480 + (w * 5 + (e >> 2)) * 1024) = yn[e >> 2];
                            SCHEDB();
                        }
                    }
                asm volatile("s_nop 7");
#pragma unroll
                for (int q = 0; q < 4; ++q) PIN(oa[q]);
            };
            outx(true);
            if (__builtin_expect(__any(__builtin_isunordered(oa[0][0], oa[1][0]) || __builtin_isunordered(oa[2][0], oa[3][0])), 0)) {
                CNT(3);
                for (int tries = 1;; ++tries) {
                    asm volatile("" : "+v"(vo), "+v"(vm) :: "memory");
                    u32 m = 0;
#pragma unroll
                    for (int i = 0; i < 20; ++i) ldp(i);
#pragma unroll
                    for (int i = 0; i < 20; ++i) m = max4(m, P[i]);
                    if (!__any(m == ~0u)) break;
                    if (tries >= cap) { cap = 1; break; }
                    __builtin_amdgcn_s_sleep(8);
                }
                outx(false);
            }
            if constexpr (!CI(MORE)) { if (c == 0 && w == 0) stamp2(p.hop, 48 + layer * 4 + set); }
            const bool own = (fr < 8) == (g < 2);
#pragma unroll
            for (int q = 0; q < 4; q += 2) {
                f32x4 o;
#pragma unroll
                for (int i = 0; i < 4; ++i) { const float t = fdpp<0x128>(oa[q + 1][i]); o[i] = own ? oa[q][i] : t; }
                *(f32x4*)(shm + C_RED + ((w * 2 + (g >> 1)) * 32 + (q + !own) * 8 + (fr & 7)) * 32 + (g & 1) * 16) = o;
            }
            RAW_BARRIER();
            TR(5);
            if constexpr (CI(MORE)) {
                k1 = sched_get((u32)peek2, mine + sch + s * 128 + 8, cap);
                wait_rt(__builtin_amdgcn_readfirstlane((u32)(peek2 >> 32)) + k1 + RECV);
                peek = sched_peek(mine + sch + (s + 1) * 128 + 4);
                SCHEDB();
            }
            typedef __attribute__((ext_vector_type(2))) float f32x2;
            const char* rp = shm + C_RED + (ln & 4) * 1024 + w * 256 + (ln >> 3) * 32 + (ln & 3) * 8;
            f32x2 a = (*(const f32x2*)rp + *(const f32x2*)(rp + 1024)) + (*(const f32x2*)(rp + 2048) + *(const f32x2*)(rp + 3072));
            a[0] += fdpp<0x104>(a[0]); a[1] += fdpp<0x104>(a[1]);
            const u32 y2 = pk2bf(a[0], a[1]), d = nosent(pk2bf(bflo(rg) + bflo(y2), bfhi(rg) + bfhi(y2)));
            const u32x4 o4 = {d, (u32)__builtin_amdgcn_mov_dpp(d, 0x55, 0xf, 0xf, true), (u32)__builtin_amdgcn_mov_dpp(d, 0xaa, 0xf, 0xf, true), (u32)__builtin_amdgcn_mov_dpp(d, 0xff, 0xf, 0xf, true)};
            if constexpr (CI(MORE)) { ynext = __builtin_amdgcn_readfirstlane(peek); asm volatile("" : "+s"(ynext)); }
            st16m((wsp(p) + O_X1) + layer * ACT + (size_t)s * 40960 + c * 512 + w * 128, ln * 2, o4, 0x0101010101010101ull);
            if (w == 1) {
                u32x4 v;
                asm volatile("v_mov_b64 %0, -1\n\tv_mov_b64 %1, -1" : "=v"(((u64*)&v)[0]), "=v"(((u64*)&v)[1]));
                st16m(other + (M_SLOT0 + (((s + 7) >> 1) & 3) * 8 + c / 40) * SLOT + (c % 40) * 128, (ln >> 4) * 2 * SLOT + (ln & 7) * 16, v, 0x00ff00ff00ff00ffull);
            }
            TRC(11); TRFLUSH(set * 128 + 32 + c);
        };
        if (s + 2 >= NSEG) { tokens(std::integral_constant<int, 0>()); break; }
        tokens(std::integral_constant<int, 1>());
    }
    TT(set * 128 + 32 + c, 0);
    asm volatile("" : "+s"(cb), "+s"(rb));
    dma_kb<10>((wsp(p) + O_WO) + cb * 81920 + 40960 + w * 10240, lds0 + C_XN + 40960 + w * 10240, lane * 16);
    if (w == 0) { const u32 t = sched_get(~0u, mine + (T_SLOT + 1) * SLOT + (50 + layer * 4) * 128, cap); *(u32*)(shm + C_EX) = t; }
    VM0(); RAW_BARRIER();
    wait_rt(__builtin_amdgcn_readfirstlane(*(const u32*)(shm + C_EX)) + T_OB + OB_STEP * w + (layer == 0 ? OB_COLD + 8 * w : 0));
    {
        int ln = lane;
        asm volatile("" : "+v"(ln));
        u32x4 O[20], R[2];
        u32 dw[8];
        auto ready = [&](const int T) {
            const char* ob = (wsp(p) + O_OB) + layer * ACT + (size_t)T * 20480 + 16384;
            const u32 off[1] = {(u32)(ln & 31) * 128};
            wait_flags<1>(ob, off, cap);
        };
        auto load = [&](const int T) {
            const char* ob0 = (wsp(p) + O_OB) + layer * ACT + (size_t)T * 20480 + 4096;
            const char* ob1 = ob0 + 8192;
            const char* ob2 = ob0 + 12288;
            const char* xb = (wsp(p) + O_X1) + layer * ACT + (size_t)(T >> 1) * 40960 + cb * 4096 + (T & 1) * 256;
            u32 vo = ln * 16, vr = (ln >> 4) * 256 + (ln & 15) * 8, vx = (ln >> 4) * 1024 + (ln & 15) * 16;
            asm volatile("" : "+v"(vo), "+v"(vr), "+v"(vx), "+s"(ob0), "+s"(ob1), "+s"(ob2), "+s"(xb) :: "memory");
#pragma unroll
            for (int k = 0; k < 16; ++k) { O[k] = ld16(k < 8 ? ob0 : ob1, vo, ((k & 7) - 4) * 1024); SCHEDB(); }
#pragma unroll
            for (int k = 0; k < 4; ++k) {
                const u64 lo = __hip_atomic_load((const gu64*)(ob2 + vr + k * 1024), __ATOMIC_RELAXED, __HIP_MEMORY_SCOPE_AGENT);
                const u64 hi = __hip_atomic_load((const gu64*)(ob2 + vr + k * 1024 + 128), __ATOMIC_RELAXED, __HIP_MEMORY_SCOPE_AGENT);
                O[16 + k] = (u32x4){(u32)lo, (u32)(lo >> 32), (u32)hi, (u32)(hi >> 32)};
            }
            R[0] = ld16(xb, vx, 0); R[1] = ld16(xb, vx, 512);
            SCHEDB();
        };
        auto gemm = [&]() {
            u32 wa0 = lds0 + C_XN + ln * 16, wa1 = wa0 + 40960;
            asm volatile("" : "+v"(wa0), "+v"(wa1));
            f32x4 acc[4];
            bf8 wf[8];
#pragma unroll
            for (int t = 0; t < 8; ++t) wf[t] = *(LDS3 const bf8*)(uintptr_t)(wa0 + t * 1024);
#pragma unroll
            for (int k = 0; k < 20; ++k) {
                if (k >= 16) { PIN(O[k]); NOPV(); }
#pragma unroll
                for (int t = 0; t < 4; ++t) {
                    if (k == 0) MMA0_VV(acc[t], wf[t], O[k]); else MMA_VV(acc[t], wf[(k & 1) * 4 + t], O[k]);
                    SCHEDB();
                    if (k < 18) wf[(k & 1) * 4 + t] = *(LDS3 const bf8*)(uintptr_t)((k < 8 ? wa0 : wa1) + (((k + 2) % 10) * 4 + t) * 1024);
                    SCHEDB();
                }
            }
            asm volatile("s_nop 7" : "+v"(acc[0]), "+v"(acc[1]), "+v"(acc[2]), "+v"(acc[3]));
            float chk = 0.f;
#pragma unroll
            for (int i = 0; i < 8; ++i) {
                const u32 r = R[i >> 2][i & 3], y = pk2bf(acc[i >> 1][(i & 1) * 2], acc[i >> 1][(i & 1) * 2 + 1]);
                const float a = bflo(r) + bflo(y), b = bfhi(r) + bfhi(y);
                dw[i] = pk2bf(a, b);
                chk += a;
            }
            return __any(chk != chk);
        };
        const int T0 = rb + 16 * w;
        load(T0);
        static_for<0, 2>([&](auto PASS) {
            constexpr int pass = CI(PASS);
            const int T = T0 + 64 * pass;
            TT(set * 128 + 32 + c, 1 + pass * 4);
            if (__builtin_expect(gemm(), 0)) {
                for (int tries = 0;; ++tries) {
                    ready(T); load(T);
                    u32 m = max4(max4(0, R[0]), R[1]);
#pragma unroll
                    for (int k = 0; k < 20; ++k) m = max4(m, O[k]);
                    if (!__any(m == ~0u)) break;
                    if (tries >= cap) { cap = 1; break; }
                    __builtin_amdgcn_s_sleep(6);
                }
                gemm();
            }
            TT(set * 128 + 32 + c, 2 + pass * 4);
            if (layer == 1) {
                st32((char*)p.out + (size_t)T * 16 * DM * 2 + cb * 128, (ln & 15) * (DM * 2) + (ln >> 4) * 32, (u32x4){dw[0], dw[1], dw[2], dw[3]}, (u32x4){dw[4], dw[5], dw[6], dw[7]});
                if constexpr (pass == 0) load(T + 64);
            } else {
            float s1 = 0.f, s2 = 0.f;
#pragma unroll
            for (int i = 0; i < 8; ++i) { const float a = bflo(dw[i]), b = bfhi(dw[i]); s1 += a + b; s2 = fmaf(a, a, fmaf(b, b, s2)); }
            s1 = grp_sum(s1); s2 = grp_sum(s2);
            char* stl = (wsp(p) + O_ST) + (size_t)T * 1280;
            st8m(stl + cb * 128, ln * 8, (u32x2){nosent(__float_as_uint(s1)), nosent(__float_as_uint(s2))}, 0xffffull);
            const size_t fo = ACT + (size_t)(T >> 1) * 40960 + (cb * 4 + (T & 1)) * 1024;
            const u32 lo = (ln >> 5) * 2048 + ((ln >> 4) & 1) * 512 + (ln & 15) * 16;
            if constexpr (pass == 0) __builtin_amdgcn_s_sleep(ST_LAG);
            else {
                wait_first_tokens(p, set, lane, cap);
                dma_kb<10>(xn + ACT + (size_t)set * 40960 + w * 10240, lds0 + C_YL + w * 10240, lane * 16);
            }
            for (int spin = 0;; ++spin) {
                u32 m = 0;
                s1 = 0.f; s2 = 0.f;
#pragma unroll
                for (int i = 0; i < 3; ++i) {
                    const int k = (ln >> 4) + 4 * i;
                    const u64 v = __hip_atomic_load((const gu64*)(stl + min(k, 9) * 128 + (ln & 15) * 8), __ATOMIC_RELAXED, __HIP_MEMORY_SCOPE_AGENT);
                    m = max(m, max((u32)v, (u32)(v >> 32)));
                    if (k < 10) { s1 += __uint_as_float((u32)v); s2 += __uint_as_float((u32)(v >> 32)); }
                }
                if (!__any(m == ~0u)) break;
                if (spin >= cap) { cap = 1; break; }
                __builtin_amdgcn_s_sleep(2);
            }
            TT(set * 128 + 32 + c, 3 + pass * 4);
            if constexpr (pass == 0) { if (w > 0) load(T + 64); }
            s1 = grp_sum(s1); s2 = grp_sum(s2);
            const float mu = s1 * (1.0f / DM), mr = rbf(mu), rs = ln_scale(fmaxf(fmaf(-mu, mu, s2 * (1.0f / DM)), 0.f));
            u32 dn[8];
#pragma unroll
            for (int i = 0; i < 8; ++i) dn[i] = ln_pair(dw[i], mr, rs);
            st16s((wsp(p) + O_XN) + fo, lo, (u32x4){dn[0], dn[4], dn[1], dn[5]}); st16s((wsp(p) + O_XN) + fo + 256, lo, (u32x4){dn[2], dn[6], dn[3], dn[7]});
            st16s((wsp(p) + O_XR) + fo, lo, (u32x4){dw[0], dw[4], dw[1], dw[5]}); st16s((wsp(p) + O_XR) + fo + 256, lo, (u32x4){dw[2], dw[6], dw[3], dw[7]});
            TT(set * 128 + 32 + c, 4 + pass * 4);
            if constexpr (pass == 0) { if (w == 0) { load(T + 64); asm volatile("s_waitcnt vmcnt(18)" ::: "memory"); st4_lane0(wsp(p) + O_FL + (rb * 10 + cb) * 128, 1u); } }
            }
        });
    }
    if (layer == 1) break;
    sch += 5 * SLOT; xn += ACT;
    TT(set * 128 + 32 + c, 9);
    }
}

constexpr int B_PQ = 0, B_IM = 131072, B_CS = 155648, B_IS = 176;
__device__ __forceinline__ void bw_role(const LaParams& p, const int hb, char* shm) {
    const int lane = threadIdx.x & 63, w = __builtin_amdgcn_readfirstlane(threadIdx.x >> 6);
    const int fr = lane & 15, g = lane >> 4;
    int cap = SPIN_CAP;
    if (hb > 0) {
        const char* src = (const char*)((hb & 1) ? p.Wk : p.Wq) + (size_t)(DM - 16 * BPF + (hb >> 1) * BPF) * HD * 2 + w * (BPF * 3072);
        for (int i = 0; i < BPF * 3; ++i) dma_gather(src + i * 1024, (u32)(uintptr_t)(LDS3 char*)shm + w * 1024, lane * 16);
        VM0();
    }
    if (hb == 0 && w == 0) {
        u32 best[2] = {~0u, ~0u};
        const u32 tc = rt32();
        for (int i = 0; i < 256; ++i) {
#pragma unroll
            for (int sd = 0; sd < 2; ++sd) {
                u64 t0, t1;
                u32 z = 0, one = 1;
                asm volatile("s_memtime %0\n\ts_mov_b64 exec, 1\n\ts_waitcnt lgkmcnt(0)\n\tglobal_atomic_add %2, %3, %2, %4 sc0 sc1\n\ts_waitcnt vmcnt(0)\n\t"
                             "s_memtime %1\n\ts_mov_b64 exec, -1\n\ts_waitcnt lgkmcnt(0)"
                             : "=&s"(t0), "=&s"(t1), "+v"(one) : "v"(z), "s"(p.hop + M0_OFF + sd * 8192 + 6144 + (i & 3) * 256) : "memory");
                best[sd] = min(best[sd], (u32)(t1 - t0));
            }
            const u32 el = rt32() - tc, lo = min(best[0], best[1]), hi = max(best[0], best[1]);
            if (el > 1900u || (el > 600u && lo < 700u && hi - lo > 130u && hi - lo < 420u)) break;
        }
        u32 xcc; asm volatile("s_getreg_b32 %0, hwreg(HW_REG_XCC_ID)" : "=s"(xcc));
        st4_lane0(wsp(p) + O_FL + 384 * 128, ((xcc >> 2) & 1) ^ (best[1] < best[0] ? 1u : 0u) ^ WRONG);
    }
    bf8 WA[64], WV[16];
    {
        const int t = threadIdx.x;
        float* xs = (float*)(shm + 81920);
        float* ph = xs + 128 * 61;
        for (int i = t; i < 400; i += 256) ph[i] = bf2f(p.wphi[hb * 400 + i]);
        const int j = 4 * w + (fr & 3), G = fr >> 2;
        for (int d0 = 0; d0 < DM; d0 += 128) {
            u32 v15[15];
#pragma unroll
            for (int k = 0; k < 15; ++k) { const int i = t + k * 256; v15[k] = *(const u32*)(p.wqkv + (size_t)(d0 + i / 30) * 1920 + ((i % 30) / 10) * 640 + hb * 20 + (i % 10) * 2); }
            __syncthreads();
#pragma unroll
            for (int k = 0; k < 15; ++k) { const int i = t + k * 256; float* o = xs + (i / 30) * 61 + (i % 30) * 2; o[0] = bflo(v15[k]); o[1] = bfhi(v15[k]); }
            __syncthreads();
            for (int kk = 0; kk < 4; ++kk) {
                float v[8];
                const float* xr = xs + (kk * 32 + g * 8) * 61;
                if (j < 10) {
                    const int m = j / 5, f = 5 * G + j % 5;
#pragma unroll
                    for (int e = 0; e < 8; ++e) v[e] = 0.f;
#pragma unroll 4
                    for (int i = 0; i < 20; ++i) {
                        const float pw = ph[i * 20 + f];
#pragma unroll
                        for (int e = 0; e < 8; ++e) v[e] = fmaf(xr[e * 61 + m * 20 + i], pw, v[e]);
                    }
                } else {
#pragma unroll
                    for (int e = 0; e < 8; ++e) v[e] = j < 15 ? xr[e * 61 + 40 + 5 * G + min(j, 14) - 10] : 1.0f;
                }
                *(u32x4*)(shm + ((d0 / 32 + kk) * 4 + w) * 1024 + lane * 16) = (u32x4){pk2bf(v[0], v[1]), pk2bf(v[2], v[3]), pk2bf(v[4], v[5]), pk2bf(v[6], v[7])};
            }
        }
        __syncthreads();
#pragma unroll
        for (int i = 0; i < 64; ++i) WA[i] = *(const bf8*)(shm + i * 1024 + lane * 16);
#pragma unroll
        for (int i = 0; i < 16; ++i) WV[i] = *(const bf8*)(shm + (64 + i) * 1024 + lane * 16);
        __syncthreads();
    }
    auto proj = [&](f32x4 (&acc)[4], f32x4& sq, auto&& fetch) {
        u32x4 X[20];
#pragma unroll
        for (int k = 0; k < 12; ++k) { X[k] = fetch(k); SCHEDB(); }
#pragma unroll
        for (int k = 0; k < 20; ++k) {
#pragma unroll
            for (int t = 0; t < 4; ++t) {
                if (k == 0) MMA0_AV(acc[t], WA[t], X[k]); else if (k < 16) MMA_AV(acc[t], WA[(k * 4 + t) & 63], X[k]); else MMA_VV(acc[t], WV[(k * 4 + t) & 15], X[k]);
            }
            if (k == 0) MMA0_VV(sq, X[k], X[k]); else MMA_VV(sq, X[k], X[k]);
            SCHEDB();
            if (k < 8) X[k + 12] = fetch(k + 12);
            SCHEDB();
        }
        asm volatile("s_nop 7" : "+v"(acc[0]), "+v"(acc[1]), "+v"(acc[2]), "+v"(acc[3]), "+v"(sq));
    };
    {
        f32x4 t, cs[4];
        u32x4 o1 = {0x3f803f80u, 0x3f803f80u, 0x3f803f80u, 0x3f803f80u};
        asm volatile("s_nop 1" : "+v"(o1));
        proj(cs, t, [&](int) { return o1; });
#pragma unroll
        for (int i = 0; i < 4; ++i) *(f32x4*)(shm + B_CS + i * 1024 + lane * 16) = cs[i];
    }
    char* im = shm + B_IM + w * 6144;
    const u32 imr = (u32)(uintptr_t)(LDS3 char*)im + (4 * g + (fr >> 2)) * B_IS + (fr & 3) * 8;
    typedef __attribute__((ext_vector_type(32))) u32 u32x32;
    for (int layer = 0; layer < 2; ++layer) {
        const char* sch = p.hop + (hb & 1) * 8192 + (S_SLOT + layer * 5 + 4) * SLOT;
        f32x4 kv[3][2];
#pragma unroll
        for (int i = 0; i < 6; ++i) ZERO4(kv[i >> 1][i & 1]);
        u32x32 q5;
        u32 tn = 0;
        float vq[5];
        auto phi = [&](const float* x, u32 (&ph)[5]) {
            const float mx = grp_max(fmaxf(fmaxf(fmaxf(x[0], x[1]), fmaxf(x[2], x[3])), x[4]));
            const float mn = -grp_max(-fminf(fminf(fminf(x[0], x[1]), fminf(x[2], x[3])), x[4]));
            float e[10], a = 0.f, b = 0.f;
#pragma unroll
            for (int j = 0; j < 5; ++j) {
                e[j] = __builtin_amdgcn_exp2f((x[j] - mx) * 1.4426950408889634f); a += e[j];
                e[5 + j] = __builtin_amdgcn_exp2f((mn - x[j]) * 1.4426950408889634f); b += e[5 + j];
            }
            a = __builtin_amdgcn_rcpf(grp_sum(a)); b = __builtin_amdgcn_rcpf(grp_sum(b));
#pragma unroll
            for (int j = 0; j < 5; ++j) { e[j] *= a; e[5 + j] *= b; }
#pragma unroll
            for (int j = 0; j < 5; ++j) ph[j] = pk2bf(e[2 * j], e[2 * j + 1]);
        };
        auto phiq = [&](const int it) {
            u32 ph[5];
            phi(vq, ph);
            *(u32x4*)(shm + B_PQ + (w * 32 + it) * 1024 + lane * 16) = (u32x4){ph[0], ph[1], ph[2], ph[3]};
            q5[it] = ph[4];
        };
        for (int it = 0; it < 32; ++it) {
            const int s = 2 * it + (w >> 1);
            if (s + 2 < NSEG) { wait_rt(tn); tn = sched_get<BSL>(sched_peek(sch + (s + 1) * 128 + 4), sch + (s + 1) * 128 + 4, cap); wait_rt(tn + B_LAG); tn += BPEEK; }
            else wait_rt(sched_get(~0u, p.hop + (hb & 1) * 8192 + (T_SLOT + 1) * SLOT + (48 + layer * 4 + (s & 1)) * 128, cap) + T_X1);
            const char* xb = (wsp(p) + O_X1) + layer * ACT + (size_t)s * 40960 + (w & 1) * 256;
            if (it >= 30) TT((hb >> 4) * 128 + 112 + (hb & 15), (it - 30) * 3);
            f32x4 acc[4], sq;
            const char* xk[5];
#pragma unroll
            for (int i = 0; i < 5; ++i) { xk[i] = xb + i * 8192 + 4096; asm volatile("" : "+s"(xk[i])); }
            for (int pass = 0;; ++pass) {
                u32 vo = g * 512 + fr * 16;
                asm volatile("" : "+v"(vo) :: "memory");
                auto fetch = [&](const int k) { return ld16(xk[k >> 2], vo, ((k & 3) - 2) * 2048); };
                proj(acc, sq, fetch);
                if (pass > 0 || !__any(acc[3][3] != acc[3][3])) break;
                CNT(4);
                for (int tries = 0;; ++tries) {
                    asm volatile("" : "+v"(vo) :: "memory");
                    u32 m = 0;
#pragma unroll
                    for (int k = 0; k < 20; k += 10) {
#pragma unroll
                        for (int i = 0; i < 10; ++i) m = max4(m, fetch(k + i));
                        SCHEDB();
                    }
                    if (!__any(m == ~0u)) break;
                    if (tries >= cap) { cap = 1; break; }
                    __builtin_amdgcn_s_sleep(6);
                }
            }
            if (it >= 30) TT((hb >> 4) * 128 + 112 + (hb & 15), (it - 30) * 3 + 1);
            if (s == NSEG - 1 && hb == 5 && w == 3) stamp2(p.hop, 50 + layer * 4);
            const float dsel = (fr & 2) ? ((fr & 1) ? sq[3] : sq[2]) : ((fr & 1) ? sq[1] : sq[0]);
            const float s2 = grp_sum(g == (fr >> 2) ? dsel : 0.f);
            const float mu = acc[3][3] * (1.0f / DM), rs = __builtin_amdgcn_rsqf(fmaxf(fmaf(-mu, mu, s2 * (1.0f / DM)), 0.f) + 1e-5f);
            float v[15];
            f32x4 cs[4];
#pragma unroll
            for (int i = 0; i < 4; ++i) cs[i] = *(const f32x4*)(shm + B_CS + i * 1024 + lane * 16);
#pragma unroll
            for (int j = 0; j < 15; ++j) v[j] = fmaf(-mu, cs[j >> 2][j & 3], acc[j >> 2][j & 3]) * rs;
            u32 ph[5];
            phi(v + 5, ph);
            char* row = im + fr * B_IS;
            *(u32x2*)(row + g * 8) = (u32x2){ph[0], ph[1]};
            *(u32x2*)(row + 32 + g * 8) = (u32x2){ph[2], ph[3]};
            *(u32x2*)(row + 64 + g * 8) = (u32x2){ph[4], 0u};
            *(u32x2*)(row + 96 + g * 8) = (u32x2){pk2bf(v[10], v[11]), pk2bf(v[12], v[13])};
            *(u32x2*)(row + 128 + g * 8) = (u32x2){pk2bf(v[14], 1.0f), 0u};
            s16x4 tf[5];
#pragma unroll
            for (int i = 0; i < 5; ++i) tf[i] = __builtin_amdgcn_ds_read_tr16_b64_v4i16((LDS3 s16x4*)(uintptr_t)(imr + i * 32));
#pragma unroll
            for (int i = 0; i < 6; ++i) MMA16(kv[i >> 1][i & 1], tf[i >> 1], tf[3 + (i & 1)]);
#pragma unroll
            for (int j = 0; j < 5; ++j) vq[j] = v[j];
            if (it < 31) phiq(it);
            if (it >= 30) TT((hb >> 4) * 128 + 112 + (hb & 15), (it - 30) * 3 + 2);
        }
        asm volatile("s_nop 7" : "+v"(kv[0][0]), "+v"(kv[0][1]), "+v"(kv[1][0]), "+v"(kv[1][1]), "+v"(kv[2][0]), "+v"(kv[2][1]));
#pragma unroll
        for (int i = 0; i < 6; ++i) *(f32x4*)(im + i * 1024 + lane * 16) = kv[i >> 1][i & 1];
        RAW_BARRIER();
        bf8 ka[2];
        u32x2 kb[2];
#pragma unroll
        for (int dt = 0; dt < 2; ++dt) {
            f32x4 t[3];
#pragma unroll
            for (int ft = 0; ft < 3; ++ft) {
                const char* a = shm + B_IM + (ft * 2 + dt) * 1024 + lane * 16;
                t[ft] = (*(const f32x4*)a + *(const f32x4*)(a + 6144)) + (*(const f32x4*)(a + 12288) + *(const f32x4*)(a + 18432));
            }
            ka[dt] = pack8(t[0], t[1]);
            kb[dt] = (u32x2){pk2bf(t[2][0], t[2][1]), pk2bf(t[2][2], t[2][3])};
            PIN(ka[dt]); PIN(kb[dt]);
        }
        char* ob = (wsp(p) + O_OB) + layer * ACT + (size_t)w * 20480 + hb * 512;
        TT((hb >> 4) * 128 + 112 + (hb & 15), 6);
        u32 rofs = g * 81920 + 16384 - hb * 384 + fr * 8, mofs = (g & 1) * 81920 + (g >> 1) * 256 + fr * 16, mofs2 = mofs + 2 * 81920;
        asm volatile("" : "+v"(rofs), "+v"(mofs), "+v"(mofs2));
        f32x4 o[2][4][2];
        bf8 qa[2][4];
        u32x2 qb[4];
        auto ldq = [&](const int gr) {
#pragma unroll
            for (int u = 0; u < 4; ++u) qa[gr & 1][u] = *(const bf8*)(shm + B_PQ + (w * 32 + gr * 4 + u) * 1024 + lane * 16);
        };
        auto mm = [&](const int gr, const int k) {
            SCHEDB();
            if (k < 4) { qb[k] = (u32x2){q5[gr * 4 + k], 0u}; PIN(qb[k]); }
            if (k < 8) MMA0_VV(o[gr & 1][k >> 1][k & 1], ka[k & 1], qa[gr & 1][k >> 1]);
            else MMA16(o[gr & 1][(k - 8) >> 1][k & 1], kb[k & 1], qb[(k - 8) >> 1]);
            SCHEDB();
        };
        ldq(0); ldq(1);
        NOPV();
#pragma unroll
        for (int k = 0; k < 16; ++k) mm(0, k);
        static_for<0, 8>([&](auto IT) {
            constexpr int gr = CI(IT);
            auto nx = [&](const int k) { if constexpr (gr < 7) mm(gr + 1, k); else { SCHEDB(); asm volatile("s_nop 0"); SCHEDB(); } };
            if constexpr (gr == 7) asm volatile("s_nop 7");
            u32 d[4][2];
            float x[4];
            nx(0); nx(1);
#pragma unroll
            for (int u = 0; u < 4; ++u) {
                f32x4& a = o[gr & 1][u][0];
                f32x4& b = o[gr & 1][u][1];
                PIN(a); PIN(b);
                float inv = __builtin_amdgcn_rcpf(b[1] + 1e-6f), y0, y1, y2, y3;
                nx(2 + 3 * u);
                asm("v_mul_f32 %0, %4, %8\n\tv_mul_f32 %1, %5, %8\n\tv_mul_f32 %2, %6, %8" : "=&v"(y0), "=&v"(y1), "=&v"(y2), "=&v"(y3) : "v"(a[0]), "v"(a[1]), "v"(a[2]), "v"(a[3]), "v"(inv));
                nx(3 + 3 * u);
                y3 = a[3] * inv; x[u] = b[0] * inv;
                d[u][0] = pk2bf(y0, y1);
                nx(4 + 3 * u);
                d[u][1] = pk2bf(y2, y3);
                if (u & 1) {
                    auto t0 = __builtin_amdgcn_permlane16_swap(d[u - 1][0], d[u][0], false, false);
                    auto t1 = __builtin_amdgcn_permlane16_swap(d[u - 1][1], d[u][1], false, false);
                    __builtin_amdgcn_global_store_b128((gu32x4*)(ob + (u == 1 ? mofs : mofs2)), (u32x4){nosent(t0[0]), nosent(t1[0]), nosent(t0[1]), nosent(t1[1])}, "agent");
                }
            }
            if constexpr (gr == 5) phiq(31);
            if constexpr (gr < 6) ldq(gr + 2);
            nx(14);
            auto a0 = __builtin_amdgcn_permlane32_swap(__float_as_uint(x[0]), __float_as_uint(x[2]), false, false);
            auto a1 = __builtin_amdgcn_permlane32_swap(__float_as_uint(x[1]), __float_as_uint(x[3]), false, false);
            nx(15);
            auto b0 = __builtin_amdgcn_permlane16_swap(a0[0], a1[0], false, false);
            auto b2 = __builtin_amdgcn_permlane16_swap(a0[1], a1[1], false, false);
            __hip_atomic_store((gu64*)(ob + rofs), (u64)nosent(pk2bf(__uint_as_float(b0[0]), __uint_as_float(b0[1]))) | ((u64)nosent(pk2bf(__uint_as_float(b2[0]), __uint_as_float(b2[1]))) << 32), __ATOMIC_RELAXED, __HIP_MEMORY_SCOPE_AGENT);
            ob += 4 * 81920;
            asm volatile("" : "+s"(ob));
        });
        RAW_BARRIER();
        TT((hb >> 4) * 128 + 112 + (hb & 15), 7);
    }
}

__global__ void __launch_bounds__(256, 1) la_kernel(LaParams p) {
    extern __shared__ __attribute__((aligned(16))) char shm[];
    p.hop = align2m(p.ws + O_HOP);
    if (threadIdx.x == 0) {
        u32 xcc; asm volatile("s_getreg_b32 %0, hwreg(HW_REG_XCC_ID)" : "=s"(xcc));
        int role = 255;
        for (int a = 0; a < 8; ++a) {
            const u32 v = (xcc + a) & 7;
            const u32 k = __hip_atomic_fetch_add((gu32*)(((u32*)(p.ws + O_CTL)) + v * 32), 1u, __ATOMIC_RELAXED, __HIP_MEMORY_SCOPE_AGENT);
            if (k < 32) { role = v * 32 + k; break; }
        }
        *(int*)shm = role;
    }
    __syncthreads();
    const int role = __builtin_amdgcn_readfirstlane(*(int*)shm);
    __syncthreads();
    const int set = role >> 7, id = (role >> 5) & 3, k = role & 31;
    if (k < 8) qk_role(p, set, id * 8 + k, shm);
    else if (k < 28) comb_role(p, set, id * 20 + k - 8, shm);
    else bw_role(p, (role >> 5) * 4 + k - 28, shm);
}

struct PrepParams { const u16* x; const u16* mem0; const u16* wo; char* ws; };
constexpr int N_LN = 128, N_WO = 200, N_FILL = 512;
__global__ void __launch_bounds__(256) prep_kernel(PrepParams q) {
    int b = blockIdx.x;
    const int t = threadIdx.x, lane = t & 63, w = t >> 6, fr = lane & 15, g = lane >> 4;
    char* hop = align2m(q.ws + O_HOP);
    if (b < N_LN) {
        const int tok = b * 16 + w * 4 + g;
        u32x4 d[3][2];
        float s = 0.f;
#pragma unroll
        for (int i = 0; i < 3; ++i) {
            const u32x4* a = (const u32x4*)(q.x + (size_t)tok * DM + min(fr + 16 * i, 39) * 16);
            d[i][0] = a[0]; d[i][1] = a[1];
        }
#pragma unroll
        for (int i = 0; i < 3; ++i)
#pragma unroll
            for (int e = 0; e < 8; ++e) if (fr + 16 * i < 40) s += bflo(d[i][e >> 2][e & 3]) + bfhi(d[i][e >> 2][e & 3]);
        const float mu = row16_sum(s) * (1.0f / DM);
        float v = 0.f;
#pragma unroll
        for (int i = 0; i < 3; ++i)
#pragma unroll
            for (int e = 0; e < 8; ++e) if (fr + 16 * i < 40) { const float a = bflo(d[i][e >> 2][e & 3]) - mu, c = bfhi(d[i][e >> 2][e & 3]) - mu; v = fmaf(a, a, fmaf(c, c, v)); }
        const float rs = ln_scale(row16_sum(v) * (1.0f / DM)), mr = rbf(mu);
#pragma unroll
        for (int i = 0; i < 3; ++i) {
            const int blk = fr + 16 * i;
            if (blk >= 40) continue;
            const size_t o = (size_t)(tok >> 5) * 40960 + ((blk >> 1) * 2 + ((tok >> 4) & 1)) * 1024 + ((blk & 1) * 32 + (tok & 15)) * 16;
            u32 n[8];
#pragma unroll
            for (int e = 0; e < 8; ++e) n[e] = ln_pair(d[i][e >> 2][e & 3], mr, rs);
            *(u32x4*)(q.ws + O_XR + o) = (u32x4){d[i][0][0], d[i][1][0], d[i][0][1], d[i][1][1]};
            *(u32x4*)(q.ws + O_XR + o + 256) = (u32x4){d[i][0][2], d[i][1][2], d[i][0][3], d[i][1][3]};
            *(u32x4*)(q.ws + O_XN + o) = (u32x4){n[0], n[4], n[1], n[5]};
            *(u32x4*)(q.ws + O_XN + o + 256) = (u32x4){n[2], n[6], n[3], n[7]};
        }
        return;
    }
    b -= N_LN;
    if (b < N_WO) {
        const int cb = b / 20, kk = b % 20;
        const u16* col = q.wo + cb * 64 + 16 * (fr >> 2) + 4 * w + (fr & 3);
        u32 v[8];
#pragma unroll
        for (int e = 0; e < 8; ++e) {
            const int row = kk < 16 ? (2 * kk + (g >> 1)) * 20 + 5 * (2 * (g & 1) + (e >> 2)) + (e & 3) : (8 * (kk - 16) + 2 * g + (e >> 2)) * 20 + 5 * (e & 3) + 4;
            v[e] = col[(size_t)row * DM];
        }
        *(u32x4*)(q.ws + O_WO + (size_t)cb * 81920 + (kk * 4 + w) * 1024 + lane * 16) = (u32x4){v[0] | v[1] << 16, v[2] | v[3] << 16, v[4] | v[5] << 16, v[6] | v[7] << 16};
        return;
    }
    b -= N_WO;
    const u32x4 SENT = {~0u, ~0u, ~0u, ~0u};
    const u32 i0 = b * 256 + t;
    if (b == 0) ((u32*)(q.ws + O_CTL))[t] = 0;
    if (i0 < 8 * 2 * 320) {
        const int sl = i0 / 640, e = i0 % 320;
        const u32x4 v = *(const u32x4*)(q.mem0 + (e & 7) * DM + ((sl & 1) * 40 + (e >> 3)) * 8);
        st16(hop + M0_OFF + (i0 / 320) * 8192 + e * 16, (u32x4){nosent(v[0]), nosent(v[1]), nosent(v[2]), nosent(v[3])});
    }
    for (u32 i = i0; i < HOP_BYTES / 16; i += N_FILL * 256) st16(hop + (size_t)i * 16, SENT);
    for (u32 i = i0; i < (O_XN - O_FL) / 16; i += N_FILL * 256) st16(q.ws + O_FL + (size_t)i * 16, SENT);
}

extern "C" void kernel_launch(void* const* d_in, const int* in_sizes, int n_in,
                              void* d_out, int out_size, void* d_ws, size_t ws_size,
                              hipStream_t stream) {
    PrepParams q;
    q.x = (const u16*)d_in[0]; q.mem0 = (const u16*)d_in[1];
    q.wo = (const u16*)d_in[10];
    q.ws = (char*)d_ws;
    prep_kernel<<<N_LN + N_WO + N_FILL, 256, 0, stream>>>(q);
    LaParams p;
    p.ws = (char*)d_ws; p.out = (u16*)d_out; p.hop = (char*)d_ws;
    p.Wq = (const u16*)d_in[2]; p.Wk = (const u16*)d_in[3]; p.Wv = (const u16*)d_in[4]; p.Wo = (const u16*)d_in[5];
    p.wqkv = (const u16*)d_in[8]; p.wphi = (const u16*)d_in[9];
    (void)hipFuncSetAttribute((const void*)la_kernel, hipFuncAttributeMaxDynamicSharedMemorySize, LA_LDS);
    la_kernel<<<256, 256, LA_LDS, stream>>>(p);
}
```

```cpp
#include <hip/hip_runtime.h>
#include <hip/hip_bf16.h>
#include <stdint.h>
#include <type_traits>

typedef unsigned short u16;
typedef unsigned int u32;
typedef unsigned long long u64;
using bf16x8 = __attribute__((ext_vector_type(8))) short;
using f32x4 = __attribute__((ext_vector_type(4))) float;

constexpr int S_TOK = 2048, DM = 640, NH = 32, DH = 192;
constexpr int MEM = 8, SEG = 32, NSEG = S_TOK / SEG;
constexpr int HD = NH * DH;

__device__ __forceinline__ float bf2f(u16 v) { return __uint_as_float(((u32)v) << 16); }
__device__ __forceinline__ u32 pk2bf(float lo, float hi) {
    typedef __attribute__((ext_vector_type(2))) float f32x2_t;
    typedef __attribute__((ext_vector_type(2))) __bf16 bf16x2_t;
    bf16x2_t r = __builtin_convertvector((f32x2_t){lo, hi}, bf16x2_t);
    return __builtin_bit_cast(u32, r);
}
__device__ __forceinline__ float rbf(float f) { return __uint_as_float(pk2bf(f, f) << 16); }
typedef __attribute__((ext_vector_type(8))) __bf16 bf8;
typedef __attribute__((ext_vector_type(16))) __bf16 bf16v;
typedef __attribute__((ext_vector_type(4))) __bf16 bf4;
typedef __attribute__((ext_vector_type(4))) short s16x4;
using u32x4 = __attribute__((ext_vector_type(4))) u32;
using u32x2 = __attribute__((ext_vector_type(2))) u32;
typedef __attribute__((address_space(1))) u32 gu32;
typedef __attribute__((address_space(1))) u64 gu64;
#define LDS3 __attribute__((address_space(3)))

constexpr int SLOT = 16384;
#define PCSV 3
constexpr int PCS = PCSV, P_SLOT0 = 0, M_SLOT0 = 16 * PCS, S_SLOT = M_SLOT0 + 32, T_SLOT = S_SLOT + 10, N_SLOT = T_SLOT + 2;
constexpr size_t HOP_BYTES = (size_t)N_SLOT * SLOT, M0_OFF = HOP_BYTES;
constexpr size_t PX_BUF = 4 * 32 * 640, PX_BYTES = 8 * PX_BUF;
constexpr int LA_LDS = 162816 + 256;
constexpr int SPIN_CAP = 1 << 16;
#define D1I 131
#define D2I 53
#define MG1 10
#define MG2 11
#define DUP 5
#define PACE 6
#define CLAG 48
#define T_PX 26
#define T_X1 75
#define T_OB 150
#define T_TOK 350
#define ST_LAG 6
#define OB_STEP 25
#define OB_COLD 20
#define NH_ 2
#define W0D 6
#define LATEP 2
#define SYNCB 1
#define PACE2 5
#define BPEEK 400
#define PLIN 1
#define SLK 6
#define FSL 8
#define E0 110
#define HOLD 4
#define DOWN 1
#define EY 35
#define BOOST 3
#define DSH "1"
#define PAUSE 160u
#define BSL 40
#define PKK 18
#define PKT 2
#define BPF 20
#define BUMP 2
#define WRONG 0u
#define RECV 4
#define B_LAG 65
#define ELATE 85

__device__ __forceinline__ void st16(void* p, u32x4 v) {
    asm volatile("global_store_dwordx4 %0, %1, off sc1\n\ts_nop 2" :: "v"(p), "v"(v) : "memory");
}
typedef __attribute__((address_space(1))) u32x4 gu32x4;
__device__ __forceinline__ u32x4 ld16(const char* base, u32 off, int imm) { return __builtin_amdgcn_global_load_b128((gu32x4*)(base + off + imm), "agent"); }
__device__ __forceinline__ u32 nosent(u32 v) { return min(v, 0xFFFFFFFEu); }
__device__ __forceinline__ u32 rt32() { return (u32)__builtin_amdgcn_s_memrealtime(); }
__device__ __forceinline__ void wait_rt(u32 t) {
    int d = (int)(t - rt32()) - 4;
    if (d > 4096 || d < 0) return;
    while (d > 7) { if (d > 18) __builtin_amdgcn_s_sleep(2); d = max((int)(t - rt32()) - 4, 0); }
    asm volatile("s_bitcmp1_b32 %0, 2\n\ts_cbranch_scc0 1f\n\ts_nop 15\n\ts_nop 7\n1:\n\ts_bitcmp1_b32 %0, 1\n\ts_cbranch_scc0 2f\n\ts_nop 12\n2:\n\t"
                 "s_bitcmp1_b32 %0, 0\n\ts_cbranch_scc0 3f\n\ts_nop 7\n3:" :: "s"(d) : "scc");
}
template <int N> __device__ __forceinline__ void dma_kb(const char* src, u32 lds_dst, u32 lane16) {
#pragma unroll
    for (int i = 0; i < N; i += 4) {
        u32 keep;
        if (N - i >= 4)
            asm volatile("s_mov_b32 %0, m0\n\ts_mov_b32 m0, %3\n\ts_nop 2\n\tglobal_load_lds_dwordx4 %1, %2 sc1\n\tglobal_load_lds_dwordx4 %1, %2 offset:1024 sc1\n\t"
                         "global_load_lds_dwordx4 %1, %2 offset:2048 sc1\n\tglobal_load_lds_dwordx4 %1, %2 offset:3072 sc1\n\ts_mov_b32 m0, %0"
                         : "=&s"(keep) : "v"(lane16), "s"(src + i * 1024), "s"(lds_dst + i * 1024) : "memory");
        else if (N - i == 2)
            asm volatile("s_mov_b32 %0, m0\n\ts_mov_b32 m0, %3\n\ts_nop 2\n\tglobal_load_lds_dwordx4 %1, %2 sc1\n\tglobal_load_lds_dwordx4 %1, %2 offset:1024 sc1\n\ts_mov_b32 m0, %0"
                         : "=&s"(keep) : "v"(lane16), "s"(src + i * 1024), "s"(lds_dst + i * 1024) : "memory");
        else
            asm volatile("s_mov_b32 %0, m0\n\ts_mov_b32 m0, %3\n\ts_nop 2\n\tglobal_load_lds_dwordx4 %1, %2 sc1\n\ts_mov_b32 m0, %0"
                         : "=&s"(keep) : "v"(lane16), "s"(src + i * 1024), "s"(lds_dst + i * 1024) : "memory");
    }
}
template <int OFF> __device__ __forceinline__ void dma_one(const char* src, u32 lds_dst, u32 lane16) {
    asm volatile("s_mov_b32 m0, %2\n\ts_nop 0\n\tglobal_load_lds_dwordx4 %0, %1 offset:%3 sc1" :: "v"(lane16), "s"(src), "s"(lds_dst), "n"(OFF) : "memory");
}
__device__ __forceinline__ u32 ringp(u32 a, u32 b) { return a + b; }
__device__ __forceinline__ void dma_gather(const char* src, u32 lds_dst, u32 off) {
    u32 keep;
    asm volatile("s_mov_b32 %0, m0\n\ts_mov_b32 m0, %3\n\ts_nop 2\n\tglobal_load_lds_dwordx4 %1, %2\n\ts_mov_b32 m0, %0"
                 : "=&s"(keep) : "v"(off), "s"(src), "s"(lds_dst) : "memory");
}
__device__ __forceinline__ void st16s(char* base, u32 off, u32x4 v) {
    asm volatile("s_nop 4\n\tglobal_store_dwordx4 %0, %1, %2 sc1\n\ts_nop 2" :: "v"(off), "v"(v), "s"(base) : "memory");
}
__device__ __forceinline__ void arm16(char* base, u32 off) {
    u32x4 v;
    asm volatile("v_mov_b64 %0, -1\n\tv_mov_b64 %1, -1" : "=v"(((u64*)&v)[0]), "=v"(((u64*)&v)[1]));
    st16s(base, off, v);
}
template <int B, int E, typename F> __device__ __forceinline__ void static_for(F&& f) {
    if constexpr (B < E) { f(std::integral_constant<int, B>()); static_for<B + 1, E>(f); }
}
#define CI(x) decltype(x)::value
#define RAW_BARRIER() do { asm volatile("s_waitcnt lgkmcnt(0)" ::: "memory"); __builtin_amdgcn_s_barrier(); asm volatile("" ::: "memory"); } while (0)
#define VM0() asm volatile("s_waitcnt vmcnt(0)" ::: "memory")
#define SCHEDB() __builtin_amdgcn_sched_barrier(0)
__device__ __forceinline__ bf8 lo8(const bf16v& v) { return __builtin_shufflevector(v, v, 0, 1, 2, 3, 4, 5, 6, 7); }
__device__ __forceinline__ bf8 hi8(const bf16v& v) { return __builtin_shufflevector(v, v, 8, 9, 10, 11, 12, 13, 14, 15); }
__device__ __forceinline__ bf16v cat8(bf8 a, bf8 b) { return __builtin_shufflevector(a, b, 0, 1, 2, 3, 4, 5, 6, 7, 8, 9, 10, 11, 12, 13, 14, 15); }
#define MMA_VV(c, a, b) asm volatile("v_mfma_f32_16x16x32_bf16 %0, %1, %2, %0" : "+v"(c) : "v"(a), "v"(b))
#define MMA_AV(c, a, b) asm volatile("v_mfma_f32_16x16x32_bf16 %0, %1, %2, %0" : "+v"(c) : "a"(a), "v"(b))
#define MMA_VA(c, a, b) asm volatile("v_mfma_f32_16x16x32_bf16 %0, %1, %2, %0" : "+v"(c) : "v"(a), "a"(b))
#define MMA0_VV(c, a, b) asm volatile("v_mfma_f32_16x16x32_bf16 %0, %1, %2, 0" : "=&v"(c) : "v"(a), "v"(b))
#define MMA0_AV(c, a, b) asm volatile("v_mfma_f32_16x16x32_bf16 %0, %1, %2, 0" : "=&v"(c) : "a"(a), "v"(b))
#define MMA0_VA(c, a, b) asm volatile("v_mfma_f32_16x16x32_bf16 %0, %1, %2, 0" : "=&v"(c) : "v"(a), "a"(b))
#define MMA16(c, a, b) asm volatile("v_mfma_f32_16x16x16_bf16 %0, %1, %2, %0" : "+v"(c) : "v"(a), "v"(b))
#define MMA16_0(c, a, b) asm volatile("v_mfma_f32_16x16x16_bf16 %0, %1, %2, 0" : "=&v"(c) : "v"(a), "v"(b))
#define SMM_V(c, a, b, i) asm volatile("v_smfmac_f32_16x16x64_bf16 %0, %1, %2, %3" : "+v"(c) : "v"(a), "v"(b), "v"(i))
#define SMM_A(c, a, b, i) asm volatile("v_smfmac_f32_16x16x64_bf16 %0, %1, %2, %3" : "+v"(c) : "v"(a), "a"(b), "v"(i))
#define NOPV() asm volatile("s_nop 1")
#define PIN(x) asm volatile("" : "+v"(x))
#define SETTLE() asm volatile("s_nop 7\n\ts_nop 3")
__device__ __forceinline__ bf8 pack8(f32x4 a, f32x4 b) {
    u32x4 r = {pk2bf(a[0], a[1]), pk2bf(a[2], a[3]), pk2bf(b[0], b[1]), pk2bf(b[2], b[3])};
    return __builtin_bit_cast(bf8, r);
}
__device__ __forceinline__ u32 max4(u32 m, u32x4 g) { m = max(max(m, g[0]), g[1]); return max(max(m, g[2]), g[3]); }
#define ZERO4(c) asm volatile("v_mov_b64 %0, 0\n\tv_mov_b64 %1, 0" : "=v"(((u64*)&(c))[0]), "=v"(((u64*)&(c))[1]))
__device__ __forceinline__ bf8 lds8(const char* shm, int off) { return *(const bf8*)(shm + off); }
__device__ __forceinline__ bf16v ldfrag(const char* p) { return cat8(*(const bf8*)p, *(const bf8*)(p + 1024)); }
template <int CTRL> __device__ __forceinline__ float fdpp(float x) {
    return __builtin_bit_cast(float, __builtin_amdgcn_mov_dpp(__builtin_bit_cast(int, x), CTRL, 0xf, 0xf, true));
}
__device__ __forceinline__ float halfsum(float a, float b) {
    auto r = __builtin_amdgcn_permlane32_swap(__float_as_uint(a), __float_as_uint(b), false, false);
    return __uint_as_float(r[0]) + __uint_as_float(r[1]);
}
__device__ __forceinline__ float grp_sum(float x) {
    auto t = __builtin_amdgcn_permlane16_swap(__float_as_uint(x), __float_as_uint(x), false, false);
    x = __uint_as_float(t[0]) + __uint_as_float(t[1]);
    return halfsum(x, x);
}
__device__ __forceinline__ float grp_max(float x) {
    auto t = __builtin_amdgcn_permlane16_swap(__float_as_uint(x), __float_as_uint(x), false, false);
    x = fmaxf(__uint_as_float(t[0]), __uint_as_float(t[1]));
    auto u = __builtin_amdgcn_permlane32_swap(__float_as_uint(x), __float_as_uint(x), false, false);
    return fmaxf(__uint_as_float(u[0]), __uint_as_float(u[1]));
}
__device__ __forceinline__ float bflo(u32 v) { return __uint_as_float(v << 16); }
__device__ __forceinline__ float bfhi(u32 v) { return __uint_as_float(v & 0xffff0000u); }
__device__ __forceinline__ float ln_scale(float var) { return __builtin_amdgcn_rcpf(rbf(__builtin_amdgcn_sqrtf(rbf(rbf(var) + 1e-5f)))); }
__device__ __forceinline__ u32 ln_pair(u32 v, float m, float rs) { const u32 d = pk2bf(bflo(v) - m, bfhi(v) - m); return pk2bf(bflo(d) * rs, bfhi(d) * rs); }
__device__ __forceinline__ void st4_lane0(char* base, u32 v) {
    u32 z, d;
    base = (char*)(((u64)(u32)__builtin_amdgcn_readfirstlane((u32)((u64)base >> 32)) << 32) | (u64)(u32)__builtin_amdgcn_readfirstlane((u32)(u64)base));
    asm volatile("s_mov_b64 exec, 1\n\tv_mov_b32 %0, 0\n\tv_mov_b32 %1, %3\n\ts_nop 1\n\tglobal_store_dword %0, %1, %2 sc1\n\ts_mov_b64 exec, -1" : "=&v"(z), "=&v"(d) : "s"(base), "s"(v) : "memory");
}
__device__ __forceinline__ void stamp2(char* hop, const int line) {
    const u32 t = nosent(rt32());
    st4_lane0(hop + (T_SLOT + 1) * SLOT + line * 128, t); st4_lane0(hop + (T_SLOT + 1) * SLOT + 8192 + line * 128, t);
}
__device__ __forceinline__ int smax(int a, int b) { int r; asm volatile("s_max_i32 %0, %1, %2" : "=s"(r) : "s"(a), "s"(b) : "scc"); return r; }
__device__ __forceinline__ int track(int d, int sample, int down) {
    int e, a, b;
    asm("s_sub_i32 %0, %3, %4\n\ts_cmp_gt_i32 %3, 0\n\ts_cselect_b32 %0, %0, 0\n\ts_min_i32 %1, %0, %6\n\ts_ashr_i32 %2, %0, " DSH "\n\ts_max_i32 %2, %2, %5\n\t"
        "s_min_i32 %2, %2, 0\n\ts_max_i32 %1, %1, %2" : "=&s"(e), "=&s"(a), "=&s"(b) : "s"(sample), "s"(d), "s"(-down), "n"(DUP) : "scc");
    return d + a;
}
__device__ __forceinline__ u32 sched_peek(const char* p) { return __hip_atomic_load((const gu32*)p, __ATOMIC_RELAXED, __HIP_MEMORY_SCOPE_AGENT); }
template <int SL = 3> __device__ __forceinline__ u32 sched_get(u32 v, const char* p, int& cap) {
    for (int spin = 0; v == ~0u; ++spin) {
        if (spin >= cap) { cap = 1; break; }
        __builtin_amdgcn_s_sleep(SL);
        v = sched_peek(p);
    }
    return __builtin_amdgcn_readfirstlane(v);
}

#define TRDECL
#define TR(i)
#define TRV(i, v)
#define TRFLUSH(role)
#define TRC(i)
constexpr size_t ACT = (size_t)S_TOK * DM * 2;
constexpr size_t O_DBG = 0, O_CTL = 2u << 20, O_FL = O_CTL + 4096, O_PX = O_FL + 640 * 128, O_ST = O_PX + PX_BYTES, O_X1 = O_ST + 128 * 1280,
                 O_OB = O_X1 + 2 * ACT, O_XN = O_OB + 2 * ACT, O_XR = O_XN + 2 * ACT, O_WO = O_XR + 2 * ACT,
                 O_WVO = O_WO + 10 * 81920, O_HOP = O_WVO + (size_t)80 * 4 * 40 * 2048, WS_BYTES = O_HOP + (4u << 20);
#define TT(role, i)
#define CNT(i)
struct LaParams { char* ws; u16* out; char* hop; const u16* Wq; const u16* Wk; const u16* Wv; const u16* Wo; const u16* wqkv; const u16* wphi; };
__device__ __forceinline__ int kperm(int g, int e) { return 16 * (g >> 1) + 8 * ((e & 3) >> 1) + 4 * (g & 1) + 2 * (e >> 2) + (e & 1); }
__device__ __forceinline__ bf8 tr8(u32 a, u32 b) {
    const s16x4 lo = __builtin_amdgcn_ds_read_tr16_b64_v4i16((LDS3 s16x4*)(uintptr_t)a), hi = __builtin_amdgcn_ds_read_tr16_b64_v4i16((LDS3 s16x4*)(uintptr_t)b);
    return __builtin_bit_cast(bf8, __builtin_shufflevector(lo, hi, 0, 1, 2, 3, 4, 5, 6, 7));
}
__device__ __forceinline__ char* wsp(const LaParams& p) { char* b = p.ws; asm volatile("" : "+s"(b)); return b; }
template <int N, int SL = 4> __device__ __forceinline__ void wait_flags(const char* f, const u32 (&off)[N], int& cap) {
    for (int spin = 0;; ++spin) {
        u32 m = 0;
#pragma unroll
        for (int i = 0; i < N; ++i) m = max(m, sched_peek(f + off[i]));
        if (!__any(m == ~0u)) break;
        if (spin >= cap) { cap = 1; break; }
        __builtin_amdgcn_s_sleep(SL);
    }
    asm volatile("" ::: "memory");
}
__device__ __forceinline__ void wait_all_tokens(const LaParams& p, u32 lane, int& cap) {
    asm volatile("" : "+v"(lane));
    const u32 off[3] = {lane * 128, lane * 128 + 8192, min(lane + 128, 159u) * 128};
    wait_flags<3>(wsp(p) + O_FL + 192 * 128, off, cap);
}
__device__ __forceinline__ void wait_first_tokens(const LaParams& p, const int set, u32 lane, int& cap) {
    asm volatile("" : "+v"(lane));
    const u32 off[1] = {(set * 20 + min(lane, 39u) + (lane >= 20 ? 20 : 0)) * 128};
    wait_flags<1>(wsp(p) + O_FL, off, cap);
}
__device__ __forceinline__ float row16_sum(float x) { x += fdpp<0x128>(x); x += fdpp<0x124>(x); x += fdpp<0x122>(x); return x + fdpp<0x121>(x); }
__device__ __forceinline__ int get_side(const LaParams& p, int& cap) { return sched_get(sched_peek(wsp(p) + O_FL + 384 * 128), wsp(p) + O_FL + 384 * 128, cap) & 1; }
__device__ __forceinline__ char* align2m(char* p) { return p + ((size_t)(-(intptr_t)p) & ((2u << 20) - 1)); }

constexpr int Q_XA = 0, Q_PART = 20480, Q_TR = 48640, Q_MK = 54784, Q_WL = 55808, Q_ST = 162304, Q_PS = 44;

#define DPP1(op, ror) op "_dpp %0, %0, %0 row_ror:" ror " row_mask:0xf bank_mask:0xf\n\t"
#define DPP2(op, ror) DPP1(op, ror) op "_dpp %1, %1, %1 row_ror:" ror " row_mask:0xf bank_mask:0xf\n\ts_nop 0\n\t"
__device__ __forceinline__ void st16m(char* base, u32 off, u32x4 v, u64 mask) {
    asm volatile("s_mov_b64 exec, %3\n\ts_nop 3\n\tglobal_store_dwordx4 %0, %1, %2 sc1\n\ts_mov_b64 exec, -1\n\ts_nop 1" :: "v"(off), "v"(v), "s"(base), "s"(mask) : "memory");
}
__device__ __forceinline__ void st16f(char* base, u32 off, u32x4 v, u64 mask) {
    asm volatile("s_mov_b64 exec, %3\n\tglobal_store_dwordx4 %0, %1, %2 sc1\n\ts_mov_b64 exec, -1\n\ts_nop 1" :: "v"(off), "v"(v), "s"(base), "s"(mask) : "memory");
}
__device__ __forceinline__ void st16f64(char* base, u32 off, u32x4 v, u64 mask) {
    asm volatile("s_mov_b64 exec, %3\n\tglobal_store_dwordx4 %0, %1, %2 offset:64 sc1\n\ts_mov_b64 exec, -1\n\ts_nop 1" :: "v"(off), "v"(v), "s"(base), "s"(mask) : "memory");
}
__device__ __forceinline__ void st32(char* base, u32 off, u32x4 a, u32x4 b) {
    asm volatile("s_nop 4\n\tglobal_store_dwordx4 %0, %1, %3\n\tglobal_store_dwordx4 %0, %2, %3 offset:16\n\ts_nop 2" :: "v"(off), "v"(a), "v"(b), "s"(base) : "memory");
}
__device__ __forceinline__ void st8m(char* base, u32 off, u32x2 v, u64 mask) {
    asm volatile("s_mov_b64 exec, %3\n\ts_nop 3\n\tglobal_store_dwordx2 %0, %1, %2 sc1\n\ts_mov_b64 exec, -1" :: "v"(off), "v"(v), "s"(base), "s"(mask) : "memory");
}
__device__ __forceinline__ u32 pk2bf_abs(float lo, float hi) {
    u32 r;
    asm("v_cvt_pk_bf16_f32 %0, |%1|, |%2|" : "=v"(r) : "v"(lo), "v"(hi));
    return r;
}
__device__ __forceinline__ u32 p_line(const int h, const int piece, const u32 gap) {
    u32 a = (h >> 4) * gap + ((h >> 1) & 7) * 1024 + (h & 1) * 128, b = 2 * gap + h * 128;
    asm volatile("" : "+s"(a), "+s"(b));
    return piece < 4 ? a + piece * 256 : b;
}
constexpr float SM_C = 0.07216878364870322f * 1.4426950408889634f, NEG_BIG = -0x1p100f;
constexpr u64 SM_MASK = 0x0155015501550155ull;
template <typename MID> __device__ __forceinline__ void softmax_mem(const float* p, char* dst, char* dst2, const u32 off, MID&& mid) {
    f32x4 a[4];
#pragma unroll
    for (int i = 0; i < 4; ++i) a[i] = *(const f32x4*)(p + i * 40 * Q_PS);
    SCHEDB(); mid(); SCHEDB();
    f32x4 x = (a[0] + a[1]) + (a[2] + a[3]);
    float ml, mx = fmaxf(fmaxf(x[0], x[1]), fmaxf(x[2], x[3])), e0, e1, e2, e3;
    asm volatile("v_mul_f32 %5, %10, %0\n\ts_nop 0\n\t"
        DPP1("v_max_f32", "8") "v_fma_f32 %1, %6, %10, -%5\n\tv_fma_f32 %2, %7, %10, -%5\n\t"
        DPP1("v_max_f32", "4") "v_fma_f32 %3, %8, %10, -%5\n\tv_fma_f32 %4, %9, %10, -%5\n\t"
        DPP1("v_max_f32", "2") "v_exp_f32 %1, %1\n\tv_exp_f32 %2, %2\n\t"
        DPP1("v_max_f32", "1") "v_exp_f32 %3, %3\n\tv_fma_f32 %5, -%0, %10, %5\n\tv_exp_f32 %4, %4\n\tv_add_f32 %0, %1, %2\n\tv_exp_f32 %5, %5\n\tv_add_f32 %0, %0, %3\n\t"
        "v_add_f32 %0, %0, %4\n\tv_mul_f32 %0, %0, %5\n\tv_mul_f32 %1, %1, %5\n\tv_mul_f32 %2, %2, %5\n\t"
        DPP1("v_add_f32", "8") "v_mul_f32 %3, %3, %5\n\tv_mul_f32 %4, %4, %5\n\t" DPP1("v_add_f32", "4") "s_nop 1\n\t"
        DPP1("v_add_f32", "2") "s_nop 1\n\t" DPP1("v_add_f32", "1") "v_rcp_f32 %0, %0\n\ts_nop 0\n\t"
        "v_mul_f32 %1, %1, %0\n\tv_mul_f32 %2, %2, %0\n\tv_mul_f32 %3, %3, %0\n\tv_mul_f32 %4, %4, %0"
        : "+v"(mx), "=&v"(e0), "=&v"(e1), "=&v"(e2), "=&v"(e3), "=&v"(ml) : "v"(x[0]), "v"(x[1]), "v"(x[2]), "v"(x[3]), "s"(SM_C) : "memory");
    u32x4 o;
    asm volatile("v_cvt_pk_bf16_f32 %4, |%4|, |%6|\n\tv_cvt_pk_bf16_f32 %5, |%5|, |%7|\n\ts_nop 0\n\t"
                 "v_mov_b32_dpp %0, %4 quad_perm:[0,0,2,2] row_mask:0xf bank_mask:0xf\n\tv_mov_b32_dpp %1, %5 quad_perm:[0,0,2,2] row_mask:0xf bank_mask:0xf\n\t"
                 "v_mov_b32_dpp %2, %4 quad_perm:[1,1,3,3] row_mask:0xf bank_mask:0xf\n\tv_mov_b32_dpp %3, %5 quad_perm:[1,1,3,3] row_mask:0xf bank_mask:0xf"
                 : "=&v"(o[0]), "=&v"(o[1]), "=&v"(o[2]), "=&v"(o[3]), "+v"(e0), "+v"(e2) : "v"(e1), "v"(e3), "s"(dst), "s"(dst2));
    asm volatile("s_mov_b64 exec, %4\n\tglobal_store_dwordx4 %0, %1, %2 sc1\n\tglobal_store_dwordx4 %0, %1, %3 sc1\n\ts_mov_b64 exec, -1\n\ts_nop 1"
                 :: "v"(off), "v"(o), "s"(dst), "s"(dst2), "s"(0x03ff03ff03ff03ffull) : "memory");
}
struct SmTok {
    float x[2][4], b1[2][4], b2[2][4], b3[2][4], mx[2], sm[2], t0, t1;
    u32 d00, d01, d10, d11;
    static constexpr int N = 88;
    __device__ __forceinline__ void load(const float* p, const int j) {
        const int n = j & 1, k = j >> 1;
        const f32x4 a = *(const f32x4*)(p + (k * 40 + n * 4) * Q_PS);
#pragma unroll
        for (int i = 0; i < 4; ++i) (k == 0 ? x : (k == 1 ? b1 : (k == 2 ? b2 : b3)))[n][i] = a[i];
    }
    template <int I> __device__ __forceinline__ void piece(char* dst, const u32 off) {
        constexpr int n = I & 1, k = (I >> 1) - 12;
        float& t = n ? t1 : t0;
#define ROR(op, ror, v) asm volatile(op "_dpp %0, %0, %0 row_ror:" ror " row_mask:0xf bank_mask:0xf" : "+v"(v))
        if constexpr (k < -8) asm volatile("v_add_f32 %0, %0, %1" : "+v"(x[n][k + 12]) : "v"(b1[n][k + 12]));
        else if constexpr (k < -4) asm volatile("v_add_f32 %0, %0, %1" : "+v"(b2[n][k + 8]) : "v"(b3[n][k + 8]));
        else if constexpr (k < 0) asm volatile("v_add_f32 %0, %0, %1" : "+v"(x[n][k + 4]) : "v"(b2[n][k + 4]));
        else if constexpr (k == 0) asm volatile("v_max_f32 %0, %1, %2" : "=v"(mx[n]) : "v"(x[n][0]), "v"(x[n][1]));
        else if constexpr (k == 1) asm volatile("v_max3_f32 %0, %0, %1, %2" : "+v"(mx[n]) : "v"(x[n][2]), "v"(x[n][3]));
        else if constexpr (k == 2) ROR("v_max_f32", "8", mx[n]);
        else if constexpr (k == 3) ROR("v_max_f32", "4", mx[n]);
        else if constexpr (k == 4) ROR("v_max_f32", "2", mx[n]);
        else if constexpr (k == 5) ROR("v_max_f32", "1", mx[n]);
        else if constexpr (k == 6) { mx[n] *= SM_C; PIN(mx[n]); }
        else if constexpr (k < 11) { x[n][k - 7] = fmaf(x[n][k - 7], SM_C, -mx[n]); PIN(x[n][k - 7]); }
        else if constexpr (k < 15) asm volatile("v_exp_f32 %0, %0" : "+v"(x[n][k - 11]));
        else if constexpr (k == 15) asm volatile("v_add_f32 %0, %1, %2" : "=v"(sm[n]) : "v"(x[n][0]), "v"(x[n][1]));
        else if constexpr (k == 16) asm volatile("v_add_f32 %0, %1, %2" : "=v"(t) : "v"(x[n][2]), "v"(x[n][3]));
        else if constexpr (k == 17) asm volatile("v_add_f32 %0, %0, %1" : "+v"(sm[n]) : "v"(t));
        else if constexpr (k == 18) ROR("v_add_f32", "8", sm[n]);
        else if constexpr (k == 19) ROR("v_add_f32", "4", sm[n]);
        else if constexpr (k == 20) ROR("v_add_f32", "2", sm[n]);
        else if constexpr (k == 21) ROR("v_add_f32", "1", sm[n]);
        else if constexpr (k == 22) asm volatile("v_rcp_f32 %0, %0" : "+v"(sm[n]));
        else if constexpr (k < 27) asm volatile("v_mul_f32 %0, %0, %1" : "+v"(x[n][k - 23]) : "v"(sm[n]));
        else if constexpr (k == 27) asm volatile("v_cvt_pk_bf16_f32 %0, |%1|, |%2|" : "=v"(n ? d10 : d00) : "v"(x[n][0]), "v"(x[n][1]));
        else if constexpr (k == 28) asm volatile("v_cvt_pk_bf16_f32 %0, |%1|, |%2|" : "=v"(n ? d11 : d01) : "v"(x[n][2]), "v"(x[n][3]));
        else if constexpr (I == 84 || I == 87) {
            u32x4 o;
            asm volatile("v_mov_b32 %0, %4\n\tv_mov_b32 %1, %5\n\tv_mov_b32_dpp %2, %4 quad_perm:[1,0,3,2] row_mask:0xf bank_mask:0xf\n\tv_mov_b32_dpp %3, %5 quad_perm:[1,0,3,2] row_mask:0xf bank_mask:0xf"
                         : "=&v"(o[0]), "=&v"(o[1]), "=&v"(o[2]), "=&v"(o[3]) : "v"(n ? d10 : d00), "v"(n ? d11 : d01), "s"(dst));
            if constexpr (n == 0) st16f(dst, off, o, SM_MASK); else st16f64(dst, off, o, SM_MASK);
        }
    }
};

__host__ __device__ constexpr int qk_slot(int T, int ks) { return ks < 5 ? ks * 6 + T : (T < 4 ? 30 + (T >> 1) * 10 + (ks - 5) * 2 + (T & 1) : 25 + T * 5 + ks); }
constexpr int NLF = 13;
__host__ __device__ constexpr int lds_slot(int n) { constexpr int t[NLF] = {2, 6, 11, 15, 20, 24, 29, 33, 37, 41, 45, 49, 55}; return t[n]; }
__host__ __device__ constexpr int slot_lds(int sl) { for (int n = 0; n < NLF; ++n) if (lds_slot(n) == sl) return n; return -1; }
__host__ __device__ constexpr int slot_frag(int sl) { if (slot_lds(sl) >= 0) return 48 + slot_lds(sl); int n = 0; for (int i = 0; i < NLF; ++i) n += lds_slot(i) < sl; return sl - n; }
__host__ __device__ constexpr int qk_frag(int T, int ks) { return slot_frag(qk_slot(T, ks)); }
__host__ __device__ constexpr int slot_T(int sl) { return sl < 30 ? sl % 6 : (sl < 50 ? ((sl - 30) / 10) * 2 + (sl & 1) : (sl - 30) / 5); }
__host__ __device__ constexpr int slot_ks(int sl) { return sl < 30 ? sl / 6 : (sl < 50 ? 5 + ((sl - 30) % 10) / 2 : 5 + sl % 5); }
__host__ __device__ constexpr int lds_rank(int T, int ks) { int n = 0; for (int t = 0; t < T; ++t) n += qk_frag(t, ks) >= 48; return n; }
__host__ __device__ constexpr int lds_nth(int ks, int n) { for (int t = 0; t < 6; ++t) if (qk_frag(t, ks) >= 48 && lds_rank(t, ks) == n) return qk_frag(t, ks) - 48; return -1; }

__device__ __forceinline__ void qk_role(const LaParams& p, const int set, const int h, char* shm) {
    const int lane = threadIdx.x & 63, w = __builtin_amdgcn_readfirstlane(threadIdx.x >> 6);
    const int fr = lane & 15, g = lane >> 4;
    const u32 lds0 = (u32)(uintptr_t)(LDS3 char*)shm;
    const int sidx = (lane & 8) ? 0xEEEE : 0x4444;
    int cap = SPIN_CAP;
    char* mine = p.hop;
    int sch = (S_SLOT + w) * SLOT;
    int phys = 0;
    char* px = (wsp(p) + O_PX);
    const char* xn = (wsp(p) + O_XN);
    char* wl = shm + Q_WL + w * (NLF * 2048) + lane * 16;
    const char* xa = shm + Q_XA + lane * 16;

    bf16v WA[32], WV[28 - NLF];
    {
        bf16v WT[NLF];
        u32 vo[6], rd[3];
#pragma unroll
        for (int i = 0; i < 6; ++i) {
            const int id = i * 64 + lane, r = w * 16 + id / 24;
            vo[i] = (32 * (r >> 5) + kperm((r >> 2) & 3, ((r >> 4) & 1) * 4 + (r & 3))) * (HD * 2) + ((id % 24) ^ (r & 6)) * 16;
        }
#pragma unroll
        for (int i = 0; i < 3; ++i) rd[i] = lds0 + (g * 4 + (fr >> 2)) * 384 + ((w * 3 + i) ^ ((g & 1) * 2 + (fr >> 3))) * 32 + (fr & 3) * 8;
        auto issue = [&](const int j) {
            const char* src = (const char*)((j & 1) ? p.Wk : p.Wq) + (size_t)(j >> 1) * 64 * HD * 2 + h * 384;
#pragma unroll
            for (int i = 0; i < 6; ++i) dma_gather(src, lds0 + (j % 6) * 24576 + w * 6144 + i * 1024, vo[i]);
        };
#pragma unroll
        for (int j = 0; j < 5; ++j) issue(j);
        static_for<0, 20>([&](auto J) {
            constexpr int j = CI(J);
            asm volatile("s_waitcnt vmcnt(%0)" :: "n"((19 - j < 4 ? 19 - j : 4) * 6) : "memory");
            RAW_BARRIER();
            if constexpr (j + 5 < 20) issue(j + 5);
            static_for<0, 3>([&](auto PR) {
                constexpr int id = qk_frag(2 * CI(PR) + (j & 1), j >> 1);
                u32 a = rd[CI(PR)] + (j % 6) * 24576;
                asm volatile("" : "+v"(a));
                const bf16v f = cat8(tr8(a, a + 16 * 384), tr8(a + 32 * 384, a + 48 * 384));
                if constexpr (id < 32) WA[id] = f; else if constexpr (id < 48) WV[id - 32] = f; else WT[id - 48] = f;
            });
        });
        RAW_BARRIER();
        dma_kb<5>(xn + (size_t)set * 40960 + w * 5120, lds0 + Q_XA + w * 5120, lane * 16);
#pragma unroll
        for (int i = 0; i < NLF; ++i) { *(bf8*)(wl + i * 2048) = lo8(WT[i]); *(bf8*)(wl + i * 2048 + 1024) = hi8(WT[i]); }
    }
    TRDECL;
    bf8 QX[2], KX[2];
    u32x2 QY[2], KY[2];
    u32 peek = ~0u;
    int d1 = D1I, d2 = D2I, k1 = D1I + MG1;
    if (w == 3) *(u64*)(shm + Q_ST + lane * 8) = lane < 63 ? 0 : (u64)D1I | ((u64)D2I << 32);
    const int lead = __builtin_amdgcn_readfirstlane((u32)(h * 4 + (w ^ 2) - 1) >> 31);
    SmTok smt;
    u32 took = 0, tvis = 0;
    u64 own;
    u32 xoff = p_line(h, fr >> 1, 8192) + g * 16, ao1 = p_line(h, min(lane >> 3, 4), 8192) + (lane & 7) * 16, ao0;
    {
        u32 a = (h >> 4) * SLOT + (h & 15) * 512, b = 2 * SLOT + h * 256;
        asm volatile("" : "+s"(a), "+s"(b));
        ao0 = PLIN ? p_line(h, min(lane >> 3, 4), SLOT) + (lane & 7) * 16 : (lane < 32 ? a + lane * 16 : b + (lane & 15) * 16);
    }
    asm volatile("" : "+v"(xoff), "+v"(ao0), "+v"(ao1));
    auto rearm = [&](const int rot, const int i, char*& a, u32x4& v) {
        if (i == 0) asm volatile("v_mov_b64 %0, -1\n\tv_mov_b64 %1, -1" : "=v"(((u64*)&v)[0]), "=v"(((u64*)&v)[1]));
        if (i == 1) { a = mine + (P_SLOT0 + (((rot + 3) & 3) * 4 + w) * PCS) * SLOT; asm volatile("" : "+s"(a)); }
        if (i == 5) { a = px + (size_t)(set * 4 + ((rot + 2) & 3)) * PX_BUF + w * 20480; asm volatile("" : "+s"(a)); }
        if (i == 3 || i == 8) __builtin_amdgcn_global_store_b128((gu32x4*)(a + (i == 3 ? ao0 : ao1)), v, "agent");
    };
    const char* mb0;
    const char* mb1;
    char* pdst;
    char* pdst2;
    int soft;
    u32 ytime;
    f32x4 tmask;
    auto next = [&](const int s, const int i) {
        const int rot = (s >> 1) & 3;
        if (i == 0) { mb0 = mine + (M_SLOT0 + rot * 8 + 2 * w) * SLOT + 2048; asm volatile("" : "+s"(mb0)); }
        if (i == 1) { mb1 = mb0 + SLOT; soft = (u32)((w - 2) & (s - (NSEG - 1))) >> 31; asm volatile("" : "+s"(mb1), "+s"(soft)); }
        if (i == 2) { pdst = mine + (P_SLOT0 + rot * 4 * PCS) * SLOT; pdst2 = pdst + 2 * PCS * SLOT; asm volatile("" : "+s"(pdst), "+s"(pdst2)); }
        if (i == 3) tmask = *(const f32x4*)(xa + Q_MK);
    };
    auto fetch_stats = [&]() {
        if (__builtin_expect(lead != 0, 0)) {
                u32 lo = lane;
                asm volatile("" : "+v"(lo));
                lo *= 128;
                u32 keep;
                asm volatile("s_mov_b32 %0, m0\n\ts_mov_b32 m0, %4\n\ts_nop 2\n\tglobal_load_lds_dword %1, %2 sc1\n\ts_add_u32 m0, m0, 256\n\ts_lshr_b64 exec, exec, 16\n\tglobal_load_lds_dword %1, %3 sc1\n\ts_mov_b64 exec, -1\n\ts_mov_b32 m0, %0"
                             : "=&s"(keep) : "v"(lo), "s"(mine + T_SLOT * SLOT), "s"(mine + (T_SLOT + 1) * SLOT), "s"(lds0 + Q_ST) : "memory", "scc");
        }
    };
    int hold = 0;
    u32 ylast = 0;
    auto xproj = [&](const int snext, const int sprev, auto SOFT, char* xdst) {
        f32x4 acc[6][2];
        TR(8);
        const char* xs;
        u32 xl;
        bf8 x0 = *(const bf8*)(xa), x1 = *(const bf8*)(xa + 1024), lw[2];
        lw[0] = *(const bf8*)(wl);
        if constexpr (CI(SOFT)) RAW_BARRIER();
        auto pack = [&](const int T, const int tt) {
            PIN(acc[T][tt]);
            const u32 a = pk2bf(acc[T][tt][0], acc[T][tt][1]), b = pk2bf(acc[T][tt][2], acc[T][tt][3]);
            if (T < 4) { u32* d = (u32*)((T & 1) ? &KX[tt] : &QX[tt]) + (T & 2); d[0] = a; d[1] = b; }
            else if (T == 4) QY[tt] = (u32x2){a, b}; else KY[tt] = (u32x2){a, b};
            SCHEDB();
        };
        char* ao;
        u32x4 sent;
        static_for<0, 20>([&](auto K) {
            constexpr int k = CI(K), l0 = lds_nth(((k + 1) >> 1) % 10, 0), l1 = lds_nth(((k + 1) >> 1) % 10, 1);
            bf8 n0, n1, nw[2];
            if constexpr (k == 9) {
                TR(9);
                if constexpr (CI(SOFT)) asm volatile("s_waitcnt vmcnt(4)" ::: "memory"); else VM0();
                RAW_BARRIER();
                TR(10);
            }
            static_for<0, 6>([&](auto TT) {
                constexpr int T = CI(TT), id = qk_frag(T, k >> 1), rk = lds_rank(T, k >> 1);
                if constexpr (k < 19) {
                    if constexpr (T == 0) n0 = *(const bf8*)(xa + (k + 1) * 2048);
                    if constexpr (T == 1) n1 = *(const bf8*)(xa + (k + 1) * 2048 + 1024);
                    if constexpr (T == 2 && l0 >= 0) nw[0] = *(const bf8*)(wl + l0 * 2048 + ((k + 1) & 1) * 1024);
                    if constexpr (T == 3 && l1 >= 0) nw[1] = *(const bf8*)(wl + l1 * 2048 + ((k + 1) & 1) * 1024);
                }
                if constexpr (k == 0 && T == 1) { xs = xn + (size_t)(snext - 2) * 40960 + 20480 + w * 5120; xl = lds0 + Q_XA + 20480 + w * 5120; asm volatile("" : "+s"(xs), "+s"(xl)); }
                if constexpr (k == 9 && T == 1) { xs = xn + (size_t)min(snext, NSEG - 1) * 40960 + w * 5120; xl -= 20480 + 4096; asm volatile("" : "+s"(xs), "+s"(xl)); }
                if constexpr ((k == 2 || k == 13) && T == 1) { xs += 4096; xl += 4096; asm volatile("" : "+s"(xs), "+s"(xl)); }
                if constexpr (k < 3 && (T == 3 || T == 5) && 2 * k + (T == 5) < 5) dma_one<((2 * k + (T == 5)) & 3) * 1024>(xs, xl, lane * 16);
                if constexpr (k >= 9 && k < 14 && T == 4) dma_one<((k - 9) & 3) * 1024>(xs, xl, lane * 16);
                if constexpr (CI(SOFT) && k == 10 && T == 1) {
                    if (w == 1) st4_lane0(mine + (T_SLOT + 1) * SLOT + (16 + h) * 128, took);
                }
                if constexpr (CI(SOFT) && k == 16 && T == 4) fetch_stats();
                if constexpr (CI(SOFT) && k == 11 && T == 1) own = __hip_atomic_load((const gu64*)(mine + sch + (sprev - 1) * 128 + 8), __ATOMIC_RELAXED, __HIP_MEMORY_SCOPE_AGENT);
                if constexpr (CI(SOFT) && k == 16 && T == 1) { tvis = __builtin_amdgcn_readfirstlane((u32)own + (u32)(own >> 32)) + RECV; asm volatile("" : "+s"(tvis)); }
                if constexpr (CI(SOFT) && k == PKK && T == PKT) peek = sched_peek(mine + sch + ((int)(rt32() - tvis) >= 0 ? sprev * 128 + 4 : (sprev - 1) * 128 + 16));
                if constexpr (CI(SOFT) && k == 15 && T >= 3) next(snext - 2, T - 3);
                if constexpr (CI(SOFT) && k == 19 && T == 3) next(snext - 2, 3);
                SCHEDB();
                static_for<0, 2>([&](auto U) {
                    constexpr int u = CI(U);
                    const bf8 xx = u ? x1 : x0;
                    if constexpr (id < 32) {
                        const bf8 wf = (k & 1) ? hi8(WA[id]) : lo8(WA[id]);
                        if constexpr (k == 0) MMA0_AV(acc[T][u], wf, xx); else MMA_AV(acc[T][u], wf, xx);
                    } else {
                        bf8 wf;
                        if constexpr (id < 48) wf = (k & 1) ? hi8(WV[id - 32]) : lo8(WV[id - 32]); else wf = lw[rk];
                        if constexpr (k == 0) MMA0_VV(acc[T][u], wf, xx); else MMA_VV(acc[T][u], wf, xx);
                    }
                    SCHEDB();
                    if constexpr (CI(SOFT) && k * 12 + T * 2 + u < SmTok::N) smt.template piece<k * 12 + T * 2 + u>(xdst, xoff);
                    if constexpr (CI(SOFT) && k == 8) rearm((snext >> 1) + 2, T * 2 + u, ao, sent);
                    if constexpr (k == 19 && T >= 2) pack(T - 2, u);
                    SCHEDB();
                });
            });
            if constexpr (k < 19) { x0 = n0; x1 = n1; if constexpr (l0 >= 0) lw[0] = nw[0]; if constexpr (l1 >= 0) lw[1] = nw[1]; }
        });
        TR(11);
        pack(4, 0); pack(4, 1);
        asm volatile("s_nop 3");
        pack(5, 0); pack(5, 1);
        if constexpr (CI(SOFT)) ytime = __builtin_amdgcn_readfirstlane(peek);
    };
    {
        const f32x4 neg = {NEG_BIG, NEG_BIG, NEG_BIG, NEG_BIG};
        *(f32x4*)((float*)(shm + Q_PART) + (w * 40 + 8 + fr) * Q_PS + g * 8 + 4) = neg;
        if (lane < 40) *(f32x4*)((float*)(shm + Q_PART) + (w * 40 + lane) * Q_PS + 40) = neg;
        *(f32x4*)(shm + Q_MK + lane * 16) = (f32x4){4 * g > fr ? NEG_BIG : 0.f, 4 * g + 1 > fr ? NEG_BIG : 0.f, 4 * g + 2 > fr ? NEG_BIG : 0.f, 4 * g + 3 > fr ? NEG_BIG : 0.f};
    }
    float* pw0 = (float*)(shm + Q_PART) + (w * 40 + (fr & 7)) * Q_PS + g * 8 + (fr >> 3) * 4;
    float* pw2 = fr < 8 && g >= 2 ? (float*)(shm + Q_PART) + (w * 40 + fr) * Q_PS + 24 + g * 4 : (float*)(shm + Q_TR + w * 1536) + lane * 4;
    const float* prd = (const float*)(shm + Q_PART) + ((w & 1) * 4 + g) * Q_PS + min(fr, 10) * 4;
    const float* prt = (const float*)(shm + Q_PART) + (8 + 8 * w + g) * Q_PS + min(fr, 10) * 4;
    u32 trw = lds0 + Q_TR + w * 1536 + lane * 8, trr = lds0 + Q_TR + w * 1536 + (fr & 3) * 128 + (4 * g + (fr >> 2)) * 8;
    u32 trh = lds0 + Q_TR + w * 1536 + 1024 + (lane & 31) * 8 + (lane >> 5) * 4;
    u32 pw3 = lds0 + Q_PART + ((w * 40 + 8 + (lane >> 5) * 16 + fr) * Q_PS + 32 + (g & 1) * 4) * 4;
    u32 pp = lds0 + Q_PART + ((w * 40 + 8 + fr) * Q_PS + g * 8) * 4;
    asm volatile("" : "+v"(trw), "+v"(trr), "+v"(trh), "+v"(pw3), "+v"(pp));
    u32 pa = (h >> 4) * SLOT + (h & 15) * 512 + (w & 1) * 256, pbm = 2 * SLOT + h * 256 + (w & 1) * 128;
    asm volatile("" : "+s"(pa), "+s"(pbm));
    const u32 poff = (PLIN ? p_line(h, fr >> 1, SLOT) + (w & 1) * 64 : (fr < 8 ? pa + (fr >> 1) * 64 : pbm)) + g * 16 + (fr & 1) * (PCS * SLOT);

    for (int layer = 0; layer < 2; ++layer) {
    if (layer > 0) {
        if (w == 0) {
            __builtin_amdgcn_s_sleep(127);
            wait_rt(sched_get(~0u, mine + (T_SLOT + 1) * SLOT + 50 * 128, cap) + T_TOK);
            wait_first_tokens(p, set, lane, cap);
        }
        RAW_BARRIER();
        sch += 5 * SLOT; xn += ACT;
        TT(set * 128 + h, 1);
        dma_kb<5>(xn + (size_t)set * 40960 + w * 5120, lds0 + Q_XA + w * 5120, lane * 16);
    }
    VM0(); RAW_BARRIER();
    TT(set * 128 + h, 2);
    xproj(set + 2, set - 1, std::integral_constant<int, 0>(), nullptr);
    if (layer == 0) { phys = set ^ get_side(p, cap); mine += phys * 8192; }
    next(set, 0);
    if (set == 0) mb0 = p.hop + M0_OFF + phys * 8192 + 2 * w * SLOT + 2048;
    next(set, 1); next(set, 2); next(set, 3);
    ytime = set ? ~0u : rt32();
    TT(set * 128 + h, 3);
    for (int s = set; s < NSEG; s += 2) {
        const int rot = (s >> 1) & 3;
        TR(0);
        int early = 0;
        if (ytime == ~0u) { early = SLK; wait_rt(tvis); ytime = sched_get(sched_peek(mine + sch + (s - 1) * 128 + 4), mine + sch + (s - 1) * 128 + 4, cap); }
        f32x4 t[3];
        t[0] = tmask;
        PIN(t[0]); PIN(QX[0]); PIN(QX[1]); PIN(KX[0]); PIN(KX[1]); PIN(QY[0]); PIN(QY[1]); PIN(KY[0]); PIN(KY[1]);
        int tight = 0;
        if (w == 0) wait_rt(ytime);
        else {
            const int sl = (int)(ytime - rt32()) + early;
            tight = SLK - smax(-smax(-sl, -SLK), 0);
        }
        __builtin_amdgcn_s_barrier();
        TR(1); TRV(7, ytime);
        f32x4 acc[6];
        u32x4 A[10];
        bf8 Ll[2], Lh[2];
        s16x4 F[4];
        u32 r0[4], r1[4], d[2];
        float v[4];
        int tries = 0;
        auto fin = [&](const int pr, const int i) {
            if (i == 0) { PIN(acc[2 * pr]); PIN(acc[2 * pr + 1]); }
            if (i < 4) { auto r = __builtin_amdgcn_permlane32_swap(__float_as_uint(acc[2 * pr][i]), __float_as_uint(acc[2 * pr + 1][i]), false, false); r0[i] = r[0]; r1[i] = r[1]; }
            const int a0 = pr == 0 ? i - 4 : i - 1;
            if (a0 >= 0 && a0 < 4) asm volatile("v_add_f32 %0, %1, %2" : "=v"(v[a0]) : "v"(r0[a0]), "v"(r1[a0]));
            if (a0 == 3) d[0] = pk2bf(v[0], v[1]);
            if (a0 == 4) {
                d[1] = pk2bf(v[2], v[3]);
                *(LDS3 u32x2*)(uintptr_t)(trw + pr * 512) = (u32x2){d[0], d[1]};
                F[pr] = __builtin_amdgcn_ds_read_tr16_b64_v4i16((LDS3 s16x4*)(uintptr_t)(trr + pr * 512));
            }
        };
        auto fin1 = [&](const int T, const int i) {
            if (i == 0) asm volatile("s_nop 1\n\tv_permlane32_swap_b32 %0, %2\n\tv_permlane32_swap_b32 %1, %3" : "+v"(acc[T][0]), "+v"(acc[T][1]), "+v"(acc[T][2]), "+v"(acc[T][3]));
            else {
                PIN(acc[T]);
                asm volatile("v_add_f32 %0, %2, %4\n\tv_add_f32 %1, %3, %5\n\tv_cvt_pk_bf16_f32 %0, %0, %1" : "=&v"(v[0]), "=&v"(v[1]) : "v"(acc[T][0]), "v"(acc[T][1]), "v"(acc[T][2]), "v"(acc[T][3]));
                *(LDS3 float*)(uintptr_t)(trh + (T - 4) * 256) = v[0];
                F[T - 2] = __builtin_amdgcn_ds_read_tr16_b64_v4i16((LDS3 s16x4*)(uintptr_t)(trr + 1024));
            }
        };
        auto slot = [&](auto SL) {
            constexpr int sl = CI(SL), T = slot_T(sl), ks = slot_ks(sl), id = slot_frag(sl);
            if constexpr (id < 32) SMM_A(acc[T], A[ks], WA[id], sidx);
            else if constexpr (id < 48) SMM_V(acc[T], A[ks], WV[id - 32], sidx);
            else { const bf16v L = cat8(Ll[id & 1], Lh[id & 1]); SMM_V(acc[T], A[ks], L, sidx); }
            SCHEDB();
            if constexpr (sl == 0) TRC(6);
            constexpr int n0 = slot_lds(sl), n1 = slot_lds(sl - 1);
            if constexpr (n0 >= 0 && n0 < NLF - 2) Ll[n0 & 1] = *(const bf8*)(wl + (n0 + 2) * 2048);
            if constexpr (n1 >= 0 && n1 < NLF - 2) Lh[n1 & 1] = *(const bf8*)(wl + (n1 + 2) * 2048 + 1024);
            if constexpr (sl >= 43 && sl < 52) fin(0, sl - 43);
            if constexpr (sl >= 52 && sl < 58) fin(1, sl - 52);
            if constexpr (sl >= 58) fin1(4, sl - 58);
            SCHEDB();
        };
        auto gather = [&](const bool first) {
            u32 vo = lane * 16;
            asm volatile("" : "+v"(vo) :: "memory");
#pragma unroll
            for (int ks = 0; ks < 10; ++ks) {
                A[ks] = ld16(ks < 5 ? mb0 : mb1, vo, (ks % 5 - 2) * 1024);
                SCHEDB();
                if (first) {
                    if (ks == 0) {
                        MMA0_VV(t[1], KX[0], QX[1]);
                        asm volatile("v_mfma_f32_16x16x32_bf16 %0, %1, %2, %3" : "=&v"(t[2]) : "v"(KX[1]), "v"(QX[1]), "v"(t[0]));
                        MMA_VV(t[0], KX[0], QX[0]);
                    }
                    if (ks == 2) { MMA16(t[1], KY[0], QY[1]); MMA16(t[2], KY[1], QY[1]); MMA16(t[0], KY[0], QY[0]); }
                    if (ks == 4) { PIN(t[1]); *(LDS3 f32x4*)(uintptr_t)(pp + 16 * Q_PS * 4) = t[1]; }
                    if (ks == 5) { PIN(t[2]); *(LDS3 f32x4*)(uintptr_t)(pp + 16 * Q_PS * 4 + 16) = t[2]; }
                    if (ks == 6) { PIN(t[0]); *(LDS3 f32x4*)(uintptr_t)pp = t[0]; }
                    if (ks == 1 || ks == 3 || ks == 7 || ks == 8) asm volatile("s_nop %0" :: "n"(PACE2));
                } else if (ks < 9) asm volatile("s_nop %0" :: "n"(PACE));
                if (ks == 3 || ks == 7) { Ll[ks >> 2] = *(const bf8*)(wl + (ks >> 2) * 2048); Lh[ks >> 2] = *(const bf8*)(wl + (ks >> 2) * 2048 + 1024); }
                if (ks == 1) { ZERO4(acc[0]); ZERO4(acc[1]); }
                if (ks >= 5 && ks < 9) ZERO4(acc[ks - 3]);
                SCHEDB();
            }
        };
        gather(true);
        static_for<0, 42>(slot);
        int passed = 0;
        auto record = [&](const u32 xt, const u32 yt) {
            u32 ln8 = lane;
            asm volatile("" : "+v"(ln8));
            ln8 *= 8192;
            const u32x4 rec = {nosent(xt), nosent(yt), (u32)(d1 + MG1), yt};
            k1 = d1 + MG1;
            char* at = p.hop + (S_SLOT + layer * 5) * SLOT + s * 128;
            asm volatile("s_waitcnt lgkmcnt(0)\n\ts_barrier\n\ts_mov_b64 exec, 0x3ff\n\ts_nop 1\n\tglobal_store_dwordx4 %0, %1, %2 sc1\n\ts_mov_b64 exec, -1\n\ts_nop 0"
                         :: "v"(ln8), "v"(rec), "s"(at) : "memory");
            passed = 1;
        };
        u64 bad, bad2;
        asm volatile("v_cmp_u_f32 %0, %2, %3\n\tv_cmp_u_f32 %1, %4, %5" : "=s"(bad), "=s"(bad2) : "v"(acc[0][0]), "v"(acc[0][1]), "v"(acc[0][2]), "v"(acc[0][3]));
        static_for<42, 43>(slot);
        asm volatile("" : "+s"(bad), "+s"(bad2));
        if (__builtin_expect((bad | bad2) != 0, 0)) {
            {
                SETTLE(); CNT(0);
                while (true) {
                    gather(false);
                    u32 m = 0;
#pragma unroll
                    for (int ks = 0; ks < 10; ++ks) m = max4(m, A[ks]);
                    ++tries;
                    if (!__any(m == ~0u)) break;
                    if (tries >= cap) { cap = 1; break; }
                    __builtin_amdgcn_s_sleep(4);
                }
                static_for<0, 43>(slot);
            }
        }
        TR(2); TRV(6, tries + 1);
        static_for<43, 60>(slot);
        TRC(7);
        bf8 F01 = __builtin_bit_cast(bf8, __builtin_shufflevector(F[0], F[1], 0, 1, 2, 3, 4, 5, 6, 7));
        f32x4 s0, s1, s2, u0, u1;
        PIN(F01);
        MMA0_VV(s1, KX[1], F01); MMA0_VV(s0, KX[0], F01);
        SCHEDB();
        fin1(5, 0); fin1(5, 1);
        SCHEDB();
        MMA16(s1, KY[1], F[2]); MMA16(s0, KY[0], F[2]);
        MMA0_VV(s2, F01, F01); MMA0_VV(u0, F01, QX[0]); MMA0_VV(u1, F01, QX[1]);
        asm volatile("" : "+v"(s0), "+v"(s1));
#pragma unroll
        for (int i = 0; i < 4; ++i) s0[i] = __int_as_float(__builtin_amdgcn_update_dpp(__float_as_int(s0[i]), __float_as_int(s1[i]), 0x128, 0xf, 0xc, false));
        SCHEDB();
        MMA16(s2, F[3], F[3]);
        SCHEDB();
        *(f32x4*)pw0 = s0; SCHEDB();
        MMA16(u0, F[3], QY[0]); MMA16(u1, F[3], QY[1]);
        asm volatile("s_nop 1");
        PIN(s2); *(f32x4*)pw2 = s2; SCHEDB();
        asm volatile("s_nop 4" : "+v"(u0), "+v"(u1));
        f32x4 u;
#pragma unroll
        for (int i = 0; i < 4; ++i) u[i] = __uint_as_float(__builtin_amdgcn_permlane32_swap(__float_as_uint(u0[i]), __float_as_uint(u1[i]), false, false)[1]);
        PIN(u);
        auto uwrite = [&]() { *(LDS3 f32x4*)(uintptr_t)pw3 = u; };
        if (__builtin_expect(soft != 0, 1)) {
            RAW_BARRIER();
            TR(3);
            softmax_mem(prd, pdst, pdst2, poff, uwrite);
            TR(4);
        } else {
            RAW_BARRIER();
            TR(3);
            uwrite();
            if (h == 0 && s < NSEG - 1) {
                hold = tries > 0 || s < 2 || ytime - ylast > 2u * (d1 + d2) + PAUSE ? HOLD : smax(hold - 1, 0);
                ylast = ytime;
                if (w == 3) {
                    const u64 dd = *(const u64*)(shm + Q_ST + 504);
                    const u32 xt = tries == 0 && s > 0 ? ytime + k1 : rt32() + (s > 0 ? ELATE : E0);
                    d1 = __builtin_amdgcn_readfirstlane((u32)dd); d2 = __builtin_amdgcn_readfirstlane((u32)(dd >> 32));
                    record(xt, xt + d2 + MG2 + (tries > 0 ? EY : 0) + hold * BOOST);
                } else {
                    u32 lo = lane * 4;
                    asm volatile("" : "+v"(lo));
                    int sta = *(const int*)(shm + Q_ST + lo), stb = *(const int*)(shm + Q_ST + 256 + lo);
                    asm volatile("s_nop 1\n\t" DPP2("v_max_i32", "8") DPP2("v_max_i32", "4") DPP2("v_max_i32", "2") DPP2("v_max_i32", "1") : "+v"(sta), "+v"(stb));
                    const int a0 = __builtin_amdgcn_readlane(sta, 0), a1 = __builtin_amdgcn_readlane(sta, 16), a2 = __builtin_amdgcn_readlane(sta, 32), a3 = __builtin_amdgcn_readlane(sta, 48);
                    const int b0 = __builtin_amdgcn_readlane(stb, 0), b1 = __builtin_amdgcn_readlane(stb, 16), b2 = __builtin_amdgcn_readlane(stb, 32);
                    SCHEDB();
                    const int down = smax(DOWN - hold * DOWN, 0);
                    d1 = track(d1, smax(b1, b2), down); d2 = track(d2, smax(smax(smax(a0, a1), smax(a2, a3)), b0), down);
                    if (hold == HOLD && s > 2) { d1 += BUMP; d2 += BUMP; }
                    *(u64*)(shm + Q_ST + 504) = (u64)(u32)d1 | ((u64)(u32)d2 << 32);
                }
            }
            if (w == 2 && s < 2 && layer > 0) wait_all_tokens(p, lane, cap);
        }
        u64 tpub = 0;
        if (__builtin_expect(passed == 0, 1)) asm volatile("s_memrealtime %0\n\ts_waitcnt lgkmcnt(0)\n\ts_barrier" : "=s"(tpub) :: "memory");
#pragma unroll
        for (int j = 0; j < 8; ++j) smt.load(prt, j);

        took = tries == 0 && s > 0 ? (u32)tpub - ytime + tight : 0;
        char* xdst = px + (size_t)(set * 4 + rot) * PX_BUF + w * 20480;
        TR(5); TRFLUSH(set * 128 + h);
        if (s + 2 >= NSEG) {
            static_for<0, SmTok::N>([&](auto I) { smt.template piece<CI(I)>(xdst, xoff); SCHEDB(); if constexpr ((CI(I) & 1) && ((CI(I) >= 26 && CI(I) < 36) || (CI(I) >= 58 && CI(I) < 68) || CI(I) > 80)) asm volatile("s_nop 0"); SCHEDB(); });
            TT(set * 128 + h, 0);
            __builtin_amdgcn_s_sleep(64);
            char* ao;
            u32x4 sent;
#pragma unroll
            for (int i = 0; i < 10; ++i) rearm(rot, i, ao, sent);
            break;
        }
        xproj(s + 4, s + 1, std::integral_constant<int, 1>(), xdst);
    }
    }
}

constexpr int C_YL = 0, C_RED = 40960, C_EX = 49152, C_XN = 53248, C_M0 = 135168;
__device__ __forceinline__ void comb_role(const LaParams& p, const int set, const int c, char* shm) {
    const int lane = threadIdx.x & 63, w = __builtin_amdgcn_readfirstlane(threadIdx.x >> 6);
    const int fr = lane & 15, g = lane >> 4;
    const u32 lds0 = (u32)(uintptr_t)(LDS3 char*)shm;
    const int sidx = (lane & 8) ? 0xEEEE : 0x4444;
    int cap = SPIN_CAP;
    char* mine = p.hop;
    char* other = p.hop;
    int phys = 0, sch = (S_SLOT + w) * SLOT;
    u32 k1 = D1I + MG1;
    char* px = (wsp(p) + O_PX);
    const char* xn = (wsp(p) + O_XN);
    char* const yl0 = shm + C_YL + lane * 16;
    const u32 pvo = PLIN ? lane * 16 : (fr >> 2) * 256 + g * 64 + (fr & 3) * 16, pvm = PLIN ? lane * 16 : g * 512 + (fr >> 2) * 128 + (fr & 3) * 16;
    int cb = (set * 80 + c) % 10, rb = (set * 80 + c) / 10;

    {
        const int t = threadIdx.x, h = (set * 80 + c) / 5, cu = c % 5;
        u32x4 st[12];
        const u16* src = p.Wo + ((size_t)h * DH + (t >> 4)) * DM + cu * 128 + (t & 15) * 8;
#pragma unroll
        for (int i = 0; i < 12; ++i) asm volatile("global_load_dwordx4 %0, %1, off" : "=v"(st[i]) : "v"(src + (size_t)i * 16 * DM) : "memory");
        u32 vo[12];
#pragma unroll
        for (int i = 0; i < 12; ++i) {
            const int id = i * 64 + lane, r = id / 24;
            vo[i] = kperm((r >> 2) & 3, (r >> 4) * 4 + (r & 3)) * (HD * 2) + ((id % 24) ^ (r & 7)) * 16;
        }
        const u32 ring = lds0 + 57344 + w * 24576;
        auto issue = [&](const int j) {
            const char* a = (const char*)p.Wv + (size_t)(((j + cu) % 5) * 4 + w) * 32 * HD * 2 + h * 384;
#pragma unroll
            for (int i = 0; i < 12; ++i) dma_gather(a, ring + (j & 1) * 12288 + i * 1024, vo[i]);
        };
        SCHEDB();
        issue(0);
        const u32 ra = ring + fr * 384 + ((g ^ (fr & 3)) + (fr & 4)) * 16;
        const int e = t >> 4, pr = ((e >> 2) & 1) * 8 + (e >> 3) * 4 + (e & 3);
        asm volatile("s_waitcnt vmcnt(12)" : "+v"(st[0]), "+v"(st[1]), "+v"(st[2]), "+v"(st[3]), "+v"(st[4]), "+v"(st[5]),
                     "+v"(st[6]), "+v"(st[7]), "+v"(st[8]), "+v"(st[9]), "+v"(st[10]), "+v"(st[11]) :: "memory");
#pragma unroll
        for (int i = 0; i < 12; ++i) *(u32x4*)(shm + (i * 16 + pr) * 288 + (t & 15) * 16) = st[i];
        issue(1);
        RAW_BARRIER();
        { const int layer = 0; TT(set * 128 + 32 + c, 12); }
        bf8 B[8][6], A[2][2][6];
        const u32 rd = lds0 + ((g >> 1) * 16 + (g & 1) * 4 + (fr >> 2)) * 288 + (fr & 3) * 8;
#pragma unroll
        for (int n = 0; n < 8; ++n)
#pragma unroll
            for (int k = 0; k < 6; ++k) B[n][k] = tr8(rd + k * 32 * 288 + n * 32, rd + (k * 32 + 8) * 288 + n * 32);
        auto lda = [&](const int j, const int k) {
#pragma unroll
            for (int u = 0; u < 2; ++u) A[j & 1][u][k] = *(LDS3 const bf8*)(uintptr_t)((ra ^ ((k & 1) * 64)) + (j & 1) * 12288 + u * 6144 + (k >> 1) * 128);
        };
        asm volatile("s_waitcnt vmcnt(12)" ::: "memory");
#pragma unroll
        for (int k = 0; k < 6; ++k) lda(0, k);
        f32x4 cc[2][2];
        char* dst;
        const char* nsrc;
        static_for<0, 41>([&](auto N) {
            constexpr int n = CI(N) % 8, j = CI(N) / 8, q = CI(N) - 1;
            if constexpr (n == 7 && j < 4) asm volatile("s_waitcnt vmcnt(%0)" :: "n"(j == 0 ? 18 : (j < 3 ? 21 : 9)) : "memory");
            if constexpr (n == 1) { dst = wsp(p) + O_WVO + ((size_t)(h * 20 + ((j + cu) % 5) * 4 + w) * 40 + cu * 8 + 4) * 1024; asm volatile("" : "+s"(dst)); }
            if constexpr (n == 0 && j < 3) { nsrc = (const char*)p.Wv + (size_t)(((j + 2 + cu) % 5) * 4 + w) * 32 * HD * 2 + h * 384; asm volatile("" : "+s"(nsrc)); }
            u32x4 o;
#pragma unroll
            for (int k = 0; k < 6; ++k) {
                if constexpr (j < 5) {
#pragma unroll
                    for (int u = 0; u < 2; ++u) { if (k == 0) MMA0_VA(cc[n & 1][u], A[j & 1][u][k], B[n][k]); else MMA_VA(cc[n & 1][u], A[j & 1][u][k], B[n][k]); }
                } else if (k == 0) asm volatile("s_nop 7");
                SCHEDB();
                if constexpr (j < 3 && n >= 1 && n < 7) {
                    if (k == 0 || k == 2) asm volatile("s_mov_b32 m0, %2\n\ts_nop 0\n\tglobal_load_lds_dwordx4 %0, %1" :: "v"(vo[2 * (n - 1) + k / 2]), "s"(nsrc), "s"(ringp(ring, (j & 1) * 12288 + (2 * (n - 1) + k / 2) * 1024)) : "memory");
                }
                if constexpr (n == 7 && j < 4) lda(j + 1, k);
                if constexpr (q >= 0) {
                    if (k == 3) { PIN(cc[q & 1][0]); o[0] = pk2bf(cc[q & 1][0][0], cc[q & 1][0][1]); o[1] = pk2bf(cc[q & 1][0][2], cc[q & 1][0][3]); }
                    if (k == 4) { PIN(cc[q & 1][1]); o[2] = pk2bf(cc[q & 1][1][0], cc[q & 1][1][1]); o[3] = pk2bf(cc[q & 1][1][2], cc[q & 1][1][3]); }
                    if (k == 5) __builtin_amdgcn_global_store_b128((gu32x4*)(dst + (u32)(lane * 16) + (q % 8 - 4) * 1024), o, "agent");
                }
                SCHEDB();
            }
        });
        { const int layer = 0; TT(set * 128 + 32 + c, 13); }
        VM0(); RAW_BARRIER();
        { const int layer = 0; TT(set * 128 + 32 + c, 14); }
        if (w == 0) st4_lane0(wsp(p) + O_FL + (400 + set * 80 + c) * 128, 1u);
        dma_kb<10>(xn + (size_t)set * 40960 + w * 10240, lds0 + C_XN + w * 10240, lane * 16);
        if (w == 0) {
            __builtin_amdgcn_s_sleep(8);
            const u32 off[1] = {(u32)(400 + (lane & 31) * 5 + (c >> 4)) * 128};
            wait_flags<1, FSL>(wsp(p) + O_FL, off, cap);
        }
        RAW_BARRIER();
        { const int layer = 0; TT(set * 128 + 32 + c, 15); }
    }
    bf16v WA[32], WV[8];
    {
        u32 vo = (w * 8 + (fr >> 3)) * 819200 + g * 256 + (fr & 7) * 16;
        asm volatile("" : "+v"(vo));
        auto ldw = [&](const int ks, const int t) {
            const char* a = wsp(p) + O_WVO + (size_t)(t * 1600 + ks * 80 + (c >> 1)) * 1024 + (c & 1) * 128;
            const char* b = a + 40960;
            asm volatile("" : "+s"(b));
            return cat8(__builtin_bit_cast(bf8, ld16(a, vo, 0)), __builtin_bit_cast(bf8, ld16(b, vo, 0)));
        };
#pragma unroll
        for (int i = 0; i < 32; ++i) WA[i] = ldw(i >> 2, i & 3);
#pragma unroll
        for (int i = 0; i < 8; ++i) WV[i] = ldw(8 + (i >> 2), i & 3);
    }
    auto yx = [&](f32x4 (&acc)[4][2], const int sy, const int tile, auto&& extra) {
        const char* xa = shm + tile + lane * 16;
        const char* xs = (sy + 2 < NSEG ? xn + (size_t)(sy + 2) * 40960 : (wsp(p) + O_WO) + cb * 81920) + w * 10240;
        u32 xl = lds0 + C_XN + (~(sy >> 1) & 1) * 40960 + w * 10240;
        bf8 x0 = *(const bf8*)(xa), x1 = *(const bf8*)(xa + 1024);
        const char* xs3[3] = {xs, xs + 4096, xs + 8192};
        u32 xl3[3] = {xl, xl + 4096, xl + 8192};
        asm volatile("" : "+s"(xs3[0]), "+s"(xs3[1]), "+s"(xs3[2]), "+s"(xl3[0]), "+s"(xl3[1]), "+s"(xl3[2]));
        static_for<0, 20>([&](auto K) {
            constexpr int k = CI(K);
            bf8 n0, n1;
            static_for<0, 4>([&](auto TT_) {
                constexpr int t = CI(TT_), id = (k >> 1) * 4 + t, dn = 2 * (k - 3) + t - 2;
                if constexpr (k < 19 && t == 0) n0 = *(const bf8*)(xa + (k + 1) * 2048);
                if constexpr (k < 19 && t == 1) n1 = *(const bf8*)(xa + (k + 1) * 2048 + 1024);
                if constexpr (t >= 2 && k >= 3 && k < 8) dma_one<(dn & 3) * 1024>(xs3[dn >> 2], xl3[dn >> 2], lane * 16);
                if constexpr (t >= 2) extra(2 * k + t - 2);
                SCHEDB();
                if constexpr (id < 32) {
                    const bf8 wf = (k & 1) ? hi8(WA[id]) : lo8(WA[id]);
                    if constexpr (k == 0) { MMA0_VA(acc[t][0], x0, wf); MMA0_VA(acc[t][1], x1, wf); } else { MMA_VA(acc[t][0], x0, wf); MMA_VA(acc[t][1], x1, wf); }
                } else {
                    const bf8 wf = (k & 1) ? hi8(WV[id - 32]) : lo8(WV[id - 32]);
                    MMA_VV(acc[t][0], x0, wf); MMA_VV(acc[t][1], x1, wf);
                }
                SCHEDB();
            });
            if constexpr (k < 19) { x0 = n0; x1 = n1; }
        });
    };

    for (int layer = 0; layer < 2; ++layer) {
    u32 ynext = ~0u;
    if (layer > 0 && set == 0) k1 = sched_get(~0u, mine + sch - 5 * SLOT + 62 * 128 + 8, cap);
    VM0(); RAW_BARRIER();
    TT(set * 128 + 32 + c, 11);
    if (layer > 0 && w == 0) st4_lane0(wsp(p) + O_FL + (192 + rb * 10 + cb) * 128, 1u);
    {
        f32x4 ya[4][2];
        yx(ya, set, layer ? C_YL : C_XN, [](int) {});
        asm volatile("s_nop 7");
        RAW_BARRIER();
        if (layer > 0) *(bf8*)(yl0 + (w * 5 + 4) * 1024) = *(const bf8*)(shm + C_M0 + w * 1024 + lane * 16);
#pragma unroll
        for (int t = 0; t < 4; ++t) { PIN(ya[t][0]); PIN(ya[t][1]); *(bf8*)(yl0 + (w * 5 + t) * 1024) = pack8(ya[t][0], ya[t][1]); }
        RAW_BARRIER();
    }
    if (layer == 0) { phys = set ^ get_side(p, cap); mine += phys * 8192; other += (phys ^ 1) * 8192; }
    TT(set * 128 + 32 + c, 10);
    for (int s = set; s < NSEG; s += 2) {
        const int rot = (s >> 1) & 3;
        char* yl = yl0 + (rot & 1) * 20480;
        TRDECL; TR(0);
        const char* mb0 = (s > 0 ? mine + (M_SLOT0 + rot * 8) * SLOT : p.hop + M0_OFF + phys * 8192) + 2 * w * SLOT + 2048;
        const char* mb1 = mb0 + SLOT;
        asm volatile("" : "+s"(mb0), "+s"(mb1));
        u32 yt = 0;
        bool late = s == 0;
        if (s > 0) {
            yt = sched_get(ynext, mine + sch + (s - 1) * 128 + 4, cap);
            wait_rt(yt + CLAG);
        }
        auto ldy = [&](const int f) { return *(const bf8*)(yl + (f < 16 ? (f >> 2) * 5 + (f & 3) : (f - 16) * 5 + 4) * 1024); };
        bf8 Y0[20 - 3 * NH_];
        TR(1);
        if (s == 0 && layer > 0) {
#pragma unroll
            for (int i = 0; i < 20 - 3 * NH_; ++i) Y0[i] = ldy(i);
        } else {
            f32x4 acc[4];
            u32x4 A[10];
            auto gather = [&]() {
                u32 vo = lane * 16;
                asm volatile("" : "+v"(vo) :: "memory");
#pragma unroll
                for (int ks = 0; ks < 10; ++ks) {
                    A[ks] = ld16(ks < 5 ? mb0 : mb1, vo, (ks % 5 - 2) * 1024);
                    SCHEDB();
                    if (ks < 4) ZERO4(acc[ks]);
                    if (ks < 9) asm volatile("s_nop %0" :: "n"(PACE));
                    SCHEDB();
                }
            };
            auto mproj = [&]() {
#pragma unroll
                for (int ks = 0; ks < 10; ++ks) {
#pragma unroll
                    for (int t = 0; t < 4; ++t) { if (ks < 8) SMM_A(acc[t], A[ks], WA[(ks * 4 + t) & 31], sidx); else SMM_V(acc[t], A[ks], WV[(ks * 4 + t) & 7], sidx); }
                }
                asm volatile("s_nop 7");
#pragma unroll
                for (int t = 0; t < 4; ++t) PIN(acc[t]);
            };
            gather();
#pragma unroll
            for (int i = 0; i < 20 - 3 * NH_; ++i) Y0[i] = ldy(i);
            SCHEDB();
            mproj();
            if (__builtin_expect(__any(__builtin_isunordered(acc[0][0], acc[0][1]) || __builtin_isunordered(acc[0][2], acc[0][3])), 0)) {
                CNT(1);
                late = true;
                for (int tries = 1;; ++tries) {
                    gather();
                    u32 m = 0;
#pragma unroll
                    for (int ks = 0; ks < 10; ++ks) m = max4(m, A[ks]);
                    if (!__any(m == ~0u)) break;
                    if (tries >= cap) { cap = 1; break; }
                    __builtin_amdgcn_s_sleep(8);
                }
                mproj();
            }
#pragma unroll
            for (int pr = 0; pr < 2; ++pr) {
                float v[4];
#pragma unroll
                for (int i = 0; i < 4; ++i) v[i] = halfsum(acc[2 * pr][i], acc[2 * pr + 1][i]);
                *(u32x2*)(shm + C_YL + (rot & 1) * 20480 + (w * 5 + 4) * 1024 + ((2 * pr + (g >> 1)) * 16 + fr) * 16 + (g & 1) * 8) = (u32x2){pk2bf(v[0], v[1]), pk2bf(v[2], v[3])};
            }
        }
        if (s == 0) { VM0(); *(bf8*)(shm + C_M0 + w * 1024 + lane * 16) = *(const bf8*)(yl0 + (w * 5 + 4) * 1024); }
        RAW_BARRIER();
        TR(2);
        u32 took = 0;
        if (s < NSEG - 1) {
            const char* pb[3];
#pragma unroll
            for (int i = 0; i < 3; ++i) { pb[i] = mine + (P_SLOT0 + (rot * 4 + c / 20) * PCS + i) * SLOT + 4096; asm volatile("" : "+s"(pb[i])); }
            auto ldp = [&](const int f, const u32 vo, const u32 vm) { return f < 16 ? ld16(pb[f >> 3], vo, ((f & 7) - 4) * 1024) : ld16(pb[2], vm, PLIN ? (f - 20) * 1024 : (f - 18) * 2048); };
            u32 xt;
            auto partial = [&](auto NF, auto F0, f32x4& acc, auto&& lastp) {
                constexpr int nf = CI(NF), f0 = CI(F0);
                bf8 (&Y)[nf] = Y0;
#pragma unroll
                for (int i = 0; i < nf; ++i) PIN(Y[i]);
                u32 vo = pvo;
                asm volatile("" : "+v"(vo));
                xt = late ? sched_get(~0u, mine + sch + s * 128, cap) : yt + k1;
                TR(3);
                if (!SYNCB || f0 == 0) wait_rt(xt);
                if (SYNCB) __builtin_amdgcn_s_barrier();
                TRV(7, xt); TRC(6);
                if (W0D > 0 && f0 == 0) asm volatile("s_nop %0" :: "n"(W0D));
                asm volatile("" ::: "memory");
                u32x4 P[nf];
#pragma unroll
                for (int i = 0; i < nf; ++i) { P[i] = ldp(f0 + i, vo, vo); SCHEDB(); }
#pragma unroll
                for (int i = 0; i < nf; ++i) {
                    if (i == nf - LATEP) lastp();
                    if (i == 0) MMA0_VV(acc, Y[i], P[i]); else MMA_VV(acc, Y[i], P[i]);
                    if (i == 0) TRC(7);
                    SCHEDB();
                }
                TRC(8);
            };
            f32x4 acc;
            if (w > 0) {
                bf8 Y[NH_];
                const char* q[NH_];
                u32 vo[NH_], ex = lds0 + C_EX + (w - 1) * 1024 + lane * 16;
#pragma unroll
                for (int i = 0; i < NH_; ++i) {
                    const int f = 20 - (4 - w) * NH_ + i;
                    Y[i] = ldy(f);
                    q[i] = pb[0] + (f >> 3) * SLOT + (f & 7) * (PLIN || f < 16 ? 1024 : 2048) - 4096;
                    vo[i] = f < 16 ? pvo : pvm;
                    asm volatile("" : "+s"(q[i]));
                    PIN(vo[i]);
                }
#pragma unroll
                for (int i = 0; i < NH_; ++i) PIN(Y[i]);
                PIN(ex);
                __builtin_amdgcn_s_barrier();
                TRC(6);
                asm volatile("" ::: "memory");
                u32x4 P[NH_];
#pragma unroll
                for (int i = 0; i < NH_; ++i) { P[i] = ld16(q[i], vo[i], 0); SCHEDB(); }
#pragma unroll
                for (int i = 0; i < NH_; ++i) { if (i == 0) MMA0_VV(acc, Y[i], P[i]); else MMA_VV(acc, Y[i], P[i]); if (i == 0) TRC(7); }
                TRC(8);
                asm volatile("s_nop 7" : "+v"(acc));
                *(LDS3 f32x4*)(uintptr_t)ex = acc;
                RAW_BARRIER();
            } else {
                const int rn = ((s + 1) >> 1) & 3;
                u32 dst = g * 2 * SLOT + (lane & 7) * 16;
                char* dstb = other + (M_SLOT0 + rn * 8 + c / 40) * SLOT + (c % 40) * 128;
                u32 exa = lds0 + C_EX + lane * 16;
                asm volatile("" : "+s"(dstb), "+v"(dst), "+v"(exa));
                f32x4 h1, h2, h3;
                partial(std::integral_constant<int, 20 - 3 * NH_>(), std::integral_constant<int, 0>(), acc, [&] {
                    __builtin_amdgcn_s_barrier();
                    asm volatile("" ::: "memory");
                    h1 = *(LDS3 const f32x4*)(uintptr_t)exa; h2 = *(LDS3 const f32x4*)(uintptr_t)(exa + 1024); h3 = *(LDS3 const f32x4*)(uintptr_t)(exa + 2048);
                    SCHEDB();
                });
                h1 = (h1 + h2) + h3;
                asm volatile("s_nop 2" : "+v"(acc), "+v"(h1));
                acc += h1;
                float v0, v1;
                auto merge = [&]() {
                    auto a = __builtin_amdgcn_permlane32_swap(__float_as_uint(acc[0]), __float_as_uint(acc[2]), false, false);
                    auto b = __builtin_amdgcn_permlane32_swap(__float_as_uint(acc[1]), __float_as_uint(acc[3]), false, false);
                    v0 = __uint_as_float(a[0]) + fdpp<0x128>(__uint_as_float(a[1]));
                    v1 = __uint_as_float(b[0]) + fdpp<0x128>(__uint_as_float(b[1]));
                };
                merge();
                int tries = 0;
                if (__builtin_expect(__any(__builtin_isunordered(v0, v1)), 0)) {
                    CNT(2);
                    wait_rt(sched_get(~0u, mine + sch + s * 128, cap));
                    while (true) {
                        u32 vo = pvo, vm = pvm, m = 0;
                        asm volatile("" : "+v"(vo), "+v"(vm) :: "memory");
                        u32x4 P[20];
#pragma unroll
                        for (int i = 0; i < 20; ++i) P[i] = ldp(i, vo, vm);
                        f32x4 hh[4];
#pragma unroll
                        for (int i = 0; i < 20; ++i) {
                            const bf8 y = ldy(i);
                            const int k = i < 20 - 3 * NH_ ? 0 : (i - 20 + 4 * NH_) / NH_;
                            if (i == 0 || i == 20 - 3 * NH_ || i == 20 - 2 * NH_ || i == 20 - NH_) MMA0_VV(hh[k], y, P[i]); else MMA_VV(hh[k], y, P[i]);
                            m = max4(m, P[i]);
                            SCHEDB();
                        }
                        asm volatile("s_nop 7" : "+v"(hh[0]), "+v"(hh[1]), "+v"(hh[2]), "+v"(hh[3]));
                        acc = hh[0] + ((hh[1] + hh[2]) + hh[3]);
                        ++tries;
                        if (!__any(m == ~0u)) break;
                        if (tries >= cap) { cap = 1; break; }
                        __builtin_amdgcn_s_sleep(4);
                    }
                    merge();
                }
                TRV(6, tries + 1);
                asm volatile("" :: "s"(dstb));
                const u32 dw = nosent(pk2bf(v0, v1));
                auto e = __builtin_amdgcn_permlane32_swap(dw, dw, false, false);
                auto e0 = __builtin_amdgcn_permlane16_swap(e[0], e[0], false, false);
                auto e1 = __builtin_amdgcn_permlane16_swap(e[1], e[1], false, false);
                st16f(dstb, dst, (u32x4){e0[0], e1[0], e0[1], e1[1]}, 0x00ff00ff00ff00ffull);
                TR(4);
                took = tries == 0 && s > 0 ? rt32() - xt : 0;
            }
            if (s < 2 && layer > 0 && w == 3) wait_all_tokens(p, lane, cap);
            if (s == NSEG - 2 || s < 2) RAW_BARRIER();
        } else wait_rt((late ? rt32() : yt + k1) + T_PX);
        auto tokens = [&](auto MORE) {
            f32x4 oa[4], ya[4][2];
            u32x4 P[20];
            u32 vo = lane * 16 + w * 4096, vm = lane * 16 + 16384 + w * 1024;
            asm volatile("" : "+v"(vo), "+v"(vm));
            const char* pb[4];
#pragma unroll
            for (int i = 0; i < 4; ++i) { pb[i] = px + (size_t)(set * 4 + rot) * PX_BUF + i * 20480; asm volatile("" : "+s"(pb[i])); }
            auto ldp = [&](const int i) { P[i] = i % 5 == 4 ? ld16(pb[i / 5], vm, 0) : ld16(pb[i / 5], vo, (i % 5) * 1024); };
            int ln = lane;
            asm volatile("" : "+v"(ln));
            u32 rg, peek;
            u64 peek2;
            bf8 y[5];
            auto side = [&](const int i) {
                if (i == 4) rg = sched_peek((wsp(p) + O_XR) + layer * ACT + (size_t)s * 40960 + ((c >> 2) * 2 + (w >> 1)) * 1024 + (c & 2) * 256 + (w & 1) * 128 + (c & 1) * 4 + (u32)((ln & 2) * 128 + (ln >> 3) * 16 + (ln & 1) * 8));
                if (i >= 16 && i < 36) ldp(i - 16);
                if (i == 2 && CI(MORE) && w == 0) st4_lane0(mine + (T_SLOT + (c >> 6)) * SLOT + (c & 63) * 128, took);
                if (i == 15 && CI(MORE)) peek2 = __hip_atomic_load((const gu64*)(mine + sch + s * 128 + 8), __ATOMIC_RELAXED, __HIP_MEMORY_SCOPE_AGENT);
                if (i >= 35) y[i - 35] = *(const bf8*)(yl + (w * 5 + i - 35) * 1024);
            };
            TRC(9);
            if constexpr (CI(MORE)) yx(ya, s + 2, C_XN + (~rot & 1) * 40960, side);
            else {
#pragma unroll
                for (int i = 0; i < 40; ++i) side(i);
            }
            TRC(10);
            bf8 yn[4];
            auto outx = [&](const bool first) {
                if (!first) {
#pragma unroll
                    for (int i = 0; i < 20; ++i) PIN(P[i]);
                    NOPV();
                }
#pragma unroll
                for (int q = 0; q < 4; ++q)
#pragma unroll
                    for (int j = 0; j < 5; ++j) {
                        if (j == 0) MMA0_VV(oa[q], y[j], P[q * 5 + j]); else MMA_VV(oa[q], y[j], P[q * 5 + j]);
                        if (CI(MORE) && first && q * 5 + j >= 3 && q * 5 + j < 19) {
                            const int e = q * 5 + j - 3;
                            SCHEDB();
                            if ((e & 1) == 0) PIN(ya[e >> 2][(e >> 1) & 1]);
                            ((u32*)&yn[e >> 2])[e & 3] = pk2bf(ya[e >> 2][(e >> 1) & 1][(e & 1) * 2], ya[e >> 2][(e >> 1) & 1][(e & 1) * 2 + 1]);
                            if ((e & 3) == 3) *(bf8*)(yl0 + (~rot & 1) * 20480 + (w * 5 + (e >> 2)) * 1024) = yn[e >> 2];
                            SCHEDB();
                        }
                    }
                asm volatile("s_nop 7");
#pragma unroll
                for (int q = 0; q < 4; ++q) PIN(oa[q]);
            };
            outx(true);
            if (__builtin_expect(__any(__builtin_isunordered(oa[0][0], oa[1][0]) || __builtin_isunordered(oa[2][0], oa[3][0])), 0)) {
                CNT(3);
                for (int tries = 1;; ++tries) {
                    asm volatile("" : "+v"(vo), "+v"(vm) :: "memory");
                    u32 m = 0;
#pragma unroll
                    for (int i = 0; i < 20; ++i) ldp(i);
#pragma unroll
                    for (int i = 0; i < 20; ++i) m = max4(m, P[i]);
                    if (!__any(m == ~0u)) break;
                    if (tries >= cap) { cap = 1; break; }
                    __builtin_amdgcn_s_sleep(8);
                }
                outx(false);
            }
            if constexpr (!CI(MORE)) { if (c == 0 && w == 0) stamp2(p.hop, 48 + layer * 4 + set); }
            const bool own = (fr < 8) == (g < 2);
#pragma unroll
            for (int q = 0; q < 4; q += 2) {
                f32x4 o;
#pragma unroll
                for (int i = 0; i < 4; ++i) { const float t = fdpp<0x128>(oa[q + 1][i]); o[i] = own ? oa[q][i] : t; }
                *(f32x4*)(shm + C_RED + ((w * 2 + (g >> 1)) * 32 + (q + !own) * 8 + (fr & 7)) * 32 + (g & 1) * 16) = o;
            }
            RAW_BARRIER();
            TR(5);
            if constexpr (CI(MORE)) {
                k1 = sched_get((u32)peek2, mine + sch + s * 128 + 8, cap);
                wait_rt(__builtin_amdgcn_readfirstlane((u32)(peek2 >> 32)) + k1 + RECV);
                peek = sched_peek(mine + sch + (s + 1) * 128 + 4);
                SCHEDB();
            }
            typedef __attribute__((ext_vector_type(2))) float f32x2;
            const char* rp = shm + C_RED + (ln & 4) * 1024 + w * 256 + (ln >> 3) * 32 + (ln & 3) * 8;
            f32x2 a = (*(const f32x2*)rp + *(const f32x2*)(rp + 1024)) + (*(const f32x2*)(rp + 2048) + *(const f32x2*)(rp + 3072));
            a[0] += fdpp<0x104>(a[0]); a[1] += fdpp<0x104>(a[1]);
            const u32 y2 = pk2bf(a[0], a[1]), d = nosent(pk2bf(bflo(rg) + bflo(y2), bfhi(rg) + bfhi(y2)));
            const u32x4 o4 = {d, (u32)__builtin_amdgcn_mov_dpp(d, 0x55, 0xf, 0xf, true), (u32)__builtin_amdgcn_mov_dpp(d, 0xaa, 0xf, 0xf, true), (u32)__builtin_amdgcn_mov_dpp(d, 0xff, 0xf, 0xf, true)};
            if constexpr (CI(MORE)) { ynext = __builtin_amdgcn_readfirstlane(peek); asm volatile("" : "+s"(ynext)); }
            st16m((wsp(p) + O_X1) + layer * ACT + (size_t)s * 40960 + c * 512 + w * 128, ln * 2, o4, 0x0101010101010101ull);
            if (w == 1) {
                u32x4 v;
                asm volatile("v_mov_b64 %0, -1\n\tv_mov_b64 %1, -1" : "=v"(((u64*)&v)[0]), "=v"(((u64*)&v)[1]));
                st16m(other + (M_SLOT0 + (((s + 7) >> 1) & 3) * 8 + c / 40) * SLOT + (c % 40) * 128, (ln >> 4) * 2 * SLOT + (ln & 7) * 16, v, 0x00ff00ff00ff00ffull);
            }
            TRC(11); TRFLUSH(set * 128 + 32 + c);
        };
        if (s + 2 >= NSEG) { tokens(std::integral_constant<int, 0>()); break; }
        tokens(std::integral_constant<int, 1>());
    }
    TT(set * 128 + 32 + c, 0);
    asm volatile("" : "+s"(cb), "+s"(rb));
    dma_kb<10>((wsp(p) + O_WO) + cb * 81920 + 40960 + w * 10240, lds0 + C_XN + 40960 + w * 10240, lane * 16);
    if (w == 0) { const u32 t = sched_get(~0u, mine + (T_SLOT + 1) * SLOT + (50 + layer * 4) * 128, cap); *(u32*)(shm + C_EX) = t; }
    VM0(); RAW_BARRIER();
    wait_rt(__builtin_amdgcn_readfirstlane(*(const u32*)(shm + C_EX)) + T_OB + OB_STEP * w + (layer == 0 ? OB_COLD + 8 * w : 0));
    {
        int ln = lane;
        asm volatile("" : "+v"(ln));
        u32x4 O[20], R[2];
        u32 dw[8];
        auto ready = [&](const int T) {
            const char* ob = (wsp(p) + O_OB) + layer * ACT + (size_t)T * 20480 + 16384;
            const u32 off[1] = {(u32)(ln & 31) * 128};
            wait_flags<1>(ob, off, cap);
        };
        auto load = [&](const int T) {
            const char* ob0 = (wsp(p) + O_OB) + layer * ACT + (size_t)T * 20480 + 4096;
            const char* ob1 = ob0 + 8192;
            const char* ob2 = ob0 + 12288;
            const char* xb = (wsp(p) + O_X1) + layer * ACT + (size_t)(T >> 1) * 40960 + cb * 4096 + (T & 1) * 256;
            u32 vo = ln * 16, vr = (ln >> 4) * 256 + (ln & 15) * 8, vx = (ln >> 4) * 1024 + (ln & 15) * 16;
            asm volatile("" : "+v"(vo), "+v"(vr), "+v"(vx), "+s"(ob0), "+s"(ob1), "+s"(ob2), "+s"(xb) :: "memory");
#pragma unroll
            for (int k = 0; k < 16; ++k) { O[k] = ld16(k < 8 ? ob0 : ob1, vo, ((k & 7) - 4) * 1024); SCHEDB(); }
#pragma unroll
            for (int k = 0; k < 4; ++k) {
                const u64 lo = __hip_atomic_load((const gu64*)(ob2 + vr + k * 1024), __ATOMIC_RELAXED, __HIP_MEMORY_SCOPE_AGENT);
                const u64 hi = __hip_atomic_load((const gu64*)(ob2 + vr + k * 1024 + 128), __ATOMIC_RELAXED, __HIP_MEMORY_SCOPE_AGENT);
                O[16 + k] = (u32x4){(u32)lo, (u32)(lo >> 32), (u32)hi, (u32)(hi >> 32)};
            }
            R[0] = ld16(xb, vx, 0); R[1] = ld16(xb, vx, 512);
            SCHEDB();
        };
        auto gemm = [&]() {
            u32 wa0 = lds0 + C_XN + ln * 16, wa1 = wa0 + 40960;
            asm volatile("" : "+v"(wa0), "+v"(wa1));
            f32x4 acc[4];
            bf8 wf[8];
#pragma unroll
            for (int t = 0; t < 8; ++t) wf[t] = *(LDS3 const bf8*)(uintptr_t)(wa0 + t * 1024);
#pragma unroll
            for (int k = 0; k < 20; ++k) {
                if (k >= 16) { PIN(O[k]); NOPV(); }
#pragma unroll
                for (int t = 0; t < 4; ++t) {
                    if (k == 0) MMA0_VV(acc[t], wf[t], O[k]); else MMA_VV(acc[t], wf[(k & 1) * 4 + t], O[k]);
                    SCHEDB();
                    if (k < 18) wf[(k & 1) * 4 + t] = *(LDS3 const bf8*)(uintptr_t)((k < 8 ? wa0 : wa1) + (((k + 2) % 10) * 4 + t) * 1024);
                    SCHEDB();
                }
            }
            asm volatile("s_nop 7" : "+v"(acc[0]), "+v"(acc[1]), "+v"(acc[2]), "+v"(acc[3]));
            float chk = 0.f;
#pragma unroll
            for (int i = 0; i < 8; ++i) {
                const u32 r = R[i >> 2][i & 3], y = pk2bf(acc[i >> 1][(i & 1) * 2], acc[i >> 1][(i & 1) * 2 + 1]);
                const float a = bflo(r) + bflo(y), b = bfhi(r) + bfhi(y);
                dw[i] = pk2bf(a, b);
                chk += a;
            }
            return __any(chk != chk);
        };
        const int T0 = rb + 16 * w;
        load(T0);
        static_for<0, 2>([&](auto PASS) {
            constexpr int pass = CI(PASS);
            const int T = T0 + 64 * pass;
            TT(set * 128 + 32 + c, 1 + pass * 4);
            if (__builtin_expect(gemm(), 0)) {
                for (int tries = 0;; ++tries) {
                    ready(T); load(T);
                    u32 m = max4(max4(0, R[0]), R[1]);
#pragma unroll
                    for (int k = 0; k < 20; ++k) m = max4(m, O[k]);
                    if (!__any(m == ~0u)) break;
                    if (tries >= cap) { cap = 1; break; }
                    __builtin_amdgcn_s_sleep(6);
                }
                gemm();
            }
            TT(set * 128 + 32 + c, 2 + pass * 4);
            if (layer == 1) {
                st32((char*)p.out + (size_t)T * 16 * DM * 2 + cb * 128, (ln & 15) * (DM * 2) + (ln >> 4) * 32, (u32x4){dw[0], dw[1], dw[2], dw[3]}, (u32x4){dw[4], dw[5], dw[6], dw[7]});
                if constexpr (pass == 0) load(T + 64);
            } else {
            float s1 = 0.f, s2 = 0.f;
#pragma unroll
            for (int i = 0; i < 8; ++i) { const float a = bflo(dw[i]), b = bfhi(dw[i]); s1 += a + b; s2 = fmaf(a, a, fmaf(b, b, s2)); }
            s1 = grp_sum(s1); s2 = grp_sum(s2);
            char* stl = (wsp(p) + O_ST) + (size_t)T * 1280;
            st8m(stl + cb * 128, ln * 8, (u32x2){nosent(__float_as_uint(s1)), nosent(__float_as_uint(s2))}, 0xffffull);
            const size_t fo = ACT + (size_t)(T >> 1) * 40960 + (cb * 4 + (T & 1)) * 1024;
            const u32 lo = (ln >> 5) * 2048 + ((ln >> 4) & 1) * 512 + (ln & 15) * 16;
            if constexpr (pass == 0) __builtin_amdgcn_s_sleep(ST_LAG);
            else {
                wait_first_tokens(p, set, lane, cap);
                dma_kb<10>(xn + ACT + (size_t)set * 40960 + w * 10240, lds0 + C_YL + w * 10240, lane * 16);
            }
            for (int spin = 0;; ++spin) {
                u32 m = 0;
                s1 = 0.f; s2 = 0.f;
#pragma unroll
                for (int i = 0; i < 3; ++i) {
                    const int k = (ln >> 4) + 4 * i;
                    const u64 v = __hip_atomic_load((const gu64*)(stl + min(k, 9) * 128 + (ln & 15) * 8), __ATOMIC_RELAXED, __HIP_MEMORY_SCOPE_AGENT);
                    m = max(m, max((u32)v, (u32)(v >> 32)));
                    if (k < 10) { s1 += __uint_as_float((u32)v); s2 += __uint_as_float((u32)(v >> 32)); }
                }
                if (!__any(m == ~0u)) break;
                if (spin >= cap) { cap = 1; break; }
                __builtin_amdgcn_s_sleep(2);
            }
            TT(set * 128 + 32 + c, 3 + pass * 4);
            if constexpr (pass == 0) { if (w > 0) load(T + 64); }
            s1 = grp_sum(s1); s2 = grp_sum(s2);
            const float mu = s1 * (1.0f / DM), mr = rbf(mu), rs = ln_scale(fmaxf(fmaf(-mu, mu, s2 * (1.0f / DM)), 0.f));
            u32 dn[8];
#pragma unroll
            for (int i = 0; i < 8; ++i) dn[i] = ln_pair(dw[i], mr, rs);
            st16s((wsp(p) + O_XN) + fo, lo, (u32x4){dn[0], dn[4], dn[1], dn[5]}); st16s((wsp(p) + O_XN) + fo + 256, lo, (u32x4){dn[2], dn[6], dn[3], dn[7]});
            st16s((wsp(p) + O_XR) + fo, lo, (u32x4){dw[0], dw[4], dw[1], dw[5]}); st16s((wsp(p) + O_XR) + fo + 256, lo, (u32x4){dw[2], dw[6], dw[3], dw[7]});
            TT(set * 128 + 32 + c, 4 + pass * 4);
            if constexpr (pass == 0) { if (w == 0) { load(T + 64); asm volatile("s_waitcnt vmcnt(18)" ::: "memory"); st4_lane0(wsp(p) + O_FL + (rb * 10 + cb) * 128, 1u); } }
            }
        });
    }
    if (layer == 1) break;
    sch += 5 * SLOT; xn += ACT;
    TT(set * 128 + 32 + c, 9);
    }
}

constexpr int B_PQ = 0, B_IM = 131072, B_CS = 155648, B_IS = 176;
__device__ __forceinline__ void bw_role(const LaParams& p, const int hb, char* shm) {
    const int lane = threadIdx.x & 63, w = __builtin_amdgcn_readfirstlane(threadIdx.x >> 6);
    const int fr = lane & 15, g = lane >> 4;
    int cap = SPIN_CAP;
    if (hb > 0) {
        const char* src = (const char*)((hb & 1) ? p.Wk : p.Wq) + (size_t)(DM - 16 * BPF + (hb >> 1) * BPF) * HD * 2 + w * (BPF * 3072);
        for (int i = 0; i < BPF * 3; ++i) dma_gather(src + i * 1024, (u32)(uintptr_t)(LDS3 char*)shm + w * 1024, lane * 16);
        VM0();
    }
    if (hb == 0 && w == 0) {
        u32 best[2] = {~0u, ~0u};
        const u32 tc = rt32();
        for (int i = 0; i < 256; ++i) {
#pragma unroll
            for (int sd = 0; sd < 2; ++sd) {
                u64 t0, t1;
                u32 z = 0, one = 1;
                asm volatile("s_memtime %0\n\ts_mov_b64 exec, 1\n\ts_waitcnt lgkmcnt(0)\n\tglobal_atomic_add %2, %3, %2, %4 sc0 sc1\n\ts_waitcnt vmcnt(0)\n\t"
                             "s_memtime %1\n\ts_mov_b64 exec, -1\n\ts_waitcnt lgkmcnt(0)"
                             : "=&s"(t0), "=&s"(t1), "+v"(one) : "v"(z), "s"(p.hop + M0_OFF + sd * 8192 + 6144 + (i & 3) * 256) : "memory");
                best[sd] = min(best[sd], (u32)(t1 - t0));
            }
            const u32 el = rt32() - tc, lo = min(best[0], best[1]), hi = max(best[0], best[1]);
            if (el > 1900u || (el > 600u && lo < 700u && hi - lo > 130u && hi - lo < 420u)) break;
        }
        u32 xcc; asm volatile("s_getreg_b32 %0, hwreg(HW_REG_XCC_ID)" : "=s"(xcc));
        st4_lane0(wsp(p) + O_FL + 384 * 128, ((xcc >> 2) & 1) ^ (best[1] < best[0] ? 1u : 0u) ^ WRONG);
    }
    bf8 WA[64], WV[16];
    {
        const int t = threadIdx.x;
        float* xs = (float*)(shm + 81920);
        float* ph = xs + 128 * 61;
        for (int i = t; i < 400; i += 256) ph[i] = bf2f(p.wphi[hb * 400 + i]);
        const int j = 4 * w + (fr & 3), G = fr >> 2;
        for (int d0 = 0; d0 < DM; d0 += 128) {
            u32 v15[15];
#pragma unroll
            for (int k = 0; k < 15; ++k) { const int i = t + k * 256; v15[k] = *(const u32*)(p.wqkv + (size_t)(d0 + i / 30) * 1920 + ((i % 30) / 10) * 640 + hb * 20 + (i % 10) * 2); }
            __syncthreads();
#pragma unroll
            for (int k = 0; k < 15; ++k) { const int i = t + k * 256; float* o = xs + (i / 30) * 61 + (i % 30) * 2; o[0] = bflo(v15[k]); o[1] = bfhi(v15[k]); }
            __syncthreads();
            for (int kk = 0; kk < 4; ++kk) {
                float v[8];
                const float* xr = xs + (kk * 32 + g * 8) * 61;
                if (j < 10) {
                    const int m = j / 5, f = 5 * G + j % 5;
#pragma unroll
                    for (int e = 0; e < 8; ++e) v[e] = 0.f;
#pragma unroll 4
                    for (int i = 0; i < 20; ++i) {
                        const float pw = ph[i * 20 + f];
#pragma unroll
                        for (int e = 0; e < 8; ++e) v[e] = fmaf(xr[e * 61 + m * 20 + i], pw, v[e]);
                    }
                } else {
#pragma unroll
                    for (int e = 0; e < 8; ++e) v[e] = j < 15 ? xr[e * 61 + 40 + 5 * G + min(j, 14) - 10] : 1.0f;
                }
                *(u32x4*)(shm + ((d0 / 32 + kk) * 4 + w) * 1024 + lane * 16) = (u32x4){pk2bf(v[0], v[1]), pk2bf(v[2], v[3]), pk2bf(v[4], v[5]), pk2bf(v[6], v[7])};
            }
        }
        __syncthreads();
#pragma unroll
        for (int i = 0; i < 64; ++i) WA[i] = *(const bf8*)(shm + i * 1024 + lane * 16);
#pragma unroll
        for (int i = 0; i < 16; ++i) WV[i] = *(const bf8*)(shm + (64 + i) * 1024 + lane * 16);
        __syncthreads();
    }
    auto proj = [&](f32x4 (&acc)[4], f32x4& sq, auto&& fetch) {
        u32x4 X[20];
#pragma unroll
        for (int k = 0; k < 12; ++k) { X[k] = fetch(k); SCHEDB(); }
#pragma unroll
        for (int k = 0; k < 20; ++k) {
#pragma unroll
            for (int t = 0; t < 4; ++t) {
                if (k == 0) MMA0_AV(acc[t], WA[t], X[k]); else if (k < 16) MMA_AV(acc[t], WA[(k * 4 + t) & 63], X[k]); else MMA_VV(acc[t], WV[(k * 4 + t) & 15], X[k]);
            }
            if (k == 0) MMA0_VV(sq, X[k], X[k]); else MMA_VV(sq, X[k], X[k]);
            SCHEDB();
            if (k < 8) X[k + 12] = fetch(k + 12);
            SCHEDB();
        }
        asm volatile("s_nop 7" : "+v"(acc[0]), "+v"(acc[1]), "+v"(acc[2]), "+v"(acc[3]), "+v"(sq));
    };
    {
        f32x4 t, cs[4];
        u32x4 o1 = {0x3f803f80u, 0x3f803f80u, 0x3f803f80u, 0x3f803f80u};
        asm volatile("s_nop 1" : "+v"(o1));
        proj(cs, t, [&](int) { return o1; });
#pragma unroll
        for (int i = 0; i < 4; ++i) *(f32x4*)(shm + B_CS + i * 1024 + lane * 16) = cs[i];
    }
    char* im = shm + B_IM + w * 6144;
    const u32 imr = (u32)(uintptr_t)(LDS3 char*)im + (4 * g + (fr >> 2)) * B_IS + (fr & 3) * 8;
    typedef __attribute__((ext_vector_type(32))) u32 u32x32;
    for (int layer = 0; layer < 2; ++layer) {
        const char* sch = p.hop + (hb & 1) * 8192 + (S_SLOT + layer * 5 + 4) * SLOT;
        f32x4 kv[3][2];
#pragma unroll
        for (int i = 0; i < 6; ++i) ZERO4(kv[i >> 1][i & 1]);
        u32x32 q5;
        u32 tn = 0;
        float vq[5];
        auto phi = [&](const float* x, u32 (&ph)[5]) {
            const float mx = grp_max(fmaxf(fmaxf(fmaxf(x[0], x[1]), fmaxf(x[2], x[3])), x[4]));
            const float mn = -grp_max(-fminf(fminf(fminf(x[0], x[1]), fminf(x[2], x[3])), x[4]));
            float e[10], a = 0.f, b = 0.f;
#pragma unroll
            for (int j = 0; j < 5; ++j) {
                e[j] = __builtin_amdgcn_exp2f((x[j] - mx) * 1.4426950408889634f); a += e[j];
                e[5 + j] = __builtin_amdgcn_exp2f((mn - x[j]) * 1.4426950408889634f); b += e[5 + j];
            }
            a = __builtin_amdgcn_rcpf(grp_sum(a)); b = __builtin_amdgcn_rcpf(grp_sum(b));
#pragma unroll
            for (int j = 0; j < 5; ++j) { e[j] *= a; e[5 + j] *= b; }
#pragma unroll
            for (int j = 0; j < 5; ++j) ph[j] = pk2bf(e[2 * j], e[2 * j + 1]);
        };
        auto phiq = [&](const int it) {
            u32 ph[5];
            phi(vq, ph);
            *(u32x4*)(shm + B_PQ + (w * 32 + it) * 1024 + lane * 16) = (u32x4){ph[0], ph[1], ph[2], ph[3]};
            q5[it] = ph[4];
        };
        for (int it = 0; it < 32; ++it) {
            const int s = 2 * it + (w >> 1);
            if (s + 2 < NSEG) { wait_rt(tn); tn = sched_get<BSL>(sched_peek(sch + (s + 1) * 128 + 4), sch + (s + 1) * 128 + 4, cap); wait_rt(tn + B_LAG); tn += BPEEK; }
            else wait_rt(sched_get(~0u, p.hop + (hb & 1) * 8192 + (T_SLOT + 1) * SLOT + (48 + layer * 4 + (s & 1)) * 128, cap) + T_X1);
            const char* xb = (wsp(p) + O_X1) + layer * ACT + (size_t)s * 40960 + (w & 1) * 256;
            if (it >= 30) TT((hb >> 4) * 128 + 112 + (hb & 15), (it - 30) * 3);
            f32x4 acc[4], sq;
            const char* xk[5];
#pragma unroll
            for (int i = 0; i < 5; ++i) { xk[i] = xb + i * 8192 + 4096; asm volatile("" : "+s"(xk[i])); }
            for (int pass = 0;; ++pass) {
                u32 vo = g * 512 + fr * 16;
                asm volatile("" : "+v"(vo) :: "memory");
                auto fetch = [&](const int k) { return ld16(xk[k >> 2], vo, ((k & 3) - 2) * 2048); };
                proj(acc, sq, fetch);
                if (pass > 0 || !__any(acc[3][3] != acc[3][3])) break;
                CNT(4);
                for (int tries = 0;; ++tries) {
                    asm volatile("" : "+v"(vo) :: "memory");
                    u32 m = 0;
#pragma unroll
                    for (int k = 0; k < 20; k += 10) {
#pragma unroll
                        for (int i = 0; i < 10; ++i) m = max4(m, fetch(k + i));
                        SCHEDB();
                    }
                    if (!__any(m == ~0u)) break;
                    if (tries >= cap) { cap = 1; break; }
                    __builtin_amdgcn_s_sleep(6);
                }
            }
            if (it >= 30) TT((hb >> 4) * 128 + 112 + (hb & 15), (it - 30) * 3 + 1);
            if (s == NSEG - 1 && hb == 5 && w == 3) stamp2(p.hop, 50 + layer * 4);
            const float dsel = (fr & 2) ? ((fr & 1) ? sq[3] : sq[2]) : ((fr & 1) ? sq[1] : sq[0]);
            const float s2 = grp_sum(g == (fr >> 2) ? dsel : 0.f);
            const float mu = acc[3][3] * (1.0f / DM), rs = __builtin_amdgcn_rsqf(fmaxf(fmaf(-mu, mu, s2 * (1.0f / DM)), 0.f) + 1e-5f);
            float v[15];
            f32x4 cs[4];
#pragma unroll
            for (int i = 0; i < 4; ++i) cs[i] = *(const f32x4*)(shm + B_CS + i * 1024 + lane * 16);
#pragma unroll
            for (int j = 0; j < 15; ++j) v[j] = fmaf(-mu, cs[j >> 2][j & 3], acc[j >> 2][j & 3]) * rs;
            u32 ph[5];
            phi(v + 5, ph);
            char* row = im + fr * B_IS;
            *(u32x2*)(row + g * 8) = (u32x2){ph[0], ph[1]};
            *(u32x2*)(row + 32 + g * 8) = (u32x2){ph[2], ph[3]};
            *(u32x2*)(row + 64 + g * 8) = (u32x2){ph[4], 0u};
            *(u32x2*)(row + 96 + g * 8) = (u32x2){pk2bf(v[10], v[11]), pk2bf(v[12], v[13])};
            *(u32x2*)(row + 128 + g * 8) = (u32x2){pk2bf(v[14], 1.0f), 0u};
            s16x4 tf[5];
#pragma unroll
            for (int i = 0; i < 5; ++i) tf[i] = __builtin_amdgcn_ds_read_tr16_b64_v4i16((LDS3 s16x4*)(uintptr_t)(imr + i * 32));
#pragma unroll
            for (int i = 0; i < 6; ++i) MMA16(kv[i >> 1][i & 1], tf[i >> 1], tf[3 + (i & 1)]);
#pragma unroll
            for (int j = 0; j < 5; ++j) vq[j] = v[j];
            if (it < 31) phiq(it);
            if (it >= 30) TT((hb >> 4) * 128 + 112 + (hb & 15), (it - 30) * 3 + 2);
        }
        asm volatile("s_nop 7" : "+v"(kv[0][0]), "+v"(kv[0][1]), "+v"(kv[1][0]), "+v"(kv[1][1]), "+v"(kv[2][0]), "+v"(kv[2][1]));
#pragma unroll
        for (int i = 0; i < 6; ++i) *(f32x4*)(im + i * 1024 + lane * 16) = kv[i >> 1][i & 1];
        RAW_BARRIER();
        bf8 ka[2];
        u32x2 kb[2];
#pragma unroll
        for (int dt = 0; dt < 2; ++dt) {
            f32x4 t[3];
#pragma unroll
            for (int ft = 0; ft < 3; ++ft) {
                const char* a = shm + B_IM + (ft * 2 + dt) * 1024 + lane * 16;
                t[ft] = (*(const f32x4*)a + *(const f32x4*)(a + 6144)) + (*(const f32x4*)(a + 12288) + *(const f32x4*)(a + 18432));
            }
            ka[dt] = pack8(t[0], t[1]);
            kb[dt] = (u32x2){pk2bf(t[2][0], t[2][1]), pk2bf(t[2][2], t[2][3])};
            PIN(ka[dt]); PIN(kb[dt]);
        }
        char* ob = (wsp(p) + O_OB) + layer * ACT + (size_t)w * 20480 + hb * 512;
        TT((hb >> 4) * 128 + 112 + (hb & 15), 6);
        u32 rofs = g * 81920 + 16384 - hb * 384 + fr * 8, mofs = (g & 1) * 81920 + (g >> 1) * 256 + fr * 16, mofs2 = mofs + 2 * 81920;
        asm volatile("" : "+v"(rofs), "+v"(mofs), "+v"(mofs2));
        f32x4 o[2][4][2];
        bf8 qa[2][4];
        u32x2 qb[4];
        auto ldq = [&](const int gr) {
#pragma unroll
            for (int u = 0; u < 4; ++u) qa[gr & 1][u] = *(const bf8*)(shm + B_PQ + (w * 32 + gr * 4 + u) * 1024 + lane * 16);
        };
        auto mm = [&](const int gr, const int k) {
            SCHEDB();
            if (k < 4) { qb[k] = (u32x2){q5[gr * 4 + k], 0u}; PIN(qb[k]); }
            if (k < 8) MMA0_VV(o[gr & 1][k >> 1][k & 1], ka[k & 1], qa[gr & 1][k >> 1]);
            else MMA16(o[gr & 1][(k - 8) >> 1][k & 1], kb[k & 1], qb[(k - 8) >> 1]);
            SCHEDB();
        };
        ldq(0); ldq(1);
        NOPV();
#pragma unroll
        for (int k = 0; k < 16; ++k) mm(0, k);
        static_for<0, 8>([&](auto IT) {
            constexpr int gr = CI(IT);
            auto nx = [&](const int k) { if constexpr (gr < 7) mm(gr + 1, k); else { SCHEDB(); asm volatile("s_nop 0"); SCHEDB(); } };
            if constexpr (gr == 7) asm volatile("s_nop 7");
            u32 d[4][2];
            float x[4];
            nx(0); nx(1);
#pragma unroll
            for (int u = 0; u < 4; ++u) {
                f32x4& a = o[gr & 1][u][0];
                f32x4& b = o[gr & 1][u][1];
                PIN(a); PIN(b);
                float inv = __builtin_amdgcn_rcpf(b[1] + 1e-6f), y0, y1, y2, y3;
                nx(2 + 3 * u);
                asm("v_mul_f32 %0, %4, %8\n\tv_mul_f32 %1, %5, %8\n\tv_mul_f32 %2, %6, %8" : "=&v"(y0), "=&v"(y1), "=&v"(y2), "=&v"(y3) : "v"(a[0]), "v"(a[1]), "v"(a[2]), "v"(a[3]), "v"(inv));
                nx(3 + 3 * u);
                y3 = a[3] * inv; x[u] = b[0] * inv;
                d[u][0] = pk2bf(y0, y1);
                nx(4 + 3 * u);
                d[u][1] = pk2bf(y2, y3);
                if (u & 1) {
                    auto t0 = __builtin_amdgcn_permlane16_swap(d[u - 1][0], d[u][0], false, false);
                    auto t1 = __builtin_amdgcn_permlane16_swap(d[u - 1][1], d[u][1], false, false);
                    __builtin_amdgcn_global_store_b128((gu32x4*)(ob + (u == 1 ? mofs : mofs2)), (u32x4){nosent(t0[0]), nosent(t1[0]), nosent(t0[1]), nosent(t1[1])}, "agent");
                }
            }
            if constexpr (gr == 5) phiq(31);
            if constexpr (gr < 6) ldq(gr + 2);
            nx(14);
            auto a0 = __builtin_amdgcn_permlane32_swap(__float_as_uint(x[0]), __float_as_uint(x[2]), false, false);
            auto a1 = __builtin_amdgcn_permlane32_swap(__float_as_uint(x[1]), __float_as_uint(x[3]), false, false);
            nx(15);
            auto b0 = __builtin_amdgcn_permlane16_swap(a0[0], a1[0], false, false);
            auto b2 = __builtin_amdgcn_permlane16_swap(a0[1], a1[1], false, false);
            __hip_atomic_store((gu64*)(ob + rofs), (u64)nosent(pk2bf(__uint_as_float(b0[0]), __uint_as_float(b0[1]))) | ((u64)nosent(pk2bf(__uint_as_float(b2[0]), __uint_as_float(b2[1]))) << 32), __ATOMIC_RELAXED, __HIP_MEMORY_SCOPE_AGENT);
            ob += 4 * 81920;
            asm volatile("" : "+s"(ob));
        });
        RAW_BARRIER();
        TT((hb >> 4) * 128 + 112 + (hb & 15), 7);
    }
}

__global__ void __launch_bounds__(256, 1) la_kernel(LaParams p) {
    extern __shared__ __attribute__((aligned(16))) char shm[];
    p.hop = align2m(p.ws + O_HOP);
    if (threadIdx.x == 0) {
        u32 xcc; asm volatile("s_getreg_b32 %0, hwreg(HW_REG_XCC_ID)" : "=s"(xcc));
        int role = 255;
        for (int a = 0; a < 8; ++a) {
            const u32 v = (xcc + a) & 7;
            const u32 k = __hip_atomic_fetch_add((gu32*)(((u32*)(p.ws + O_CTL)) + v * 32), 1u, __ATOMIC_RELAXED, __HIP_MEMORY_SCOPE_AGENT);
            if (k < 32) { role = v * 32 + k; break; }
        }
        *(int*)shm = role;
    }
    __syncthreads();
    const int role = __builtin_amdgcn_readfirstlane(*(int*)shm);
    __syncthreads();
    const int set = role >> 7, id = (role >> 5) & 3, k = role & 31;
    if (k < 8) qk_role(p, set, id * 8 + k, shm);
    else if (k < 28) comb_role(p, set, id * 20 + k - 8, shm);
    else bw_role(p, (role >> 5) * 4 + k - 28, shm);
}

#define PFN 1
struct PrepParams { const u16* W[2]; const u16* x; const u16* mem0; const u16* wo; char* ws; };
constexpr int N_PF = 128 * PFN, N_LN = 128, N_WO = 200, N_FILL = 512;
__global__ void __launch_bounds__(256) prep_kernel(PrepParams q) {
    int b = blockIdx.x;
    const int t = threadIdx.x, lane = t & 63, w = t >> 6, fr = lane & 15, g = lane >> 4;
    char* hop = align2m(q.ws + O_HOP);
    if (b < N_LN) {
        const int tok = b * 16 + w * 4 + g;
        u32x4 d[3][2];
        float s = 0.f;
#pragma unroll
        for (int i = 0; i < 3; ++i) {
            const u32x4* a = (const u32x4*)(q.x + (size_t)tok * DM + min(fr + 16 * i, 39) * 16);
            d[i][0] = a[0]; d[i][1] = a[1];
        }
#pragma unroll
        for (int i = 0; i < 3; ++i)
#pragma unroll
            for (int e = 0; e < 8; ++e) if (fr + 16 * i < 40) s += bflo(d[i][e >> 2][e & 3]) + bfhi(d[i][e >> 2][e & 3]);
        const float mu = row16_sum(s) * (1.0f / DM);
        float v = 0.f;
#pragma unroll
        for (int i = 0; i < 3; ++i)
#pragma unroll
            for (int e = 0; e < 8; ++e) if (fr + 16 * i < 40) { const float a = bflo(d[i][e >> 2][e & 3]) - mu, c = bfhi(d[i][e >> 2][e & 3]) - mu; v = fmaf(a, a, fmaf(c, c, v)); }
        const float rs = ln_scale(row16_sum(v) * (1.0f / DM)), mr = rbf(mu);
#pragma unroll
        for (int i = 0; i < 3; ++i) {
            const int blk = fr + 16 * i;
            if (blk >= 40) continue;
            const size_t o = (size_t)(tok >> 5) * 40960 + ((blk >> 1) * 2 + ((tok >> 4) & 1)) * 1024 + ((blk & 1) * 32 + (tok & 15)) * 16;
            u32 n[8];
#pragma unroll
            for (int e = 0; e < 8; ++e) n[e] = ln_pair(d[i][e >> 2][e & 3], mr, rs);
            *(u32x4*)(q.ws + O_XR + o) = (u32x4){d[i][0][0], d[i][1][0], d[i][0][1], d[i][1][1]};
            *(u32x4*)(q.ws + O_XR + o + 256) = (u32x4){d[i][0][2], d[i][1][2], d[i][0][3], d[i][1][3]};
            *(u32x4*)(q.ws + O_XN + o) = (u32x4){n[0], n[4], n[1], n[5]};
            *(u32x4*)(q.ws + O_XN + o + 256) = (u32x4){n[2], n[6], n[3], n[7]};
        }
        return;
    }
    b -= N_LN;
    if (b < N_PF) {
        const u32x4* src = (const u32x4*)q.W[b >> 7] + (size_t)(b & 127) * 3840 + t;
        u32x4 v[15];
#pragma unroll
        for (int i = 0; i < 15; ++i) v[i] = src[i * 256];
#pragma unroll
        for (int i = 0; i < 15; ++i) asm volatile("" :: "v"(v[i]));
        return;
    }
    b -= N_PF;
    if (b < N_WO) {
        const int cb = b / 20, kk = b % 20;
        const u16* col = q.wo + cb * 64 + 16 * (fr >> 2) + 4 * w + (fr & 3);
        u32 v[8];
#pragma unroll
        for (int e = 0; e < 8; ++e) {
            const int row = kk < 16 ? (2 * kk + (g >> 1)) * 20 + 5 * (2 * (g & 1) + (e >> 2)) + (e & 3) : (8 * (kk - 16) + 2 * g + (e >> 2)) * 20 + 5 * (e & 3) + 4;
            v[e] = col[(size_t)row * DM];
        }
        *(u32x4*)(q.ws + O_WO + (size_t)cb * 81920 + (kk * 4 + w) * 1024 + lane * 16) = (u32x4){v[0] | v[1] << 16, v[2] | v[3] << 16, v[4] | v[5] << 16, v[6] | v[7] << 16};
        return;
    }
    b -= N_WO;
    const u32x4 SENT = {~0u, ~0u, ~0u, ~0u};
    const u32 i0 = b * 256 + t;
    if (b == 0) ((u32*)(q.ws + O_CTL))[t] = 0;
    if (i0 < 8 * 2 * 320) {
        const int sl = i0 / 640, e = i0 % 320;
        const u32x4 v = *(const u32x4*)(q.mem0 + (e & 7) * DM + ((sl & 1) * 40 + (e >> 3)) * 8);
        st16(hop + M0_OFF + (i0 / 320) * 8192 + e * 16, (u32x4){nosent(v[0]), nosent(v[1]), nosent(v[2]), nosent(v[3])});
    }
    for (u32 i = i0; i < HOP_BYTES / 16; i += N_FILL * 256) st16(hop + (size_t)i * 16, SENT);
    for (u32 i = i0; i < (O_XN - O_FL) / 16; i += N_FILL * 256) st16(q.ws + O_FL + (size_t)i * 16, SENT);
}

extern "C" void kernel_launch(void* const* d_in, const int* in_sizes, int n_in,
                              void* d_out, int out_size, void* d_ws, size_t ws_size,
                              hipStream_t stream) {
    PrepParams q;
    q.x = (const u16*)d_in[0]; q.mem0 = (const u16*)d_in[1];
    q.wo = (const u16*)d_in[10];
    q.W[0] = (const u16*)d_in[5]; q.W[1] = (const u16*)d_in[4];
    q.ws = (char*)d_ws;
    prep_kernel<<<N_PF + N_LN + N_WO + N_FILL, 256, 0, stream>>>(q);
    LaParams p;
    p.ws = (char*)d_ws; p.out = (u16*)d_out; p.hop = (char*)d_ws;
    p.Wq = (const u16*)d_in[2]; p.Wk = (const u16*)d_in[3]; p.Wv = (const u16*)d_in[4]; p.Wo = (const u16*)d_in[5];
    p.wqkv = (const u16*)d_in[8]; p.wphi = (const u16*)d_in[9];
    (void)hipFuncSetAttribute((const void*)la_kernel, hipFuncAttributeMaxDynamicSharedMemorySize, LA_LDS);
    la_kernel<<<256, 256, LA_LDS, stream>>>(p);
}
```

```cpp
#include <hip/hip_runtime.h>
#include <hip/hip_bf16.h>
#include <stdint.h>
#include <type_traits>

typedef unsigned short u16;
typedef unsigned int u32;
typedef unsigned long long u64;
using bf16x8 = __attribute__((ext_vector_type(8))) short;
using f32x4 = __attribute__((ext_vector_type(4))) float;

constexpr int S_TOK = 2048, DM = 640, NH = 32, DH = 192;
constexpr int MEM = 8, SEG = 32, NSEG = S_TOK / SEG;
constexpr int HD = NH * DH;

__device__ __forceinline__ float bf2f(u16 v) { return __uint_as_float(((u32)v) << 16); }
__device__ __forceinline__ u32 pk2bf(float lo, float hi) {
    typedef __attribute__((ext_vector_type(2))) float f32x2_t;
    typedef __attribute__((ext_vector_type(2))) __bf16 bf16x2_t;
    bf16x2_t r = __builtin_convertvector((f32x2_t){lo, hi}, bf16x2_t);
    return __builtin_bit_cast(u32, r);
}
__device__ __forceinline__ float rbf(float f) { return __uint_as_float(pk2bf(f, f) << 16); }
typedef __attribute__((ext_vector_type(8))) __bf16 bf8;
typedef __attribute__((ext_vector_type(16))) __bf16 bf16v;
typedef __attribute__((ext_vector_type(4))) __bf16 bf4;
typedef __attribute__((ext_vector_type(4))) short s16x4;
using u32x4 = __attribute__((ext_vector_type(4))) u32;
using u32x2 = __attribute__((ext_vector_type(2))) u32;
typedef __attribute__((address_space(1))) u32 gu32;
typedef __attribute__((address_space(1))) u64 gu64;
#define LDS3 __attribute__((address_space(3)))

constexpr int SLOT = 16384;
#define PCSV 3
constexpr int PCS = PCSV, P_SLOT0 = 0, M_SLOT0 = 16 * PCS, S_SLOT = M_SLOT0 + 32, T_SLOT = S_SLOT + 10, N_SLOT = T_SLOT + 2;
constexpr size_t HOP_BYTES = (size_t)N_SLOT * SLOT, M0_OFF = HOP_BYTES;
constexpr size_t PX_BUF = 4 * 32 * 640, PX_BYTES = 8 * PX_BUF;
constexpr int LA_LDS = 162816 + 256;
constexpr int SPIN_CAP = 1 << 16;
#define D1I 131
#define D2I 53
#define MG1 7
#define MG2 10
#define DUP 5
#define PACE 6
#define CLAG 48
#define T_PX 26
#define T_X1 75
#define T_OB 150
#define T_TOK 350
#define ST_LAG 6
#define OB_STEP 25
#define OB_COLD 20
#define W0D 3
#define LATEP 3
#define PACE2 5
#define BPEEK 400
#define SLK 6
#define FSL 8
#define E0 110
#define HOLD 4
#define DOWN 1
#define EY 35
#define BOOST 3
#define DSH "1"
#define PAUSE 160u
#define BSL 40
#define PKK 18
#define PKT 2
#define BPF 20
#define BUMP 2
#define WRONG 0u
#define RECV 4
#define F4S 58
#define B_LAG 65
#define ELATE 85

__device__ __forceinline__ void st16(void* p, u32x4 v) {
    asm volatile("global_store_dwordx4 %0, %1, off sc1\n\ts_nop 2" :: "v"(p), "v"(v) : "memory");
}
typedef __attribute__((address_space(1))) u32x4 gu32x4;
__device__ __forceinline__ u32x4 ld16(const char* base, u32 off, int imm) { return __builtin_amdgcn_global_load_b128((gu32x4*)(base + off + imm), "agent"); }
__device__ __forceinline__ u32 nosent(u32 v) { return min(v, 0xFFFFFFFEu); }
__device__ __forceinline__ u32 rt32() { return (u32)__builtin_amdgcn_s_memrealtime(); }
__device__ __forceinline__ void wait_rt(u32 t) {
    int d = (int)(t - rt32()) - 4;
    if (d > 4096 || d < 0) return;
    while (d > 7) { if (d > 18) __builtin_amdgcn_s_sleep(2); d = max((int)(t - rt32()) - 4, 0); }
    asm volatile("s_bitcmp1_b32 %0, 2\n\ts_cbranch_scc0 1f\n\ts_nop 15\n\ts_nop 7\n1:\n\ts_bitcmp1_b32 %0, 1\n\ts_cbranch_scc0 2f\n\ts_nop 12\n2:\n\t"
                 "s_bitcmp1_b32 %0, 0\n\ts_cbranch_scc0 3f\n\ts_nop 7\n3:" :: "s"(d) : "scc");
}
template <int N> __device__ __forceinline__ void dma_kb(const char* src, u32 lds_dst, u32 lane16) {
#pragma unroll
    for (int i = 0; i < N; i += 4) {
        u32 keep;
        if (N - i >= 4)
            asm volatile("s_mov_b32 %0, m0\n\ts_mov_b32 m0, %3\n\ts_nop 2\n\tglobal_load_lds_dwordx4 %1, %2 sc1\n\tglobal_load_lds_dwordx4 %1, %2 offset:1024 sc1\n\t"
                         "global_load_lds_dwordx4 %1, %2 offset:2048 sc1\n\tglobal_load_lds_dwordx4 %1, %2 offset:3072 sc1\n\ts_mov_b32 m0, %0"
                         : "=&s"(keep) : "v"(lane16), "s"(src + i * 1024), "s"(lds_dst + i * 1024) : "memory");
        else if (N - i == 2)
            asm volatile("s_mov_b32 %0, m0\n\ts_mov_b32 m0, %3\n\ts_nop 2\n\tglobal_load_lds_dwordx4 %1, %2 sc1\n\tglobal_load_lds_dwordx4 %1, %2 offset:1024 sc1\n\ts_mov_b32 m0, %0"
                         : "=&s"(keep) : "v"(lane16), "s"(src + i * 1024), "s"(lds_dst + i * 1024) : "memory");
        else
            asm volatile("s_mov_b32 %0, m0\n\ts_mov_b32 m0, %3\n\ts_nop 2\n\tglobal_load_lds_dwordx4 %1, %2 sc1\n\ts_mov_b32 m0, %0"
                         : "=&s"(keep) : "v"(lane16), "s"(src + i * 1024), "s"(lds_dst + i * 1024) : "memory");
    }
}
template <int OFF> __device__ __forceinline__ void dma_one(const char* src, u32 lds_dst, u32 lane16) {
    asm volatile("s_mov_b32 m0, %2\n\ts_nop 0\n\tglobal_load_lds_dwordx4 %0, %1 offset:%3 sc1" :: "v"(lane16), "s"(src), "s"(lds_dst), "n"(OFF) : "memory");
}
__device__ __forceinline__ u32 ringp(u32 a, u32 b) { return a + b; }
__device__ __forceinline__ void dma_gather(const char* src, u32 lds_dst, u32 off) {
    u32 keep;
    asm volatile("s_mov_b32 %0, m0\n\ts_mov_b32 m0, %3\n\ts_nop 2\n\tglobal_load_lds_dwordx4 %1, %2\n\ts_mov_b32 m0, %0"
                 : "=&s"(keep) : "v"(off), "s"(src), "s"(lds_dst) : "memory");
}
__device__ __forceinline__ void st16s(char* base, u32 off, u32x4 v) {
    asm volatile("s_nop 4\n\tglobal_store_dwordx4 %0, %1, %2 sc1\n\ts_nop 2" :: "v"(off), "v"(v), "s"(base) : "memory");
}
__device__ __forceinline__ void arm16(char* base, u32 off) {
    u32x4 v;
    asm volatile("v_mov_b64 %0, -1\n\tv_mov_b64 %1, -1" : "=v"(((u64*)&v)[0]), "=v"(((u64*)&v)[1]));
    st16s(base, off, v);
}
template <int B, int E, typename F> __device__ __forceinline__ void static_for(F&& f) {
    if constexpr (B < E) { f(std::integral_constant<int, B>()); static_for<B + 1, E>(f); }
}
#define CI(x) decltype(x)::value
#define RAW_BARRIER() do { asm volatile("s_waitcnt lgkmcnt(0)" ::: "memory"); __builtin_amdgcn_s_barrier(); asm volatile("" ::: "memory"); } while (0)
#define VM0() asm volatile("s_waitcnt vmcnt(0)" ::: "memory")
#define SCHEDB() __builtin_amdgcn_sched_barrier(0)
__device__ __forceinline__ bf8 lo8(const bf16v& v) { return __builtin_shufflevector(v, v, 0, 1, 2, 3, 4, 5, 6, 7); }
__device__ __forceinline__ bf8 hi8(const bf16v& v) { return __builtin_shufflevector(v, v, 8, 9, 10, 11, 12, 13, 14, 15); }
__device__ __forceinline__ bf16v cat8(bf8 a, bf8 b) { return __builtin_shufflevector(a, b, 0, 1, 2, 3, 4, 5, 6, 7, 8, 9, 10, 11, 12, 13, 14, 15); }
#define MMA_VV(c, a, b) asm volatile("v_mfma_f32_16x16x32_bf16 %0, %1, %2, %0" : "+v"(c) : "v"(a), "v"(b))
#define MMA_AV(c, a, b) asm volatile("v_mfma_f32_16x16x32_bf16 %0, %1, %2, %0" : "+v"(c) : "a"(a), "v"(b))
#define MMA_VA(c, a, b) asm volatile("v_mfma_f32_16x16x32_bf16 %0, %1, %2, %0" : "+v"(c) : "v"(a), "a"(b))
#define MMA0_VV(c, a, b) asm volatile("v_mfma_f32_16x16x32_bf16 %0, %1, %2, 0" : "=&v"(c) : "v"(a), "v"(b))
#define MMA0_AV(c, a, b) asm volatile("v_mfma_f32_16x16x32_bf16 %0, %1, %2, 0" : "=&v"(c) : "a"(a), "v"(b))
#define MMA0_VA(c, a, b) asm volatile("v_mfma_f32_16x16x32_bf16 %0, %1, %2, 0" : "=&v"(c) : "v"(a), "a"(b))
#define MMA16(c, a, b) asm volatile("v_mfma_f32_16x16x16_bf16 %0, %1, %2, %0" : "+v"(c) : "v"(a), "v"(b))
#define MMA16_0(c, a, b) asm volatile("v_mfma_f32_16x16x16_bf16 %0, %1, %2, 0" : "=&v"(c) : "v"(a), "v"(b))
#define SMM_V(c, a, b, i) asm volatile("v_smfmac_f32_16x16x64_bf16 %0, %1, %2, %3" : "+v"(c) : "v"(a), "v"(b), "v"(i))
#define SMM_A(c, a, b, i) asm volatile("v_smfmac_f32_16x16x64_bf16 %0, %1, %2, %3" : "+v"(c) : "v"(a), "a"(b), "v"(i))
#define NOPV() asm volatile("s_nop 1")
#define PIN(x) asm volatile("" : "+v"(x))
#define SETTLE() asm volatile("s_nop 7\n\ts_nop 3")
__device__ __forceinline__ bf8 pack8(f32x4 a, f32x4 b) {
    u32x4 r = {pk2bf(a[0], a[1]), pk2bf(a[2], a[3]), pk2bf(b[0], b[1]), pk2bf(b[2], b[3])};
    return __builtin_bit_cast(bf8, r);
}
__device__ __forceinline__ u32 max4(u32 m, u32x4 g) { m = max(max(m, g[0]), g[1]); return max(max(m, g[2]), g[3]); }
#define ZERO4(c) asm volatile("v_mov_b64 %0, 0\n\tv_mov_b64 %1, 0" : "=v"(((u64*)&(c))[0]), "=v"(((u64*)&(c))[1]))
__device__ __forceinline__ bf8 lds8(const char* shm, int off) { return *(const bf8*)(shm + off); }
__device__ __forceinline__ bf16v ldfrag(const char* p) { return cat8(*(const bf8*)p, *(const bf8*)(p + 1024)); }
template <int CTRL> __device__ __forceinline__ float fdpp(float x) {
    return __builtin_bit_cast(float, __builtin_amdgcn_mov_dpp(__builtin_bit_cast(int, x), CTRL, 0xf, 0xf, true));
}
__device__ __forceinline__ float halfsum(float a, float b) {
    auto r = __builtin_amdgcn_permlane32_swap(__float_as_uint(a), __float_as_uint(b), false, false);
    return __uint_as_float(r[0]) + __uint_as_float(r[1]);
}
__device__ __forceinline__ float grp_sum(float x) {
    auto t = __builtin_amdgcn_permlane16_swap(__float_as_uint(x), __float_as_uint(x), false, false);
    x = __uint_as_float(t[0]) + __uint_as_float(t[1]);
    return halfsum(x, x);
}
__device__ __forceinline__ float grp_max(float x) {
    auto t = __builtin_amdgcn_permlane16_swap(__float_as_uint(x), __float_as_uint(x), false, false);
    x = fmaxf(__uint_as_float(t[0]), __uint_as_float(t[1]));
    auto u = __builtin_amdgcn_permlane32_swap(__float_as_uint(x), __float_as_uint(x), false, false);
    return fmaxf(__uint_as_float(u[0]), __uint_as_float(u[1]));
}
__device__ __forceinline__ float bflo(u32 v) { return __uint_as_float(v << 16); }
__device__ __forceinline__ float bfhi(u32 v) { return __uint_as_float(v & 0xffff0000u); }
__device__ __forceinline__ float ln_scale(float var) { return __builtin_amdgcn_rcpf(rbf(__builtin_amdgcn_sqrtf(rbf(rbf(var) + 1e-5f)))); }
__device__ __forceinline__ u32 ln_pair(u32 v, float m, float rs) { const u32 d = pk2bf(bflo(v) - m, bfhi(v) - m); return pk2bf(bflo(d) * rs, bfhi(d) * rs); }
__device__ __forceinline__ void st4_lane0(char* base, u32 v) {
    u32 z, d;
    base = (char*)(((u64)(u32)__builtin_amdgcn_readfirstlane((u32)((u64)base >> 32)) << 32) | (u64)(u32)__builtin_amdgcn_readfirstlane((u32)(u64)base));
    asm volatile("s_mov_b64 exec, 1\n\tv_mov_b32 %0, 0\n\tv_mov_b32 %1, %3\n\ts_nop 1\n\tglobal_store_dword %0, %1, %2 sc1\n\ts_mov_b64 exec, -1" : "=&v"(z), "=&v"(d) : "s"(base), "s"(v) : "memory");
}
__device__ __forceinline__ void stamp2(char* hop, const int line) {
    const u32 t = nosent(rt32());
    st4_lane0(hop + (T_SLOT + 1) * SLOT + line * 128, t); st4_lane0(hop + (T_SLOT + 1) * SLOT + 8192 + line * 128, t);
}
__device__ __forceinline__ int smax(int a, int b) { int r; asm volatile("s_max_i32 %0, %1, %2" : "=s"(r) : "s"(a), "s"(b) : "scc"); return r; }
__device__ __forceinline__ int track(int d, int sample, int down) {
    int e, a, b;
    asm("s_sub_i32 %0, %3, %4\n\ts_cmp_gt_i32 %3, 0\n\ts_cselect_b32 %0, %0, 0\n\ts_min_i32 %1, %0, %6\n\ts_ashr_i32 %2, %0, " DSH "\n\ts_max_i32 %2, %2, %5\n\t"
        "s_min_i32 %2, %2, 0\n\ts_max_i32 %1, %1, %2" : "=&s"(e), "=&s"(a), "=&s"(b) : "s"(sample), "s"(d), "s"(-down), "n"(DUP) : "scc");
    return d + a;
}
__device__ __forceinline__ u32 sched_peek(const char* p) { return __hip_atomic_load((const gu32*)p, __ATOMIC_RELAXED, __HIP_MEMORY_SCOPE_AGENT); }
template <int SL = 3> __device__ __forceinline__ u32 sched_get(u32 v, const char* p, int& cap) {
    for (int spin = 0; v == ~0u; ++spin) {
        if (spin >= cap) { cap = 1; break; }
        __builtin_amdgcn_s_sleep(SL);
        v = sched_peek(p);
    }
    return __builtin_amdgcn_readfirstlane(v);
}

#define TRDECL
#define TR(i)
#define TRV(i, v)
#define TRFLUSH(role)
#define TRC(i)
constexpr size_t ACT = (size_t)S_TOK * DM * 2;
constexpr size_t O_DBG = 0, O_CTL = 2u << 20, O_FL = O_CTL + 4096, O_PX = O_FL + 640 * 128, O_ST = O_PX + PX_BYTES, O_X1 = O_ST + 128 * 1280,
                 O_OB = O_X1 + 2 * ACT, O_XN = O_OB + 2 * ACT, O_XR = O_XN + 2 * ACT, O_WO = O_XR + 2 * ACT,
                 O_WVO = O_WO + 10 * 81920, O_HOP = O_WVO + (size_t)80 * 4 * 40 * 2048, WS_BYTES = O_HOP + (4u << 20);
#define TT(role, i)
#define CNT(i)
struct LaParams { char* ws; u16* out; char* hop; const u16* Wq; const u16* Wk; const u16* Wv; const u16* Wo; const u16* wqkv; const u16* wphi; };
__device__ __forceinline__ int kperm(int g, int e) { return 16 * (g >> 1) + 8 * ((e & 3) >> 1) + 4 * (g & 1) + 2 * (e >> 2) + (e & 1); }
__device__ __forceinline__ bf8 tr8(u32 a, u32 b) {
    const s16x4 lo = __builtin_amdgcn_ds_read_tr16_b64_v4i16((LDS3 s16x4*)(uintptr_t)a), hi = __builtin_amdgcn_ds_read_tr16_b64_v4i16((LDS3 s16x4*)(uintptr_t)b);
    return __builtin_bit_cast(bf8, __builtin_shufflevector(lo, hi, 0, 1, 2, 3, 4, 5, 6, 7));
}
__device__ __forceinline__ char* wsp(const LaParams& p) { char* b = p.ws; asm volatile("" : "+s"(b)); return b; }
template <int N, int SL = 4> __device__ __forceinline__ void wait_flags(const char* f, const u32 (&off)[N], int& cap) {
    for (int spin = 0;; ++spin) {
        u32 m = 0;
#pragma unroll
        for (int i = 0; i < N; ++i) m = max(m, sched_peek(f + off[i]));
        if (!__any(m == ~0u)) break;
        if (spin >= cap) { cap = 1; break; }
        __builtin_amdgcn_s_sleep(SL);
    }
    asm volatile("" ::: "memory");
}
__device__ __forceinline__ void wait_all_tokens(const LaParams& p, u32 lane, int& cap) {
    asm volatile("" : "+v"(lane));
    const u32 off[3] = {lane * 128, lane * 128 + 8192, min(lane + 128, 159u) * 128};
    wait_flags<3>(wsp(p) + O_FL + 192 * 128, off, cap);
}
__device__ __forceinline__ void wait_first_tokens(const LaParams& p, const int set, u32 lane, int& cap) {
    asm volatile("" : "+v"(lane));
    const u32 off[1] = {(set * 20 + min(lane, 39u) + (lane >= 20 ? 20 : 0)) * 128};
    wait_flags<1>(wsp(p) + O_FL, off, cap);
}
__device__ __forceinline__ float row16_sum(float x) { x += fdpp<0x128>(x); x += fdpp<0x124>(x); x += fdpp<0x122>(x); return x + fdpp<0x121>(x); }
__device__ __forceinline__ int get_side(const LaParams& p, int& cap) { return sched_get(sched_peek(wsp(p) + O_FL + 384 * 128), wsp(p) + O_FL + 384 * 128, cap) & 1; }
__device__ __forceinline__ char* align2m(char* p) { return p + ((size_t)(-(intptr_t)p) & ((2u << 20) - 1)); }

constexpr int Q_XA = 0, Q_PART = 20480, Q_TR = 48640, Q_MK = 54784, Q_WL = 55808, Q_ST = 162304, Q_PS = 44;

#define DPP1(op, ror) op "_dpp %0, %0, %0 row_ror:" ror " row_mask:0xf bank_mask:0xf\n\t"
#define DPP2(op, ror) DPP1(op, ror) op "_dpp %1, %1, %1 row_ror:" ror " row_mask:0xf bank_mask:0xf\n\ts_nop 0\n\t"
__device__ __forceinline__ void st16m(char* base, u32 off, u32x4 v, u64 mask) {
    asm volatile("s_mov_b64 exec, %3\n\ts_nop 3\n\tglobal_store_dwordx4 %0, %1, %2 sc1\n\ts_mov_b64 exec, -1\n\ts_nop 1" :: "v"(off), "v"(v), "s"(base), "s"(mask) : "memory");
}
__device__ __forceinline__ void st16f(char* base, u32 off, u32x4 v, u64 mask) {
    asm volatile("s_mov_b64 exec, %3\n\tglobal_store_dwordx4 %0, %1, %2 sc1\n\ts_mov_b64 exec, -1\n\ts_nop 1" :: "v"(off), "v"(v), "s"(base), "s"(mask) : "memory");
}
__device__ __forceinline__ void st16f64(char* base, u32 off, u32x4 v, u64 mask) {
    asm volatile("s_mov_b64 exec, %3\n\tglobal_store_dwordx4 %0, %1, %2 offset:64 sc1\n\ts_mov_b64 exec, -1\n\ts_nop 1" :: "v"(off), "v"(v), "s"(base), "s"(mask) : "memory");
}
__device__ __forceinline__ void st32(char* base, u32 off, u32x4 a, u32x4 b) {
    asm volatile("s_nop 4\n\tglobal_store_dwordx4 %0, %1, %3\n\tglobal_store_dwordx4 %0, %2, %3 offset:16\n\ts_nop 2" :: "v"(off), "v"(a), "v"(b), "s"(base) : "memory");
}
__device__ __forceinline__ void st8m(char* base, u32 off, u32x2 v, u64 mask) {
    asm volatile("s_mov_b64 exec, %3\n\ts_nop 3\n\tglobal_store_dwordx2 %0, %1, %2 sc1\n\ts_mov_b64 exec, -1" :: "v"(off), "v"(v), "s"(base), "s"(mask) : "memory");
}
__device__ __forceinline__ u32 pk2bf_abs(float lo, float hi) {
    u32 r;
    asm("v_cvt_pk_bf16_f32 %0, |%1|, |%2|" : "=v"(r) : "v"(lo), "v"(hi));
    return r;
}
__device__ __forceinline__ u32 p_line(const int h, const int piece, const u32 gap) {
    u32 a = (h >> 4) * gap + ((h >> 1) & 7) * 1024 + (h & 1) * 128, b = 2 * gap + h * 128;
    asm volatile("" : "+s"(a), "+s"(b));
    return piece < 4 ? a + piece * 256 : b;
}
constexpr float SM_C = 0.07216878364870322f * 1.4426950408889634f, NEG_BIG = -0x1p100f;
constexpr u64 SM_MASK = 0x0155015501550155ull;
template <typename MID> __device__ __forceinline__ void softmax_mem(const float* p, char* dst, char* dst2, const u32 off, MID&& mid) {
    f32x4 a[4];
#pragma unroll
    for (int i = 0; i < 4; ++i) a[i] = *(const f32x4*)(p + i * 40 * Q_PS);
    SCHEDB(); mid(); SCHEDB();
    f32x4 x = (a[0] + a[1]) + (a[2] + a[3]);
    float ml, mx = fmaxf(fmaxf(x[0], x[1]), fmaxf(x[2], x[3])), e0, e1, e2, e3;
    asm volatile("v_mul_f32 %5, %10, %0\n\ts_nop 0\n\t"
        DPP1("v_max_f32", "8") "v_fma_f32 %1, %6, %10, -%5\n\tv_fma_f32 %2, %7, %10, -%5\n\t"
        DPP1("v_max_f32", "4") "v_fma_f32 %3, %8, %10, -%5\n\tv_fma_f32 %4, %9, %10, -%5\n\t"
        DPP1("v_max_f32", "2") "v_exp_f32 %1, %1\n\tv_exp_f32 %2, %2\n\t"
        DPP1("v_max_f32", "1") "v_exp_f32 %3, %3\n\tv_fma_f32 %5, -%0, %10, %5\n\tv_exp_f32 %4, %4\n\tv_add_f32 %0, %1, %2\n\tv_exp_f32 %5, %5\n\tv_add_f32 %0, %0, %3\n\t"
        "v_add_f32 %0, %0, %4\n\tv_mul_f32 %0, %0, %5\n\tv_mul_f32 %1, %1, %5\n\tv_mul_f32 %2, %2, %5\n\t"
        DPP1("v_add_f32", "8") "v_mul_f32 %3, %3, %5\n\tv_mul_f32 %4, %4, %5\n\t" DPP1("v_add_f32", "4") "s_nop 1\n\t"
        DPP1("v_add_f32", "2") "s_nop 1\n\t" DPP1("v_add_f32", "1") "v_rcp_f32 %0, %0\n\ts_nop 0\n\t"
        "v_mul_f32 %1, %1, %0\n\tv_mul_f32 %2, %2, %0\n\tv_mul_f32 %3, %3, %0\n\tv_mul_f32 %4, %4, %0"
        : "+v"(mx), "=&v"(e0), "=&v"(e1), "=&v"(e2), "=&v"(e3), "=&v"(ml) : "v"(x[0]), "v"(x[1]), "v"(x[2]), "v"(x[3]), "s"(SM_C) : "memory");
    u32x4 o;
    asm volatile("v_cvt_pk_bf16_f32 %4, |%4|, |%6|\n\tv_cvt_pk_bf16_f32 %5, |%5|, |%7|\n\ts_nop 0\n\t"
                 "v_mov_b32_dpp %0, %4 quad_perm:[0,0,2,2] row_mask:0xf bank_mask:0xf\n\tv_mov_b32_dpp %1, %5 quad_perm:[0,0,2,2] row_mask:0xf bank_mask:0xf\n\t"
                 "v_mov_b32_dpp %2, %4 quad_perm:[1,1,3,3] row_mask:0xf bank_mask:0xf\n\tv_mov_b32_dpp %3, %5 quad_perm:[1,1,3,3] row_mask:0xf bank_mask:0xf"
                 : "=&v"(o[0]), "=&v"(o[1]), "=&v"(o[2]), "=&v"(o[3]), "+v"(e0), "+v"(e2) : "v"(e1), "v"(e3), "s"(dst), "s"(dst2));
    asm volatile("s_mov_b64 exec, %4\n\tglobal_store_dwordx4 %0, %1, %2 sc1\n\tglobal_store_dwordx4 %0, %1, %3 sc1\n\ts_mov_b64 exec, -1\n\ts_nop 1"
                 :: "v"(off), "v"(o), "s"(dst), "s"(dst2), "s"(0x03ff03ff03ff03ffull) : "memory");
}
struct SmTok {
    float x[2][4], b1[2][4], b2[2][4], b3[2][4], mx[2], sm[2], t0, t1;
    u32 d00, d01, d10, d11;
    static constexpr int N = 88;
    __device__ __forceinline__ void load(const float* p, const int j) {
        const int n = j & 1, k = j >> 1;
        const f32x4 a = *(const f32x4*)(p + (k * 40 + n * 4) * Q_PS);
#pragma unroll
        for (int i = 0; i < 4; ++i) (k == 0 ? x : (k == 1 ? b1 : (k == 2 ? b2 : b3)))[n][i] = a[i];
    }
    template <int I> __device__ __forceinline__ void piece(char* dst, const u32 off) {
        constexpr int n = I & 1, k = (I >> 1) - 12;
        float& t = n ? t1 : t0;
#define ROR(op, ror, v) asm volatile(op "_dpp %0, %0, %0 row_ror:" ror " row_mask:0xf bank_mask:0xf" : "+v"(v))
        if constexpr (k < -8) asm volatile("v_add_f32 %0, %0, %1" : "+v"(x[n][k + 12]) : "v"(b1[n][k + 12]));
        else if constexpr (k < -4) asm volatile("v_add_f32 %0, %0, %1" : "+v"(b2[n][k + 8]) : "v"(b3[n][k + 8]));
        else if constexpr (k < 0) asm volatile("v_add_f32 %0, %0, %1" : "+v"(x[n][k + 4]) : "v"(b2[n][k + 4]));
        else if constexpr (k == 0) asm volatile("v_max_f32 %0, %1, %2" : "=v"(mx[n]) : "v"(x[n][0]), "v"(x[n][1]));
        else if constexpr (k == 1) asm volatile("v_max3_f32 %0, %0, %1, %2" : "+v"(mx[n]) : "v"(x[n][2]), "v"(x[n][3]));
        else if constexpr (k == 2) ROR("v_max_f32", "8", mx[n]);
        else if constexpr (k == 3) ROR("v_max_f32", "4", mx[n]);
        else if constexpr (k == 4) ROR("v_max_f32", "2", mx[n]);
        else if constexpr (k == 5) ROR("v_max_f32", "1", mx[n]);
        else if constexpr (k == 6) { mx[n] *= SM_C; PIN(mx[n]); }
        else if constexpr (k < 11) { x[n][k - 7] = fmaf(x[n][k - 7], SM_C, -mx[n]); PIN(x[n][k - 7]); }
        else if constexpr (k < 15) asm volatile("v_exp_f32 %0, %0" : "+v"(x[n][k - 11]));
        else if constexpr (k == 15) asm volatile("v_add_f32 %0, %1, %2" : "=v"(sm[n]) : "v"(x[n][0]), "v"(x[n][1]));
        else if constexpr (k == 16) asm volatile("v_add_f32 %0, %1, %2" : "=v"(t) : "v"(x[n][2]), "v"(x[n][3]));
        else if constexpr (k == 17) asm volatile("v_add_f32 %0, %0, %1" : "+v"(sm[n]) : "v"(t));
        else if constexpr (k == 18) ROR("v_add_f32", "8", sm[n]);
        else if constexpr (k == 19) ROR("v_add_f32", "4", sm[n]);
        else if constexpr (k == 20) ROR("v_add_f32", "2", sm[n]);
        else if constexpr (k == 21) ROR("v_add_f32", "1", sm[n]);
        else if constexpr (k == 22) asm volatile("v_rcp_f32 %0, %0" : "+v"(sm[n]));
        else if constexpr (k < 27) asm volatile("v_mul_f32 %0, %0, %1" : "+v"(x[n][k - 23]) : "v"(sm[n]));
        else if constexpr (k == 27) asm volatile("v_cvt_pk_bf16_f32 %0, |%1|, |%2|" : "=v"(n ? d10 : d00) : "v"(x[n][0]), "v"(x[n][1]));
        else if constexpr (k == 28) asm volatile("v_cvt_pk_bf16_f32 %0, |%1|, |%2|" : "=v"(n ? d11 : d01) : "v"(x[n][2]), "v"(x[n][3]));
        else if constexpr (I == 84 || I == 87) {
            u32x4 o;
            asm volatile("v_mov_b32 %0, %4\n\tv_mov_b32 %1, %5\n\tv_mov_b32_dpp %2, %4 quad_perm:[1,0,3,2] row_mask:0xf bank_mask:0xf\n\tv_mov_b32_dpp %3, %5 quad_perm:[1,0,3,2] row_mask:0xf bank_mask:0xf"
                         : "=&v"(o[0]), "=&v"(o[1]), "=&v"(o[2]), "=&v"(o[3]) : "v"(n ? d10 : d00), "v"(n ? d11 : d01), "s"(dst));
            if constexpr (n == 0) st16f(dst, off, o, SM_MASK); else st16f64(dst, off, o, SM_MASK);
        }
    }
};

__host__ __device__ constexpr int qk_slot(int T, int ks) { return ks < 5 ? ks * 6 + T : (T < 4 ? 30 + (T >> 1) * 10 + (ks - 5) * 2 + (T & 1) : 25 + T * 5 + ks); }
constexpr int NLF = 13;
__host__ __device__ constexpr int lds_slot(int n) { constexpr int t[NLF] = {2, 6, 11, 15, 20, 24, 29, 33, 37, 41, 45, 49, 55}; return t[n]; }
__host__ __device__ constexpr int slot_lds(int sl) { for (int n = 0; n < NLF; ++n) if (lds_slot(n) == sl) return n; return -1; }
__host__ __device__ constexpr int slot_frag(int sl) { if (slot_lds(sl) >= 0) return 48 + slot_lds(sl); int n = 0; for (int i = 0; i < NLF; ++i) n += lds_slot(i) < sl; return sl - n; }
__host__ __device__ constexpr int qk_frag(int T, int ks) { return slot_frag(qk_slot(T, ks)); }
__host__ __device__ constexpr int slot_T(int sl) { return sl < 30 ? sl % 6 : (sl < 50 ? ((sl - 30) / 10) * 2 + (sl & 1) : (sl - 30) / 5); }
__host__ __device__ constexpr int slot_ks(int sl) { return sl < 30 ? sl / 6 : (sl < 50 ? 5 + ((sl - 30) % 10) / 2 : 5 + sl % 5); }
__host__ __device__ constexpr int lds_rank(int T, int ks) { int n = 0; for (int t = 0; t < T; ++t) n += qk_frag(t, ks) >= 48; return n; }
__host__ __device__ constexpr int lds_nth(int ks, int n) { for (int t = 0; t < 6; ++t) if (qk_frag(t, ks) >= 48 && lds_rank(t, ks) == n) return qk_frag(t, ks) - 48; return -1; }

__device__ __forceinline__ void qk_role(const LaParams& p, const int set, const int h, char* shm) {
    const int lane = threadIdx.x & 63, w = __builtin_amdgcn_readfirstlane(threadIdx.x >> 6);
    const int fr = lane & 15, g = lane >> 4;
    const u32 lds0 = (u32)(uintptr_t)(LDS3 char*)shm;
    const int sidx = (lane & 8) ? 0xEEEE : 0x4444;
    int cap = SPIN_CAP;
    char* mine = p.hop;
    int sch = (S_SLOT + w) * SLOT;
    int phys = 0;
    char* px = (wsp(p) + O_PX);
    const char* xn = (wsp(p) + O_XN);
    char* wl = shm + Q_WL + w * (NLF * 2048) + lane * 16;
    const char* xa = shm + Q_XA + lane * 16;

    bf16v WA[32], WV[28 - NLF];
    {
        bf16v WT[NLF];
        u32 vo[6], rd[3];
#pragma unroll
        for (int i = 0; i < 6; ++i) {
            const int id = i * 64 + lane, r = w * 16 + id / 24;
            vo[i] = (32 * (r >> 5) + kperm((r >> 2) & 3, ((r >> 4) & 1) * 4 + (r & 3))) * (HD * 2) + ((id % 24) ^ (r & 6)) * 16;
        }
#pragma unroll
        for (int i = 0; i < 3; ++i) rd[i] = lds0 + (g * 4 + (fr >> 2)) * 384 + ((w * 3 + i) ^ ((g & 1) * 2 + (fr >> 3))) * 32 + (fr & 3) * 8;
        auto issue = [&](const int j) {
            const char* src = (const char*)((j & 1) ? p.Wk : p.Wq) + (size_t)(j >> 1) * 64 * HD * 2 + h * 384;
#pragma unroll
            for (int i = 0; i < 6; ++i) dma_gather(src, lds0 + (j % 6) * 24576 + w * 6144 + i * 1024, vo[i]);
        };
#pragma unroll
        for (int j = 0; j < 5; ++j) issue(j);
        static_for<0, 20>([&](auto J) {
            constexpr int j = CI(J);
            asm volatile("s_waitcnt vmcnt(%0)" :: "n"((19 - j < 4 ? 19 - j : 4) * 6) : "memory");
            RAW_BARRIER();
            if constexpr (j + 5 < 20) issue(j + 5);
            static_for<0, 3>([&](auto PR) {
                constexpr int id = qk_frag(2 * CI(PR) + (j & 1), j >> 1);
                u32 a = rd[CI(PR)] + (j % 6) * 24576;
                asm volatile("" : "+v"(a));
                const bf16v f = cat8(tr8(a, a + 16 * 384), tr8(a + 32 * 384, a + 48 * 384));
                if constexpr (id < 32) WA[id] = f; else if constexpr (id < 48) WV[id - 32] = f; else WT[id - 48] = f;
            });
        });
        RAW_BARRIER();
        dma_kb<5>(xn + (size_t)set * 40960 + w * 5120, lds0 + Q_XA + w * 5120, lane * 16);
#pragma unroll
        for (int i = 0; i < NLF; ++i) { *(bf8*)(wl + i * 2048) = lo8(WT[i]); *(bf8*)(wl + i * 2048 + 1024) = hi8(WT[i]); }
    }
    TRDECL;
    bf8 QX[2], KX[2];
    u32x2 QY[2], KY[2];
    u32 peek = ~0u;
    int d1 = D1I, d2 = D2I, k1 = D1I + MG1;
    if (w == 3) *(u64*)(shm + Q_ST + lane * 8) = lane < 63 ? 0 : (u64)D1I | ((u64)D2I << 32);
    const int lead = __builtin_amdgcn_readfirstlane((u32)(h * 4 + (w ^ 2) - 1) >> 31);
    SmTok smt;
    u32 took = 0, tvis = 0;
    u64 own;
    u32 xoff = p_line(h, fr >> 1, 8192) + g * 16, ao1 = p_line(h, min(lane >> 3, 4), 8192) + (lane & 7) * 16, ao0 = p_line(h, min(lane >> 3, 4), SLOT) + (lane & 7) * 16;
    asm volatile("" : "+v"(xoff), "+v"(ao0), "+v"(ao1));
    auto rearm = [&](const int rot, const int i, char*& a, u32x4& v) {
        if (i == 0) asm volatile("v_mov_b64 %0, -1\n\tv_mov_b64 %1, -1" : "=v"(((u64*)&v)[0]), "=v"(((u64*)&v)[1]));
        if (i == 1) { a = mine + (P_SLOT0 + (((rot + 3) & 3) * 4 + w) * PCS) * SLOT; asm volatile("" : "+s"(a)); }
        if (i == 5) { a = px + (size_t)(set * 4 + ((rot + 2) & 3)) * PX_BUF + w * 20480; asm volatile("" : "+s"(a)); }
        if (i == 3 || i == 8) __builtin_amdgcn_global_store_b128((gu32x4*)(a + (i == 3 ? ao0 : ao1)), v, "agent");
    };
    const char* mb0;
    const char* mb1;
    char* pdst;
    char* pdst2;
    int soft;
    u32 ytime;
    f32x4 tmask;
    auto next = [&](const int s, const int i) {
        const int rot = (s >> 1) & 3;
        if (i == 0) { mb0 = mine + (M_SLOT0 + rot * 8 + 2 * w) * SLOT + 2048; asm volatile("" : "+s"(mb0)); }
        if (i == 1) { mb1 = mb0 + SLOT; soft = (u32)((w - 2) & (s - (NSEG - 1))) >> 31; asm volatile("" : "+s"(mb1), "+s"(soft)); }
        if (i == 2) { pdst = mine + (P_SLOT0 + rot * 4 * PCS) * SLOT; pdst2 = pdst + 2 * PCS * SLOT; asm volatile("" : "+s"(pdst), "+s"(pdst2)); }
        if (i == 3) tmask = *(const f32x4*)(xa + Q_MK);
    };
    auto fetch_stats = [&]() {
        if (__builtin_expect(lead != 0, 0)) {
                u32 lo = lane;
                asm volatile("" : "+v"(lo));
                lo *= 128;
                u32 keep;
                asm volatile("s_mov_b32 %0, m0\n\ts_mov_b32 m0, %4\n\ts_nop 2\n\tglobal_load_lds_dword %1, %2 sc1\n\ts_add_u32 m0, m0, 256\n\ts_lshr_b64 exec, exec, 16\n\tglobal_load_lds_dword %1, %3 sc1\n\ts_mov_b64 exec, -1\n\ts_mov_b32 m0, %0"
                             : "=&s"(keep) : "v"(lo), "s"(mine + T_SLOT * SLOT), "s"(mine + (T_SLOT + 1) * SLOT), "s"(lds0 + Q_ST) : "memory", "scc");
        }
    };
    int hold = 0;
    u32 ylast = 0;
    auto xproj = [&](const int snext, const int sprev, auto SOFT, char* xdst) {
        f32x4 acc[6][2];
        TR(8);
        const char* xs;
        u32 xl;
        bf8 x0 = *(const bf8*)(xa), x1 = *(const bf8*)(xa + 1024), lw[2];
        lw[0] = *(const bf8*)(wl);
        if constexpr (CI(SOFT)) RAW_BARRIER();
        auto pack = [&](const int T, const int tt) {
            PIN(acc[T][tt]);
            const u32 a = pk2bf(acc[T][tt][0], acc[T][tt][1]), b = pk2bf(acc[T][tt][2], acc[T][tt][3]);
            if (T < 4) { u32* d = (u32*)((T & 1) ? &KX[tt] : &QX[tt]) + (T & 2); d[0] = a; d[1] = b; }
            else if (T == 4) QY[tt] = (u32x2){a, b}; else KY[tt] = (u32x2){a, b};
            SCHEDB();
        };
        char* ao;
        u32x4 sent;
        static_for<0, 20>([&](auto K) {
            constexpr int k = CI(K), l0 = lds_nth(((k + 1) >> 1) % 10, 0), l1 = lds_nth(((k + 1) >> 1) % 10, 1);
            bf8 n0, n1, nw[2];
            if constexpr (k == 9) {
                TR(9);
                if constexpr (CI(SOFT)) asm volatile("s_waitcnt vmcnt(4)" ::: "memory"); else VM0();
                RAW_BARRIER();
                TR(10);
            }
            static_for<0, 6>([&](auto TT) {
                constexpr int T = CI(TT), id = qk_frag(T, k >> 1), rk = lds_rank(T, k >> 1);
                if constexpr (k < 19) {
                    if constexpr (T == 0) n0 = *(const bf8*)(xa + (k + 1) * 2048);
                    if constexpr (T == 1) n1 = *(const bf8*)(xa + (k + 1) * 2048 + 1024);
                    if constexpr (T == 2 && l0 >= 0) nw[0] = *(const bf8*)(wl + l0 * 2048 + ((k + 1) & 1) * 1024);
                    if constexpr (T == 3 && l1 >= 0) nw[1] = *(const bf8*)(wl + l1 * 2048 + ((k + 1) & 1) * 1024);
                }
                if constexpr (k == 0 && T == 1) { xs = xn + (size_t)(snext - 2) * 40960 + 20480 + w * 5120; xl = lds0 + Q_XA + 20480 + w * 5120; asm volatile("" : "+s"(xs), "+s"(xl)); }
                if constexpr (k == 9 && T == 1) { xs = xn + (size_t)min(snext, NSEG - 1) * 40960 + w * 5120; xl -= 20480 + 4096; asm volatile("" : "+s"(xs), "+s"(xl)); }
                if constexpr ((k == 2 || k == 13) && T == 1) { xs += 4096; xl += 4096; asm volatile("" : "+s"(xs), "+s"(xl)); }
                if constexpr (k < 3 && (T == 3 || T == 5) && 2 * k + (T == 5) < 5) dma_one<((2 * k + (T == 5)) & 3) * 1024>(xs, xl, lane * 16);
                if constexpr (k >= 9 && k < 14 && T == 4) dma_one<((k - 9) & 3) * 1024>(xs, xl, lane * 16);
                if constexpr (CI(SOFT) && k == 10 && T == 1) {
                    if (w == 1) st4_lane0(mine + (T_SLOT + 1) * SLOT + (16 + h) * 128, took);
                }
                if constexpr (CI(SOFT) && k == 16 && T == 4) fetch_stats();
                if constexpr (CI(SOFT) && k == 11 && T == 1) own = __hip_atomic_load((const gu64*)(mine + sch + (sprev - 1) * 128 + 8), __ATOMIC_RELAXED, __HIP_MEMORY_SCOPE_AGENT);
                if constexpr (CI(SOFT) && k == 16 && T == 1) { tvis = __builtin_amdgcn_readfirstlane((u32)own + (u32)(own >> 32)) + RECV; asm volatile("" : "+s"(tvis)); }
                if constexpr (CI(SOFT) && k == PKK && T == PKT) peek = sched_peek(mine + sch + ((int)(rt32() - tvis) >= 0 ? sprev * 128 + 4 : (sprev - 1) * 128 + 16));
                if constexpr (CI(SOFT) && k == 15 && T >= 3) next(snext - 2, T - 3);
                if constexpr (CI(SOFT) && k == 19 && T == 3) next(snext - 2, 3);
                SCHEDB();
                static_for<0, 2>([&](auto U) {
                    constexpr int u = CI(U);
                    const bf8 xx = u ? x1 : x0;
                    if constexpr (id < 32) {
                        const bf8 wf = (k & 1) ? hi8(WA[id]) : lo8(WA[id]);
                        if constexpr (k == 0) MMA0_AV(acc[T][u], wf, xx); else MMA_AV(acc[T][u], wf, xx);
                    } else {
                        bf8 wf;
                        if constexpr (id < 48) wf = (k & 1) ? hi8(WV[id - 32]) : lo8(WV[id - 32]); else wf = lw[rk];
                        if constexpr (k == 0) MMA0_VV(acc[T][u], wf, xx); else MMA_VV(acc[T][u], wf, xx);
                    }
                    SCHEDB();
                    if constexpr (CI(SOFT) && k * 12 + T * 2 + u < SmTok::N) smt.template piece<k * 12 + T * 2 + u>(xdst, xoff);
                    if constexpr (CI(SOFT) && k == 8) rearm((snext >> 1) + 2, T * 2 + u, ao, sent);
                    if constexpr (k == 19 && T >= 2) pack(T - 2, u);
                    SCHEDB();
                });
            });
            if constexpr (k < 19) { x0 = n0; x1 = n1; if constexpr (l0 >= 0) lw[0] = nw[0]; if constexpr (l1 >= 0) lw[1] = nw[1]; }
        });
        TR(11);
        pack(4, 0); pack(4, 1);
        asm volatile("s_nop 3");
        pack(5, 0); pack(5, 1);
        if constexpr (CI(SOFT)) ytime = __builtin_amdgcn_readfirstlane(peek);
    };
    {
        const f32x4 neg = {NEG_BIG, NEG_BIG, NEG_BIG, NEG_BIG};
        *(f32x4*)((float*)(shm + Q_PART) + (w * 40 + 8 + fr) * Q_PS + g * 8 + 4) = neg;
        if (lane < 40) *(f32x4*)((float*)(shm + Q_PART) + (w * 40 + lane) * Q_PS + 40) = neg;
        *(f32x4*)(shm + Q_MK + lane * 16) = (f32x4){4 * g > fr ? NEG_BIG : 0.f, 4 * g + 1 > fr ? NEG_BIG : 0.f, 4 * g + 2 > fr ? NEG_BIG : 0.f, 4 * g + 3 > fr ? NEG_BIG : 0.f};
    }
    float* pw0 = (float*)(shm + Q_PART) + (w * 40 + (fr & 7)) * Q_PS + g * 8 + (fr >> 3) * 4;
    float* pw2 = fr < 8 && g >= 2 ? (float*)(shm + Q_PART) + (w * 40 + fr) * Q_PS + 24 + g * 4 : (float*)(shm + Q_TR + w * 1536) + lane * 4;
    const float* prd = (const float*)(shm + Q_PART) + ((w & 1) * 4 + g) * Q_PS + min(fr, 10) * 4;
    const float* prt = (const float*)(shm + Q_PART) + (8 + 8 * w + g) * Q_PS + min(fr, 10) * 4;
    u32 trw = lds0 + Q_TR + w * 1536 + lane * 8, trr = lds0 + Q_TR + w * 1536 + (fr & 3) * 128 + (4 * g + (fr >> 2)) * 8;
    u32 trh = lds0 + Q_TR + w * 1536 + 1024 + (lane & 31) * 8 + (lane >> 5) * 4;
    u32 pw3 = lds0 + Q_PART + ((w * 40 + 8 + (lane >> 5) * 16 + fr) * Q_PS + 32 + (g & 1) * 4) * 4;
    u32 pp = lds0 + Q_PART + ((w * 40 + 8 + fr) * Q_PS + g * 8) * 4;
    asm volatile("" : "+v"(trw), "+v"(trr), "+v"(trh), "+v"(pw3), "+v"(pp));
    const u32 poff = p_line(h, fr >> 1, SLOT) + (w & 1) * 64 + g * 16 + (fr & 1) * (PCS * SLOT);

    for (int layer = 0; layer < 2; ++layer) {
    if (layer > 0) {
        if (w == 0) {
            __builtin_amdgcn_s_sleep(127);
            wait_rt(sched_get(~0u, mine + (T_SLOT + 1) * SLOT + 50 * 128, cap) + T_TOK);
            wait_first_tokens(p, set, lane, cap);
        }
        RAW_BARRIER();
        sch += 5 * SLOT; xn += ACT;
        TT(set * 128 + h, 1);
        dma_kb<5>(xn + (size_t)set * 40960 + w * 5120, lds0 + Q_XA + w * 5120, lane * 16);
    }
    VM0(); RAW_BARRIER();
    TT(set * 128 + h, 2);
    xproj(set + 2, set - 1, std::integral_constant<int, 0>(), nullptr);
    if (layer == 0) { phys = set ^ get_side(p, cap); mine += phys * 8192; }
    next(set, 0);
    if (set == 0) mb0 = p.hop + M0_OFF + phys * 8192 + 2 * w * SLOT + 2048;
    next(set, 1); next(set, 2); next(set, 3);
    ytime = set ? ~0u : rt32();
    TT(set * 128 + h, 3);
    for (int s = set; s < NSEG; s += 2) {
        const int rot = (s >> 1) & 3;
        TR(0);
        int early = 0;
        if (ytime == ~0u) { early = SLK; wait_rt(tvis); ytime = sched_get(sched_peek(mine + sch + (s - 1) * 128 + 4), mine + sch + (s - 1) * 128 + 4, cap); }
        f32x4 t[3];
        t[0] = tmask;
        PIN(t[0]); PIN(QX[0]); PIN(QX[1]); PIN(KX[0]); PIN(KX[1]); PIN(QY[0]); PIN(QY[1]); PIN(KY[0]); PIN(KY[1]);
        int tight = 0;
        if (w == 0) wait_rt(ytime);
        else {
            const int sl = (int)(ytime - rt32()) + early;
            tight = SLK - smax(-smax(-sl, -SLK), 0);
        }
        __builtin_amdgcn_s_barrier();
        TR(1); TRV(7, ytime);
        f32x4 acc[6];
        u32x4 A[10];
        bf8 Ll[2], Lh[2];
        s16x4 F[4];
        u32 r0[4], r1[4], d[2];
        float v[4];
        int tries = 0;
        auto fin = [&](const int pr, const int i) {
            if (i == 0) { PIN(acc[2 * pr]); PIN(acc[2 * pr + 1]); }
            if (i < 4) { auto r = __builtin_amdgcn_permlane32_swap(__float_as_uint(acc[2 * pr][i]), __float_as_uint(acc[2 * pr + 1][i]), false, false); r0[i] = r[0]; r1[i] = r[1]; }
            const int a0 = pr == 0 ? i - 4 : i - 1;
            if (a0 >= 0 && a0 < 4) asm volatile("v_add_f32 %0, %1, %2" : "=v"(v[a0]) : "v"(r0[a0]), "v"(r1[a0]));
            if (a0 == 3) d[0] = pk2bf(v[0], v[1]);
            if (a0 == 4) {
                d[1] = pk2bf(v[2], v[3]);
                *(LDS3 u32x2*)(uintptr_t)(trw + pr * 512) = (u32x2){d[0], d[1]};
                F[pr] = __builtin_amdgcn_ds_read_tr16_b64_v4i16((LDS3 s16x4*)(uintptr_t)(trr + pr * 512));
            }
        };
        auto fin1 = [&](const int T) {
            asm volatile("v_permlane32_swap_b32 %2, %4\n\tv_permlane32_swap_b32 %3, %5\n\tv_add_f32 %0, %2, %4\n\tv_add_f32 %1, %3, %5\n\tv_cvt_pk_bf16_f32 %0, %0, %1"
                         : "=&v"(v[0]), "=&v"(v[1]) : "v"(acc[T][0]), "v"(acc[T][1]), "v"(acc[T][2]), "v"(acc[T][3]));
            *(LDS3 float*)(uintptr_t)(trh + (T - 4) * 256) = v[0];
            F[T - 2] = __builtin_amdgcn_ds_read_tr16_b64_v4i16((LDS3 s16x4*)(uintptr_t)(trr + 1024));
        };
        auto slot = [&](auto SL) {
            constexpr int sl = CI(SL), T = slot_T(sl), ks = slot_ks(sl), id = slot_frag(sl);
            if constexpr (id < 32) SMM_A(acc[T], A[ks], WA[id], sidx);
            else if constexpr (id < 48) SMM_V(acc[T], A[ks], WV[id - 32], sidx);
            else { const bf16v L = cat8(Ll[id & 1], Lh[id & 1]); SMM_V(acc[T], A[ks], L, sidx); }
            SCHEDB();
            if constexpr (sl == 0) TRC(6);
            constexpr int n0 = slot_lds(sl), n1 = slot_lds(sl - 1);
            if constexpr (n0 >= 0 && n0 < NLF - 2) Ll[n0 & 1] = *(const bf8*)(wl + (n0 + 2) * 2048);
            if constexpr (n1 >= 0 && n1 < NLF - 2) Lh[n1 & 1] = *(const bf8*)(wl + (n1 + 2) * 2048 + 1024);
            if constexpr (sl >= 43 && sl < 52) fin(0, sl - 43);
            if constexpr (sl >= 52 && sl < 58) fin(1, sl - 52);
            if constexpr (sl == F4S) fin1(4);
            SCHEDB();
        };
        auto gather = [&](const bool first) {
            u32 vo = lane * 16;
            asm volatile("" : "+v"(vo) :: "memory");
#pragma unroll
            for (int ks = 0; ks < 10; ++ks) {
                A[ks] = ld16(ks < 5 ? mb0 : mb1, vo, (ks % 5 - 2) * 1024);
                SCHEDB();
                if (first) {
                    if (ks == 0) {
                        MMA0_VV(t[1], KX[0], QX[1]);
                        asm volatile("v_mfma_f32_16x16x32_bf16 %0, %1, %2, %3" : "=&v"(t[2]) : "v"(KX[1]), "v"(QX[1]), "v"(t[0]));
                        MMA_VV(t[0], KX[0], QX[0]);
                    }
                    if (ks == 2) { MMA16(t[1], KY[0], QY[1]); MMA16(t[2], KY[1], QY[1]); MMA16(t[0], KY[0], QY[0]); }
                    if (ks == 4) { PIN(t[1]); *(LDS3 f32x4*)(uintptr_t)(pp + 16 * Q_PS * 4) = t[1]; }
                    if (ks == 5) { PIN(t[2]); *(LDS3 f32x4*)(uintptr_t)(pp + 16 * Q_PS * 4 + 16) = t[2]; }
                    if (ks == 6) { PIN(t[0]); *(LDS3 f32x4*)(uintptr_t)pp = t[0]; }
                    if (ks == 1 || ks == 3 || ks == 7 || ks == 8) asm volatile("s_nop %0" :: "n"(PACE2));
                } else if (ks < 9) asm volatile("s_nop %0" :: "n"(PACE));
                if (ks == 3 || ks == 7) { Ll[ks >> 2] = *(const bf8*)(wl + (ks >> 2) * 2048); Lh[ks >> 2] = *(const bf8*)(wl + (ks >> 2) * 2048 + 1024); }
                if (ks == 1) { ZERO4(acc[0]); ZERO4(acc[1]); }
                if (ks >= 5 && ks < 9) ZERO4(acc[ks - 3]);
                SCHEDB();
            }
        };
        gather(true);
        static_for<0, 42>(slot);
        int passed = 0;
        auto record = [&](const u32 xt, const u32 yt) {
            u32 ln8 = lane;
            asm volatile("" : "+v"(ln8));
            ln8 *= 8192;
            const u32x4 rec = {nosent(xt), nosent(yt), (u32)(d1 + MG1), yt};
            k1 = d1 + MG1;
            char* at = p.hop + (S_SLOT + layer * 5) * SLOT + s * 128;
            asm volatile("s_waitcnt lgkmcnt(0)\n\ts_barrier\n\ts_mov_b64 exec, 0x3ff\n\ts_nop 1\n\tglobal_store_dwordx4 %0, %1, %2 sc1\n\ts_mov_b64 exec, -1\n\ts_nop 0"
                         :: "v"(ln8), "v"(rec), "s"(at) : "memory");
            passed = 1;
        };
        u64 bad, bad2;
        asm volatile("v_cmp_u_f32 %0, %2, %3\n\tv_cmp_u_f32 %1, %4, %5" : "=s"(bad), "=s"(bad2) : "v"(acc[0][0]), "v"(acc[0][1]), "v"(acc[0][2]), "v"(acc[0][3]));
        static_for<42, 43>(slot);
        asm volatile("" : "+s"(bad), "+s"(bad2));
        if (__builtin_expect((bad | bad2) != 0, 0)) {
            {
                SETTLE(); CNT(0);
                while (true) {
                    gather(false);
                    u32 m = 0;
#pragma unroll
                    for (int ks = 0; ks < 10; ++ks) m = max4(m, A[ks]);
                    ++tries;
                    if (!__any(m == ~0u)) break;
                    if (tries >= cap) { cap = 1; break; }
                    __builtin_amdgcn_s_sleep(4);
                }
                static_for<0, 43>(slot);
            }
        }
        TR(2); TRV(6, tries + 1);
        static_for<43, 60>(slot);
        TRC(7);
        bf8 F01 = __builtin_bit_cast(bf8, __builtin_shufflevector(F[0], F[1], 0, 1, 2, 3, 4, 5, 6, 7));
        f32x4 s0, s1, s2, u0, u1;
        PIN(F01);
        MMA0_VV(s1, KX[1], F01); MMA0_VV(s0, KX[0], F01);
        SCHEDB();
        fin1(5);
        SCHEDB();
        MMA16(s1, KY[1], F[2]); MMA16(s0, KY[0], F[2]);
        MMA0_VV(s2, F01, F01); MMA0_VV(u0, F01, QX[0]); MMA0_VV(u1, F01, QX[1]);
        asm volatile("" : "+v"(s0), "+v"(s1));
#pragma unroll
        for (int i = 0; i < 4; ++i) s0[i] = __int_as_float(__builtin_amdgcn_update_dpp(__float_as_int(s0[i]), __float_as_int(s1[i]), 0x128, 0xf, 0xc, false));
        SCHEDB();
        MMA16(s2, F[3], F[3]);
        SCHEDB();
        *(f32x4*)pw0 = s0; SCHEDB();
        MMA16(u0, F[3], QY[0]); MMA16(u1, F[3], QY[1]);
        asm volatile("s_nop 1");
        PIN(s2); *(f32x4*)pw2 = s2; SCHEDB();
        asm volatile("s_nop 4" : "+v"(u0), "+v"(u1));
        f32x4 u;
#pragma unroll
        for (int i = 0; i < 4; ++i) u[i] = __uint_as_float(__builtin_amdgcn_permlane32_swap(__float_as_uint(u0[i]), __float_as_uint(u1[i]), false, false)[1]);
        PIN(u);
        auto uwrite = [&]() { *(LDS3 f32x4*)(uintptr_t)pw3 = u; };
        if (__builtin_expect(soft != 0, 1)) {
            RAW_BARRIER();
            TR(3);
            softmax_mem(prd, pdst, pdst2, poff, uwrite);
            TR(4);
        } else {
            RAW_BARRIER();
            TR(3);
            uwrite();
            if (h == 0 && s < NSEG - 1) {
                hold = tries > 0 || s < 2 || ytime - ylast > 2u * (d1 + d2) + PAUSE ? HOLD : smax(hold - 1, 0);
                ylast = ytime;
                if (w == 3) {
                    const u64 dd = *(const u64*)(shm + Q_ST + 504);
                    const u32 xt = tries == 0 && s > 0 ? ytime + k1 : rt32() + (s > 0 ? ELATE : E0);
                    d1 = __builtin_amdgcn_readfirstlane((u32)dd); d2 = __builtin_amdgcn_readfirstlane((u32)(dd >> 32));
                    record(xt, xt + d2 + MG2 + (tries > 0 ? EY : 0) + hold * BOOST);
                } else {
                    u32 lo = lane * 4;
                    asm volatile("" : "+v"(lo));
                    int sta = *(const int*)(shm + Q_ST + lo), stb = *(const int*)(shm + Q_ST + 256 + lo);
                    asm volatile("s_nop 1\n\t" DPP2("v_max_i32", "8") DPP2("v_max_i32", "4") DPP2("v_max_i32", "2") DPP2("v_max_i32", "1") : "+v"(sta), "+v"(stb));
                    const int a0 = __builtin_amdgcn_readlane(sta, 0), a1 = __builtin_amdgcn_readlane(sta, 16), a2 = __builtin_amdgcn_readlane(sta, 32), a3 = __builtin_amdgcn_readlane(sta, 48);
                    const int b0 = __builtin_amdgcn_readlane(stb, 0), b1 = __builtin_amdgcn_readlane(stb, 16), b2 = __builtin_amdgcn_readlane(stb, 32);
                    SCHEDB();
                    const int down = smax(DOWN - hold * DOWN, 0);
                    if (hold == HOLD && s > 2) { d1 += BUMP; d2 += BUMP; }
                    else { d1 = track(d1, smax(b1, b2), down); d2 = track(d2, smax(smax(smax(a0, a1), smax(a2, a3)), b0), down); }
                    *(u64*)(shm + Q_ST + 504) = (u64)(u32)d1 | ((u64)(u32)d2 << 32);
                }
            }
            if (w == 2 && s < 2 && layer > 0) wait_all_tokens(p, lane, cap);
        }
        u64 tpub = 0;
        if (__builtin_expect(passed == 0, 1)) asm volatile("s_memrealtime %0\n\ts_waitcnt lgkmcnt(0)\n\ts_barrier" : "=s"(tpub) :: "memory");
#pragma unroll
        for (int j = 0; j < 8; ++j) smt.load(prt, j);

        took = tries == 0 && s > 0 ? (u32)tpub - ytime + tight : 0;
        char* xdst = px + (size_t)(set * 4 + rot) * PX_BUF + w * 20480;
        TR(5); TRFLUSH(set * 128 + h);
        if (s + 2 >= NSEG) {
            static_for<0, SmTok::N>([&](auto I) { smt.template piece<CI(I)>(xdst, xoff); SCHEDB(); if constexpr ((CI(I) & 1) && ((CI(I) >= 26 && CI(I) < 36) || (CI(I) >= 58 && CI(I) < 68) || CI(I) > 80)) asm volatile("s_nop 0"); SCHEDB(); });
            TT(set * 128 + h, 0);
            __builtin_amdgcn_s_sleep(64);
            char* ao;
            u32x4 sent;
#pragma unroll
            for (int i = 0; i < 10; ++i) rearm(rot, i, ao, sent);
            break;
        }
        xproj(s + 4, s + 1, std::integral_constant<int, 1>(), xdst);
    }
    }
}

typedef __attribute__((ext_vector_type(2))) float f32x2;
__device__ __forceinline__ f32x2 merge2(const f32x4& a) {
    f32x2 r;
    asm volatile("s_nop 7\n\tv_permlane32_swap_b32 %2, %4\n\tv_permlane32_swap_b32 %3, %5\n\ts_nop 0\n\t"
                 "v_add_f32_dpp %0, %4, %2 row_ror:8 row_mask:0xf bank_mask:0xf\n\tv_add_f32_dpp %1, %5, %3 row_ror:8 row_mask:0xf bank_mask:0xf"
                 : "=&v"(r[0]), "=&v"(r[1]) : "v"(a[0]), "v"(a[1]), "v"(a[2]), "v"(a[3]));
    return r;
}
constexpr int C_YL = 0, C_RED = 40960, C_EX = 49152, C_XN = 53248, C_M0 = 135168;
__device__ __forceinline__ void comb_role(const LaParams& p, const int set, const int c, char* shm) {
    const int lane = threadIdx.x & 63, w = __builtin_amdgcn_readfirstlane(threadIdx.x >> 6);
    const int fr = lane & 15, g = lane >> 4;
    const u32 lds0 = (u32)(uintptr_t)(LDS3 char*)shm;
    const int sidx = (lane & 8) ? 0xEEEE : 0x4444;
    int cap = SPIN_CAP;
    char* mine = p.hop;
    char* other = p.hop;
    int phys = 0, sch = (S_SLOT + w) * SLOT;
    u32 k1 = D1I + MG1;
    char* px = (wsp(p) + O_PX);
    const char* xn = (wsp(p) + O_XN);
    char* const yl0 = shm + C_YL + lane * 16;
    int cb = (set * 80 + c) % 10, rb = (set * 80 + c) / 10;

    {
        const int t = threadIdx.x, h = (set * 80 + c) / 5, cu = c % 5;
        u32x4 st[12];
        const u16* src = p.Wo + ((size_t)h * DH + (t >> 4)) * DM + cu * 128 + (t & 15) * 8;
#pragma unroll
        for (int i = 0; i < 12; ++i) asm volatile("global_load_dwordx4 %0, %1, off" : "=v"(st[i]) : "v"(src + (size_t)i * 16 * DM) : "memory");
        u32 vo[12];
#pragma unroll
        for (int i = 0; i < 12; ++i) {
            const int id = i * 64 + lane, r = id / 24;
            vo[i] = kperm((r >> 2) & 3, (r >> 4) * 4 + (r & 3)) * (HD * 2) + ((id % 24) ^ (r & 7)) * 16;
        }
        const u32 ring = lds0 + 57344 + w * 24576;
        auto issue = [&](const int j) {
            const char* a = (const char*)p.Wv + (size_t)(((j + cu) % 5) * 4 + w) * 32 * HD * 2 + h * 384;
#pragma unroll
            for (int i = 0; i < 12; ++i) dma_gather(a, ring + (j & 1) * 12288 + i * 1024, vo[i]);
        };
        SCHEDB();
        issue(0);
        const u32 ra = ring + fr * 384 + ((g ^ (fr & 3)) + (fr & 4)) * 16;
        const int e = t >> 4, pr = ((e >> 2) & 1) * 8 + (e >> 3) * 4 + (e & 3);
        asm volatile("s_waitcnt vmcnt(12)" : "+v"(st[0]), "+v"(st[1]), "+v"(st[2]), "+v"(st[3]), "+v"(st[4]), "+v"(st[5]),
                     "+v"(st[6]), "+v"(st[7]), "+v"(st[8]), "+v"(st[9]), "+v"(st[10]), "+v"(st[11]) :: "memory");
#pragma unroll
        for (int i = 0; i < 12; ++i) *(u32x4*)(shm + (i * 16 + pr) * 288 + (t & 15) * 16) = st[i];
        issue(1);
        RAW_BARRIER();
        { const int layer = 0; TT(set * 128 + 32 + c, 12); }
        bf8 B[8][6], A[2][2][6];
        const u32 rd = lds0 + ((g >> 1) * 16 + (g & 1) * 4 + (fr >> 2)) * 288 + (fr & 3) * 8;
#pragma unroll
        for (int n = 0; n < 8; ++n)
#pragma unroll
            for (int k = 0; k < 6; ++k) B[n][k] = tr8(rd + k * 32 * 288 + n * 32, rd + (k * 32 + 8) * 288 + n * 32);
        auto lda = [&](const int j, const int k) {
#pragma unroll
            for (int u = 0; u < 2; ++u) A[j & 1][u][k] = *(LDS3 const bf8*)(uintptr_t)((ra ^ ((k & 1) * 64)) + (j & 1) * 12288 + u * 6144 + (k >> 1) * 128);
        };
        asm volatile("s_waitcnt vmcnt(12)" ::: "memory");
#pragma unroll
        for (int k = 0; k < 6; ++k) lda(0, k);
        f32x4 cc[2][2];
        char* dst;
        const char* nsrc;
        static_for<0, 41>([&](auto N) {
            constexpr int n = CI(N) % 8, j = CI(N) / 8, q = CI(N) - 1;
            if constexpr (n == 7 && j < 4) asm volatile("s_waitcnt vmcnt(%0)" :: "n"(j == 0 ? 18 : (j < 3 ? 21 : 9)) : "memory");
            if constexpr (n == 1) { dst = wsp(p) + O_WVO + ((size_t)(h * 20 + ((j + cu) % 5) * 4 + w) * 40 + cu * 8 + 4) * 1024; asm volatile("" : "+s"(dst)); }
            if constexpr (n == 0 && j < 3) { nsrc = (const char*)p.Wv + (size_t)(((j + 2 + cu) % 5) * 4 + w) * 32 * HD * 2 + h * 384; asm volatile("" : "+s"(nsrc)); }
            u32x4 o;
#pragma unroll
            for (int k = 0; k < 6; ++k) {
                if constexpr (j < 5) {
#pragma unroll
                    for (int u = 0; u < 2; ++u) { if (k == 0) MMA0_VA(cc[n & 1][u], A[j & 1][u][k], B[n][k]); else MMA_VA(cc[n & 1][u], A[j & 1][u][k], B[n][k]); }
                } else if (k == 0) asm volatile("s_nop 7");
                SCHEDB();
                if constexpr (j < 3 && n >= 1 && n < 7) {
                    if (k == 0 || k == 2) asm volatile("s_mov_b32 m0, %2\n\ts_nop 0\n\tglobal_load_lds_dwordx4 %0, %1" :: "v"(vo[2 * (n - 1) + k / 2]), "s"(nsrc), "s"(ringp(ring, (j & 1) * 12288 + (2 * (n - 1) + k / 2) * 1024)) : "memory");
                }
                if constexpr (n == 7 && j < 4) lda(j + 1, k);
                if constexpr (q >= 0) {
                    if (k == 3) { PIN(cc[q & 1][0]); o[0] = pk2bf(cc[q & 1][0][0], cc[q & 1][0][1]); o[1] = pk2bf(cc[q & 1][0][2], cc[q & 1][0][3]); }
                    if (k == 4) { PIN(cc[q & 1][1]); o[2] = pk2bf(cc[q & 1][1][0], cc[q & 1][1][1]); o[3] = pk2bf(cc[q & 1][1][2], cc[q & 1][1][3]); }
                    if (k == 5) __builtin_amdgcn_global_store_b128((gu32x4*)(dst + (u32)(lane * 16) + (q % 8 - 4) * 1024), o, "agent");
                }
                SCHEDB();
            }
        });
        { const int layer = 0; TT(set * 128 + 32 + c, 13); }
        VM0(); RAW_BARRIER();
        { const int layer = 0; TT(set * 128 + 32 + c, 14); }
        if (w == 0) st4_lane0(wsp(p) + O_FL + (400 + set * 80 + c) * 128, 1u);
        dma_kb<10>(xn + (size_t)set * 40960 + w * 10240, lds0 + C_XN + w * 10240, lane * 16);
        if (w == 0) {
            __builtin_amdgcn_s_sleep(8);
            const u32 off[1] = {(u32)(400 + (lane & 31) * 5 + (c >> 4)) * 128};
            wait_flags<1, FSL>(wsp(p) + O_FL, off, cap);
        }
        RAW_BARRIER();
        { const int layer = 0; TT(set * 128 + 32 + c, 15); }
    }
    bf16v WA[32], WV[8];
    {
        u32 vo = (w * 8 + (fr >> 3)) * 819200 + g * 256 + (fr & 7) * 16;
        asm volatile("" : "+v"(vo));
        auto ldw = [&](const int ks, const int t) {
            const char* a = wsp(p) + O_WVO + (size_t)(t * 1600 + ks * 80 + (c >> 1)) * 1024 + (c & 1) * 128;
            const char* b = a + 40960;
            asm volatile("" : "+s"(b));
            return cat8(__builtin_bit_cast(bf8, ld16(a, vo, 0)), __builtin_bit_cast(bf8, ld16(b, vo, 0)));
        };
#pragma unroll
        for (int i = 0; i < 32; ++i) WA[i] = ldw(i >> 2, i & 3);
#pragma unroll
        for (int i = 0; i < 8; ++i) WV[i] = ldw(8 + (i >> 2), i & 3);
    }
    auto yx = [&](f32x4 (&acc)[4][2], const int sy, const int tile, auto&& extra) {
        const char* xa = shm + tile + lane * 16;
        const char* xs = (sy + 2 < NSEG ? xn + (size_t)(sy + 2) * 40960 : (wsp(p) + O_WO) + cb * 81920) + w * 10240;
        u32 xl = lds0 + C_XN + (~(sy >> 1) & 1) * 40960 + w * 10240;
        bf8 x0 = *(const bf8*)(xa), x1 = *(const bf8*)(xa + 1024);
        const char* xs3[3] = {xs, xs + 4096, xs + 8192};
        u32 xl3[3] = {xl, xl + 4096, xl + 8192};
        asm volatile("" : "+s"(xs3[0]), "+s"(xs3[1]), "+s"(xs3[2]), "+s"(xl3[0]), "+s"(xl3[1]), "+s"(xl3[2]));
        static_for<0, 20>([&](auto K) {
            constexpr int k = CI(K);
            bf8 n0, n1;
            static_for<0, 4>([&](auto TT_) {
                constexpr int t = CI(TT_), id = (k >> 1) * 4 + t, dn = 2 * (k - 3) + t - 2;
                if constexpr (k < 19 && t == 0) n0 = *(const bf8*)(xa + (k + 1) * 2048);
                if constexpr (k < 19 && t == 1) n1 = *(const bf8*)(xa + (k + 1) * 2048 + 1024);
                if constexpr (t >= 2 && k >= 3 && k < 8) dma_one<(dn & 3) * 1024>(xs3[dn >> 2], xl3[dn >> 2], lane * 16);
                if constexpr (t >= 2) extra(2 * k + t - 2);
                SCHEDB();
                if constexpr (id < 32) {
                    const bf8 wf = (k & 1) ? hi8(WA[id]) : lo8(WA[id]);
                    if constexpr (k == 0) { MMA0_VA(acc[t][0], x0, wf); MMA0_VA(acc[t][1], x1, wf); } else { MMA_VA(acc[t][0], x0, wf); MMA_VA(acc[t][1], x1, wf); }
                } else {
                    const bf8 wf = (k & 1) ? hi8(WV[id - 32]) : lo8(WV[id - 32]);
                    MMA_VV(acc[t][0], x0, wf); MMA_VV(acc[t][1], x1, wf);
                }
                SCHEDB();
            });
            if constexpr (k < 19) { x0 = n0; x1 = n1; }
        });
    };

    for (int layer = 0; layer < 2; ++layer) {
    u32 ynext = ~0u;
    if (layer > 0 && set == 0) k1 = sched_get(~0u, mine + sch - 5 * SLOT + 62 * 128 + 8, cap);
    VM0(); RAW_BARRIER();
    TT(set * 128 + 32 + c, 11);
    if (layer > 0 && w == 0) st4_lane0(wsp(p) + O_FL + (192 + rb * 10 + cb) * 128, 1u);
    {
        f32x4 ya[4][2];
        yx(ya, set, layer ? C_YL : C_XN, [](int) {});
        asm volatile("s_nop 7");
        RAW_BARRIER();
        if (layer > 0) *(bf8*)(yl0 + (w * 5 + 4) * 1024) = *(const bf8*)(shm + C_M0 + w * 1024 + lane * 16);
#pragma unroll
        for (int t = 0; t < 4; ++t) { PIN(ya[t][0]); PIN(ya[t][1]); *(bf8*)(yl0 + (w * 5 + t) * 1024) = pack8(ya[t][0], ya[t][1]); }
        RAW_BARRIER();
    }
    if (layer == 0) { phys = set ^ get_side(p, cap); mine += phys * 8192; other += (phys ^ 1) * 8192; }
    TT(set * 128 + 32 + c, 10);
    for (int s = set; s < NSEG; s += 2) {
        const int rot = (s >> 1) & 3;
        char* yl = yl0 + (rot & 1) * 20480;
        TRDECL; TR(0);
        const char* mb0 = (s > 0 ? mine + (M_SLOT0 + rot * 8) * SLOT : p.hop + M0_OFF + phys * 8192) + 2 * w * SLOT + 2048;
        const char* mb1 = mb0 + SLOT;
        const char* pb[3];
#pragma unroll
        for (int i = 0; i < 3; ++i) pb[i] = mine + (P_SLOT0 + (rot * 4 + c / 20) * PCS + i) * SLOT + 4096;
        const char* ph = w == 1 ? pb[1] + 2048 : pb[2] + (w - 4) * 2048;
        char* dstb = other + (M_SLOT0 + (((s + 1) >> 1) & 3) * 8 + c / 40) * SLOT + (c % 40) * 128;
        asm volatile("" : "+s"(mb0), "+s"(mb1), "+s"(pb[0]), "+s"(pb[1]), "+s"(pb[2]), "+s"(ph), "+s"(dstb));
        u32 yt = 0;
        bool late = s == 0;
        if (s > 0) {
            yt = sched_get(ynext, mine + sch + (s - 1) * 128 + 4, cap);
            wait_rt(yt + CLAG);
        }
        auto ldy = [&](const int f) { return *(const bf8*)(yl + (f < 16 ? (f >> 2) * 5 + (f & 3) : (f - 16) * 5 + 4) * 1024); };
        bf8 Y0[14];
        TR(1);
        if (s == 0 && layer > 0) {
#pragma unroll
            for (int i = 0; i < 14; ++i) Y0[i] = ldy(i);
        } else {
            f32x4 acc[4];
            u32x4 A[10];
            auto gather = [&]() {
                u32 vo = lane * 16;
                asm volatile("" : "+v"(vo) :: "memory");
#pragma unroll
                for (int ks = 0; ks < 10; ++ks) {
                    A[ks] = ld16(ks < 5 ? mb0 : mb1, vo, (ks % 5 - 2) * 1024);
                    SCHEDB();
                    if (ks < 4) ZERO4(acc[ks]);
                    if (ks < 9) asm volatile("s_nop %0" :: "n"(PACE));
                    SCHEDB();
                }
            };
            auto mproj = [&]() {
#pragma unroll
                for (int ks = 0; ks < 10; ++ks) {
#pragma unroll
                    for (int t = 0; t < 4; ++t) { if (ks < 8) SMM_A(acc[t], A[ks], WA[(ks * 4 + t) & 31], sidx); else SMM_V(acc[t], A[ks], WV[(ks * 4 + t) & 7], sidx); }
                }
                asm volatile("s_nop 7");
#pragma unroll
                for (int t = 0; t < 4; ++t) PIN(acc[t]);
            };
            gather();
#pragma unroll
            for (int i = 0; i < 14; ++i) Y0[i] = ldy(i);
            SCHEDB();
            mproj();
            if (__builtin_expect(__any(__builtin_isunordered(acc[0][0], acc[0][1]) || __builtin_isunordered(acc[0][2], acc[0][3])), 0)) {
                CNT(1);
                late = true;
                for (int tries = 1;; ++tries) {
                    gather();
                    u32 m = 0;
#pragma unroll
                    for (int ks = 0; ks < 10; ++ks) m = max4(m, A[ks]);
                    if (!__any(m == ~0u)) break;
                    if (tries >= cap) { cap = 1; break; }
                    __builtin_amdgcn_s_sleep(8);
                }
                mproj();
            }
#pragma unroll
            for (int pr = 0; pr < 2; ++pr) {
                float v[4];
#pragma unroll
                for (int i = 0; i < 4; ++i) v[i] = halfsum(acc[2 * pr][i], acc[2 * pr + 1][i]);
                *(u32x2*)(shm + C_YL + (rot & 1) * 20480 + (w * 5 + 4) * 1024 + ((2 * pr + (g >> 1)) * 16 + fr) * 16 + (g & 1) * 8) = (u32x2){pk2bf(v[0], v[1]), pk2bf(v[2], v[3])};
            }
        }
        if (s == 0) { VM0(); *(bf8*)(shm + C_M0 + w * 1024 + lane * 16) = *(const bf8*)(yl0 + (w * 5 + 4) * 1024); }
        TR(2);
        u32 took = 0, xt = yt + k1;
        if (w == 0 && s < NSEG - 1) {
            if (__builtin_expect(late, 0)) xt = sched_get(~0u, mine + sch + s * 128, cap);
            TR(3);
            wait_rt(xt);
        }
        u32 vo = lane * 16;
        PIN(vo);
        RAW_BARRIER();
        TRV(7, xt); TRC(6);
        if (s < NSEG - 1) {
            auto ldp = [&](const int f, const u32 v) { return ld16(pb[f >> 3], v, ((f & 7) - 4) * 1024); };
            f32x4 acc;
            if (w > 0) {
                const u32x4 P0 = ld16(ph, vo, 0);
                SCHEDB();
                const u32x4 P1 = ld16(ph, vo, 1024);
                SCHEDB();
                const bf8 Ya = ldy(12 + 2 * w), Yb = ldy(13 + 2 * w);
                u32 ex = lds0 + C_EX + (w - 1) * 512 + lane * 8;
                PIN(ex);
                MMA0_VV(acc, Ya, P0);
                TRC(7);
                MMA_VV(acc, Yb, P1);
                TRC(8);
                *(LDS3 f32x2*)(uintptr_t)ex = merge2(acc);
                RAW_BARRIER();
            } else {
                asm volatile("s_nop %0" :: "n"(W0D));
                u32x4 P[14];
#pragma unroll
                for (int i = 0; i < 14; ++i) { P[i] = ldp(i, vo); SCHEDB(); }
                u32 dst = g * 2 * SLOT + (lane & 7) * 16, exa = lds0 + C_EX + lane * 8;
                asm volatile("" : "+v"(dst), "+v"(exa));
                f32x2 h1, h2, h3;
#pragma unroll
                for (int i = 0; i < 14; ++i) {
                    if (i == 14 - LATEP) {
                        __builtin_amdgcn_s_barrier();
                        asm volatile("" ::: "memory");
                        h1 = *(LDS3 const f32x2*)(uintptr_t)exa; h2 = *(LDS3 const f32x2*)(uintptr_t)(exa + 512); h3 = *(LDS3 const f32x2*)(uintptr_t)(exa + 1024);
                        SCHEDB();
                    }
                    if (i == 0) MMA0_VV(acc, Y0[i], P[i]); else MMA_VV(acc, Y0[i], P[i]);
                    if (i == 0) TRC(7);
                    SCHEDB();
                }
                TRC(8);
                u32x4 o4;
                u64 bad;
                auto finish = [&]() {
                    asm volatile("v_add_f32 %0, %9, %11\n\tv_add_f32 %1, %10, %12\n\tv_add_f32 %0, %0, %13\n\tv_add_f32 %1, %1, %14\n\ts_nop 3\n\t"
                                 "v_permlane32_swap_b32 %5, %7\n\tv_permlane32_swap_b32 %6, %8\n\tv_add_f32 %0, %5, %0\n\tv_add_f32 %1, %6, %1\n\t"
                                 "v_add_f32_dpp %0, %7, %0 row_ror:8 row_mask:0xf bank_mask:0xf\n\tv_add_f32_dpp %1, %8, %1 row_ror:8 row_mask:0xf bank_mask:0xf\n\t"
                                 "v_cvt_pk_bf16_f32 %2, %0, %1\n\tv_cmp_u_f32 %4, %0, %1\n\tv_min_u32 %0, -2, %2\n\tv_min_u32 %1, -2, %2\n\ts_nop 1\n\t"
                                 "v_permlane32_swap_b32 %0, %1\n\tv_mov_b32 %2, %0\n\tv_mov_b32 %3, %1\n\ts_nop 0\n\tv_permlane16_swap_b32 %0, %2\n\tv_permlane16_swap_b32 %1, %3"
                                 : "=&v"(o4[0]), "=&v"(o4[1]), "=&v"(o4[2]), "=&v"(o4[3]), "=&s"(bad)
                                 : "v"(acc[0]), "v"(acc[1]), "v"(acc[2]), "v"(acc[3]), "v"(h1[0]), "v"(h1[1]), "v"(h2[0]), "v"(h2[1]), "v"(h3[0]), "v"(h3[1]), "s"(dstb));
                };
                finish();
                int tries = 0;
                if (__builtin_expect(bad != 0, 0)) {
                    CNT(2);
                    wait_rt(sched_get(~0u, mine + sch + s * 128, cap));
                    while (true) {
                        u32 m = 0;
                        asm volatile("" : "+v"(vo) :: "memory");
                        u32x4 P[20];
#pragma unroll
                        for (int i = 0; i < 20; ++i) P[i] = ldp(i, vo);
                        f32x4 hh[4];
#pragma unroll
                        for (int i = 0; i < 20; ++i) {
                            const bf8 y = ldy(i);
                            const int k = i < 14 ? 0 : (i - 12) / 2;
                            if (i == 0 || i == 14 || i == 16 || i == 18) MMA0_VV(hh[k], y, P[i]); else MMA_VV(hh[k], y, P[i]);
                            m = max4(m, P[i]);
                            SCHEDB();
                        }
                        asm volatile("s_nop 7" : "+v"(hh[0]), "+v"(hh[1]), "+v"(hh[2]), "+v"(hh[3]));
                        acc = hh[0]; h1 = merge2(hh[1]); h2 = merge2(hh[2]); h3 = merge2(hh[3]);
                        ++tries;
                        if (!__any(m == ~0u)) break;
                        if (tries >= cap) { cap = 1; break; }
                        __builtin_amdgcn_s_sleep(4);
                    }
                    finish();
                }
                TRV(6, tries + 1);
                st16f(dstb, dst, o4, 0x00ff00ff00ff00ffull);
                TR(4);
                took = tries == 0 && s > 0 ? rt32() - xt : 0;
            }
            if (s < 2 && layer > 0 && w == 3) wait_all_tokens(p, lane, cap);
            if (s == NSEG - 2 || s < 2) RAW_BARRIER();
        } else wait_rt((late ? rt32() : yt + k1) + T_PX);
        auto tokens = [&](auto MORE) {
            f32x4 oa[4], ya[4][2];
            u32x4 P[20];
            u32 vo = lane * 16 + w * 4096, vm = lane * 16 + 16384 + w * 1024;
            asm volatile("" : "+v"(vo), "+v"(vm));
            const char* pb[4];
#pragma unroll
            for (int i = 0; i < 4; ++i) { pb[i] = px + (size_t)(set * 4 + rot) * PX_BUF + i * 20480; asm volatile("" : "+s"(pb[i])); }
            auto ldp = [&](const int i) { P[i] = i % 5 == 4 ? ld16(pb[i / 5], vm, 0) : ld16(pb[i / 5], vo, (i % 5) * 1024); };
            int ln = lane;
            asm volatile("" : "+v"(ln));
            u32 rg, peek;
            u64 peek2;
            bf8 y[5];
            auto side = [&](const int i) {
                if (i == 4) rg = sched_peek((wsp(p) + O_XR) + layer * ACT + (size_t)s * 40960 + ((c >> 2) * 2 + (w >> 1)) * 1024 + (c & 2) * 256 + (w & 1) * 128 + (c & 1) * 4 + (u32)((ln & 2) * 128 + (ln >> 3) * 16 + (ln & 1) * 8));
                if (i >= 16 && i < 36) ldp(i - 16);
                if (i == 2 && CI(MORE) && w == 0) st4_lane0(mine + (T_SLOT + (c >> 6)) * SLOT + (c & 63) * 128, took);
                if (i == 15 && CI(MORE)) peek2 = __hip_atomic_load((const gu64*)(mine + sch + s * 128 + 8), __ATOMIC_RELAXED, __HIP_MEMORY_SCOPE_AGENT);
                if (i >= 35) y[i - 35] = *(const bf8*)(yl + (w * 5 + i - 35) * 1024);
            };
            TRC(9);
            if constexpr (CI(MORE)) yx(ya, s + 2, C_XN + (~rot & 1) * 40960, side);
            else {
#pragma unroll
                for (int i = 0; i < 40; ++i) side(i);
            }
            TRC(10);
            bf8 yn[4];
            auto outx = [&](const bool first) {
                if (!first) {
#pragma unroll
                    for (int i = 0; i < 20; ++i) PIN(P[i]);
                    NOPV();
                }
#pragma unroll
                for (int q = 0; q < 4; ++q)
#pragma unroll
                    for (int j = 0; j < 5; ++j) {
                        if (j == 0) MMA0_VV(oa[q], y[j], P[q * 5 + j]); else MMA_VV(oa[q], y[j], P[q * 5 + j]);
                        if (CI(MORE) && first && q * 5 + j >= 3 && q * 5 + j < 19) {
                            const int e = q * 5 + j - 3;
                            SCHEDB();
                            if ((e & 1) == 0) PIN(ya[e >> 2][(e >> 1) & 1]);
                            ((u32*)&yn[e >> 2])[e & 3] = pk2bf(ya[e >> 2][(e >> 1) & 1][(e & 1) * 2], ya[e >> 2][(e >> 1) & 1][(e & 1) * 2 + 1]);
                            if ((e & 3) == 3) *(bf8*)(yl0 + (~rot & 1) * 20480 + (w * 5 + (e >> 2)) * 1024) = yn[e >> 2];
                            SCHEDB();
                        }
                    }
                asm volatile("s_nop 7");
#pragma unroll
                for (int q = 0; q < 4; ++q) PIN(oa[q]);
            };
            outx(true);
            if (__builtin_expect(__any(__builtin_isunordered(oa[0][0], oa[1][0]) || __builtin_isunordered(oa[2][0], oa[3][0])), 0)) {
                CNT(3);
                for (int tries = 1;; ++tries) {
                    asm volatile("" : "+v"(vo), "+v"(vm) :: "memory");
                    u32 m = 0;
#pragma unroll
                    for (int i = 0; i < 20; ++i) ldp(i);
#pragma unroll
                    for (int i = 0; i < 20; ++i) m = max4(m, P[i]);
                    if (!__any(m == ~0u)) break;
                    if (tries >= cap) { cap = 1; break; }
                    __builtin_amdgcn_s_sleep(8);
                }
                outx(false);
            }
            if constexpr (!CI(MORE)) { if (c == 0 && w == 0) stamp2(p.hop, 48 + layer * 4 + set); }
            const bool own = (fr < 8) == (g < 2);
#pragma unroll
            for (int q = 0; q < 4; q += 2) {
                f32x4 o;
#pragma unroll
                for (int i = 0; i < 4; ++i) { const float t = fdpp<0x128>(oa[q + 1][i]); o[i] = own ? oa[q][i] : t; }
                *(f32x4*)(shm + C_RED + ((w * 2 + (g >> 1)) * 32 + (q + !own) * 8 + (fr & 7)) * 32 + (g & 1) * 16) = o;
            }
            RAW_BARRIER();
            TR(5);
            if constexpr (CI(MORE)) {
                k1 = sched_get((u32)peek2, mine + sch + s * 128 + 8, cap);
                wait_rt(__builtin_amdgcn_readfirstlane((u32)(peek2 >> 32)) + k1 + RECV);
                peek = sched_peek(mine + sch + (s + 1) * 128 + 4);
                SCHEDB();
            }
            const char* rp = shm + C_RED + (ln & 4) * 1024 + w * 256 + (ln >> 3) * 32 + (ln & 3) * 8;
            f32x2 a = (*(const f32x2*)rp + *(const f32x2*)(rp + 1024)) + (*(const f32x2*)(rp + 2048) + *(const f32x2*)(rp + 3072));
            a[0] += fdpp<0x104>(a[0]); a[1] += fdpp<0x104>(a[1]);
            const u32 y2 = pk2bf(a[0], a[1]), d = nosent(pk2bf(bflo(rg) + bflo(y2), bfhi(rg) + bfhi(y2)));
            const u32x4 o4 = {d, (u32)__builtin_amdgcn_mov_dpp(d, 0x55, 0xf, 0xf, true), (u32)__builtin_amdgcn_mov_dpp(d, 0xaa, 0xf, 0xf, true), (u32)__builtin_amdgcn_mov_dpp(d, 0xff, 0xf, 0xf, true)};
            if constexpr (CI(MORE)) { ynext = __builtin_amdgcn_readfirstlane(peek); asm volatile("" : "+s"(ynext)); }
            st16m((wsp(p) + O_X1) + layer * ACT + (size_t)s * 40960 + c * 512 + w * 128, ln * 2, o4, 0x0101010101010101ull);
            if (w == 1) {
                u32x4 v;
                asm volatile("v_mov_b64 %0, -1\n\tv_mov_b64 %1, -1" : "=v"(((u64*)&v)[0]), "=v"(((u64*)&v)[1]));
                st16m(other + (M_SLOT0 + (((s + 7) >> 1) & 3) * 8 + c / 40) * SLOT + (c % 40) * 128, (ln >> 4) * 2 * SLOT + (ln & 7) * 16, v, 0x00ff00ff00ff00ffull);
            }
            TRC(11); TRFLUSH(set * 128 + 32 + c);
        };
        if (s + 2 >= NSEG) { tokens(std::integral_constant<int, 0>()); break; }
        tokens(std::integral_constant<int, 1>());
    }
    TT(set * 128 + 32 + c, 0);
    asm volatile("" : "+s"(cb), "+s"(rb));
    dma_kb<10>((wsp(p) + O_WO) + cb * 81920 + 40960 + w * 10240, lds0 + C_XN + 40960 + w * 10240, lane * 16);
    if (w == 0) { const u32 t = sched_get(~0u, mine + (T_SLOT + 1) * SLOT + (50 + layer * 4) * 128, cap); *(u32*)(shm + C_EX) = t; }
    VM0(); RAW_BARRIER();
    wait_rt(__builtin_amdgcn_readfirstlane(*(const u32*)(shm + C_EX)) + T_OB + OB_STEP * w + (layer == 0 ? OB_COLD + 8 * w : 0));
    {
        int ln = lane;
        asm volatile("" : "+v"(ln));
        u32x4 O[20], R[2];
        u32 dw[8];
        auto ready = [&](const int T) {
            const char* ob = (wsp(p) + O_OB) + layer * ACT + (size_t)T * 20480 + 16384;
            const u32 off[1] = {(u32)(ln & 31) * 128};
            wait_flags<1>(ob, off, cap);
        };
        auto load = [&](const int T) {
            const char* ob0 = (wsp(p) + O_OB) + layer * ACT + (size_t)T * 20480 + 4096;
            const char* ob1 = ob0 + 8192;
            const char* ob2 = ob0 + 12288;
            const char* xb = (wsp(p) + O_X1) + layer * ACT + (size_t)(T >> 1) * 40960 + cb * 4096 + (T & 1) * 256;
            u32 vo = ln * 16, vr = (ln >> 4) * 256 + (ln & 15) * 8, vx = (ln >> 4) * 1024 + (ln & 15) * 16;
            asm volatile("" : "+v"(vo), "+v"(vr), "+v"(vx), "+s"(ob0), "+s"(ob1), "+s"(ob2), "+s"(xb) :: "memory");
#pragma unroll
            for (int k = 0; k < 16; ++k) { O[k] = ld16(k < 8 ? ob0 : ob1, vo, ((k & 7) - 4) * 1024); SCHEDB(); }
#pragma unroll
            for (int k = 0; k < 4; ++k) {
                const u64 lo = __hip_atomic_load((const gu64*)(ob2 + vr + k * 1024), __ATOMIC_RELAXED, __HIP_MEMORY_SCOPE_AGENT);
                const u64 hi = __hip_atomic_load((const gu64*)(ob2 + vr + k * 1024 + 128), __ATOMIC_RELAXED, __HIP_MEMORY_SCOPE_AGENT);
                O[16 + k] = (u32x4){(u32)lo, (u32)(lo >> 32), (u32)hi, (u32)(hi >> 32)};
            }
            R[0] = ld16(xb, vx, 0); R[1] = ld16(xb, vx, 512);
            SCHEDB();
        };
        auto gemm = [&]() {
            u32 wa0 = lds0 + C_XN + ln * 16, wa1 = wa0 + 40960;
            asm volatile("" : "+v"(wa0), "+v"(wa1));
            f32x4 acc[4];
            bf8 wf[8];
#pragma unroll
            for (int t = 0; t < 8; ++t) wf[t] = *(LDS3 const bf8*)(uintptr_t)(wa0 + t * 1024);
#pragma unroll
            for (int k = 0; k < 20; ++k) {
                if (k >= 16) { PIN(O[k]); NOPV(); }
#pragma unroll
                for (int t = 0; t < 4; ++t) {
                    if (k == 0) MMA0_VV(acc[t], wf[t], O[k]); else MMA_VV(acc[t], wf[(k & 1) * 4 + t], O[k]);
                    SCHEDB();
                    if (k < 18) wf[(k & 1) * 4 + t] = *(LDS3 const bf8*)(uintptr_t)((k < 8 ? wa0 : wa1) + (((k + 2) % 10) * 4 + t) * 1024);
                    SCHEDB();
                }
            }
            asm volatile("s_nop 7" : "+v"(acc[0]), "+v"(acc[1]), "+v"(acc[2]), "+v"(acc[3]));
            float chk = 0.f;
#pragma unroll
            for (int i = 0; i < 8; ++i) {
                const u32 r = R[i >> 2][i & 3], y = pk2bf(acc[i >> 1][(i & 1) * 2], acc[i >> 1][(i & 1) * 2 + 1]);
                const float a = bflo(r) + bflo(y), b = bfhi(r) + bfhi(y);
                dw[i] = pk2bf(a, b);
                chk += a;
            }
            return __any(chk != chk);
        };
        const int T0 = rb + 16 * w;
        load(T0);
        static_for<0, 2>([&](auto PASS) {
            constexpr int pass = CI(PASS);
            const int T = T0 + 64 * pass;
            TT(set * 128 + 32 + c, 1 + pass * 4);
            if (__builtin_expect(gemm(), 0)) {
                for (int tries = 0;; ++tries) {
                    ready(T); load(T);
                    u32 m = max4(max4(0, R[0]), R[1]);
#pragma unroll
                    for (int k = 0; k < 20; ++k) m = max4(m, O[k]);
                    if (!__any(m == ~0u)) break;
                    if (tries >= cap) { cap = 1; break; }
                    __builtin_amdgcn_s_sleep(6);
                }
                gemm();
            }
            TT(set * 128 + 32 + c, 2 + pass * 4);
            if (layer == 1) {
                st32((char*)p.out + (size_t)T * 16 * DM * 2 + cb * 128, (ln & 15) * (DM * 2) + (ln >> 4) * 32, (u32x4){dw[0], dw[1], dw[2], dw[3]}, (u32x4){dw[4], dw[5], dw[6], dw[7]});
                if constexpr (pass == 0) load(T + 64);
            } else {
            float s1 = 0.f, s2 = 0.f;
#pragma unroll
            for (int i = 0; i < 8; ++i) { const float a = bflo(dw[i]), b = bfhi(dw[i]); s1 += a + b; s2 = fmaf(a, a, fmaf(b, b, s2)); }
            s1 = grp_sum(s1); s2 = grp_sum(s2);
            char* stl = (wsp(p) + O_ST) + (size_t)T * 1280;
            st8m(stl + cb * 128, ln * 8, (u32x2){nosent(__float_as_uint(s1)), nosent(__float_as_uint(s2))}, 0xffffull);
            const size_t fo = ACT + (size_t)(T >> 1) * 40960 + (cb * 4 + (T & 1)) * 1024;
            const u32 lo = (ln >> 5) * 2048 + ((ln >> 4) & 1) * 512 + (ln & 15) * 16;
            if constexpr (pass == 0) __builtin_amdgcn_s_sleep(ST_LAG);
            else {
                wait_first_tokens(p, set, lane, cap);
                dma_kb<10>(xn + ACT + (size_t)set * 40960 + w * 10240, lds0 + C_YL + w * 10240, lane * 16);
            }
            for (int spin = 0;; ++spin) {
                u32 m = 0;
                s1 = 0.f; s2 = 0.f;
#pragma unroll
                for (int i = 0; i < 3; ++i) {
                    const int k = (ln >> 4) + 4 * i;
                    const u64 v = __hip_atomic_load((const gu64*)(stl + min(k, 9) * 128 + (ln & 15) * 8), __ATOMIC_RELAXED, __HIP_MEMORY_SCOPE_AGENT);
                    m = max(m, max((u32)v, (u32)(v >> 32)));
                    if (k < 10) { s1 += __uint_as_float((u32)v); s2 += __uint_as_float((u32)(v >> 32)); }
                }
                if (!__any(m == ~0u)) break;
                if (spin >= cap) { cap = 1; break; }
                __builtin_amdgcn_s_sleep(2);
            }
            TT(set * 128 + 32 + c, 3 + pass * 4);
            if constexpr (pass == 0) { if (w > 0) load(T + 64); }
            s1 = grp_sum(s1); s2 = grp_sum(s2);
            const float mu = s1 * (1.0f / DM), mr = rbf(mu), rs = ln_scale(fmaxf(fmaf(-mu, mu, s2 * (1.0f / DM)), 0.f));
            u32 dn[8];
#pragma unroll
            for (int i = 0; i < 8; ++i) dn[i] = ln_pair(dw[i], mr, rs);
            st16s((wsp(p) + O_XN) + fo, lo, (u32x4){dn[0], dn[4], dn[1], dn[5]}); st16s((wsp(p) + O_XN) + fo + 256, lo, (u32x4){dn[2], dn[6], dn[3], dn[7]});
            st16s((wsp(p) + O_XR) + fo, lo, (u32x4){dw[0], dw[4], dw[1], dw[5]}); st16s((wsp(p) + O_XR) + fo + 256, lo, (u32x4){dw[2], dw[6], dw[3], dw[7]});
            TT(set * 128 + 32 + c, 4 + pass * 4);
            if constexpr (pass == 0) { if (w == 0) { load(T + 64); asm volatile("s_waitcnt vmcnt(18)" ::: "memory"); st4_lane0(wsp(p) + O_FL + (rb * 10 + cb) * 128, 1u); } }
            }
        });
    }
    if (layer == 1) break;
    sch += 5 * SLOT; xn += ACT;
    TT(set * 128 + 32 + c, 9);
    }
}

constexpr int B_PQ = 0, B_IM = 131072, B_CS = 155648, B_IS = 176;
__device__ __forceinline__ void bw_role(const LaParams& p, const int hb, char* shm) {
    const int lane = threadIdx.x & 63, w = __builtin_amdgcn_readfirstlane(threadIdx.x >> 6);
    const int fr = lane & 15, g = lane >> 4;
    int cap = SPIN_CAP;
    if (hb > 0) {
        const char* src = (const char*)((hb & 1) ? p.Wk : p.Wq) + (size_t)(DM - 16 * BPF + (hb >> 1) * BPF) * HD * 2 + w * (BPF * 3072);
        for (int i = 0; i < BPF * 3; ++i) dma_gather(src + i * 1024, (u32)(uintptr_t)(LDS3 char*)shm + w * 1024, lane * 16);
        VM0();
    }
    if (hb == 0 && w == 0) {
        u32 best[2] = {~0u, ~0u};
        const u32 tc = rt32();
        for (int i = 0; i < 256; ++i) {
#pragma unroll
            for (int sd = 0; sd < 2; ++sd) {
                u64 t0, t1;
                u32 z = 0, one = 1;
                asm volatile("s_memtime %0\n\ts_mov_b64 exec, 1\n\ts_waitcnt lgkmcnt(0)\n\tglobal_atomic_add %2, %3, %2, %4 sc0 sc1\n\ts_waitcnt vmcnt(0)\n\t"
                             "s_memtime %1\n\ts_mov_b64 exec, -1\n\ts_waitcnt lgkmcnt(0)"
                             : "=&s"(t0), "=&s"(t1), "+v"(one) : "v"(z), "s"(p.hop + M0_OFF + sd * 8192 + 6144 + (i & 3) * 256) : "memory");
                best[sd] = min(best[sd], (u32)(t1 - t0));
            }
            const u32 el = rt32() - tc, lo = min(best[0], best[1]), hi = max(best[0], best[1]);
            if (el > 1900u || (el > 600u && lo < 700u && hi - lo > 130u && hi - lo < 420u)) break;
        }
        u32 xcc; asm volatile("s_getreg_b32 %0, hwreg(HW_REG_XCC_ID)" : "=s"(xcc));
        st4_lane0(wsp(p) + O_FL + 384 * 128, ((xcc >> 2) & 1) ^ (best[1] < best[0] ? 1u : 0u) ^ WRONG);
    }
    bf8 WA[64], WV[16];
    {
        const int t = threadIdx.x;
        float* xs = (float*)(shm + 81920);
        float* ph = xs + 128 * 61;
        for (int i = t; i < 400; i += 256) ph[i] = bf2f(p.wphi[hb * 400 + i]);
        const int j = 4 * w + (fr & 3), G = fr >> 2;
        for (int d0 = 0; d0 < DM; d0 += 128) {
            u32 v15[15];
#pragma unroll
            for (int k = 0; k < 15; ++k) { const int i = t + k * 256; v15[k] = *(const u32*)(p.wqkv + (size_t)(d0 + i / 30) * 1920 + ((i % 30) / 10) * 640 + hb * 20 + (i % 10) * 2); }
            __syncthreads();
#pragma unroll
            for (int k = 0; k < 15; ++k) { const int i = t + k * 256; float* o = xs + (i / 30) * 61 + (i % 30) * 2; o[0] = bflo(v15[k]); o[1] = bfhi(v15[k]); }
            __syncthreads();
            for (int kk = 0; kk < 4; ++kk) {
                float v[8];
                const float* xr = xs + (kk * 32 + g * 8) * 61;
                if (j < 10) {
                    const int m = j / 5, f = 5 * G + j % 5;
#pragma unroll
                    for (int e = 0; e < 8; ++e) v[e] = 0.f;
#pragma unroll 4
                    for (int i = 0; i < 20; ++i) {
                        const float pw = ph[i * 20 + f];
#pragma unroll
                        for (int e = 0; e < 8; ++e) v[e] = fmaf(xr[e * 61 + m * 20 + i], pw, v[e]);
                    }
                } else {
#pragma unroll
                    for (int e = 0; e < 8; ++e) v[e] = j < 15 ? xr[e * 61 + 40 + 5 * G + min(j, 14) - 10] : 1.0f;
                }
                *(u32x4*)(shm + ((d0 / 32 + kk) * 4 + w) * 1024 + lane * 16) = (u32x4){pk2bf(v[0], v[1]), pk2bf(v[2], v[3]), pk2bf(v[4], v[5]), pk2bf(v[6], v[7])};
            }
        }
        __syncthreads();
#pragma unroll
        for (int i = 0; i < 64; ++i) WA[i] = *(const bf8*)(shm + i * 1024 + lane * 16);
#pragma unroll
        for (int i = 0; i < 16; ++i) WV[i] = *(const bf8*)(shm + (64 + i) * 1024 + lane * 16);
        __syncthreads();
    }
    auto proj = [&](f32x4 (&acc)[4], f32x4& sq, auto&& fetch) {
        u32x4 X[20];
#pragma unroll
        for (int k = 0; k < 12; ++k) { X[k] = fetch(k); SCHEDB(); }
#pragma unroll
        for (int k = 0; k < 20; ++k) {
#pragma unroll
            for (int t = 0; t < 4; ++t) {
                if (k == 0) MMA0_AV(acc[t], WA[t], X[k]); else if (k < 16) MMA_AV(acc[t], WA[(k * 4 + t) & 63], X[k]); else MMA_VV(acc[t], WV[(k * 4 + t) & 15], X[k]);
            }
            if (k == 0) MMA0_VV(sq, X[k], X[k]); else MMA_VV(sq, X[k], X[k]);
            SCHEDB();
            if (k < 8) X[k + 12] = fetch(k + 12);
            SCHEDB();
        }
        asm volatile("s_nop 7" : "+v"(acc[0]), "+v"(acc[1]), "+v"(acc[2]), "+v"(acc[3]), "+v"(sq));
    };
    {
        f32x4 t, cs[4];
        u32x4 o1 = {0x3f803f80u, 0x3f803f80u, 0x3f803f80u, 0x3f803f80u};
        asm volatile("s_nop 1" : "+v"(o1));
        proj(cs, t, [&](int) { return o1; });
#pragma unroll
        for (int i = 0; i < 4; ++i) *(f32x4*)(shm + B_CS + i * 1024 + lane * 16) = cs[i];
    }
    char* im = shm + B_IM + w * 6144;
    const u32 imr = (u32)(uintptr_t)(LDS3 char*)im + (4 * g + (fr >> 2)) * B_IS + (fr & 3) * 8;
    typedef __attribute__((ext_vector_type(32))) u32 u32x32;
    for (int layer = 0; layer < 2; ++layer) {
        const char* sch = p.hop + (hb & 1) * 8192 + (S_SLOT + layer * 5 + 4) * SLOT;
        f32x4 kv[3][2];
#pragma unroll
        for (int i = 0; i < 6; ++i) ZERO4(kv[i >> 1][i & 1]);
        u32x32 q5;
        u32 tn = 0;
        float vq[5];
        auto phi = [&](const float* x, u32 (&ph)[5]) {
            const float mx = grp_max(fmaxf(fmaxf(fmaxf(x[0], x[1]), fmaxf(x[2], x[3])), x[4]));
            const float mn = -grp_max(-fminf(fminf(fminf(x[0], x[1]), fminf(x[2], x[3])), x[4]));
            float e[10], a = 0.f, b = 0.f;
#pragma unroll
            for (int j = 0; j < 5; ++j) {
                e[j] = __builtin_amdgcn_exp2f((x[j] - mx) * 1.4426950408889634f); a += e[j];
                e[5 + j] = __builtin_amdgcn_exp2f((mn - x[j]) * 1.4426950408889634f); b += e[5 + j];
            }
            a = __builtin_amdgcn_rcpf(grp_sum(a)); b = __builtin_amdgcn_rcpf(grp_sum(b));
#pragma unroll
            for (int j = 0; j < 5; ++j) { e[j] *= a; e[5 + j] *= b; }
#pragma unroll
            for (int j = 0; j < 5; ++j) ph[j] = pk2bf(e[2 * j], e[2 * j + 1]);
        };
        auto phiq = [&](const int it) {
            u32 ph[5];
            phi(vq, ph);
            *(u32x4*)(shm + B_PQ + (w * 32 + it) * 1024 + lane * 16) = (u32x4){ph[0], ph[1], ph[2], ph[3]};
            q5[it] = ph[4];
        };
        for (int it = 0; it < 32; ++it) {
            const int s = 2 * it + (w >> 1);
            if (s + 2 < NSEG) { wait_rt(tn); tn = sched_get<BSL>(sched_peek(sch + (s + 1) * 128 + 4), sch + (s + 1) * 128 + 4, cap); wait_rt(tn + B_LAG); tn += BPEEK; }
            else wait_rt(sched_get(~0u, p.hop + (hb & 1) * 8192 + (T_SLOT + 1) * SLOT + (48 + layer * 4 + (s & 1)) * 128, cap) + T_X1);
            const char* xb = (wsp(p) + O_X1) + layer * ACT + (size_t)s * 40960 + (w & 1) * 256;
            if (it >= 30) TT((hb >> 4) * 128 + 112 + (hb & 15), (it - 30) * 3);
            f32x4 acc[4], sq;
            const char* xk[5];
#pragma unroll
            for (int i = 0; i < 5; ++i) { xk[i] = xb + i * 8192 + 4096; asm volatile("" : "+s"(xk[i])); }
            for (int pass = 0;; ++pass) {
                u32 vo = g * 512 + fr * 16;
                asm volatile("" : "+v"(vo) :: "memory");
                auto fetch = [&](const int k) { return ld16(xk[k >> 2], vo, ((k & 3) - 2) * 2048); };
                proj(acc, sq, fetch);
                if (pass > 0 || !__any(acc[3][3] != acc[3][3])) break;
                CNT(4);
                for (int tries = 0;; ++tries) {
                    asm volatile("" : "+v"(vo) :: "memory");
                    u32 m = 0;
#pragma unroll
                    for (int k = 0; k < 20; k += 10) {
#pragma unroll
                        for (int i = 0; i < 10; ++i) m = max4(m, fetch(k + i));
                        SCHEDB();
                    }
                    if (!__any(m == ~0u)) break;
                    if (tries >= cap) { cap = 1; break; }
                    __builtin_amdgcn_s_sleep(6);
                }
            }
            if (it >= 30) TT((hb >> 4) * 128 + 112 + (hb & 15), (it - 30) * 3 + 1);
            if (s == NSEG - 1 && hb == 5 && w == 3) stamp2(p.hop, 50 + layer * 4);
            const float dsel = (fr & 2) ? ((fr & 1) ? sq[3] : sq[2]) : ((fr & 1) ? sq[1] : sq[0]);
            const float s2 = grp_sum(g == (fr >> 2) ? dsel : 0.f);
            const float mu = acc[3][3] * (1.0f / DM), rs = __builtin_amdgcn_rsqf(fmaxf(fmaf(-mu, mu, s2 * (1.0f / DM)), 0.f) + 1e-5f);
            float v[15];
            f32x4 cs[4];
#pragma unroll
            for (int i = 0; i < 4; ++i) cs[i] = *(const f32x4*)(shm + B_CS + i * 1024 + lane * 16);
#pragma unroll
            for (int j = 0; j < 15; ++j) v[j] = fmaf(-mu, cs[j >> 2][j & 3], acc[j >> 2][j & 3]) * rs;
            u32 ph[5];
            phi(v + 5, ph);
            char* row = im + fr * B_IS;
            *(u32x2*)(row + g * 8) = (u32x2){ph[0], ph[1]};
            *(u32x2*)(row + 32 + g * 8) = (u32x2){ph[2], ph[3]};
            *(u32x2*)(row + 64 + g * 8) = (u32x2){ph[4], 0u};
            *(u32x2*)(row + 96 + g * 8) = (u32x2){pk2bf(v[10], v[11]), pk2bf(v[12], v[13])};
            *(u32x2*)(row + 128 + g * 8) = (u32x2){pk2bf(v[14], 1.0f), 0u};
            s16x4 tf[5];
#pragma unroll
            for (int i = 0; i < 5; ++i) tf[i] = __builtin_amdgcn_ds_read_tr16_b64_v4i16((LDS3 s16x4*)(uintptr_t)(imr + i * 32));
#pragma unroll
            for (int i = 0; i < 6; ++i) MMA16(kv[i >> 1][i & 1], tf[i >> 1], tf[3 + (i & 1)]);
#pragma unroll
            for (int j = 0; j < 5; ++j) vq[j] = v[j];
            if (it < 31) phiq(it);
            if (it >= 30) TT((hb >> 4) * 128 + 112 + (hb & 15), (it - 30) * 3 + 2);
        }
        asm volatile("s_nop 7" : "+v"(kv[0][0]), "+v"(kv[0][1]), "+v"(kv[1][0]), "+v"(kv[1][1]), "+v"(kv[2][0]), "+v"(kv[2][1]));
#pragma unroll
        for (int i = 0; i < 6; ++i) *(f32x4*)(im + i * 1024 + lane * 16) = kv[i >> 1][i & 1];
        RAW_BARRIER();
        bf8 ka[2];
        u32x2 kb[2];
#pragma unroll
        for (int dt = 0; dt < 2; ++dt) {
            f32x4 t[3];
#pragma unroll
            for (int ft = 0; ft < 3; ++ft) {
                const char* a = shm + B_IM + (ft * 2 + dt) * 1024 + lane * 16;
                t[ft] = (*(const f32x4*)a + *(const f32x4*)(a + 6144)) + (*(const f32x4*)(a + 12288) + *(const f32x4*)(a + 18432));
            }
            ka[dt] = pack8(t[0], t[1]);
            kb[dt] = (u32x2){pk2bf(t[2][0], t[2][1]), pk2bf(t[2][2], t[2][3])};
            PIN(ka[dt]); PIN(kb[dt]);
        }
        char* ob = (wsp(p) + O_OB) + layer * ACT + (size_t)w * 20480 + hb * 512;
        TT((hb >> 4) * 128 + 112 + (hb & 15), 6);
        u32 rofs = g * 81920 + 16384 - hb * 384 + fr * 8, mofs = (g & 1) * 81920 + (g >> 1) * 256 + fr * 16, mofs2 = mofs + 2 * 81920;
        asm volatile("" : "+v"(rofs), "+v"(mofs), "+v"(mofs2));
        f32x4 o[2][4][2];
        bf8 qa[2][4];
        u32x2 qb[4];
        auto ldq = [&](const int gr) {
#pragma unroll
            for (int u = 0; u < 4; ++u) qa[gr & 1][u] = *(const bf8*)(shm + B_PQ + (w * 32 + gr * 4 + u) * 1024 + lane * 16);
        };
        auto mm = [&](const int gr, const int k) {
            SCHEDB();
            if (k < 4) { qb[k] = (u32x2){q5[gr * 4 + k], 0u}; PIN(qb[k]); }
            if (k < 8) MMA0_VV(o[gr & 1][k >> 1][k & 1], ka[k & 1], qa[gr & 1][k >> 1]);
            else MMA16(o[gr & 1][(k - 8) >> 1][k & 1], kb[k & 1], qb[(k - 8) >> 1]);
            SCHEDB();
        };
        ldq(0); ldq(1);
        NOPV();
#pragma unroll
        for (int k = 0; k < 16; ++k) mm(0, k);
        static_for<0, 8>([&](auto IT) {
            constexpr int gr = CI(IT);
            auto nx = [&](const int k) { if constexpr (gr < 7) mm(gr + 1, k); else { SCHEDB(); asm volatile("s_nop 0"); SCHEDB(); } };
            if constexpr (gr == 7) asm volatile("s_nop 7");
            u32 d[4][2];
            float x[4];
            nx(0); nx(1);
#pragma unroll
            for (int u = 0; u < 4; ++u) {
                f32x4& a = o[gr & 1][u][0];
                f32x4& b = o[gr & 1][u][1];
                PIN(a); PIN(b);
                float inv = __builtin_amdgcn_rcpf(b[1] + 1e-6f), y0, y1, y2, y3;
                nx(2 + 3 * u);
                asm("v_mul_f32 %0, %4, %8\n\tv_mul_f32 %1, %5, %8\n\tv_mul_f32 %2, %6, %8" : "=&v"(y0), "=&v"(y1), "=&v"(y2), "=&v"(y3) : "v"(a[0]), "v"(a[1]), "v"(a[2]), "v"(a[3]), "v"(inv));
                nx(3 + 3 * u);
                y3 = a[3] * inv; x[u] = b[0] * inv;
                d[u][0] = pk2bf(y0, y1);
                nx(4 + 3 * u);
                d[u][1] = pk2bf(y2, y3);
                if (u & 1) {
                    auto t0 = __builtin_amdgcn_permlane16_swap(d[u - 1][0], d[u][0], false, false);
                    auto t1 = __builtin_amdgcn_permlane16_swap(d[u - 1][1], d[u][1], false, false);
                    __builtin_amdgcn_global_store_b128((gu32x4*)(ob + (u == 1 ? mofs : mofs2)), (u32x4){nosent(t0[0]), nosent(t1[0]), nosent(t0[1]), nosent(t1[1])}, "agent");
                }
            }
            if constexpr (gr == 5) phiq(31);
            if constexpr (gr < 6) ldq(gr + 2);
            nx(14);
            auto a0 = __builtin_amdgcn_permlane32_swap(__float_as_uint(x[0]), __float_as_uint(x[2]), false, false);
            auto a1 = __builtin_amdgcn_permlane32_swap(__float_as_uint(x[1]), __float_as_uint(x[3]), false, false);
            nx(15);
            auto b0 = __builtin_amdgcn_permlane16_swap(a0[0], a1[0], false, false);
            auto b2 = __builtin_amdgcn_permlane16_swap(a0[1], a1[1], false, false);
            __hip_atomic_store((gu64*)(ob + rofs), (u64)nosent(pk2bf(__uint_as_float(b0[0]), __uint_as_float(b0[1]))) | ((u64)nosent(pk2bf(__uint_as_float(b2[0]), __uint_as_float(b2[1]))) << 32), __ATOMIC_RELAXED, __HIP_MEMORY_SCOPE_AGENT);
            ob += 4 * 81920;
            asm volatile("" : "+s"(ob));
        });
        RAW_BARRIER();
        TT((hb >> 4) * 128 + 112 + (hb & 15), 7);
    }
}

__global__ void __launch_bounds__(256, 1) la_kernel(LaParams p) {
    extern __shared__ __attribute__((aligned(16))) char shm[];
    p.hop = align2m(p.ws + O_HOP);
    if (threadIdx.x == 0) {
        u32 xcc; asm volatile("s_getreg_b32 %0, hwreg(HW_REG_XCC_ID)" : "=s"(xcc));
        int role = 255;
        for (int a = 0; a < 8; ++a) {
            const u32 v = (xcc + a) & 7;
            const u32 k = __hip_atomic_fetch_add((gu32*)(((u32*)(p.ws + O_CTL)) + v * 32), 1u, __ATOMIC_RELAXED, __HIP_MEMORY_SCOPE_AGENT);
            if (k < 32) { role = v * 32 + k; break; }
        }
        *(int*)shm = role;
    }
    __syncthreads();
    const int role = __builtin_amdgcn_readfirstlane(*(int*)shm);
    __syncthreads();
    const int set = role >> 7, id = (role >> 5) & 3, k = role & 31;
    if (k < 8) qk_role(p, set, id * 8 + k, shm);
    else if (k < 28) comb_role(p, set, id * 20 + k - 8, shm);
    else bw_role(p, (role >> 5) * 4 + k - 28, shm);
}

#define PFN 1
struct PrepParams { const u16* W[2]; const u16* x; const u16* mem0; const u16* wo; char* ws; };
constexpr int N_PF = 128 * PFN, N_LN = 128, N_WO = 200, N_FILL = 512;
__global__ void __launch_bounds__(256) prep_kernel(PrepParams q) {
    int b = blockIdx.x;
    const int t = threadIdx.x, lane = t & 63, w = t >> 6, fr = lane & 15, g = lane >> 4;
    char* hop = align2m(q.ws + O_HOP);
    if (b < N_LN) {
        const int tok = b * 16 + w * 4 + g;
        u32x4 d[3][2];
        float s = 0.f;
#pragma unroll
        for (int i = 0; i < 3; ++i) {
            const u32x4* a = (const u32x4*)(q.x + (size_t)tok * DM + min(fr + 16 * i, 39) * 16);
            d[i][0] = a[0]; d[i][1] = a[1];
        }
#pragma unroll
        for (int i = 0; i < 3; ++i)
#pragma unroll
            for (int e = 0; e < 8; ++e) if (fr + 16 * i < 40) s += bflo(d[i][e >> 2][e & 3]) + bfhi(d[i][e >> 2][e & 3]);
        const float mu = row16_sum(s) * (1.0f / DM);
        float v = 0.f;
#pragma unroll
        for (int i = 0; i < 3; ++i)
#pragma unroll
            for (int e = 0; e < 8; ++e) if (fr + 16 * i < 40) { const float a = bflo(d[i][e >> 2][e & 3]) - mu, c = bfhi(d[i][e >> 2][e & 3]) - mu; v = fmaf(a, a, fmaf(c, c, v)); }
        const float rs = ln_scale(row16_sum(v) * (1.0f / DM)), mr = rbf(mu);
#pragma unroll
        for (int i = 0; i < 3; ++i) {
            const int blk = fr + 16 * i;
            if (blk >= 40) continue;
            const size_t o = (size_t)(tok >> 5) * 40960 + ((blk >> 1) * 2 + ((tok >> 4) & 1)) * 1024 + ((blk & 1) * 32 + (tok & 15)) * 16;
            u32 n[8];
#pragma unroll
            for (int e = 0; e < 8; ++e) n[e] = ln_pair(d[i][e >> 2][e & 3], mr, rs);
            *(u32x4*)(q.ws + O_XR + o) = (u32x4){d[i][0][0], d[i][1][0], d[i][0][1], d[i][1][1]};
            *(u32x4*)(q.ws + O_XR + o + 256) = (u32x4){d[i][0][2], d[i][1][2], d[i][0][3], d[i][1][3]};
            *(u32x4*)(q.ws + O_XN + o) = (u32x4){n[0], n[4], n[1], n[5]};
            *(u32x4*)(q.ws + O_XN + o + 256) = (u32x4){n[2], n[6], n[3], n[7]};
        }
        return;
    }
    b -= N_LN;
    if (b < N_PF) {
        const u32x4* src = (const u32x4*)q.W[b >> 7] + (size_t)(b & 127) * 3840 + t;
        u32x4 v[15];
#pragma unroll
        for (int i = 0; i < 15; ++i) v[i] = src[i * 256];
#pragma unroll
        for (int i = 0; i < 15; ++i) asm volatile("" :: "v"(v[i]));
        return;
    }
    b -= N_PF;
    if (b < N_WO) {
        const int cb = b / 20, kk = b % 20;
        const u16* col = q.wo + cb * 64 + 16 * (fr >> 2) + 4 * w + (fr & 3);
        u32 v[8];
#pragma unroll
        for (int e = 0; e < 8; ++e) {
            const int row = kk < 16 ? (2 * kk + (g >> 1)) * 20 + 5 * (2 * (g & 1) + (e >> 2)) + (e & 3) : (8 * (kk - 16) + 2 * g + (e >> 2)) * 20 + 5 * (e & 3) + 4;
            v[e] = col[(size_t)row * DM];
        }
        *(u32x4*)(q.ws + O_WO + (size_t)cb * 81920 + (kk * 4 + w) * 1024 + lane * 16) = (u32x4){v[0] | v[1] << 16, v[2] | v[3] << 16, v[4] | v[5] << 16, v[6] | v[7] << 16};
        return;
    }
    b -= N_WO;
    const u32x4 SENT = {~0u, ~0u, ~0u, ~0u};
    const u32 i0 = b * 256 + t;
    if (b == 0) ((u32*)(q.ws + O_CTL))[t] = 0;
    if (i0 < 8 * 2 * 320) {
        const int sl = i0 / 640, e = i0 % 320;
        const u32x4 v = *(const u32x4*)(q.mem0 + (e & 7) * DM + ((sl & 1) * 40 + (e >> 3)) * 8);
        st16(hop + M0_OFF + (i0 / 320) * 8192 + e * 16, (u32x4){nosent(v[0]), nosent(v[1]), nosent(v[2]), nosent(v[3])});
    }
    for (u32 i = i0; i < HOP_BYTES / 16; i += N_FILL * 256) st16(hop + (size_t)i * 16, SENT);
    for (u32 i = i0; i < (O_XN - O_FL) / 16; i += N_FILL * 256) st16(q.ws + O_FL + (size_t)i * 16, SENT);
}

extern "C" void kernel_launch(void* const* d_in, const int* in_sizes, int n_in,
                              void* d_out, int out_size, void* d_ws, size_t ws_size,
                              hipStream_t stream) {
    PrepParams q;
    q.x = (const u16*)d_in[0]; q.mem0 = (const u16*)d_in[1];
    q.wo = (const u16*)d_in[10];
    q.W[0] = (const u16*)d_in[5]; q.W[1] = (const u16*)d_in[4];
    q.ws = (char*)d_ws;
    prep_kernel<<<N_PF + N_LN + N_WO + N_FILL, 256, 0, stream>>>(q);
    LaParams p;
    p.ws = (char*)d_ws; p.out = (u16*)d_out; p.hop = (char*)d_ws;
    p.Wq = (const u16*)d_in[2]; p.Wk = (const u16*)d_in[3]; p.Wv = (const u16*)d_in[4]; p.Wo = (const u16*)d_in[5];
    p.wqkv = (const u16*)d_in[8]; p.wphi = (const u16*)d_in[9];
    (void)hipFuncSetAttribute((const void*)la_kernel, hipFuncAttributeMaxDynamicSharedMemorySize, LA_LDS);
    la_kernel<<<256, 256, LA_LDS, stream>>>(p);
}
```

```cpp
#include <hip/hip_runtime.h>
#include <hip/hip_bf16.h>
#include <stdint.h>
#include <type_traits>

typedef unsigned short u16;
typedef unsigned int u32;
typedef unsigned long long u64;
using bf16x8 = __attribute__((ext_vector_type(8))) short;
using f32x4 = __attribute__((ext_vector_type(4))) float;

constexpr int S_TOK = 2048, DM = 640, NH = 32, DH = 192;
constexpr int MEM = 8, SEG = 32, NSEG = S_TOK / SEG;
constexpr int HD = NH * DH;

__device__ __forceinline__ float bf2f(u16 v) { return __uint_as_float(((u32)v) << 16); }
__device__ __forceinline__ u32 pk2bf(float lo, float hi) {
    typedef __attribute__((ext_vector_type(2))) float f32x2_t;
    typedef __attribute__((ext_vector_type(2))) __bf16 bf16x2_t;
    bf16x2_t r = __builtin_convertvector((f32x2_t){lo, hi}, bf16x2_t);
    return __builtin_bit_cast(u32, r);
}
__device__ __forceinline__ float rbf(float f) { return __uint_as_float(pk2bf(f, f) << 16); }
typedef __attribute__((ext_vector_type(8))) __bf16 bf8;
typedef __attribute__((ext_vector_type(16))) __bf16 bf16v;
typedef __attribute__((ext_vector_type(4))) __bf16 bf4;
typedef __attribute__((ext_vector_type(4))) short s16x4;
using u32x4 = __attribute__((ext_vector_type(4))) u32;
using u32x2 = __attribute__((ext_vector_type(2))) u32;
typedef __attribute__((address_space(1))) u32 gu32;
typedef __attribute__((address_space(1))) u64 gu64;
#define LDS3 __attribute__((address_space(3)))

constexpr int SLOT = 16384;
#define PCSV 3
constexpr int PCS = PCSV, P_SLOT0 = 0, M_SLOT0 = 16 * PCS, S_SLOT = M_SLOT0 + 32, T_SLOT = S_SLOT + 10, N_SLOT = T_SLOT + 2;
constexpr size_t HOP_BYTES = (size_t)N_SLOT * SLOT, M0_OFF = HOP_BYTES;
constexpr size_t PX_BUF = 4 * 32 * 640, PX_BYTES = 8 * PX_BUF;
constexpr int LA_LDS = 162816 + 256;
constexpr int SPIN_CAP = 1 << 16;
#define D1I 131
#define D2I 53
#define MG1 8
#define MG2 10
#define DUP 5
#define PACE 6
#define CLAG 48
#define T_PX 26
#define T_X1 75
#define T_OB 150
#define T_TOK 350
#define ST_LAG 6
#define OB_STEP 25
#define OB_COLD 20
#define W0D 3
#define LATEP 3
#define PACE2 5
#define BPEEK 400
#define SLK 6
#define FSL 8
#define E0 110
#define HOLD 4
#define DOWN 1
#define EY 35
#define BOOST 3
#define DSH "1"
#define PAUSE 160u
#define BSL 40
#define PKK 18
#define PKT 2
#define BPF 20
#define BUMP 2
#define RECV 4
#define F4S 59
#define B_LAG 65
#define ELATE 85

__device__ __forceinline__ void st16(void* p, u32x4 v) {
    asm volatile("global_store_dwordx4 %0, %1, off sc1\n\ts_nop 2" :: "v"(p), "v"(v) : "memory");
}
typedef __attribute__((address_space(1))) u32x4 gu32x4;
__device__ __forceinline__ u32x4 ld16(const char* base, u32 off, int imm) { return __builtin_amdgcn_global_load_b128((gu32x4*)(base + off + imm), "agent"); }
__device__ __forceinline__ u32 nosent(u32 v) { return min(v, 0xFFFFFFFEu); }
__device__ __forceinline__ u32 rt32() { return (u32)__builtin_amdgcn_s_memrealtime(); }
__device__ __forceinline__ void wait_rt(u32 t) {
    int d = (int)(t - rt32()) - 4;
    if (d > 4096 || d < 0) return;
    while (d > 7) { if (d > 18) __builtin_amdgcn_s_sleep(2); d = max((int)(t - rt32()) - 4, 0); }
    asm volatile("s_bitcmp1_b32 %0, 2\n\ts_cbranch_scc0 1f\n\ts_nop 15\n\ts_nop 7\n1:\n\ts_bitcmp1_b32 %0, 1\n\ts_cbranch_scc0 2f\n\ts_nop 12\n2:\n\t"
                 "s_bitcmp1_b32 %0, 0\n\ts_cbranch_scc0 3f\n\ts_nop 7\n3:" :: "s"(d) : "scc");
}
template <int N> __device__ __forceinline__ void dma_kb(const char* src, u32 lds_dst, u32 lane16) {
#pragma unroll
    for (int i = 0; i < N; i += 4) {
        u32 keep;
        if (N - i >= 4)
            asm volatile("s_mov_b32 %0, m0\n\ts_mov_b32 m0, %3\n\ts_nop 2\n\tglobal_load_lds_dwordx4 %1, %2 sc1\n\tglobal_load_lds_dwordx4 %1, %2 offset:1024 sc1\n\t"
                         "global_load_lds_dwordx4 %1, %2 offset:2048 sc1\n\tglobal_load_lds_dwordx4 %1, %2 offset:3072 sc1\n\ts_mov_b32 m0, %0"
                         : "=&s"(keep) : "v"(lane16), "s"(src + i * 1024), "s"(lds_dst + i * 1024) : "memory");
        else if (N - i == 2)
            asm volatile("s_mov_b32 %0, m0\n\ts_mov_b32 m0, %3\n\ts_nop 2\n\tglobal_load_lds_dwordx4 %1, %2 sc1\n\tglobal_load_lds_dwordx4 %1, %2 offset:1024 sc1\n\ts_mov_b32 m0, %0"
                         : "=&s"(keep) : "v"(lane16), "s"(src + i * 1024), "s"(lds_dst + i * 1024) : "memory");
        else
            asm volatile("s_mov_b32 %0, m0\n\ts_mov_b32 m0, %3\n\ts_nop 2\n\tglobal_load_lds_dwordx4 %1, %2 sc1\n\ts_mov_b32 m0, %0"
                         : "=&s"(keep) : "v"(lane16), "s"(src + i * 1024), "s"(lds_dst + i * 1024) : "memory");
    }
}
template <int OFF> __device__ __forceinline__ void dma_one(const char* src, u32 lds_dst, u32 lane16) {
    asm volatile("s_mov_b32 m0, %2\n\ts_nop 0\n\tglobal_load_lds_dwordx4 %0, %1 offset:%3 sc1" :: "v"(lane16), "s"(src), "s"(lds_dst), "n"(OFF) : "memory");
}
__device__ __forceinline__ u32 ringp(u32 a, u32 b) { return a + b; }
__device__ __forceinline__ void dma_gather(const char* src, u32 lds_dst, u32 off) {
    u32 keep;
    asm volatile("s_mov_b32 %0, m0\n\ts_mov_b32 m0, %3\n\ts_nop 2\n\tglobal_load_lds_dwordx4 %1, %2\n\ts_mov_b32 m0, %0"
                 : "=&s"(keep) : "v"(off), "s"(src), "s"(lds_dst) : "memory");
}
__device__ __forceinline__ void st16s(char* base, u32 off, u32x4 v) {
    asm volatile("s_nop 4\n\tglobal_store_dwordx4 %0, %1, %2 sc1\n\ts_nop 2" :: "v"(off), "v"(v), "s"(base) : "memory");
}
__device__ __forceinline__ void arm16(char* base, u32 off) {
    u32x4 v;
    asm volatile("v_mov_b64 %0, -1\n\tv_mov_b64 %1, -1" : "=v"(((u64*)&v)[0]), "=v"(((u64*)&v)[1]));
    st16s(base, off, v);
}
template <int B, int E, typename F> __device__ __forceinline__ void static_for(F&& f) {
    if constexpr (B < E) { f(std::integral_constant<int, B>()); static_for<B + 1, E>(f); }
}
#define CI(x) decltype(x)::value
#define RAW_BARRIER() do { asm volatile("s_waitcnt lgkmcnt(0)" ::: "memory"); __builtin_amdgcn_s_barrier(); asm volatile("" ::: "memory"); } while (0)
#define VM0() asm volatile("s_waitcnt vmcnt(0)" ::: "memory")
#define SCHEDB() __builtin_amdgcn_sched_barrier(0)
__device__ __forceinline__ bf8 lo8(const bf16v& v) { return __builtin_shufflevector(v, v, 0, 1, 2, 3, 4, 5, 6, 7); }
__device__ __forceinline__ bf8 hi8(const bf16v& v) { return __builtin_shufflevector(v, v, 8, 9, 10, 11, 12, 13, 14, 15); }
__device__ __forceinline__ bf16v cat8(bf8 a, bf8 b) { return __builtin_shufflevector(a, b, 0, 1, 2, 3, 4, 5, 6, 7, 8, 9, 10, 11, 12, 13, 14, 15); }
#define MMA_VV(c, a, b) asm volatile("v_mfma_f32_16x16x32_bf16 %0, %1, %2, %0" : "+v"(c) : "v"(a), "v"(b))
#define MMA_AV(c, a, b) asm volatile("v_mfma_f32_16x16x32_bf16 %0, %1, %2, %0" : "+v"(c) : "a"(a), "v"(b))
#define MMA_VA(c, a, b) asm volatile("v_mfma_f32_16x16x32_bf16 %0, %1, %2, %0" : "+v"(c) : "v"(a), "a"(b))
#define MMA0_VV(c, a, b) asm volatile("v_mfma_f32_16x16x32_bf16 %0, %1, %2, 0" : "=&v"(c) : "v"(a), "v"(b))
#define MMA0_AV(c, a, b) asm volatile("v_mfma_f32_16x16x32_bf16 %0, %1, %2, 0" : "=&v"(c) : "a"(a), "v"(b))
#define MMA0_VA(c, a, b) asm volatile("v_mfma_f32_16x16x32_bf16 %0, %1, %2, 0" : "=&v"(c) : "v"(a), "a"(b))
#define MMA16(c, a, b) asm volatile("v_mfma_f32_16x16x16_bf16 %0, %1, %2, %0" : "+v"(c) : "v"(a), "v"(b))
#define MMA16_0(c, a, b) asm volatile("v_mfma_f32_16x16x16_bf16 %0, %1, %2, 0" : "=&v"(c) : "v"(a), "v"(b))
#define SMM_V(c, a, b, i) asm volatile("v_smfmac_f32_16x16x64_bf16 %0, %1, %2, %3" : "+v"(c) : "v"(a), "v"(b), "v"(i))
#define SMM_A(c, a, b, i) asm volatile("v_smfmac_f32_16x16x64_bf16 %0, %1, %2, %3" : "+v"(c) : "v"(a), "a"(b), "v"(i))
#define NOPV() asm volatile("s_nop 1")
#define PIN(x) asm volatile("" : "+v"(x))
#define SETTLE() asm volatile("s_nop 7\n\ts_nop 3")
__device__ __forceinline__ bf8 pack8(f32x4 a, f32x4 b) {
    u32x4 r = {pk2bf(a[0], a[1]), pk2bf(a[2], a[3]), pk2bf(b[0], b[1]), pk2bf(b[2], b[3])};
    return __builtin_bit_cast(bf8, r);
}
__device__ __forceinline__ u32 max4(u32 m, u32x4 g) { m = max(max(m, g[0]), g[1]); return max(max(m, g[2]), g[3]); }
#define ZERO4(c) asm volatile("v_mov_b64 %0, 0\n\tv_mov_b64 %1, 0" : "=v"(((u64*)&(c))[0]), "=v"(((u64*)&(c))[1]))
__device__ __forceinline__ bf8 lds8(const char* shm, int off) { return *(const bf8*)(shm + off); }
__device__ __forceinline__ bf16v ldfrag(const char* p) { return cat8(*(const bf8*)p, *(const bf8*)(p + 1024)); }
template <int CTRL> __device__ __forceinline__ float fdpp(float x) {
    return __builtin_bit_cast(float, __builtin_amdgcn_mov_dpp(__builtin_bit_cast(int, x), CTRL, 0xf, 0xf, true));
}
__device__ __forceinline__ float halfsum(float a, float b) {
    auto r = __builtin_amdgcn_permlane32_swap(__float_as_uint(a), __float_as_uint(b), false, false);
    return __uint_as_float(r[0]) + __uint_as_float(r[1]);
}
__device__ __forceinline__ float grp_sum(float x) {
    auto t = __builtin_amdgcn_permlane16_swap(__float_as_uint(x), __float_as_uint(x), false, false);
    x = __uint_as_float(t[0]) + __uint_as_float(t[1]);
    return halfsum(x, x);
}
__device__ __forceinline__ float grp_max(float x) {
    auto t = __builtin_amdgcn_permlane16_swap(__float_as_uint(x), __float_as_uint(x), false, false);
    x = fmaxf(__uint_as_float(t[0]), __uint_as_float(t[1]));
    auto u = __builtin_amdgcn_permlane32_swap(__float_as_uint(x), __float_as_uint(x), false, false);
    return fmaxf(__uint_as_float(u[0]), __uint_as_float(u[1]));
}
__device__ __forceinline__ float bflo(u32 v) { return __uint_as_float(v << 16); }
__device__ __forceinline__ float bfhi(u32 v) { return __uint_as_float(v & 0xffff0000u); }
__device__ __forceinline__ float ln_scale(float var) { return __builtin_amdgcn_rcpf(rbf(__builtin_amdgcn_sqrtf(rbf(rbf(var) + 1e-5f)))); }
__device__ __forceinline__ u32 ln_pair(u32 v, float m, float rs) { const u32 d = pk2bf(bflo(v) - m, bfhi(v) - m); return pk2bf(bflo(d) * rs, bfhi(d) * rs); }
__device__ __forceinline__ void st4_lane0(char* base, u32 v) {
    u32 z, d;
    base = (char*)(((u64)(u32)__builtin_amdgcn_readfirstlane((u32)((u64)base >> 32)) << 32) | (u64)(u32)__builtin_amdgcn_readfirstlane((u32)(u64)base));
    asm volatile("s_mov_b64 exec, 1\n\tv_mov_b32 %0, 0\n\tv_mov_b32 %1, %3\n\ts_nop 1\n\tglobal_store_dword %0, %1, %2 sc1\n\ts_mov_b64 exec, -1" : "=&v"(z), "=&v"(d) : "s"(base), "s"(v) : "memory");
}
__device__ __forceinline__ void stamp2(char* hop, const int line) {
    const u32 t = nosent(rt32());
    st4_lane0(hop + (T_SLOT + 1) * SLOT + line * 128, t); st4_lane0(hop + (T_SLOT + 1) * SLOT + 8192 + line * 128, t);
}
__device__ __forceinline__ int smax(int a, int b) { int r; asm volatile("s_max_i32 %0, %1, %2" : "=s"(r) : "s"(a), "s"(b) : "scc"); return r; }
__device__ __forceinline__ int track(int d, int sample, int down) {
    int e, a, b;
    asm("s_sub_i32 %0, %3, %4\n\ts_cmp_gt_i32 %3, 0\n\ts_cselect_b32 %0, %0, 0\n\ts_min_i32 %1, %0, %6\n\ts_ashr_i32 %2, %0, " DSH "\n\ts_max_i32 %2, %2, %5\n\t"
        "s_min_i32 %2, %2, 0\n\ts_max_i32 %1, %1, %2" : "=&s"(e), "=&s"(a), "=&s"(b) : "s"(sample), "s"(d), "s"(-down), "n"(DUP) : "scc");
    return d + a;
}
__device__ __forceinline__ u32 sched_peek(const char* p) { return __hip_atomic_load((const gu32*)p, __ATOMIC_RELAXED, __HIP_MEMORY_SCOPE_AGENT); }
template <int SL = 3> __device__ __forceinline__ u32 sched_get(u32 v, const char* p, int& cap) {
    for (int spin = 0; v == ~0u; ++spin) {
        if (spin >= cap) { cap = 1; break; }
        __builtin_amdgcn_s_sleep(SL);
        v = sched_peek(p);
    }
    return __builtin_amdgcn_readfirstlane(v);
}

constexpr size_t ACT = (size_t)S_TOK * DM * 2;
constexpr size_t O_DBG = 0, O_CTL = 2u << 20, O_FL = O_CTL + 4096, O_PX = O_FL + 640 * 128, O_ST = O_PX + PX_BYTES, O_X1 = O_ST + 128 * 1280,
                 O_OB = O_X1 + 2 * ACT, O_XN = O_OB + 2 * ACT, O_XR = O_XN + 2 * ACT, O_WO = O_XR + 2 * ACT,
                 O_WVO = O_WO + 10 * 81920, O_HOP = O_WVO + (size_t)80 * 4 * 40 * 2048, WS_BYTES = O_HOP + (4u << 20);
struct LaParams { char* ws; u16* out; char* hop; const u16* Wq; const u16* Wk; const u16* Wv; const u16* Wo; const u16* wqkv; const u16* wphi; };
__device__ __forceinline__ int kperm(int g, int e) { return 16 * (g >> 1) + 8 * ((e & 3) >> 1) + 4 * (g & 1) + 2 * (e >> 2) + (e & 1); }
__device__ __forceinline__ bf8 tr8(u32 a, u32 b) {
    const s16x4 lo = __builtin_amdgcn_ds_read_tr16_b64_v4i16((LDS3 s16x4*)(uintptr_t)a), hi = __builtin_amdgcn_ds_read_tr16_b64_v4i16((LDS3 s16x4*)(uintptr_t)b);
    return __builtin_bit_cast(bf8, __builtin_shufflevector(lo, hi, 0, 1, 2, 3, 4, 5, 6, 7));
}
__device__ __forceinline__ char* wsp(const LaParams& p) { char* b = p.ws; asm volatile("" : "+s"(b)); return b; }
template <int N, int SL = 4> __device__ __forceinline__ void wait_flags(const char* f, const u32 (&off)[N], int& cap) {
    for (int spin = 0;; ++spin) {
        u32 m = 0;
#pragma unroll
        for (int i = 0; i < N; ++i) m = max(m, sched_peek(f + off[i]));
        if (!__any(m == ~0u)) break;
        if (spin >= cap) { cap = 1; break; }
        __builtin_amdgcn_s_sleep(SL);
    }
    asm volatile("" ::: "memory");
}
__device__ __forceinline__ void wait_all_tokens(const LaParams& p, u32 lane, int& cap) {
    asm volatile("" : "+v"(lane));
    const u32 off[3] = {lane * 128, lane * 128 + 8192, min(lane + 128, 159u) * 128};
    wait_flags<3>(wsp(p) + O_FL + 192 * 128, off, cap);
}
__device__ __forceinline__ void wait_first_tokens(const LaParams& p, const int set, u32 lane, int& cap) {
    asm volatile("" : "+v"(lane));
    const u32 off[1] = {(set * 20 + min(lane, 39u) + (lane >= 20 ? 20 : 0)) * 128};
    wait_flags<1>(wsp(p) + O_FL, off, cap);
}
__device__ __forceinline__ float row16_sum(float x) { x += fdpp<0x128>(x); x += fdpp<0x124>(x); x += fdpp<0x122>(x); return x + fdpp<0x121>(x); }
__device__ __forceinline__ int get_side(const LaParams& p, int& cap) { return sched_get(sched_peek(wsp(p) + O_FL + 384 * 128), wsp(p) + O_FL + 384 * 128, cap) & 1; }
__device__ __forceinline__ char* align2m(char* p) { return p + ((size_t)(-(intptr_t)p) & ((2u << 20) - 1)); }

constexpr int Q_XA = 0, Q_PART = 20480, Q_TR = 48640, Q_MK = 54784, Q_WL = 55808, Q_ST = 162304, Q_PS = 44;

#define DPP1(op, ror) op "_dpp %0, %0, %0 row_ror:" ror " row_mask:0xf bank_mask:0xf\n\t"
#define DPP2(op, ror) DPP1(op, ror) op "_dpp %1, %1, %1 row_ror:" ror " row_mask:0xf bank_mask:0xf\n\ts_nop 0\n\t"
__device__ __forceinline__ void st16m(char* base, u32 off, u32x4 v, u64 mask) {
    asm volatile("s_mov_b64 exec, %3\n\ts_nop 3\n\tglobal_store_dwordx4 %0, %1, %2 sc1\n\ts_mov_b64 exec, -1\n\ts_nop 1" :: "v"(off), "v"(v), "s"(base), "s"(mask) : "memory");
}
__device__ __forceinline__ void st16f(char* base, u32 off, u32x4 v, u64 mask) {
    asm volatile("s_mov_b64 exec, %3\n\tglobal_store_dwordx4 %0, %1, %2 sc1\n\ts_mov_b64 exec, -1\n\ts_nop 1" :: "v"(off), "v"(v), "s"(base), "s"(mask) : "memory");
}
__device__ __forceinline__ void st16f64(char* base, u32 off, u32x4 v, u64 mask) {
    asm volatile("s_mov_b64 exec, %3\n\tglobal_store_dwordx4 %0, %1, %2 offset:64 sc1\n\ts_mov_b64 exec, -1\n\ts_nop 1" :: "v"(off), "v"(v), "s"(base), "s"(mask) : "memory");
}
__device__ __forceinline__ void st32(char* base, u32 off, u32x4 a, u32x4 b) {
    asm volatile("s_nop 4\n\tglobal_store_dwordx4 %0, %1, %3\n\tglobal_store_dwordx4 %0, %2, %3 offset:16\n\ts_nop 2" :: "v"(off), "v"(a), "v"(b), "s"(base) : "memory");
}
__device__ __forceinline__ void st8m(char* base, u32 off, u32x2 v, u64 mask) {
    asm volatile("s_mov_b64 exec, %3\n\ts_nop 3\n\tglobal_store_dwordx2 %0, %1, %2 sc1\n\ts_mov_b64 exec, -1" :: "v"(off), "v"(v), "s"(base), "s"(mask) : "memory");
}
__device__ __forceinline__ u32 pk2bf_abs(float lo, float hi) {
    u32 r;
    asm("v_cvt_pk_bf16_f32 %0, |%1|, |%2|" : "=v"(r) : "v"(lo), "v"(hi));
    return r;
}
__device__ __forceinline__ u32 p_line(const int h, const int piece, const u32 gap) {
    u32 a = (h >> 4) * gap + ((h >> 1) & 7) * 1024 + (h & 1) * 128, b = 2 * gap + h * 128;
    asm volatile("" : "+s"(a), "+s"(b));
    return piece < 4 ? a + piece * 256 : b;
}
constexpr float SM_C = 0.07216878364870322f * 1.4426950408889634f, NEG_BIG = -0x1p100f;
constexpr u64 SM_MASK = 0x0155015501550155ull;
__device__ __forceinline__ void softmax_mem(const f32x4 (&a)[4], char* dst, char* dst2, const u32 off) {
    f32x4 x = (a[0] + a[1]) + (a[2] + a[3]);
    float ml, mx = fmaxf(fmaxf(x[0], x[1]), fmaxf(x[2], x[3])), e0, e1, e2, e3;
    asm volatile("v_mul_f32 %5, %10, %0\n\ts_nop 0\n\t"
        DPP1("v_max_f32", "8") "v_fma_f32 %1, %6, %10, -%5\n\tv_fma_f32 %2, %7, %10, -%5\n\t"
        DPP1("v_max_f32", "4") "v_fma_f32 %3, %8, %10, -%5\n\tv_fma_f32 %4, %9, %10, -%5\n\t"
        DPP1("v_max_f32", "2") "v_exp_f32 %1, %1\n\tv_exp_f32 %2, %2\n\t"
        DPP1("v_max_f32", "1") "v_exp_f32 %3, %3\n\tv_fma_f32 %5, -%0, %10, %5\n\tv_exp_f32 %4, %4\n\tv_add_f32 %0, %1, %2\n\tv_exp_f32 %5, %5\n\tv_add_f32 %0, %0, %3\n\t"
        "v_add_f32 %0, %0, %4\n\tv_mul_f32 %0, %0, %5\n\tv_mul_f32 %1, %1, %5\n\tv_mul_f32 %2, %2, %5\n\t"
        DPP1("v_add_f32", "8") "v_mul_f32 %3, %3, %5\n\tv_mul_f32 %4, %4, %5\n\t" DPP1("v_add_f32", "4") "s_nop 1\n\t"
        DPP1("v_add_f32", "2") "s_nop 1\n\t" DPP1("v_add_f32", "1") "v_rcp_f32 %0, %0\n\ts_nop 0\n\t"
        "v_mul_f32 %1, %1, %0\n\tv_mul_f32 %2, %2, %0\n\tv_mul_f32 %3, %3, %0\n\tv_mul_f32 %4, %4, %0"
        : "+v"(mx), "=&v"(e0), "=&v"(e1), "=&v"(e2), "=&v"(e3), "=&v"(ml) : "v"(x[0]), "v"(x[1]), "v"(x[2]), "v"(x[3]), "s"(SM_C) : "memory");
    u32x4 o;
    asm volatile("v_cvt_pk_bf16_f32 %4, |%4|, |%6|\n\tv_cvt_pk_bf16_f32 %5, |%5|, |%7|\n\ts_nop 0\n\t"
                 "v_mov_b32_dpp %0, %4 quad_perm:[0,0,2,2] row_mask:0xf bank_mask:0xf\n\tv_mov_b32_dpp %1, %5 quad_perm:[0,0,2,2] row_mask:0xf bank_mask:0xf\n\t"
                 "v_mov_b32_dpp %2, %4 quad_perm:[1,1,3,3] row_mask:0xf bank_mask:0xf\n\tv_mov_b32_dpp %3, %5 quad_perm:[1,1,3,3] row_mask:0xf bank_mask:0xf"
                 : "=&v"(o[0]), "=&v"(o[1]), "=&v"(o[2]), "=&v"(o[3]), "+v"(e0), "+v"(e2) : "v"(e1), "v"(e3), "s"(dst), "s"(dst2));
    asm volatile("s_mov_b64 exec, %4\n\tglobal_store_dwordx4 %0, %1, %2 sc1\n\tglobal_store_dwordx4 %0, %1, %3 sc1\n\ts_mov_b64 exec, -1\n\ts_nop 1"
                 :: "v"(off), "v"(o), "s"(dst), "s"(dst2), "s"(0x03ff03ff03ff03ffull) : "memory");
}
struct SmTok {
    float x[2][4], b1[2][4], b2[2][4], b3[2][4], mx[2], sm[2], t0, t1;
    u32 d00, d01, d10, d11;
    static constexpr int N = 88;
    __device__ __forceinline__ void load(const float* p, const int j) {
        const int n = j & 1, k = j >> 1;
        const f32x4 a = *(const f32x4*)(p + (k * 40 + n * 4) * Q_PS);
#pragma unroll
        for (int i = 0; i < 4; ++i) (k == 0 ? x : (k == 1 ? b1 : (k == 2 ? b2 : b3)))[n][i] = a[i];
    }
    template <int I> __device__ __forceinline__ void piece(char* dst, const u32 off) {
        constexpr int n = I & 1, k = (I >> 1) - 12;
        float& t = n ? t1 : t0;
#define ROR(op, ror, v) asm volatile(op "_dpp %0, %0, %0 row_ror:" ror " row_mask:0xf bank_mask:0xf" : "+v"(v))
        if constexpr (k < -8) asm volatile("v_add_f32 %0, %0, %1" : "+v"(x[n][k + 12]) : "v"(b1[n][k + 12]));
        else if constexpr (k < -4) asm volatile("v_add_f32 %0, %0, %1" : "+v"(b2[n][k + 8]) : "v"(b3[n][k + 8]));
        else if constexpr (k < 0) asm volatile("v_add_f32 %0, %0, %1" : "+v"(x[n][k + 4]) : "v"(b2[n][k + 4]));
        else if constexpr (k == 0) asm volatile("v_max_f32 %0, %1, %2" : "=v"(mx[n]) : "v"(x[n][0]), "v"(x[n][1]));
        else if constexpr (k == 1) asm volatile("v_max3_f32 %0, %0, %1, %2" : "+v"(mx[n]) : "v"(x[n][2]), "v"(x[n][3]));
        else if constexpr (k == 2) ROR("v_max_f32", "8", mx[n]);
        else if constexpr (k == 3) ROR("v_max_f32", "4", mx[n]);
        else if constexpr (k == 4) ROR("v_max_f32", "2", mx[n]);
        else if constexpr (k == 5) ROR("v_max_f32", "1", mx[n]);
        else if constexpr (k == 6) { mx[n] *= SM_C; PIN(mx[n]); }
        else if constexpr (k < 11) { x[n][k - 7] = fmaf(x[n][k - 7], SM_C, -mx[n]); PIN(x[n][k - 7]); }
        else if constexpr (k < 15) asm volatile("v_exp_f32 %0, %0" : "+v"(x[n][k - 11]));
        else if constexpr (k == 15) asm volatile("v_add_f32 %0, %1, %2" : "=v"(sm[n]) : "v"(x[n][0]), "v"(x[n][1]));
        else if constexpr (k == 16) asm volatile("v_add_f32 %0, %1, %2" : "=v"(t) : "v"(x[n][2]), "v"(x[n][3]));
        else if constexpr (k == 17) asm volatile("v_add_f32 %0, %0, %1" : "+v"(sm[n]) : "v"(t));
        else if constexpr (k == 18) ROR("v_add_f32", "8", sm[n]);
        else if constexpr (k == 19) ROR("v_add_f32", "4", sm[n]);
        else if constexpr (k == 20) ROR("v_add_f32", "2", sm[n]);
        else if constexpr (k == 21) ROR("v_add_f32", "1", sm[n]);
        else if constexpr (k == 22) asm volatile("v_rcp_f32 %0, %0" : "+v"(sm[n]));
        else if constexpr (k < 27) asm volatile("v_mul_f32 %0, %0, %1" : "+v"(x[n][k - 23]) : "v"(sm[n]));
        else if constexpr (k == 27) asm volatile("v_cvt_pk_bf16_f32 %0, |%1|, |%2|" : "=v"(n ? d10 : d00) : "v"(x[n][0]), "v"(x[n][1]));
        else if constexpr (k == 28) asm volatile("v_cvt_pk_bf16_f32 %0, |%1|, |%2|" : "=v"(n ? d11 : d01) : "v"(x[n][2]), "v"(x[n][3]));
        else if constexpr (I == 84 || I == 87) {
            u32x4 o;
            asm volatile("v_mov_b32 %0, %4\n\tv_mov_b32 %1, %5\n\tv_mov_b32_dpp %2, %4 quad_perm:[1,0,3,2] row_mask:0xf bank_mask:0xf\n\tv_mov_b32_dpp %3, %5 quad_perm:[1,0,3,2] row_mask:0xf bank_mask:0xf"
                         : "=&v"(o[0]), "=&v"(o[1]), "=&v"(o[2]), "=&v"(o[3]) : "v"(n ? d10 : d00), "v"(n ? d11 : d01), "s"(dst));
            if constexpr (n == 0) st16f(dst, off, o, SM_MASK); else st16f64(dst, off, o, SM_MASK);
        }
    }
};

__host__ __device__ constexpr int qk_slot(int T, int ks) { return ks < 5 ? ks * 6 + T : (T < 4 ? 30 + (T >> 1) * 10 + (ks - 5) * 2 + (T & 1) : 25 + T * 5 + ks); }
constexpr int NLF = 13;
__host__ __device__ constexpr int lds_slot(int n) { constexpr int t[NLF] = {2, 6, 11, 15, 20, 24, 29, 33, 37, 41, 45, 49, 55}; return t[n]; }
__host__ __device__ constexpr int slot_lds(int sl) { for (int n = 0; n < NLF; ++n) if (lds_slot(n) == sl) return n; return -1; }
__host__ __device__ constexpr int slot_frag(int sl) { if (slot_lds(sl) >= 0) return 48 + slot_lds(sl); int n = 0; for (int i = 0; i < NLF; ++i) n += lds_slot(i) < sl; return sl - n; }
__host__ __device__ constexpr int qk_frag(int T, int ks) { return slot_frag(qk_slot(T, ks)); }
__host__ __device__ constexpr int slot_T(int sl) { return sl < 30 ? sl % 6 : (sl < 50 ? ((sl - 30) / 10) * 2 + (sl & 1) : (sl - 30) / 5); }
__host__ __device__ constexpr int slot_ks(int sl) { return sl < 30 ? sl / 6 : (sl < 50 ? 5 + ((sl - 30) % 10) / 2 : 5 + sl % 5); }
__host__ __device__ constexpr int lds_rank(int T, int ks) { int n = 0; for (int t = 0; t < T; ++t) n += qk_frag(t, ks) >= 48; return n; }
__host__ __device__ constexpr int lds_nth(int ks, int n) { for (int t = 0; t < 6; ++t) if (qk_frag(t, ks) >= 48 && lds_rank(t, ks) == n) return qk_frag(t, ks) - 48; return -1; }

__device__ __forceinline__ void qk_role(const LaParams& p, const int set, const int h, char* shm) {
    const int lane = threadIdx.x & 63, w = __builtin_amdgcn_readfirstlane(threadIdx.x >> 6);
    const int fr = lane & 15, g = lane >> 4;
    const u32 lds0 = (u32)(uintptr_t)(LDS3 char*)shm;
    const int sidx = (lane & 8) ? 0xEEEE : 0x4444;
    int cap = SPIN_CAP;
    char* mine = p.hop;
    int sch = (S_SLOT + w) * SLOT;
    int phys = 0;
    char* px = (wsp(p) + O_PX);
    const char* xn = (wsp(p) + O_XN);
    char* wl = shm + Q_WL + w * (NLF * 2048) + lane * 16;
    const char* xa = shm + Q_XA + lane * 16;

    bf16v WA[32], WV[28 - NLF];
    {
        bf16v WT[NLF];
        u32 vo[6], rd[3];
#pragma unroll
        for (int i = 0; i < 6; ++i) {
            const int id = i * 64 + lane, r = w * 16 + id / 24;
            vo[i] = (32 * (r >> 5) + kperm((r >> 2) & 3, ((r >> 4) & 1) * 4 + (r & 3))) * (HD * 2) + ((id % 24) ^ (r & 6)) * 16;
        }
#pragma unroll
        for (int i = 0; i < 3; ++i) rd[i] = lds0 + (g * 4 + (fr >> 2)) * 384 + ((w * 3 + i) ^ ((g & 1) * 2 + (fr >> 3))) * 32 + (fr & 3) * 8;
        auto issue = [&](const int j) {
            const char* src = (const char*)((j & 1) ? p.Wk : p.Wq) + (size_t)(j >> 1) * 64 * HD * 2 + h * 384;
#pragma unroll
            for (int i = 0; i < 6; ++i) dma_gather(src, lds0 + (j % 6) * 24576 + w * 6144 + i * 1024, vo[i]);
        };
#pragma unroll
        for (int j = 0; j < 5; ++j) issue(j);
        static_for<0, 20>([&](auto J) {
            constexpr int j = CI(J);
            asm volatile("s_waitcnt vmcnt(%0)" :: "n"((19 - j < 4 ? 19 - j : 4) * 6) : "memory");
            RAW_BARRIER();
            if constexpr (j + 5 < 20) issue(j + 5);
            static_for<0, 3>([&](auto PR) {
                constexpr int id = qk_frag(2 * CI(PR) + (j & 1), j >> 1);
                u32 a = rd[CI(PR)] + (j % 6) * 24576;
                asm volatile("" : "+v"(a));
                const bf16v f = cat8(tr8(a, a + 16 * 384), tr8(a + 32 * 384, a + 48 * 384));
                if constexpr (id < 32) WA[id] = f; else if constexpr (id < 48) WV[id - 32] = f; else WT[id - 48] = f;
            });
        });
        RAW_BARRIER();
        dma_kb<5>(xn + (size_t)set * 40960 + w * 5120, lds0 + Q_XA + w * 5120, lane * 16);
#pragma unroll
        for (int i = 0; i < NLF; ++i) { *(bf8*)(wl + i * 2048) = lo8(WT[i]); *(bf8*)(wl + i * 2048 + 1024) = hi8(WT[i]); }
    }

    bf8 QX[2], KX[2];
    u32x2 QY[2], KY[2];
    u32 peek = ~0u;
    int d1 = D1I, d2 = D2I, k1 = D1I + MG1;
    if (w == 3) *(u64*)(shm + Q_ST + lane * 8) = lane < 63 ? 0 : (u64)D1I | ((u64)D2I << 32);
    const int lead = __builtin_amdgcn_readfirstlane((u32)(h * 4 + (w ^ 2) - 1) >> 31);
    SmTok smt;
    u32 took = 0, tvis = 0;
    u64 own;
    u32 xoff = p_line(h, fr >> 1, 8192) + g * 16, ao1 = p_line(h, min(lane >> 3, 4), 8192) + (lane & 7) * 16, ao0 = p_line(h, min(lane >> 3, 4), SLOT) + (lane & 7) * 16;
    asm volatile("" : "+v"(xoff), "+v"(ao0), "+v"(ao1));
    auto rearm = [&](const int rot, const int i, char*& a, u32x4& v) {
        if (i == 0) asm volatile("v_mov_b64 %0, -1\n\tv_mov_b64 %1, -1" : "=v"(((u64*)&v)[0]), "=v"(((u64*)&v)[1]));
        if (i == 1) { a = mine + (P_SLOT0 + (((rot + 3) & 3) * 4 + w) * PCS) * SLOT; asm volatile("" : "+s"(a)); }
        if (i == 5) { a = px + (size_t)(set * 4 + ((rot + 2) & 3)) * PX_BUF + w * 20480; asm volatile("" : "+s"(a)); }
        if (i == 3 || i == 8) __builtin_amdgcn_global_store_b128((gu32x4*)(a + (i == 3 ? ao0 : ao1)), v, "agent");
    };
    const char* mb0;
    const char* mb1;
    char* pdst;
    char* pdst2;
    int soft;
    u32 ytime;
    f32x4 tmask;
    auto next = [&](const int s, const int i) {
        const int rot = (s >> 1) & 3;
        if (i == 0) { mb0 = mine + (M_SLOT0 + rot * 8 + 2 * w) * SLOT + 2048; asm volatile("" : "+s"(mb0)); }
        if (i == 1) { mb1 = mb0 + SLOT; soft = (u32)((w - 2) & (s - (NSEG - 1))) >> 31; asm volatile("" : "+s"(mb1), "+s"(soft)); }
        if (i == 2) { pdst = mine + (P_SLOT0 + rot * 4 * PCS) * SLOT; pdst2 = pdst + 2 * PCS * SLOT; asm volatile("" : "+s"(pdst), "+s"(pdst2)); }
        if (i == 3) tmask = *(const f32x4*)(xa + Q_MK);
    };
    auto fetch_stats = [&]() {
        if (__builtin_expect(lead != 0, 0)) {
                u32 lo = lane;
                asm volatile("" : "+v"(lo));
                lo *= 128;
                u32 keep;
                asm volatile("s_mov_b32 %0, m0\n\ts_mov_b32 m0, %4\n\ts_nop 2\n\tglobal_load_lds_dword %1, %2 sc1\n\ts_add_u32 m0, m0, 256\n\ts_lshr_b64 exec, exec, 16\n\tglobal_load_lds_dword %1, %3 sc1\n\ts_mov_b64 exec, -1\n\ts_mov_b32 m0, %0"
                             : "=&s"(keep) : "v"(lo), "s"(mine + T_SLOT * SLOT), "s"(mine + (T_SLOT + 1) * SLOT), "s"(lds0 + Q_ST) : "memory", "scc");
        }
    };
    int hold = 0;
    u32 ylast = 0;
    auto xproj = [&](const int snext, const int sprev, auto SOFT, char* xdst) {
        f32x4 acc[6][2];
        const char* xs;
        u32 xl;
        bf8 x0 = *(const bf8*)(xa), x1 = *(const bf8*)(xa + 1024), lw[2];
        lw[0] = *(const bf8*)(wl);
        if constexpr (CI(SOFT)) RAW_BARRIER();
        auto pack = [&](const int T, const int tt) {
            PIN(acc[T][tt]);
            const u32 a = pk2bf(acc[T][tt][0], acc[T][tt][1]), b = pk2bf(acc[T][tt][2], acc[T][tt][3]);
            if (T < 4) { u32* d = (u32*)((T & 1) ? &KX[tt] : &QX[tt]) + (T & 2); d[0] = a; d[1] = b; }
            else if (T == 4) QY[tt] = (u32x2){a, b}; else KY[tt] = (u32x2){a, b};
            SCHEDB();
        };
        char* ao;
        u32x4 sent;
        static_for<0, 20>([&](auto K) {
            constexpr int k = CI(K), l0 = lds_nth(((k + 1) >> 1) % 10, 0), l1 = lds_nth(((k + 1) >> 1) % 10, 1);
            bf8 n0, n1, nw[2];
            if constexpr (k == 9) {
                if constexpr (CI(SOFT)) asm volatile("s_waitcnt vmcnt(4)" ::: "memory"); else VM0();
                RAW_BARRIER();

            }
            static_for<0, 6>([&](auto TT) {
                constexpr int T = CI(TT), id = qk_frag(T, k >> 1), rk = lds_rank(T, k >> 1);
                if constexpr (k < 19) {
                    if constexpr (T == 0) n0 = *(const bf8*)(xa + (k + 1) * 2048);
                    if constexpr (T == 1) n1 = *(const bf8*)(xa + (k + 1) * 2048 + 1024);
                    if constexpr (T == 2 && l0 >= 0) nw[0] = *(const bf8*)(wl + l0 * 2048 + ((k + 1) & 1) * 1024);
                    if constexpr (T == 3 && l1 >= 0) nw[1] = *(const bf8*)(wl + l1 * 2048 + ((k + 1) & 1) * 1024);
                }
                if constexpr (k == 0 && T == 1) { xs = xn + (size_t)(snext - 2) * 40960 + 20480 + w * 5120; xl = lds0 + Q_XA + 20480 + w * 5120; asm volatile("" : "+s"(xs), "+s"(xl)); }
                if constexpr (k == 9 && T == 1) { xs = xn + (size_t)min(snext, NSEG - 1) * 40960 + w * 5120; xl -= 20480 + 4096; asm volatile("" : "+s"(xs), "+s"(xl)); }
                if constexpr ((k == 2 || k == 13) && T == 1) { xs += 4096; xl += 4096; asm volatile("" : "+s"(xs), "+s"(xl)); }
                if constexpr (k < 3 && (T == 3 || T == 5) && 2 * k + (T == 5) < 5) dma_one<((2 * k + (T == 5)) & 3) * 1024>(xs, xl, lane * 16);
                if constexpr (k >= 9 && k < 14 && T == 4) dma_one<((k - 9) & 3) * 1024>(xs, xl, lane * 16);
                if constexpr (CI(SOFT) && k == 10 && T == 1) {
                    if (w == 1) st4_lane0(mine + (T_SLOT + 1) * SLOT + (16 + h) * 128, took);
                }
                if constexpr (CI(SOFT) && k == 16 && T == 4) fetch_stats();
                if constexpr (CI(SOFT) && k == 11 && T == 1) own = __hip_atomic_load((const gu64*)(mine + sch + (sprev - 1) * 128 + 8), __ATOMIC_RELAXED, __HIP_MEMORY_SCOPE_AGENT);
                if constexpr (CI(SOFT) && k == 16 && T == 1) { tvis = __builtin_amdgcn_readfirstlane((u32)own + (u32)(own >> 32)) + RECV; asm volatile("" : "+s"(tvis)); }
                if constexpr (CI(SOFT) && k == PKK && T == PKT) peek = sched_peek(mine + sch + ((int)(rt32() - tvis) >= 0 ? sprev * 128 + 4 : (sprev - 1) * 128 + 16));
                if constexpr (CI(SOFT) && k == 15 && T >= 3) next(snext - 2, T - 3);
                if constexpr (CI(SOFT) && k == 19 && T == 3) next(snext - 2, 3);
                SCHEDB();
                static_for<0, 2>([&](auto U) {
                    constexpr int u = CI(U);
                    const bf8 xx = u ? x1 : x0;
                    if constexpr (id < 32) {
                        const bf8 wf = (k & 1) ? hi8(WA[id]) : lo8(WA[id]);
                        if constexpr (k == 0) MMA0_AV(acc[T][u], wf, xx); else MMA_AV(acc[T][u], wf, xx);
                    } else {
                        bf8 wf;
                        if constexpr (id < 48) wf = (k & 1) ? hi8(WV[id - 32]) : lo8(WV[id - 32]); else wf = lw[rk];
                        if constexpr (k == 0) MMA0_VV(acc[T][u], wf, xx); else MMA_VV(acc[T][u], wf, xx);
                    }
                    SCHEDB();
                    if constexpr (CI(SOFT) && k * 12 + T * 2 + u < SmTok::N) smt.template piece<k * 12 + T * 2 + u>(xdst, xoff);
                    if constexpr (CI(SOFT) && k == 8) rearm((snext >> 1) + 2, T * 2 + u, ao, sent);
                    if constexpr (k == 19 && T >= 2) pack(T - 2, u);
                    SCHEDB();
                });
            });
            if constexpr (k < 19) { x0 = n0; x1 = n1; if constexpr (l0 >= 0) lw[0] = nw[0]; if constexpr (l1 >= 0) lw[1] = nw[1]; }
        });
        pack(4, 0); pack(4, 1);
        asm volatile("s_nop 3");
        pack(5, 0); pack(5, 1);
        if constexpr (CI(SOFT)) ytime = __builtin_amdgcn_readfirstlane(peek);
    };
    {
        const f32x4 neg = {NEG_BIG, NEG_BIG, NEG_BIG, NEG_BIG};
        *(f32x4*)((float*)(shm + Q_PART) + (w * 40 + 8 + fr) * Q_PS + g * 8 + 4) = neg;
        if (lane < 40) *(f32x4*)((float*)(shm + Q_PART) + (w * 40 + lane) * Q_PS + 40) = neg;
        *(f32x4*)(shm + Q_MK + lane * 16) = (f32x4){4 * g > fr ? NEG_BIG : 0.f, 4 * g + 1 > fr ? NEG_BIG : 0.f, 4 * g + 2 > fr ? NEG_BIG : 0.f, 4 * g + 3 > fr ? NEG_BIG : 0.f};
    }
    float* pw0 = (float*)(shm + Q_PART) + (w * 40 + (fr & 7)) * Q_PS + g * 8 + (fr >> 3) * 4;
    float* pw2 = fr < 8 && g >= 2 ? (float*)(shm + Q_PART) + (w * 40 + fr) * Q_PS + 24 + g * 4 : (float*)(shm + Q_TR + w * 1536) + lane * 4;
    const float* prd = (const float*)(shm + Q_PART) + ((w & 1) * 4 + g) * Q_PS + min(fr, 10) * 4;
    const float* prt = (const float*)(shm + Q_PART) + (8 + 8 * w + g) * Q_PS + min(fr, 10) * 4;
    u32 trw = lds0 + Q_TR + w * 1536 + lane * 8, trr = lds0 + Q_TR + w * 1536 + (fr & 3) * 128 + (4 * g + (fr >> 2)) * 8;
    u32 trh = lds0 + Q_TR + w * 1536 + 1024 + (lane & 31) * 8 + (lane >> 5) * 4;
    u32 pw3 = lds0 + Q_PART + ((w * 40 + 8 + (lane >> 5) * 16 + fr) * Q_PS + 32 + (g & 1) * 4) * 4;
    u32 pp = lds0 + Q_PART + ((w * 40 + 8 + fr) * Q_PS + g * 8) * 4;
    asm volatile("" : "+v"(trw), "+v"(trr), "+v"(trh), "+v"(pw3), "+v"(pp));
    const u32 poff = p_line(h, fr >> 1, SLOT) + (w & 1) * 64 + g * 16 + (fr & 1) * (PCS * SLOT);

    for (int layer = 0; layer < 2; ++layer) {
    if (layer > 0) {
        if (w == 0) {
            __builtin_amdgcn_s_sleep(127);
            wait_rt(sched_get(~0u, mine + (T_SLOT + 1) * SLOT + 50 * 128, cap) + T_TOK);
            wait_first_tokens(p, set, lane, cap);
        }
        RAW_BARRIER();
        sch += 5 * SLOT; xn += ACT;
        dma_kb<5>(xn + (size_t)set * 40960 + w * 5120, lds0 + Q_XA + w * 5120, lane * 16);
    }
    VM0(); RAW_BARRIER();

    xproj(set + 2, set - 1, std::integral_constant<int, 0>(), nullptr);
    if (layer == 0) { phys = set ^ get_side(p, cap); mine += phys * 8192; }
    next(set, 0);
    if (set == 0) mb0 = p.hop + M0_OFF + phys * 8192 + 2 * w * SLOT + 2048;
    next(set, 1); next(set, 2); next(set, 3);
    ytime = set ? ~0u : rt32();

    for (int s = set; s < NSEG; s += 2) {
        const int rot = (s >> 1) & 3;
        int early = 0;
        if (ytime == ~0u) { early = SLK; wait_rt(tvis); ytime = sched_get(sched_peek(mine + sch + (s - 1) * 128 + 4), mine + sch + (s - 1) * 128 + 4, cap); }
        f32x4 t[3];
        t[0] = tmask;
        PIN(t[0]); PIN(QX[0]); PIN(QX[1]); PIN(KX[0]); PIN(KX[1]); PIN(QY[0]); PIN(QY[1]); PIN(KY[0]); PIN(KY[1]);
        int tight = 0;
        if (w == 0) wait_rt(ytime);
        else {
            const int sl = (int)(ytime - rt32()) + early;
            tight = SLK - smax(-smax(-sl, -SLK), 0);
        }
        __builtin_amdgcn_s_barrier();

        f32x4 acc[6];
        u32x4 A[10];
        bf8 Ll[2], Lh[2];
        s16x4 F[4];
        u32 r0[4], r1[4], d[2];
        float v[4];
        int tries = 0;
        auto fin = [&](const int pr, const int i) {
            if (i == 0) { PIN(acc[2 * pr]); PIN(acc[2 * pr + 1]); }
            if (i < 4) { auto r = __builtin_amdgcn_permlane32_swap(__float_as_uint(acc[2 * pr][i]), __float_as_uint(acc[2 * pr + 1][i]), false, false); r0[i] = r[0]; r1[i] = r[1]; }
            const int a0 = pr == 0 ? i - 4 : i - 1;
            if (a0 >= 0 && a0 < 4) asm volatile("v_add_f32 %0, %1, %2" : "=v"(v[a0]) : "v"(r0[a0]), "v"(r1[a0]));
            if (a0 == 3) d[0] = pk2bf(v[0], v[1]);
            if (a0 == 4) {
                d[1] = pk2bf(v[2], v[3]);
                *(LDS3 u32x2*)(uintptr_t)(trw + pr * 512) = (u32x2){d[0], d[1]};
                F[pr] = __builtin_amdgcn_ds_read_tr16_b64_v4i16((LDS3 s16x4*)(uintptr_t)(trr + pr * 512));
            }
        };
        auto fin1 = [&](const int T) {
            asm volatile("v_permlane32_swap_b32 %2, %4\n\tv_permlane32_swap_b32 %3, %5\n\tv_add_f32 %0, %2, %4\n\tv_add_f32 %1, %3, %5\n\tv_cvt_pk_bf16_f32 %0, %0, %1"
                         : "=&v"(v[0]), "=&v"(v[1]) : "v"(acc[T][0]), "v"(acc[T][1]), "v"(acc[T][2]), "v"(acc[T][3]));
            *(LDS3 float*)(uintptr_t)(trh + (T - 4) * 256) = v[0];
            F[T - 2] = __builtin_amdgcn_ds_read_tr16_b64_v4i16((LDS3 s16x4*)(uintptr_t)(trr + 1024));
        };
        auto slot = [&](auto SL) {
            constexpr int sl = CI(SL), T = slot_T(sl), ks = slot_ks(sl), id = slot_frag(sl);
            if constexpr (id < 32) SMM_A(acc[T], A[ks], WA[id], sidx);
            else if constexpr (id < 48) SMM_V(acc[T], A[ks], WV[id - 32], sidx);
            else { const bf16v L = cat8(Ll[id & 1], Lh[id & 1]); SMM_V(acc[T], A[ks], L, sidx); }
            SCHEDB();
            constexpr int n0 = slot_lds(sl), n1 = slot_lds(sl - 1);
            if constexpr (n0 >= 0 && n0 < NLF - 2) Ll[n0 & 1] = *(const bf8*)(wl + (n0 + 2) * 2048);
            if constexpr (n1 >= 0 && n1 < NLF - 2) Lh[n1 & 1] = *(const bf8*)(wl + (n1 + 2) * 2048 + 1024);
            if constexpr (sl >= 43 && sl < 52) fin(0, sl - 43);
            if constexpr (sl >= 52 && sl < 58) fin(1, sl - 52);
            if constexpr (sl == F4S) fin1(4);
            SCHEDB();
        };
        auto gather = [&](const bool first) {
            u32 vo = lane * 16;
            asm volatile("" : "+v"(vo) :: "memory");
#pragma unroll
            for (int ks = 0; ks < 10; ++ks) {
                A[ks] = ld16(ks < 5 ? mb0 : mb1, vo, (ks % 5 - 2) * 1024);
                SCHEDB();
                if (first) {
                    if (ks == 0) {
                        MMA0_VV(t[1], KX[0], QX[1]);
                        asm volatile("v_mfma_f32_16x16x32_bf16 %0, %1, %2, %3" : "=&v"(t[2]) : "v"(KX[1]), "v"(QX[1]), "v"(t[0]));
                        MMA_VV(t[0], KX[0], QX[0]);
                    }
                    if (ks == 2) { MMA16(t[1], KY[0], QY[1]); MMA16(t[2], KY[1], QY[1]); MMA16(t[0], KY[0], QY[0]); }
                    if (ks == 4) { PIN(t[1]); *(LDS3 f32x4*)(uintptr_t)(pp + 16 * Q_PS * 4) = t[1]; }
                    if (ks == 5) { PIN(t[2]); *(LDS3 f32x4*)(uintptr_t)(pp + 16 * Q_PS * 4 + 16) = t[2]; }
                    if (ks == 6) { PIN(t[0]); *(LDS3 f32x4*)(uintptr_t)pp = t[0]; }
                    if (ks == 1 || ks == 3 || ks == 7 || ks == 8) asm volatile("s_nop %0" :: "n"(PACE2));
                } else if (ks < 9) asm volatile("s_nop %0" :: "n"(PACE));
                if (ks == 3 || ks == 7) { Ll[ks >> 2] = *(const bf8*)(wl + (ks >> 2) * 2048); Lh[ks >> 2] = *(const bf8*)(wl + (ks >> 2) * 2048 + 1024); }
                if (ks == 1) { ZERO4(acc[0]); ZERO4(acc[1]); }
                if (ks >= 5 && ks < 9) ZERO4(acc[ks - 3]);
                SCHEDB();
            }
        };
        gather(true);
        static_for<0, 42>(slot);
        int passed = 0;
        auto record = [&](const u32 xt, const u32 yt) {
            u32 ln8 = lane;
            asm volatile("" : "+v"(ln8));
            ln8 *= 8192;
            const u32x4 rec = {nosent(xt), nosent(yt), (u32)(d1 + MG1), yt};
            k1 = d1 + MG1;
            char* at = p.hop + (S_SLOT + layer * 5) * SLOT + s * 128;
            asm volatile("s_waitcnt lgkmcnt(0)\n\ts_barrier\n\ts_mov_b64 exec, 0x3ff\n\ts_nop 1\n\tglobal_store_dwordx4 %0, %1, %2 sc1\n\ts_mov_b64 exec, -1\n\ts_nop 0"
                         :: "v"(ln8), "v"(rec), "s"(at) : "memory");
            passed = 1;
        };
        u64 bad, bad2;
        asm volatile("v_cmp_u_f32 %0, %2, %3\n\tv_cmp_u_f32 %1, %4, %5" : "=s"(bad), "=s"(bad2) : "v"(acc[0][0]), "v"(acc[0][1]), "v"(acc[0][2]), "v"(acc[0][3]));
        static_for<42, 43>(slot);
        asm volatile("" : "+s"(bad), "+s"(bad2));
        if (__builtin_expect((bad | bad2) != 0, 0)) {
            {
                SETTLE();
                while (true) {
                    gather(false);
                    u32 m = 0;
#pragma unroll
                    for (int ks = 0; ks < 10; ++ks) m = max4(m, A[ks]);
                    ++tries;
                    if (!__any(m == ~0u)) break;
                    if (tries >= cap) { cap = 1; break; }
                    __builtin_amdgcn_s_sleep(4);
                }
                static_for<0, 43>(slot);
            }
        }
        static_for<43, 60>(slot);
        bf8 F01 = __builtin_bit_cast(bf8, __builtin_shufflevector(F[0], F[1], 0, 1, 2, 3, 4, 5, 6, 7));
        f32x4 s0, s1, s2, u0, u1;
        PIN(F01);
        MMA0_VV(s1, KX[1], F01); MMA0_VV(s0, KX[0], F01);
        asm volatile("s_nop 0");
        SCHEDB();
        fin1(5);
        SCHEDB();
        MMA16(s1, KY[1], F[2]); MMA16(s0, KY[0], F[2]);
        MMA0_VV(s2, F01, F01);
        asm volatile("s_nop 1" : "+v"(s0), "+v"(s1));
#pragma unroll
        for (int i = 0; i < 4; ++i) s0[i] = __int_as_float(__builtin_amdgcn_update_dpp(__float_as_int(s0[i]), __float_as_int(s1[i]), 0x128, 0xf, 0xc, false));
        SCHEDB();
        MMA16(s2, F[3], F[3]);
        SCHEDB();
        *(f32x4*)pw0 = s0; SCHEDB();
        MMA0_VV(u0, F01, QX[0]); MMA0_VV(u1, F01, QX[1]);
        asm volatile("s_nop 0" : "+v"(s2)); *(f32x4*)pw2 = s2; SCHEDB();
        asm volatile("s_nop 5");
        MMA16(u0, F[3], QY[0]); MMA16(u1, F[3], QY[1]);
        RAW_BARRIER();

        f32x4 pa[4];
        if (__builtin_expect(soft != 0, 1)) {
#pragma unroll
            for (int i = 0; i < 4; ++i) pa[i] = *(const f32x4*)(prd + i * 40 * Q_PS);
        } else {
#pragma unroll
            for (int i = 0; i < 4; ++i) asm volatile("s_nop 0" : "=v"(pa[i]));
        }
        {
            asm volatile("s_nop 0" : "+v"(u0), "+v"(u1));
            f32x4 u;
#pragma unroll
            for (int i = 0; i < 4; ++i) u[i] = __uint_as_float(__builtin_amdgcn_permlane32_swap(__float_as_uint(u0[i]), __float_as_uint(u1[i]), false, false)[1]);
            *(LDS3 f32x4*)(uintptr_t)pw3 = u;
            SCHEDB();
        }
        asm volatile("" : "+s"(soft));
        if (__builtin_expect(soft != 0, 1)) {
            softmax_mem(pa, pdst, pdst2, poff);
        } else {
            if (h == 0 && s < NSEG - 1) {
                hold = tries > 0 || s < 2 || ytime - ylast > 2u * (d1 + d2) + PAUSE ? HOLD : smax(hold - 1, 0);
                ylast = ytime;
                if (w == 3) {
                    const u64 dd = *(const u64*)(shm + Q_ST + 504);
                    const u32 xt = tries == 0 && s > 0 ? ytime + k1 : rt32() + (s > 0 ? ELATE : E0);
                    d1 = __builtin_amdgcn_readfirstlane((u32)dd); d2 = __builtin_amdgcn_readfirstlane((u32)(dd >> 32));
                    record(xt, xt + d2 + MG2 + (tries > 0 ? EY : 0) + hold * BOOST);
                } else {
                    u32 lo = lane * 4;
                    asm volatile("" : "+v"(lo));
                    int sta = *(const int*)(shm + Q_ST + lo), stb = *(const int*)(shm + Q_ST + 256 + lo);
                    asm volatile("s_nop 1\n\t" DPP2("v_max_i32", "8") DPP2("v_max_i32", "4") DPP2("v_max_i32", "2") DPP2("v_max_i32", "1") : "+v"(sta), "+v"(stb));
                    const int a0 = __builtin_amdgcn_readlane(sta, 0), a1 = __builtin_amdgcn_readlane(sta, 16), a2 = __builtin_amdgcn_readlane(sta, 32), a3 = __builtin_amdgcn_readlane(sta, 48);
                    const int b0 = __builtin_amdgcn_readlane(stb, 0), b1 = __builtin_amdgcn_readlane(stb, 16), b2 = __builtin_amdgcn_readlane(stb, 32);
                    SCHEDB();
                    const int down = smax(DOWN - hold * DOWN, 0);
                    if (hold == HOLD && s > 2) { d1 += BUMP; d2 += BUMP; }
                    else { d1 = track(d1, smax(b1, b2), down); d2 = track(d2, smax(smax(smax(a0, a1), smax(a2, a3)), b0), down); }
                    *(u64*)(shm + Q_ST + 504) = (u64)(u32)d1 | ((u64)(u32)d2 << 32);
                }
            }
            if (w == 2 && s < 2 && layer > 0) wait_all_tokens(p, lane, cap);
        }
        u64 tpub = 0;
        if (__builtin_expect(passed == 0, 1)) asm volatile("s_memrealtime %0\n\ts_waitcnt lgkmcnt(0)\n\ts_barrier" : "=s"(tpub) :: "memory");
#pragma unroll
        for (int j = 0; j < 8; ++j) smt.load(prt, j);
        took = tries == 0 && s > 0 ? (u32)tpub - ytime + tight : 0;
        char* xdst = px + (size_t)(set * 4 + rot) * PX_BUF + w * 20480;
        if (s + 2 >= NSEG) {
            static_for<0, SmTok::N>([&](auto I) { smt.template piece<CI(I)>(xdst, xoff); SCHEDB(); if constexpr ((CI(I) & 1) && ((CI(I) >= 26 && CI(I) < 36) || (CI(I) >= 58 && CI(I) < 68) || CI(I) > 80)) asm volatile("s_nop 0"); SCHEDB(); });
            __builtin_amdgcn_s_sleep(64);
            char* ao;
            u32x4 sent;
#pragma unroll
            for (int i = 0; i < 10; ++i) rearm(rot, i, ao, sent);
            break;
        }
        xproj(s + 4, s + 1, std::integral_constant<int, 1>(), xdst);
    }
    }
}

typedef __attribute__((ext_vector_type(2))) float f32x2;
__device__ __forceinline__ f32x2 merge2(const f32x4& a) {
    f32x2 r;
    asm volatile("s_nop 7\n\tv_permlane32_swap_b32 %2, %4\n\tv_permlane32_swap_b32 %3, %5\n\ts_nop 0\n\t"
                 "v_add_f32_dpp %0, %4, %2 row_ror:8 row_mask:0xf bank_mask:0xf\n\tv_add_f32_dpp %1, %5, %3 row_ror:8 row_mask:0xf bank_mask:0xf"
                 : "=&v"(r[0]), "=&v"(r[1]) : "v"(a[0]), "v"(a[1]), "v"(a[2]), "v"(a[3]));
    return r;
}
constexpr int C_YL = 0, C_RED = 40960, C_EX = 49152, C_XN = 53248, C_M0 = 135168;
__device__ __forceinline__ void comb_role(const LaParams& p, const int set, const int c, char* shm) {
    const int lane = threadIdx.x & 63, w = __builtin_amdgcn_readfirstlane(threadIdx.x >> 6);
    const int fr = lane & 15, g = lane >> 4;
    const u32 lds0 = (u32)(uintptr_t)(LDS3 char*)shm;
    const int sidx = (lane & 8) ? 0xEEEE : 0x4444;
    int cap = SPIN_CAP;
    char* mine = p.hop;
    char* other = p.hop;
    int phys = 0, sch = (S_SLOT + w) * SLOT;
    u32 k1 = D1I + MG1;
    char* px = (wsp(p) + O_PX);
    const char* xn = (wsp(p) + O_XN);
    char* const yl0 = shm + C_YL + lane * 16;
    int cb = (set * 80 + c) % 10, rb = (set * 80 + c) / 10;

    {
        const int t = threadIdx.x, h = (set * 80 + c) / 5, cu = c % 5;
        u32x4 st[12];
        const u16* src = p.Wo + ((size_t)h * DH + (t >> 4)) * DM + cu * 128 + (t & 15) * 8;
#pragma unroll
        for (int i = 0; i < 12; ++i) asm volatile("global_load_dwordx4 %0, %1, off" : "=v"(st[i]) : "v"(src + (size_t)i * 16 * DM) : "memory");
        u32 vo[12];
#pragma unroll
        for (int i = 0; i < 12; ++i) {
            const int id = i * 64 + lane, r = id / 24;
            vo[i] = kperm((r >> 2) & 3, (r >> 4) * 4 + (r & 3)) * (HD * 2) + ((id % 24) ^ (r & 7)) * 16;
        }
        const u32 ring = lds0 + 57344 + w * 24576;
        auto issue = [&](const int j) {
            const char* a = (const char*)p.Wv + (size_t)(((j + cu) % 5) * 4 + w) * 32 * HD * 2 + h * 384;
#pragma unroll
            for (int i = 0; i < 12; ++i) dma_gather(a, ring + (j & 1) * 12288 + i * 1024, vo[i]);
        };
        SCHEDB();
        issue(0);
        const u32 ra = ring + fr * 384 + ((g ^ (fr & 3)) + (fr & 4)) * 16;
        const int e = t >> 4, pr = ((e >> 2) & 1) * 8 + (e >> 3) * 4 + (e & 3);
        asm volatile("s_waitcnt vmcnt(12)" : "+v"(st[0]), "+v"(st[1]), "+v"(st[2]), "+v"(st[3]), "+v"(st[4]), "+v"(st[5]),
                     "+v"(st[6]), "+v"(st[7]), "+v"(st[8]), "+v"(st[9]), "+v"(st[10]), "+v"(st[11]) :: "memory");
#pragma unroll
        for (int i = 0; i < 12; ++i) *(u32x4*)(shm + (i * 16 + pr) * 288 + (t & 15) * 16) = st[i];
        issue(1);
        RAW_BARRIER();
        bf8 B[8][6], A[2][2][6];
        const u32 rd = lds0 + ((g >> 1) * 16 + (g & 1) * 4 + (fr >> 2)) * 288 + (fr & 3) * 8;
#pragma unroll
        for (int n = 0; n < 8; ++n)
#pragma unroll
            for (int k = 0; k < 6; ++k) B[n][k] = tr8(rd + k * 32 * 288 + n * 32, rd + (k * 32 + 8) * 288 + n * 32);
        auto lda = [&](const int j, const int k) {
#pragma unroll
            for (int u = 0; u < 2; ++u) A[j & 1][u][k] = *(LDS3 const bf8*)(uintptr_t)((ra ^ ((k & 1) * 64)) + (j & 1) * 12288 + u * 6144 + (k >> 1) * 128);
        };
        asm volatile("s_waitcnt vmcnt(12)" ::: "memory");
#pragma unroll
        for (int k = 0; k < 6; ++k) lda(0, k);
        f32x4 cc[2][2];
        char* dst;
        const char* nsrc;
        static_for<0, 41>([&](auto N) {
            constexpr int n = CI(N) % 8, j = CI(N) / 8, q = CI(N) - 1;
            if constexpr (n == 7 && j < 4) asm volatile("s_waitcnt vmcnt(%0)" :: "n"(j == 0 ? 18 : (j < 3 ? 21 : 9)) : "memory");
            if constexpr (n == 1) { dst = wsp(p) + O_WVO + ((size_t)(h * 20 + ((j + cu) % 5) * 4 + w) * 40 + cu * 8 + 4) * 1024; asm volatile("" : "+s"(dst)); }
            if constexpr (n == 0 && j < 3) { nsrc = (const char*)p.Wv + (size_t)(((j + 2 + cu) % 5) * 4 + w) * 32 * HD * 2 + h * 384; asm volatile("" : "+s"(nsrc)); }
            u32x4 o;
#pragma unroll
            for (int k = 0; k < 6; ++k) {
                if constexpr (j < 5) {
#pragma unroll
                    for (int u = 0; u < 2; ++u) { if (k == 0) MMA0_VA(cc[n & 1][u], A[j & 1][u][k], B[n][k]); else MMA_VA(cc[n & 1][u], A[j & 1][u][k], B[n][k]); }
                } else if (k == 0) asm volatile("s_nop 7");
                SCHEDB();
                if constexpr (j < 3 && n >= 1 && n < 7) {
                    if (k == 0 || k == 2) asm volatile("s_mov_b32 m0, %2\n\ts_nop 0\n\tglobal_load_lds_dwordx4 %0, %1" :: "v"(vo[2 * (n - 1) + k / 2]), "s"(nsrc), "s"(ringp(ring, (j & 1) * 12288 + (2 * (n - 1) + k / 2) * 1024)) : "memory");
                }
                if constexpr (n == 7 && j < 4) lda(j + 1, k);
                if constexpr (q >= 0) {
                    if (k == 3) { PIN(cc[q & 1][0]); o[0] = pk2bf(cc[q & 1][0][0], cc[q & 1][0][1]); o[1] = pk2bf(cc[q & 1][0][2], cc[q & 1][0][3]); }
                    if (k == 4) { PIN(cc[q & 1][1]); o[2] = pk2bf(cc[q & 1][1][0], cc[q & 1][1][1]); o[3] = pk2bf(cc[q & 1][1][2], cc[q & 1][1][3]); }
                    if (k == 5) __builtin_amdgcn_global_store_b128((gu32x4*)(dst + (u32)(lane * 16) + (q % 8 - 4) * 1024), o, "agent");
                }
                SCHEDB();
            }
        });
        VM0(); RAW_BARRIER();
        if (w == 0) st4_lane0(wsp(p) + O_FL + (400 + set * 80 + c) * 128, 1u);
        dma_kb<10>(xn + (size_t)set * 40960 + w * 10240, lds0 + C_XN + w * 10240, lane * 16);
        if (w == 0) {
            __builtin_amdgcn_s_sleep(8);
            const u32 off[1] = {(u32)(400 + (lane & 31) * 5 + (c >> 4)) * 128};
            wait_flags<1, FSL>(wsp(p) + O_FL, off, cap);
        }
        RAW_BARRIER();

    }
    bf16v WA[32], WV[8];
    {
        u32 vo = (w * 8 + (fr >> 3)) * 819200 + g * 256 + (fr & 7) * 16;
        asm volatile("" : "+v"(vo));
        auto ldw = [&](const int ks, const int t) {
            const char* a = wsp(p) + O_WVO + (size_t)(t * 1600 + ks * 80 + (c >> 1)) * 1024 + (c & 1) * 128;
            const char* b = a + 40960;
            asm volatile("" : "+s"(b));
            return cat8(__builtin_bit_cast(bf8, ld16(a, vo, 0)), __builtin_bit_cast(bf8, ld16(b, vo, 0)));
        };
#pragma unroll
        for (int i = 0; i < 32; ++i) WA[i] = ldw(i >> 2, i & 3);
#pragma unroll
        for (int i = 0; i < 8; ++i) WV[i] = ldw(8 + (i >> 2), i & 3);
    }
    auto yx = [&](f32x4 (&acc)[4][2], const int sy, const int tile, auto&& extra) {
        const char* xa = shm + tile + lane * 16;
        const char* xs = (sy + 2 < NSEG ? xn + (size_t)(sy + 2) * 40960 : (wsp(p) + O_WO) + cb * 81920) + w * 10240;
        u32 xl = lds0 + C_XN + (~(sy >> 1) & 1) * 40960 + w * 10240;
        bf8 x0 = *(const bf8*)(xa), x1 = *(const bf8*)(xa + 1024);
        const char* xs3[3] = {xs, xs + 4096, xs + 8192};
        u32 xl3[3] = {xl, xl + 4096, xl + 8192};
        asm volatile("" : "+s"(xs3[0]), "+s"(xs3[1]), "+s"(xs3[2]), "+s"(xl3[0]), "+s"(xl3[1]), "+s"(xl3[2]));
        static_for<0, 20>([&](auto K) {
            constexpr int k = CI(K);
            bf8 n0, n1;
            static_for<0, 4>([&](auto TT_) {
                constexpr int t = CI(TT_), id = (k >> 1) * 4 + t, dn = 2 * (k - 3) + t - 2;
                if constexpr (k < 19 && t == 0) n0 = *(const bf8*)(xa + (k + 1) * 2048);
                if constexpr (k < 19 && t == 1) n1 = *(const bf8*)(xa + (k + 1) * 2048 + 1024);
                if constexpr (t >= 2 && k >= 3 && k < 8) dma_one<(dn & 3) * 1024>(xs3[dn >> 2], xl3[dn >> 2], lane * 16);
                if constexpr (t >= 2) extra(2 * k + t - 2);
                SCHEDB();
                if constexpr (id < 32) {
                    const bf8 wf = (k & 1) ? hi8(WA[id]) : lo8(WA[id]);
                    if constexpr (k == 0) { MMA0_VA(acc[t][0], x0, wf); MMA0_VA(acc[t][1], x1, wf); } else { MMA_VA(acc[t][0], x0, wf); MMA_VA(acc[t][1], x1, wf); }
                } else {
                    const bf8 wf = (k & 1) ? hi8(WV[id - 32]) : lo8(WV[id - 32]);
                    MMA_VV(acc[t][0], x0, wf); MMA_VV(acc[t][1], x1, wf);
                }
                SCHEDB();
            });
            if constexpr (k < 19) { x0 = n0; x1 = n1; }
        });
    };

    for (int layer = 0; layer < 2; ++layer) {
    u32 ynext = ~0u;
    if (layer > 0 && set == 0) k1 = sched_get(~0u, mine + sch - 5 * SLOT + 62 * 128 + 8, cap);
    VM0(); RAW_BARRIER();

    if (layer > 0 && w == 0) st4_lane0(wsp(p) + O_FL + (192 + rb * 10 + cb) * 128, 1u);
    {
        f32x4 ya[4][2];
        yx(ya, set, layer ? C_YL : C_XN, [](int) {});
        asm volatile("s_nop 7");
        RAW_BARRIER();
        if (layer > 0) *(bf8*)(yl0 + (w * 5 + 4) * 1024) = *(const bf8*)(shm + C_M0 + w * 1024 + lane * 16);
#pragma unroll
        for (int t = 0; t < 4; ++t) { PIN(ya[t][0]); PIN(ya[t][1]); *(bf8*)(yl0 + (w * 5 + t) * 1024) = pack8(ya[t][0], ya[t][1]); }
        RAW_BARRIER();
    }
    if (layer == 0) { phys = set ^ get_side(p, cap); mine += phys * 8192; other += (phys ^ 1) * 8192; }

    for (int s = set; s < NSEG; s += 2) {
        const int rot = (s >> 1) & 3;
        char* yl = yl0 + (rot & 1) * 20480;
        const char* mb0 = (s > 0 ? mine + (M_SLOT0 + rot * 8) * SLOT : p.hop + M0_OFF + phys * 8192) + 2 * w * SLOT + 2048;
        const char* mb1 = mb0 + SLOT;
        const char* pb[3];
#pragma unroll
        for (int i = 0; i < 3; ++i) pb[i] = mine + (P_SLOT0 + (rot * 4 + c / 20) * PCS + i) * SLOT + 4096;
        const char* ph = w == 1 ? pb[1] + 2048 : pb[2] + (w - 4) * 2048;
        char* dstb = other + (M_SLOT0 + (((s + 1) >> 1) & 3) * 8 + c / 40) * SLOT + (c % 40) * 128;
        asm volatile("" : "+s"(mb0), "+s"(mb1), "+s"(pb[0]), "+s"(pb[1]), "+s"(pb[2]), "+s"(ph), "+s"(dstb));
        u32 yt = 0;
        bool late = s == 0;
        if (s > 0) {
            yt = sched_get(ynext, mine + sch + (s - 1) * 128 + 4, cap);
            wait_rt(yt + CLAG);
        }
        auto ldy = [&](const int f) { return *(const bf8*)(yl + (f < 16 ? (f >> 2) * 5 + (f & 3) : (f - 16) * 5 + 4) * 1024); };
        bf8 Y0[14];
        if (s == 0 && layer > 0) {
#pragma unroll
            for (int i = 0; i < 14; ++i) Y0[i] = ldy(i);
        } else {
            f32x4 acc[4];
            u32x4 A[10];
            auto gather = [&]() {
                u32 vo = lane * 16;
                asm volatile("" : "+v"(vo) :: "memory");
#pragma unroll
                for (int ks = 0; ks < 10; ++ks) {
                    A[ks] = ld16(ks < 5 ? mb0 : mb1, vo, (ks % 5 - 2) * 1024);
                    SCHEDB();
                    if (ks < 4) ZERO4(acc[ks]);
                    if (ks < 9) asm volatile("s_nop %0" :: "n"(PACE));
                    SCHEDB();
                }
            };
            auto mproj = [&]() {
#pragma unroll
                for (int ks = 0; ks < 10; ++ks) {
#pragma unroll
                    for (int t = 0; t < 4; ++t) { if (ks < 8) SMM_A(acc[t], A[ks], WA[(ks * 4 + t) & 31], sidx); else SMM_V(acc[t], A[ks], WV[(ks * 4 + t) & 7], sidx); }
                }
                asm volatile("s_nop 7");
#pragma unroll
                for (int t = 0; t < 4; ++t) PIN(acc[t]);
            };
            gather();
#pragma unroll
            for (int i = 0; i < 14; ++i) Y0[i] = ldy(i);
            SCHEDB();
            mproj();
            if (__builtin_expect(__any(__builtin_isunordered(acc[0][0], acc[0][1]) || __builtin_isunordered(acc[0][2], acc[0][3])), 0)) {
                late = true;
                for (int tries = 1;; ++tries) {
                    gather();
                    u32 m = 0;
#pragma unroll
                    for (int ks = 0; ks < 10; ++ks) m = max4(m, A[ks]);
                    if (!__any(m == ~0u)) break;
                    if (tries >= cap) { cap = 1; break; }
                    __builtin_amdgcn_s_sleep(8);
                }
                mproj();
            }
#pragma unroll
            for (int pr = 0; pr < 2; ++pr) {
                float v[4];
#pragma unroll
                for (int i = 0; i < 4; ++i) v[i] = halfsum(acc[2 * pr][i], acc[2 * pr + 1][i]);
                *(u32x2*)(shm + C_YL + (rot & 1) * 20480 + (w * 5 + 4) * 1024 + ((2 * pr + (g >> 1)) * 16 + fr) * 16 + (g & 1) * 8) = (u32x2){pk2bf(v[0], v[1]), pk2bf(v[2], v[3])};
            }
        }
        if (s == 0) { VM0(); *(bf8*)(shm + C_M0 + w * 1024 + lane * 16) = *(const bf8*)(yl0 + (w * 5 + 4) * 1024); }
        u32 took = 0, xt = yt + k1;
        if (w == 0 && s < NSEG - 1) {
            if (__builtin_expect(late, 0)) xt = sched_get(~0u, mine + sch + s * 128, cap);
            wait_rt(xt);
        }
        u32 vo = lane * 16;
        PIN(vo);
        RAW_BARRIER();

        if (s < NSEG - 1) {
            auto ldp = [&](const int f, const u32 v) { return ld16(pb[f >> 3], v, ((f & 7) - 4) * 1024); };
            f32x4 acc;
            if (w > 0) {
                const u32x4 P0 = ld16(ph, vo, 0);
                SCHEDB();
                const u32x4 P1 = ld16(ph, vo, 1024);
                SCHEDB();
                const bf8 Ya = ldy(12 + 2 * w), Yb = ldy(13 + 2 * w);
                u32 ex = lds0 + C_EX + (w - 1) * 512 + lane * 8;
                PIN(ex);
                MMA0_VV(acc, Ya, P0);
                MMA_VV(acc, Yb, P1);
                *(LDS3 f32x2*)(uintptr_t)ex = merge2(acc);
                RAW_BARRIER();
            } else {
                asm volatile("s_nop %0" :: "n"(W0D));
                u32x4 P[14];
#pragma unroll
                for (int i = 0; i < 14; ++i) { P[i] = ldp(i, vo); SCHEDB(); }
                u32 dst = g * 2 * SLOT + (lane & 7) * 16, exa = lds0 + C_EX + lane * 8;
                asm volatile("" : "+v"(dst), "+v"(exa));
                f32x2 h1, h2, h3;
#pragma unroll
                for (int i = 0; i < 14; ++i) {
                    if (i == 14 - LATEP) {
                        __builtin_amdgcn_s_barrier();
                        asm volatile("" ::: "memory");
                        h1 = *(LDS3 const f32x2*)(uintptr_t)exa; h2 = *(LDS3 const f32x2*)(uintptr_t)(exa + 512); h3 = *(LDS3 const f32x2*)(uintptr_t)(exa + 1024);
                        SCHEDB();
                    }
                    if (i == 0) MMA0_VV(acc, Y0[i], P[i]); else MMA_VV(acc, Y0[i], P[i]);
                    SCHEDB();
                }
                u32x4 o4;
                u64 bad;
                auto finish = [&]() {
                    asm volatile("v_add_f32 %0, %9, %11\n\tv_add_f32 %1, %10, %12\n\tv_add_f32 %0, %0, %13\n\tv_add_f32 %1, %1, %14\n\ts_nop 3\n\t"
                                 "v_permlane32_swap_b32 %5, %7\n\tv_permlane32_swap_b32 %6, %8\n\tv_add_f32 %0, %5, %0\n\tv_add_f32 %1, %6, %1\n\t"
                                 "v_add_f32_dpp %0, %7, %0 row_ror:8 row_mask:0xf bank_mask:0xf\n\tv_add_f32_dpp %1, %8, %1 row_ror:8 row_mask:0xf bank_mask:0xf\n\t"
                                 "v_cvt_pk_bf16_f32 %2, %0, %1\n\tv_cmp_u_f32 %4, %0, %1\n\tv_min_u32 %0, -2, %2\n\tv_min_u32 %1, -2, %2\n\ts_nop 1\n\t"
                                 "v_permlane32_swap_b32 %0, %1\n\tv_mov_b32 %2, %0\n\tv_mov_b32 %3, %1\n\ts_nop 0\n\tv_permlane16_swap_b32 %0, %2\n\tv_permlane16_swap_b32 %1, %3"
                                 : "=&v"(o4[0]), "=&v"(o4[1]), "=&v"(o4[2]), "=&v"(o4[3]), "=&s"(bad)
                                 : "v"(acc[0]), "v"(acc[1]), "v"(acc[2]), "v"(acc[3]), "v"(h1[0]), "v"(h1[1]), "v"(h2[0]), "v"(h2[1]), "v"(h3[0]), "v"(h3[1]), "s"(dstb));
                };
                finish();
                int tries = 0;
                if (__builtin_expect(bad != 0, 0)) {

                    wait_rt(sched_get(~0u, mine + sch + s * 128, cap));
                    while (true) {
                        u32 m = 0;
                        asm volatile("" : "+v"(vo) :: "memory");
                        u32x4 P[20];
#pragma unroll
                        for (int i = 0; i < 20; ++i) P[i] = ldp(i, vo);
                        f32x4 hh[4];
#pragma unroll
                        for (int i = 0; i < 20; ++i) {
                            const bf8 y = ldy(i);
                            const int k = i < 14 ? 0 : (i - 12) / 2;
                            if (i == 0 || i == 14 || i == 16 || i == 18) MMA0_VV(hh[k], y, P[i]); else MMA_VV(hh[k], y, P[i]);
                            m = max4(m, P[i]);
                            SCHEDB();
                        }
                        asm volatile("s_nop 7" : "+v"(hh[0]), "+v"(hh[1]), "+v"(hh[2]), "+v"(hh[3]));
                        acc = hh[0]; h1 = merge2(hh[1]); h2 = merge2(hh[2]); h3 = merge2(hh[3]);
                        ++tries;
                        if (!__any(m == ~0u)) break;
                        if (tries >= cap) { cap = 1; break; }
                        __builtin_amdgcn_s_sleep(4);
                    }
                    finish();
                }
                st16f(dstb, dst, o4, 0x00ff00ff00ff00ffull);
                took = tries == 0 && s > 0 ? rt32() - xt : 0;
            }
            if (s < 2 && layer > 0 && w == 3) wait_all_tokens(p, lane, cap);
            if (s == NSEG - 2 || s < 2) RAW_BARRIER();
        } else wait_rt((late ? rt32() : yt + k1) + T_PX);
        auto tokens = [&](auto MORE) {
            f32x4 oa[4], ya[4][2];
            u32x4 P[20];
            u32 vo = lane * 16 + w * 4096, vm = lane * 16 + 16384 + w * 1024;
            asm volatile("" : "+v"(vo), "+v"(vm));
            const char* pb[4];
#pragma unroll
            for (int i = 0; i < 4; ++i) { pb[i] = px + (size_t)(set * 4 + rot) * PX_BUF + i * 20480; asm volatile("" : "+s"(pb[i])); }
            auto ldp = [&](const int i) { P[i] = i % 5 == 4 ? ld16(pb[i / 5], vm, 0) : ld16(pb[i / 5], vo, (i % 5) * 1024); };
            int ln = lane;
            asm volatile("" : "+v"(ln));
            u32 rg, peek;
            u64 peek2;
            bf8 y[5];
            auto side = [&](const int i) {
                if (i == 4) rg = sched_peek((wsp(p) + O_XR) + layer * ACT + (size_t)s * 40960 + ((c >> 2) * 2 + (w >> 1)) * 1024 + (c & 2) * 256 + (w & 1) * 128 + (c & 1) * 4 + (u32)((ln & 2) * 128 + (ln >> 3) * 16 + (ln & 1) * 8));
                if (i >= 16 && i < 36) ldp(i - 16);
                if (i == 2 && CI(MORE) && w == 0) st4_lane0(mine + (T_SLOT + (c >> 6)) * SLOT + (c & 63) * 128, took);
                if (i == 15 && CI(MORE)) peek2 = __hip_atomic_load((const gu64*)(mine + sch + s * 128 + 8), __ATOMIC_RELAXED, __HIP_MEMORY_SCOPE_AGENT);
                if (i >= 35) y[i - 35] = *(const bf8*)(yl + (w * 5 + i - 35) * 1024);
            };
            if constexpr (CI(MORE)) yx(ya, s + 2, C_XN + (~rot & 1) * 40960, side);
            else {
#pragma unroll
                for (int i = 0; i < 40; ++i) side(i);
            }
            bf8 yn[4];
            auto outx = [&](const bool first) {
                if (!first) {
#pragma unroll
                    for (int i = 0; i < 20; ++i) PIN(P[i]);
                    NOPV();
                }
#pragma unroll
                for (int q = 0; q < 4; ++q)
#pragma unroll
                    for (int j = 0; j < 5; ++j) {
                        if (j == 0) MMA0_VV(oa[q], y[j], P[q * 5 + j]); else MMA_VV(oa[q], y[j], P[q * 5 + j]);
                        if (CI(MORE) && first && q * 5 + j >= 3 && q * 5 + j < 19) {
                            const int e = q * 5 + j - 3;
                            SCHEDB();
                            if ((e & 1) == 0) PIN(ya[e >> 2][(e >> 1) & 1]);
                            ((u32*)&yn[e >> 2])[e & 3] = pk2bf(ya[e >> 2][(e >> 1) & 1][(e & 1) * 2], ya[e >> 2][(e >> 1) & 1][(e & 1) * 2 + 1]);
                            if ((e & 3) == 3) *(bf8*)(yl0 + (~rot & 1) * 20480 + (w * 5 + (e >> 2)) * 1024) = yn[e >> 2];
                            SCHEDB();
                        }
                    }
                asm volatile("s_nop 7");
#pragma unroll
                for (int q = 0; q < 4; ++q) PIN(oa[q]);
            };
            outx(true);
            if (__builtin_expect(__any(__builtin_isunordered(oa[0][0], oa[1][0]) || __builtin_isunordered(oa[2][0], oa[3][0])), 0)) {
                for (int tries = 1;; ++tries) {
                    asm volatile("" : "+v"(vo), "+v"(vm) :: "memory");
                    u32 m = 0;
#pragma unroll
                    for (int i = 0; i < 20; ++i) ldp(i);
#pragma unroll
                    for (int i = 0; i < 20; ++i) m = max4(m, P[i]);
                    if (!__any(m == ~0u)) break;
                    if (tries >= cap) { cap = 1; break; }
                    __builtin_amdgcn_s_sleep(8);
                }
                outx(false);
            }
            if constexpr (!CI(MORE)) { if (c == 0 && w == 0) stamp2(p.hop, 48 + layer * 4 + set); }
            const bool own = (fr < 8) == (g < 2);
#pragma unroll
            for (int q = 0; q < 4; q += 2) {
                f32x4 o;
#pragma unroll
                for (int i = 0; i < 4; ++i) { const float t = fdpp<0x128>(oa[q + 1][i]); o[i] = own ? oa[q][i] : t; }
                *(f32x4*)(shm + C_RED + ((w * 2 + (g >> 1)) * 32 + (q + !own) * 8 + (fr & 7)) * 32 + (g & 1) * 16) = o;
            }
            RAW_BARRIER();

            if constexpr (CI(MORE)) {
                k1 = sched_get((u32)peek2, mine + sch + s * 128 + 8, cap);
                wait_rt(__builtin_amdgcn_readfirstlane((u32)(peek2 >> 32)) + k1 + RECV);
                peek = sched_peek(mine + sch + (s + 1) * 128 + 4);
                SCHEDB();
            }
            const char* rp = shm + C_RED + (ln & 4) * 1024 + w * 256 + (ln >> 3) * 32 + (ln & 3) * 8;
            f32x2 a = (*(const f32x2*)rp + *(const f32x2*)(rp + 1024)) + (*(const f32x2*)(rp + 2048) + *(const f32x2*)(rp + 3072));
            a[0] += fdpp<0x104>(a[0]); a[1] += fdpp<0x104>(a[1]);
            const u32 y2 = pk2bf(a[0], a[1]), d = nosent(pk2bf(bflo(rg) + bflo(y2), bfhi(rg) + bfhi(y2)));
            const u32x4 o4 = {d, (u32)__builtin_amdgcn_mov_dpp(d, 0x55, 0xf, 0xf, true), (u32)__builtin_amdgcn_mov_dpp(d, 0xaa, 0xf, 0xf, true), (u32)__builtin_amdgcn_mov_dpp(d, 0xff, 0xf, 0xf, true)};
            if constexpr (CI(MORE)) { ynext = __builtin_amdgcn_readfirstlane(peek); asm volatile("" : "+s"(ynext)); }
            st16m((wsp(p) + O_X1) + layer * ACT + (size_t)s * 40960 + c * 512 + w * 128, ln * 2, o4, 0x0101010101010101ull);
            if (w == 1) {
                u32x4 v;
                asm volatile("v_mov_b64 %0, -1\n\tv_mov_b64 %1, -1" : "=v"(((u64*)&v)[0]), "=v"(((u64*)&v)[1]));
                st16m(other + (M_SLOT0 + (((s + 7) >> 1) & 3) * 8 + c / 40) * SLOT + (c % 40) * 128, (ln >> 4) * 2 * SLOT + (ln & 7) * 16, v, 0x00ff00ff00ff00ffull);
            }
        };
        if (s + 2 >= NSEG) { tokens(std::integral_constant<int, 0>()); break; }
        tokens(std::integral_constant<int, 1>());
    }

    asm volatile("" : "+s"(cb), "+s"(rb));
    dma_kb<10>((wsp(p) + O_WO) + cb * 81920 + 40960 + w * 10240, lds0 + C_XN + 40960 + w * 10240, lane * 16);
    if (w == 0) { const u32 t = sched_get(~0u, mine + (T_SLOT + 1) * SLOT + (50 + layer * 4) * 128, cap); *(u32*)(shm + C_EX) = t; }
    VM0(); RAW_BARRIER();
    wait_rt(__builtin_amdgcn_readfirstlane(*(const u32*)(shm + C_EX)) + T_OB + OB_STEP * w + (layer == 0 ? OB_COLD + 8 * w : 0));
    {
        int ln = lane;
        asm volatile("" : "+v"(ln));
        u32x4 O[20], R[2];
        u32 dw[8];
        auto ready = [&](const int T) {
            const char* ob = (wsp(p) + O_OB) + layer * ACT + (size_t)T * 20480 + 16384;
            const u32 off[1] = {(u32)(ln & 31) * 128};
            wait_flags<1>(ob, off, cap);
        };
        auto load = [&](const int T) {
            const char* ob0 = (wsp(p) + O_OB) + layer * ACT + (size_t)T * 20480 + 4096;
            const char* ob1 = ob0 + 8192;
            const char* ob2 = ob0 + 12288;
            const char* xb = (wsp(p) + O_X1) + layer * ACT + (size_t)(T >> 1) * 40960 + cb * 4096 + (T & 1) * 256;
            u32 vo = ln * 16, vr = (ln >> 4) * 256 + (ln & 15) * 8, vx = (ln >> 4) * 1024 + (ln & 15) * 16;
            asm volatile("" : "+v"(vo), "+v"(vr), "+v"(vx), "+s"(ob0), "+s"(ob1), "+s"(ob2), "+s"(xb) :: "memory");
#pragma unroll
            for (int k = 0; k < 16; ++k) { O[k] = ld16(k < 8 ? ob0 : ob1, vo, ((k & 7) - 4) * 1024); SCHEDB(); }
#pragma unroll
            for (int k = 0; k < 4; ++k) {
                const u64 lo = __hip_atomic_load((const gu64*)(ob2 + vr + k * 1024), __ATOMIC_RELAXED, __HIP_MEMORY_SCOPE_AGENT);
                const u64 hi = __hip_atomic_load((const gu64*)(ob2 + vr + k * 1024 + 128), __ATOMIC_RELAXED, __HIP_MEMORY_SCOPE_AGENT);
                O[16 + k] = (u32x4){(u32)lo, (u32)(lo >> 32), (u32)hi, (u32)(hi >> 32)};
            }
            R[0] = ld16(xb, vx, 0); R[1] = ld16(xb, vx, 512);
            SCHEDB();
        };
        auto gemm = [&]() {
            u32 wa0 = lds0 + C_XN + ln * 16, wa1 = wa0 + 40960;
            asm volatile("" : "+v"(wa0), "+v"(wa1));
            f32x4 acc[4];
            bf8 wf[8];
#pragma unroll
            for (int t = 0; t < 8; ++t) wf[t] = *(LDS3 const bf8*)(uintptr_t)(wa0 + t * 1024);
#pragma unroll
            for (int k = 0; k < 20; ++k) {
                if (k >= 16) { PIN(O[k]); NOPV(); }
#pragma unroll
                for (int t = 0; t < 4; ++t) {
                    if (k == 0) MMA0_VV(acc[t], wf[t], O[k]); else MMA_VV(acc[t], wf[(k & 1) * 4 + t], O[k]);
                    SCHEDB();
                    if (k < 18) wf[(k & 1) * 4 + t] = *(LDS3 const bf8*)(uintptr_t)((k < 8 ? wa0 : wa1) + (((k + 2) % 10) * 4 + t) * 1024);
                    SCHEDB();
                }
            }
            asm volatile("s_nop 7" : "+v"(acc[0]), "+v"(acc[1]), "+v"(acc[2]), "+v"(acc[3]));
            float chk = 0.f;
#pragma unroll
            for (int i = 0; i < 8; ++i) {
                const u32 r = R[i >> 2][i & 3], y = pk2bf(acc[i >> 1][(i & 1) * 2], acc[i >> 1][(i & 1) * 2 + 1]);
                const float a = bflo(r) + bflo(y), b = bfhi(r) + bfhi(y);
                dw[i] = pk2bf(a, b);
                chk += a;
            }
            return __any(chk != chk);
        };
        const int T0 = rb + 16 * w;
        load(T0);
        static_for<0, 2>([&](auto PASS) {
            constexpr int pass = CI(PASS);
            const int T = T0 + 64 * pass;
            if (__builtin_expect(gemm(), 0)) {
                for (int tries = 0;; ++tries) {
                    ready(T); load(T);
                    u32 m = max4(max4(0, R[0]), R[1]);
#pragma unroll
                    for (int k = 0; k < 20; ++k) m = max4(m, O[k]);
                    if (!__any(m == ~0u)) break;
                    if (tries >= cap) { cap = 1; break; }
                    __builtin_amdgcn_s_sleep(6);
                }
                gemm();
            }
            if (layer == 1) {
                st32((char*)p.out + (size_t)T * 16 * DM * 2 + cb * 128, (ln & 15) * (DM * 2) + (ln >> 4) * 32, (u32x4){dw[0], dw[1], dw[2], dw[3]}, (u32x4){dw[4], dw[5], dw[6], dw[7]});
                if constexpr (pass == 0) load(T + 64);
            } else {
            float s1 = 0.f, s2 = 0.f;
#pragma unroll
            for (int i = 0; i < 8; ++i) { const float a = bflo(dw[i]), b = bfhi(dw[i]); s1 += a + b; s2 = fmaf(a, a, fmaf(b, b, s2)); }
            s1 = grp_sum(s1); s2 = grp_sum(s2);
            char* stl = (wsp(p) + O_ST) + (size_t)T * 1280;
            st8m(stl + cb * 128, ln * 8, (u32x2){nosent(__float_as_uint(s1)), nosent(__float_as_uint(s2))}, 0xffffull);
            const size_t fo = ACT + (size_t)(T >> 1) * 40960 + (cb * 4 + (T & 1)) * 1024;
            const u32 lo = (ln >> 5) * 2048 + ((ln >> 4) & 1) * 512 + (ln & 15) * 16;
            if constexpr (pass == 0) __builtin_amdgcn_s_sleep(ST_LAG);
            else {
                wait_first_tokens(p, set, lane, cap);
                dma_kb<10>(xn + ACT + (size_t)set * 40960 + w * 10240, lds0 + C_YL + w * 10240, lane * 16);
            }
            for (int spin = 0;; ++spin) {
                u32 m = 0;
                s1 = 0.f; s2 = 0.f;
#pragma unroll
                for (int i = 0; i < 3; ++i) {
                    const int k = (ln >> 4) + 4 * i;
                    const u64 v = __hip_atomic_load((const gu64*)(stl + min(k, 9) * 128 + (ln & 15) * 8), __ATOMIC_RELAXED, __HIP_MEMORY_SCOPE_AGENT);
                    m = max(m, max((u32)v, (u32)(v >> 32)));
                    if (k < 10) { s1 += __uint_as_float((u32)v); s2 += __uint_as_float((u32)(v >> 32)); }
                }
                if (!__any(m == ~0u)) break;
                if (spin >= cap) { cap = 1; break; }
                __builtin_amdgcn_s_sleep(2);
            }
            if constexpr (pass == 0) { if (w > 0) load(T + 64); }
            s1 = grp_sum(s1); s2 = grp_sum(s2);
            const float mu = s1 * (1.0f / DM), mr = rbf(mu), rs = ln_scale(fmaxf(fmaf(-mu, mu, s2 * (1.0f / DM)), 0.f));
            u32 dn[8];
#pragma unroll
            for (int i = 0; i < 8; ++i) dn[i] = ln_pair(dw[i], mr, rs);
            st16s((wsp(p) + O_XN) + fo, lo, (u32x4){dn[0], dn[4], dn[1], dn[5]}); st16s((wsp(p) + O_XN) + fo + 256, lo, (u32x4){dn[2], dn[6], dn[3], dn[7]});
            st16s((wsp(p) + O_XR) + fo, lo, (u32x4){dw[0], dw[4], dw[1], dw[5]}); st16s((wsp(p) + O_XR) + fo + 256, lo, (u32x4){dw[2], dw[6], dw[3], dw[7]});
            if constexpr (pass == 0) { if (w == 0) { load(T + 64); asm volatile("s_waitcnt vmcnt(18)" ::: "memory"); st4_lane0(wsp(p) + O_FL + (rb * 10 + cb) * 128, 1u); } }
            }
        });
    }
    if (layer == 1) break;
    sch += 5 * SLOT; xn += ACT;

    }
}

constexpr int B_PQ = 0, B_IM = 131072, B_CS = 155648, B_IS = 176;
__device__ __forceinline__ void bw_role(const LaParams& p, const int hb, char* shm) {
    const int lane = threadIdx.x & 63, w = __builtin_amdgcn_readfirstlane(threadIdx.x >> 6);
    const int fr = lane & 15, g = lane >> 4;
    int cap = SPIN_CAP;
    if (hb > 0) {
        const char* src = (const char*)((hb & 1) ? p.Wk : p.Wq) + (size_t)(DM - 16 * BPF + (hb >> 1) * BPF) * HD * 2 + w * (BPF * 3072);
        for (int i = 0; i < BPF * 3; ++i) dma_gather(src + i * 1024, (u32)(uintptr_t)(LDS3 char*)shm + w * 1024, lane * 16);
        VM0();
    }
    if (hb == 0 && w == 0) {
        u32 best[2] = {~0u, ~0u};
        const u32 tc = rt32();
        for (int i = 0; i < 256; ++i) {
#pragma unroll
            for (int sd = 0; sd < 2; ++sd) {
                u64 t0, t1;
                u32 z = 0, one = 1;
                asm volatile("s_memtime %0\n\ts_mov_b64 exec, 1\n\ts_waitcnt lgkmcnt(0)\n\tglobal_atomic_add %2, %3, %2, %4 sc0 sc1\n\ts_waitcnt vmcnt(0)\n\t"
                             "s_memtime %1\n\ts_mov_b64 exec, -1\n\ts_waitcnt lgkmcnt(0)"
                             : "=&s"(t0), "=&s"(t1), "+v"(one) : "v"(z), "s"(p.hop + M0_OFF + sd * 8192 + 6144 + (i & 3) * 256) : "memory");
                best[sd] = min(best[sd], (u32)(t1 - t0));
            }
            const u32 el = rt32() - tc, lo = min(best[0], best[1]), hi = max(best[0], best[1]);
            if (el > 1900u || (el > 600u && lo < 700u && hi - lo > 130u && hi - lo < 420u)) break;
        }
        u32 xcc; asm volatile("s_getreg_b32 %0, hwreg(HW_REG_XCC_ID)" : "=s"(xcc));
        st4_lane0(wsp(p) + O_FL + 384 * 128, ((xcc >> 2) & 1) ^ (best[1] < best[0] ? 1u : 0u));
    }
    bf8 WA[64], WV[16];
    {
        const int t = threadIdx.x;
        float* xs = (float*)(shm + 81920);
        float* ph = xs + 128 * 61;
        for (int i = t; i < 400; i += 256) ph[i] = bf2f(p.wphi[hb * 400 + i]);
        const int j = 4 * w + (fr & 3), G = fr >> 2;
        for (int d0 = 0; d0 < DM; d0 += 128) {
            u32 v15[15];
#pragma unroll
            for (int k = 0; k < 15; ++k) { const int i = t + k * 256; v15[k] = *(const u32*)(p.wqkv + (size_t)(d0 + i / 30) * 1920 + ((i % 30) / 10) * 640 + hb * 20 + (i % 10) * 2); }
            __syncthreads();
#pragma unroll
            for (int k = 0; k < 15; ++k) { const int i = t + k * 256; float* o = xs + (i / 30) * 61 + (i % 30) * 2; o[0] = bflo(v15[k]); o[1] = bfhi(v15[k]); }
            __syncthreads();
            for (int kk = 0; kk < 4; ++kk) {
                float v[8];
                const float* xr = xs + (kk * 32 + g * 8) * 61;
                if (j < 10) {
                    const int m = j / 5, f = 5 * G + j % 5;
#pragma unroll
                    for (int e = 0; e < 8; ++e) v[e] = 0.f;
#pragma unroll 4
                    for (int i = 0; i < 20; ++i) {
                        const float pw = ph[i * 20 + f];
#pragma unroll
                        for (int e = 0; e < 8; ++e) v[e] = fmaf(xr[e * 61 + m * 20 + i], pw, v[e]);
                    }
                } else {
#pragma unroll
                    for (int e = 0; e < 8; ++e) v[e] = j < 15 ? xr[e * 61 + 40 + 5 * G + min(j, 14) - 10] : 1.0f;
                }
                *(u32x4*)(shm + ((d0 / 32 + kk) * 4 + w) * 1024 + lane * 16) = (u32x4){pk2bf(v[0], v[1]), pk2bf(v[2], v[3]), pk2bf(v[4], v[5]), pk2bf(v[6], v[7])};
            }
        }
        __syncthreads();
#pragma unroll
        for (int i = 0; i < 64; ++i) WA[i] = *(const bf8*)(shm + i * 1024 + lane * 16);
#pragma unroll
        for (int i = 0; i < 16; ++i) WV[i] = *(const bf8*)(shm + (64 + i) * 1024 + lane * 16);
        __syncthreads();
    }
    auto proj = [&](f32x4 (&acc)[4], f32x4& sq, auto&& fetch) {
        u32x4 X[20];
#pragma unroll
        for (int k = 0; k < 12; ++k) { X[k] = fetch(k); SCHEDB(); }
#pragma unroll
        for (int k = 0; k < 20; ++k) {
#pragma unroll
            for (int t = 0; t < 4; ++t) {
                if (k == 0) MMA0_AV(acc[t], WA[t], X[k]); else if (k < 16) MMA_AV(acc[t], WA[(k * 4 + t) & 63], X[k]); else MMA_VV(acc[t], WV[(k * 4 + t) & 15], X[k]);
            }
            if (k == 0) MMA0_VV(sq, X[k], X[k]); else MMA_VV(sq, X[k], X[k]);
            SCHEDB();
            if (k < 8) X[k + 12] = fetch(k + 12);
            SCHEDB();
        }
        asm volatile("s_nop 7" : "+v"(acc[0]), "+v"(acc[1]), "+v"(acc[2]), "+v"(acc[3]), "+v"(sq));
    };
    {
        f32x4 t, cs[4];
        u32x4 o1 = {0x3f803f80u, 0x3f803f80u, 0x3f803f80u, 0x3f803f80u};
        asm volatile("s_nop 1" : "+v"(o1));
        proj(cs, t, [&](int) { return o1; });
#pragma unroll
        for (int i = 0; i < 4; ++i) *(f32x4*)(shm + B_CS + i * 1024 + lane * 16) = cs[i];
    }
    char* im = shm + B_IM + w * 6144;
    const u32 imr = (u32)(uintptr_t)(LDS3 char*)im + (4 * g + (fr >> 2)) * B_IS + (fr & 3) * 8;
    typedef __attribute__((ext_vector_type(32))) u32 u32x32;
    for (int layer = 0; layer < 2; ++layer) {
        const char* sch = p.hop + (hb & 1) * 8192 + (S_SLOT + layer * 5 + 4) * SLOT;
        f32x4 kv[3][2];
#pragma unroll
        for (int i = 0; i < 6; ++i) ZERO4(kv[i >> 1][i & 1]);
        u32x32 q5;
        u32 tn = 0;
        float vq[5];
        auto phi = [&](const float* x, u32 (&ph)[5]) {
            const float mx = grp_max(fmaxf(fmaxf(fmaxf(x[0], x[1]), fmaxf(x[2], x[3])), x[4]));
            const float mn = -grp_max(-fminf(fminf(fminf(x[0], x[1]), fminf(x[2], x[3])), x[4]));
            float e[10], a = 0.f, b = 0.f;
#pragma unroll
            for (int j = 0; j < 5; ++j) {
                e[j] = __builtin_amdgcn_exp2f((x[j] - mx) * 1.4426950408889634f); a += e[j];
                e[5 + j] = __builtin_amdgcn_exp2f((mn - x[j]) * 1.4426950408889634f); b += e[5 + j];
            }
            a = __builtin_amdgcn_rcpf(grp_sum(a)); b = __builtin_amdgcn_rcpf(grp_sum(b));
#pragma unroll
            for (int j = 0; j < 5; ++j) { e[j] *= a; e[5 + j] *= b; }
#pragma unroll
            for (int j = 0; j < 5; ++j) ph[j] = pk2bf(e[2 * j], e[2 * j + 1]);
        };
        auto phiq = [&](const int it) {
            u32 ph[5];
            phi(vq, ph);
            *(u32x4*)(shm + B_PQ + (w * 32 + it) * 1024 + lane * 16) = (u32x4){ph[0], ph[1], ph[2], ph[3]};
            q5[it] = ph[4];
        };
        for (int it = 0; it < 32; ++it) {
            const int s = 2 * it + (w >> 1);
            if (s + 2 < NSEG) { wait_rt(tn); tn = sched_get<BSL>(sched_peek(sch + (s + 1) * 128 + 4), sch + (s + 1) * 128 + 4, cap); wait_rt(tn + B_LAG); tn += BPEEK; }
            else wait_rt(sched_get(~0u, p.hop + (hb & 1) * 8192 + (T_SLOT + 1) * SLOT + (48 + layer * 4 + (s & 1)) * 128, cap) + T_X1);
            const char* xb = (wsp(p) + O_X1) + layer * ACT + (size_t)s * 40960 + (w & 1) * 256;
            f32x4 acc[4], sq;
            const char* xk[5];
#pragma unroll
            for (int i = 0; i < 5; ++i) { xk[i] = xb + i * 8192 + 4096; asm volatile("" : "+s"(xk[i])); }
            for (int pass = 0;; ++pass) {
                u32 vo = g * 512 + fr * 16;
                asm volatile("" : "+v"(vo) :: "memory");
                auto fetch = [&](const int k) { return ld16(xk[k >> 2], vo, ((k & 3) - 2) * 2048); };
                proj(acc, sq, fetch);
                if (pass > 0 || !__any(acc[3][3] != acc[3][3])) break;
                for (int tries = 0;; ++tries) {
                    asm volatile("" : "+v"(vo) :: "memory");
                    u32 m = 0;
#pragma unroll
                    for (int k = 0; k < 20; k += 10) {
#pragma unroll
                        for (int i = 0; i < 10; ++i) m = max4(m, fetch(k + i));
                        SCHEDB();
                    }
                    if (!__any(m == ~0u)) break;
                    if (tries >= cap) { cap = 1; break; }
                    __builtin_amdgcn_s_sleep(6);
                }
            }
            if (s == NSEG - 1 && hb == 5 && w == 3) stamp2(p.hop, 50 + layer * 4);
            const float dsel = (fr & 2) ? ((fr & 1) ? sq[3] : sq[2]) : ((fr & 1) ? sq[1] : sq[0]);
            const float s2 = grp_sum(g == (fr >> 2) ? dsel : 0.f);
            const float mu = acc[3][3] * (1.0f / DM), rs = __builtin_amdgcn_rsqf(fmaxf(fmaf(-mu, mu, s2 * (1.0f / DM)), 0.f) + 1e-5f);
            float v[15];
            f32x4 cs[4];
#pragma unroll
            for (int i = 0; i < 4; ++i) cs[i] = *(const f32x4*)(shm + B_CS + i * 1024 + lane * 16);
#pragma unroll
            for (int j = 0; j < 15; ++j) v[j] = fmaf(-mu, cs[j >> 2][j & 3], acc[j >> 2][j & 3]) * rs;
            u32 ph[5];
            phi(v + 5, ph);
            char* row = im + fr * B_IS;
            *(u32x2*)(row + g * 8) = (u32x2){ph[0], ph[1]};
            *(u32x2*)(row + 32 + g * 8) = (u32x2){ph[2], ph[3]};
            *(u32x2*)(row + 64 + g * 8) = (u32x2){ph[4], 0u};
            *(u32x2*)(row + 96 + g * 8) = (u32x2){pk2bf(v[10], v[11]), pk2bf(v[12], v[13])};
            *(u32x2*)(row + 128 + g * 8) = (u32x2){pk2bf(v[14], 1.0f), 0u};
            s16x4 tf[5];
#pragma unroll
            for (int i = 0; i < 5; ++i) tf[i] = __builtin_amdgcn_ds_read_tr16_b64_v4i16((LDS3 s16x4*)(uintptr_t)(imr + i * 32));
#pragma unroll
            for (int i = 0; i < 6; ++i) MMA16(kv[i >> 1][i & 1], tf[i >> 1], tf[3 + (i & 1)]);
#pragma unroll
            for (int j = 0; j < 5; ++j) vq[j] = v[j];
            if (it < 31) phiq(it);

        }
        asm volatile("s_nop 7" : "+v"(kv[0][0]), "+v"(kv[0][1]), "+v"(kv[1][0]), "+v"(kv[1][1]), "+v"(kv[2][0]), "+v"(kv[2][1]));
#pragma unroll
        for (int i = 0; i < 6; ++i) *(f32x4*)(im + i * 1024 + lane * 16) = kv[i >> 1][i & 1];
        RAW_BARRIER();
        bf8 ka[2];
        u32x2 kb[2];
#pragma unroll
        for (int dt = 0; dt < 2; ++dt) {
            f32x4 t[3];
#pragma unroll
            for (int ft = 0; ft < 3; ++ft) {
                const char* a = shm + B_IM + (ft * 2 + dt) * 1024 + lane * 16;
                t[ft] = (*(const f32x4*)a + *(const f32x4*)(a + 6144)) + (*(const f32x4*)(a + 12288) + *(const f32x4*)(a + 18432));
            }
            ka[dt] = pack8(t[0], t[1]);
            kb[dt] = (u32x2){pk2bf(t[2][0], t[2][1]), pk2bf(t[2][2], t[2][3])};
            PIN(ka[dt]); PIN(kb[dt]);
        }
        char* ob = (wsp(p) + O_OB) + layer * ACT + (size_t)w * 20480 + hb * 512;
        u32 rofs = g * 81920 + 16384 - hb * 384 + fr * 8, mofs = (g & 1) * 81920 + (g >> 1) * 256 + fr * 16, mofs2 = mofs + 2 * 81920;
        asm volatile("" : "+v"(rofs), "+v"(mofs), "+v"(mofs2));
        f32x4 o[2][4][2];
        bf8 qa[2][4];
        u32x2 qb[4];
        auto ldq = [&](const int gr) {
#pragma unroll
            for (int u = 0; u < 4; ++u) qa[gr & 1][u] = *(const bf8*)(shm + B_PQ + (w * 32 + gr * 4 + u) * 1024 + lane * 16);
        };
        auto mm = [&](const int gr, const int k) {
            SCHEDB();
            if (k < 4) { qb[k] = (u32x2){q5[gr * 4 + k], 0u}; PIN(qb[k]); }
            if (k < 8) MMA0_VV(o[gr & 1][k >> 1][k & 1], ka[k & 1], qa[gr & 1][k >> 1]);
            else MMA16(o[gr & 1][(k - 8) >> 1][k & 1], kb[k & 1], qb[(k - 8) >> 1]);
            SCHEDB();
        };
        ldq(0); ldq(1);
        NOPV();
#pragma unroll
        for (int k = 0; k < 16; ++k) mm(0, k);
        static_for<0, 8>([&](auto IT) {
            constexpr int gr = CI(IT);
            auto nx = [&](const int k) { if constexpr (gr < 7) mm(gr + 1, k); else { SCHEDB(); asm volatile("s_nop 0"); SCHEDB(); } };
            if constexpr (gr == 7) asm volatile("s_nop 7");
            u32 d[4][2];
            float x[4];
            nx(0); nx(1);
#pragma unroll
            for (int u = 0; u < 4; ++u) {
                f32x4& a = o[gr & 1][u][0];
                f32x4& b = o[gr & 1][u][1];
                PIN(a); PIN(b);
                float inv = __builtin_amdgcn_rcpf(b[1] + 1e-6f), y0, y1, y2, y3;
                nx(2 + 3 * u);
                asm("v_mul_f32 %0, %4, %8\n\tv_mul_f32 %1, %5, %8\n\tv_mul_f32 %2, %6, %8" : "=&v"(y0), "=&v"(y1), "=&v"(y2), "=&v"(y3) : "v"(a[0]), "v"(a[1]), "v"(a[2]), "v"(a[3]), "v"(inv));
                nx(3 + 3 * u);
                y3 = a[3] * inv; x[u] = b[0] * inv;
                d[u][0] = pk2bf(y0, y1);
                nx(4 + 3 * u);
                d[u][1] = pk2bf(y2, y3);
                if (u & 1) {
                    auto t0 = __builtin_amdgcn_permlane16_swap(d[u - 1][0], d[u][0], false, false);
                    auto t1 = __builtin_amdgcn_permlane16_swap(d[u - 1][1], d[u][1], false, false);
                    __builtin_amdgcn_global_store_b128((gu32x4*)(ob + (u == 1 ? mofs : mofs2)), (u32x4){nosent(t0[0]), nosent(t1[0]), nosent(t0[1]), nosent(t1[1])}, "agent");
                }
            }
            if constexpr (gr == 5) phiq(31);
            if constexpr (gr < 6) ldq(gr + 2);
            nx(14);
            auto a0 = __builtin_amdgcn_permlane32_swap(__float_as_uint(x[0]), __float_as_uint(x[2]), false, false);
            auto a1 = __builtin_amdgcn_permlane32_swap(__float_as_uint(x[1]), __float_as_uint(x[3]), false, false);
            nx(15);
            auto b0 = __builtin_amdgcn_permlane16_swap(a0[0], a1[0], false, false);
            auto b2 = __builtin_amdgcn_permlane16_swap(a0[1], a1[1], false, false);
            __hip_atomic_store((gu64*)(ob + rofs), (u64)nosent(pk2bf(__uint_as_float(b0[0]), __uint_as_float(b0[1]))) | ((u64)nosent(pk2bf(__uint_as_float(b2[0]), __uint_as_float(b2[1]))) << 32), __ATOMIC_RELAXED, __HIP_MEMORY_SCOPE_AGENT);
            ob += 4 * 81920;
            asm volatile("" : "+s"(ob));
        });
        RAW_BARRIER();

    }
}

__global__ void __launch_bounds__(256, 1) la_kernel(LaParams p) {
    extern __shared__ __attribute__((aligned(16))) char shm[];
    p.hop = align2m(p.ws + O_HOP);
    if (threadIdx.x == 0) {
        u32 xcc; asm volatile("s_getreg_b32 %0, hwreg(HW_REG_XCC_ID)" : "=s"(xcc));
        int role = 255;
        for (int a = 0; a < 8; ++a) {
            const u32 v = (xcc + a) & 7;
            const u32 k = __hip_atomic_fetch_add((gu32*)(((u32*)(p.ws + O_CTL)) + v * 32), 1u, __ATOMIC_RELAXED, __HIP_MEMORY_SCOPE_AGENT);
            if (k < 32) { role = v * 32 + k; break; }
        }
        *(int*)shm = role;
    }
    __syncthreads();
    const int role = __builtin_amdgcn_readfirstlane(*(int*)shm);
    __syncthreads();
    const int set = role >> 7, id = (role >> 5) & 3, k = role & 31;
    if (k < 8) qk_role(p, set, id * 8 + k, shm);
    else if (k < 28) comb_role(p, set, id * 20 + k - 8, shm);
    else bw_role(p, (role >> 5) * 4 + k - 28, shm);
}

#define PFN 1
struct PrepParams { const u16* W[2]; const u16* x; const u16* mem0; const u16* wo; char* ws; };
constexpr int N_PF = 128 * PFN, N_LN = 128, N_WO = 200, N_FILL = 512;
__global__ void __launch_bounds__(256) prep_kernel(PrepParams q) {
    int b = blockIdx.x;
    const int t = threadIdx.x, lane = t & 63, w = t >> 6, fr = lane & 15, g = lane >> 4;
    char* hop = align2m(q.ws + O_HOP);
    if (b < N_LN) {
        const int tok = b * 16 + w * 4 + g;
        u32x4 d[3][2];
        float s = 0.f;
#pragma unroll
        for (int i = 0; i < 3; ++i) {
            const u32x4* a = (const u32x4*)(q.x + (size_t)tok * DM + min(fr + 16 * i, 39) * 16);
            d[i][0] = a[0]; d[i][1] = a[1];
        }
#pragma unroll
        for (int i = 0; i < 3; ++i)
#pragma unroll
            for (int e = 0; e < 8; ++e) if (fr + 16 * i < 40) s += bflo(d[i][e >> 2][e & 3]) + bfhi(d[i][e >> 2][e & 3]);
        const float mu = row16_sum(s) * (1.0f / DM);
        float v = 0.f;
#pragma unroll
        for (int i = 0; i < 3; ++i)
#pragma unroll
            for (int e = 0; e < 8; ++e) if (fr + 16 * i < 40) { const float a = bflo(d[i][e >> 2][e & 3]) - mu, c = bfhi(d[i][e >> 2][e & 3]) - mu; v = fmaf(a, a, fmaf(c, c, v)); }
        const float rs = ln_scale(row16_sum(v) * (1.0f / DM)), mr = rbf(mu);
#pragma unroll
        for (int i = 0; i < 3; ++i) {
            const int blk = fr + 16 * i;
            if (blk >= 40) continue;
            const size_t o = (size_t)(tok >> 5) * 40960 + ((blk >> 1) * 2 + ((tok >> 4) & 1)) * 1024 + ((blk & 1) * 32 + (tok & 15)) * 16;
            u32 n[8];
#pragma unroll
            for (int e = 0; e < 8; ++e) n[e] = ln_pair(d[i][e >> 2][e & 3], mr, rs);
            *(u32x4*)(q.ws + O_XR + o) = (u32x4){d[i][0][0], d[i][1][0], d[i][0][1], d[i][1][1]};
            *(u32x4*)(q.ws + O_XR + o + 256) = (u32x4){d[i][0][2], d[i][1][2], d[i][0][3], d[i][1][3]};
            *(u32x4*)(q.ws + O_XN + o) = (u32x4){n[0], n[4], n[1], n[5]};
            *(u32x4*)(q.ws + O_XN + o + 256) = (u32x4){n[2], n[6], n[3], n[7]};
        }
        return;
    }
    b -= N_LN;
    if (b < N_PF) {
        const u32x4* src = (const u32x4*)q.W[b >> 7] + (size_t)(b & 127) * 3840 + t;
        u32x4 v[15];
#pragma unroll
        for (int i = 0; i < 15; ++i) v[i] = src[i * 256];
#pragma unroll
        for (int i = 0; i < 15; ++i) asm volatile("" :: "v"(v[i]));
        return;
    }
    b -= N_PF;
    if (b < N_WO) {
        const int cb = b / 20, kk = b % 20;
        const u16* col = q.wo + cb * 64 + 16 * (fr >> 2) + 4 * w + (fr & 3);
        u32 v[8];
#pragma unroll
        for (int e = 0; e < 8; ++e) {
            const int row = kk < 16 ? (2 * kk + (g >> 1)) * 20 + 5 * (2 * (g & 1) + (e >> 2)) + (e & 3) : (8 * (kk - 16) + 2 * g + (e >> 2)) * 20 + 5 * (e & 3) + 4;
            v[e] = col[(size_t)row * DM];
        }
        *(u32x4*)(q.ws + O_WO + (size_t)cb * 81920 + (kk * 4 + w) * 1024 + lane * 16) = (u32x4){v[0] | v[1] << 16, v[2] | v[3] << 16, v[4] | v[5] << 16, v[6] | v[7] << 16};
        return;
    }
    b -= N_WO;
    const u32x4 SENT = {~0u, ~0u, ~0u, ~0u};
    const u32 i0 = b * 256 + t;
    if (b == 0) ((u32*)(q.ws + O_CTL))[t] = 0;
    if (i0 < 8 * 2 * 320) {
        const int sl = i0 / 640, e = i0 % 320;
        const u32x4 v = *(const u32x4*)(q.mem0 + (e & 7) * DM + ((sl & 1) * 40 + (e >> 3)) * 8);
        st16(hop + M0_OFF + (i0 / 320) * 8192 + e * 16, (u32x4){nosent(v[0]), nosent(v[1]), nosent(v[2]), nosent(v[3])});
    }
    for (u32 i = i0; i < HOP_BYTES / 16; i += N_FILL * 256) st16(hop + (size_t)i * 16, SENT);
    for (u32 i = i0; i < (O_XN - O_FL) / 16; i += N_FILL * 256) st16(q.ws + O_FL + (size_t)i * 16, SENT);
}

extern "C" void kernel_launch(void* const* d_in, const int* in_sizes, int n_in,
                              void* d_out, int out_size, void* d_ws, size_t ws_size,
                              hipStream_t stream) {
    PrepParams q;
    q.x = (const u16*)d_in[0]; q.mem0 = (const u16*)d_in[1];
    q.wo = (const u16*)d_in[10];
    q.W[0] = (const u16*)d_in[5]; q.W[1] = (const u16*)d_in[4];
    q.ws = (char*)d_ws;
    prep_kernel<<<N_PF + N_LN + N_WO + N_FILL, 256, 0, stream>>>(q);
    LaParams p;
    p.ws = (char*)d_ws; p.out = (u16*)d_out; p.hop = (char*)d_ws;
    p.Wq = (const u16*)d_in[2]; p.Wk = (const u16*)d_in[3]; p.Wv = (const u16*)d_in[4]; p.Wo = (const u16*)d_in[5];
    p.wqkv = (const u16*)d_in[8]; p.wphi = (const u16*)d_in[9];
    (void)hipFuncSetAttribute((const void*)la_kernel, hipFuncAttributeMaxDynamicSharedMemorySize, LA_LDS);
    la_kernel<<<256, 256, LA_LDS, stream>>>(p);
}
```
